```python
import torch

def _softmax(x, axis=-1):
    return torch.softmax(x, dim=axis)
def _gelu(x):
    return torch.nn.functional.gelu(x, approximate="tanh")
def _silu(x):
    return torch.nn.functional.silu(x)
def _sigmoid(x):
    return torch.sigmoid(x)
def _layernorm(x, axis=-1, eps=1e-5):
    m = x.mean(dim=axis, keepdim=True)
    v = x.var(dim=axis, keepdim=True, unbiased=False)
    return (x - m) / torch.sqrt(v + eps)
def _scatter_add(tgt, idx, src, axis=0):
    return tgt.clone().index_add_(axis, idx, src)
def _segment_sum(src, seg, n):
    out = torch.zeros((n,) + src.shape[1:], dtype=src.dtype)
    return out.index_add_(0, seg, src)
def _pad(x, pw):
    flat = tuple(v for pair in reversed(pw) for v in pair)
    return torch.nn.functional.pad(x, flat)

kernel_name = 'standalone__normalfloat_lut_af4__prompt'

def setup_inputs():

    _w = (lambda a: (lambda t: t.to(torch.bfloat16) if t.is_floating_point() else t)(torch.as_tensor(a)))
    B, S, D, F = 1, 2048, 1024, 3584
    G = -1 if -1 > 0 else D
    D = (D // G) * G
    ng = D // G
    nf4 = torch.tensor([
        -1.0, -0.6961928009986877, -0.5250730514526367, -0.39491748809814453,
        -0.28444138169288635, -0.18477343022823334, -0.09105003625154495, 0.0,
        0.07958029955625534, 0.16093020141124725, 0.24611230194568634,
        0.33791524171829224, 0.44070982933044434, 0.5626170039176941,
        0.7229568362236023, 1.0,
    ], dtype=float)
    qw = torch.randint(0, 16, (D, F)).to(torch.int64)
    return {
        "x": _w((lambda *s: torch.randn(*s, dtype=torch.bfloat16) * 0.05)(B, S, D)),
        "qw": _w(qw),
        "lut": _w(nf4),
        "scales": _w(torch.abs((lambda *s: torch.randn(*s, dtype=torch.bfloat16) * 0.05)(ng, F)).to(float) + 0.01),
    }

def reference(x, qw, lut, scales):
    B, S, D = x.shape
    ng, F = scales.shape
    G = D // ng
    deq = lut[qw]
    s = (lambda x, n, axis: torch.repeat_interleave(x, n, dim=axis))(scales, G, axis=0)
    w = (lambda x, dt: x.to(dt))(deq, x.dtype) * (lambda x, dt: x.to(dt))(s, x.dtype)
    return torch.einsum("bsd,df->bsf", x, w)
```

```cpp
#include <hip/hip_runtime.h>
#include <stdint.h>

extern "C" __device__ unsigned long long llvm_dispatch_id() __asm("llvm.amdgcn.dispatch.id");

namespace {
typedef unsigned long long u64;
__global__ __launch_bounds__(512, 2)
void nf4_gemm_kernel(const void* q, u64 qd, const void* x, u64 xd, void* w, u64 wd, void* o, u64 od, const void* lut, const void* scales, void* dbg) {
    __shared__ __attribute__((aligned(16))) unsigned char smem[163840];
    asm volatile(
        ".Ln_start:\n"
        "s_load_dwordx16 s[8:23], s[2:3], 0x0\n"
        "s_load_dwordx4 s[24:27], s[2:3], 0x40\n"
        "v_readfirstlane_b32 s30, v0\n"
        "v_and_b32 v252, 63, v0\n"
        "s_getpc_b64 s[86:87]\n"
        ".Ln_here:\n"
        "s_cmp_lt_u32 s30, 448\n"
        "s_cbranch_scc1 1f\n"
        "s_lshr_b32 s35, s6, 3\n"
        "s_lshl_b32 s35, s35, 7\n"
        "v_lshl_add_u32 v3, v252, 12, s35\n"
        "v_min_u32 v3, .Ln_end-.Ln_here, v3\n"
        "global_load_dword v100, v3, s[86:87]\n"
        "1:\n"
        "s_and_b32 s32, s6, 3\n"
        "s_lshl_b32 s32, s32, 2\n"
        "s_bfe_u32 s35, s6, 0x20003\n"
        "s_or_b32 s32, s32, s35\n"
        "s_and_b32 s33, s6, 4\n"
        "s_lshl_b32 s33, s33, 1\n"
        "s_lshr_b32 s35, s6, 5\n"
        "s_or_b32 s33, s33, s35\n"
        "s_lshr_b32 s30, s30, 6\n"
        "s_and_b32 s31, s30, 3\n"
        "v_lshrrev_b32 v1, 3, v252\n"
        "v_and_b32 v2, 7, v252\n"
        "v_mul_u32_u24 v4, 0x1c000, v1\n"
        "v_lshl_add_u32 v249, v2, 4, v4\n"
        "s_cmp_ge_u32 s30, 4\n"
        "s_cbranch_scc1 .Ln_helper\n"
        "s_lshl_b32 s72, s30, 4\n"
        "s_add_u32 s72, s72, s32\n"
        "s_mul_i32 s35, s72, 9363\n"
        "s_lshr_b32 s35, s35, 16\n"
        "s_mul_i32 s36, s35, 7\n"
        "s_sub_u32 s36, s72, s36\n"
        "s_mul_i32 s40, s35, 917504\n"
        "s_mul_i32 s37, s33, 896\n"
        "s_lshl_b32 s38, s36, 7\n"
        "s_add_u32 s40, s40, s37\n"
        "s_add_u32 s40, s40, s38\n"
        "s_add_u32 s41, s40, 14336\n"
        "s_add_u32 s42, s41, 14336\n"
        "s_add_u32 s43, s42, 14336\n"
        "s_add_u32 s44, s43, 14336\n"
        "s_add_u32 s45, s44, 14336\n"
        "s_add_u32 s46, s45, 14336\n"
        "s_add_u32 s47, s46, 14336\n"
        "s_cmp_ge_u32 s30, 2\n"
        "s_cbranch_scc1 2f\n"
        "s_waitcnt lgkmcnt(0)\n"
        "buffer_load_dwordx4 v[112:115], v249, s[8:11], s40 offen\n"
        "buffer_load_dwordx4 v[116:119], v249, s[8:11], s41 offen\n"
        "buffer_load_dwordx4 v[120:123], v249, s[8:11], s42 offen\n"
        "buffer_load_dwordx4 v[124:127], v249, s[8:11], s43 offen\n"
        "buffer_load_dwordx4 v[128:131], v249, s[8:11], s44 offen\n"
        "buffer_load_dwordx4 v[132:135], v249, s[8:11], s45 offen\n"
        "buffer_load_dwordx4 v[136:139], v249, s[8:11], s46 offen\n"
        "buffer_load_dwordx4 v[140:143], v249, s[8:11], s47 offen\n"
        "2:\n"
        "s_waitcnt lgkmcnt(0)\n"
        "s_lshl_b32 s37, s31, 3\n"
        "s_load_dwordx8 s[48:55], s[24:25], 0x0\n"
        "s_load_dwordx2 s[56:57], s[24:25], s37\n"
        "s_lshl_b32 s75, s33, 4\n"
        "s_add_u32 s75, s75, s35\n"
        "s_lshl_b32 s75, s75, 3\n"
        "s_add_u32 s75, s75, s36\n"
        "s_lshl_b32 s75, s75, 11\n"
        "s_add_u32 s75, s75, 114688\n"
        "s_mul_i32 s34, s4, 0x7f4a7c15\n"
        "s_mul_hi_u32 s39, s4, 0x7f4a7c15\n"
        "s_mul_i32 s35, s4, 0x9e3779b9\n"
        "s_mul_i32 s36, s5, 0x7f4a7c15\n"
        "s_add_u32 s39, s39, s35\n"
        "s_add_u32 s39, s39, s36\n"
        "s_xor_b32 s34, s34, s0\n"
        "s_xor_b32 s39, s39, s1\n"
        "s_xor_b32 s34, s34, 0x5bd1e995\n"
        "s_mov_b32 s73, 0\n"
        "s_mov_b32 s74, 20000\n"
        "s_lshr_b32 s66, s31, 1\n"
        "s_and_b32 s67, s31, 1\n"
        "s_lshl_b32 s68, s66, 2\n"
        "s_add_u32 s68, s68, s67\n"
        "s_lshl_b32 s64, s33, 18\n"
        "s_add_u32 s65, s64, 0x4000\n"
        "v_and_b32 v184, 15, v252\n"
        "v_lshrrev_b32 v185, 4, v252\n"
        "v_and_b32 v186, 3, v1\n"
        "v_lshlrev_b32 v186, 8, v186\n"
        "v_lshl_add_u32 v186, v2, 4, v186\n"
        "v_lshrrev_b32 v250, 5, v252\n"
        "v_lshl_add_u32 v250, v250, 7, v186\n"
        "v_xor_b32 v251, 0xc0, v250\n"
        "v_mov_b32 v178, s34\n"
        "v_mov_b32 v179, s39\n"
        "v_mov_b32 v182, s34\n"
        "v_mov_b32 v183, s39\n"
        "v_and_b32 v192, 3, v1\n"
        "v_mul_u32_u24 v193, 0x202, v192\n"
        "v_add_u32 v193, 0x0c0c0100, v193\n"
        "v_lshlrev_b32 v192, 8, v192\n"
        "v_lshrrev_b32 v144, 5, v252\n"
        "v_lshl_add_u32 v192, v144, 12, v192\n"
        "v_mul_u32_u24 v144, 0x202, v144\n"
        "v_lshlrev_b32 v144, 16, v144\n"
        "v_add_u32 v144, 0x01000c0c, v144\n"
        "v_lshl_add_u32 v192, v2, 4, v192\n"
        "s_lshl_b32 s35, s31, 14\n"
        "v_add_u32 v192, s35, v192\n"
        "v_and_b32 v186, 1, v184\n"
        "v_lshrrev_b32 v187, 1, v184\n"
        "v_lshl_add_u32 v187, v185, 4, v187\n"
        "v_mul_u32_u24 v188, 0x4c, v186\n"
        "v_xor_b32 v187, v187, v188\n"
        "s_lshl_b32 s36, s68, 11\n"
        "s_add_u32 s36, s36, 114688\n"
        "v_lshl_add_u32 v232, v187, 4, s36\n"
        "v_xor_b32 v233, 0x80, v232\n"
        "v_lshrrev_b32 v187, 2, v184\n"
        "v_lshl_add_u32 v187, v185, 4, v187\n"
        "v_lshl_add_u32 v187, v186, 3, v187\n"
        "v_bfe_u32 v188, v184, 1, 1\n"
        "v_mul_u32_u24 v188, 0x4c, v188\n"
        "s_lshl_b32 s36, s67, 2\n"
        "v_xor_b32 v188, s36, v188\n"
        "v_xor_b32 v187, v188, v187\n"
        "s_mov_b32 s36, 120832\n"
        "v_lshl_add_u32 v235, v187, 4, s36\n"
        "s_lshl_b32 s35, s66, 13\n"
        "s_sub_u32 s35, s35, 2048\n"
        "v_add_u32 v234, s35, v235\n"
        "v_and_b32 v236, 31, v252\n"
        "v_lshlrev_b32 v236, 2, v236\n"
        "v_bfe_u32 v186, v184, 1, 3\n"
        "v_xor_b32 v186, v186, v185\n"
        "v_lshlrev_b32 v186, 4, v186\n"
        "v_lshl_add_u32 v186, v184, 7, v186\n"
        "s_lshl_b32 s35, s66, 13\n"
        "s_add_u32 s35, s35, 65536\n"
        "v_add_u32 v228, s35, v186\n"
        "v_xor_b32 v229, 64, v228\n"
        "v_xor_b32 v230, 0x2000, v228\n"
        "v_xor_b32 v231, 0x2000, v229\n"
        "s_waitcnt lgkmcnt(0)\n"
        "v_mov_b32 v194, s48\n"
        "v_mov_b32 v195, s50\n"
        "v_mov_b32 v196, s52\n"
        "v_mov_b32 v197, s54\n"
        "v_perm_b32 v194, s49, v194, v193\n"
        "v_perm_b32 v195, s51, v195, v193\n"
        "v_perm_b32 v196, s53, v196, v193\n"
        "v_perm_b32 v197, s55, v197, v193\n"
        "v_perm_b32 v198, s56, s56, v144\n"
        "v_perm_b32 v199, s57, s57, v144\n"
        "v_or_b32 v144, v194, v198\n"
        "v_or_b32 v145, v194, v198\n"
        "v_or_b32 v146, v194, v198\n"
        "v_or_b32 v147, v194, v198\n"
        "ds_write_b128 v192, v[144:147] offset:0\n"
        "v_or_b32 v148, v195, v198\n"
        "v_or_b32 v149, v195, v198\n"
        "v_or_b32 v150, v195, v198\n"
        "v_or_b32 v151, v195, v198\n"
        "ds_write_b128 v192, v[148:151] offset:1024\n"
        "v_or_b32 v152, v196, v198\n"
        "v_or_b32 v153, v196, v198\n"
        "v_or_b32 v154, v196, v198\n"
        "v_or_b32 v155, v196, v198\n"
        "ds_write_b128 v192, v[152:155] offset:2048\n"
        "v_or_b32 v156, v197, v198\n"
        "v_or_b32 v157, v197, v198\n"
        "v_or_b32 v158, v197, v198\n"
        "v_or_b32 v159, v197, v198\n"
        "ds_write_b128 v192, v[156:159] offset:3072\n"
        "v_or_b32 v160, v194, v199\n"
        "v_or_b32 v161, v194, v199\n"
        "v_or_b32 v162, v194, v199\n"
        "v_or_b32 v163, v194, v199\n"
        "ds_write_b128 v192, v[160:163] offset:8192\n"
        "v_or_b32 v164, v195, v199\n"
        "v_or_b32 v165, v195, v199\n"
        "v_or_b32 v166, v195, v199\n"
        "v_or_b32 v167, v195, v199\n"
        "ds_write_b128 v192, v[164:167] offset:9216\n"
        "v_or_b32 v168, v196, v199\n"
        "v_or_b32 v169, v196, v199\n"
        "v_or_b32 v170, v196, v199\n"
        "v_or_b32 v171, v196, v199\n"
        "ds_write_b128 v192, v[168:171] offset:10240\n"
        "v_or_b32 v172, v197, v199\n"
        "v_or_b32 v173, v197, v199\n"
        "v_or_b32 v174, v197, v199\n"
        "v_or_b32 v175, v197, v199\n"
        "ds_write_b128 v192, v[172:175] offset:11264\n"
        "s_mul_i32 s35, s33, 448\n"
        "s_lshl_b32 s36, s68, 6\n"
        "s_add_u32 s36, s36, s35\n"
        "v_lshl_add_u32 v108, v185, 4, s36\n"
        "s_lshl_b32 s36, s67, 5\n"
        "s_add_u32 s35, s35, s36\n"
        "v_lshl_add_u32 v109, v185, 3, s35\n"
        "s_lshl_b32 s36, s66, 8\n"
        "v_add_u32 v110, s36, v109\n"
        "v_and_b32 v186, 7, v184\n"
        "s_lshl_b32 s35, s66, 12\n"
        "v_lshl_add_u32 v187, v184, 8, s35\n"
        "s_add_u32 s36, s66, s67\n"
        "s_lshl_b32 s36, s36, 2\n"
        "v_add_u32 v188, s36, v185\n"
        "v_xor_b32 v188, v188, v186\n"
        "v_lshl_add_u32 v246, v188, 4, v187\n"
        "v_and_b32 v188, 1, v185\n"
        "v_lshl_add_u32 v187, v188, 3, v187\n"
        "v_lshrrev_b32 v185, 1, v185\n"
        "s_lshl_b32 s35, s67, 1\n"
        "s_lshl_b32 s36, s66, 2\n"
        "s_add_u32 s36, s36, s35\n"
        "s_add_u32 s36, s36, 8\n"
        "v_add_u32 v188, s36, v185\n"
        "v_xor_b32 v188, v188, v186\n"
        "v_lshl_add_u32 v247, v188, 4, v187\n"
        "s_mul_i32 s36, s66, 12\n"
        "s_sub_u32 s36, s35, s36\n"
        "s_add_u32 s36, s36, 12\n"
        "v_add_u32 v188, s36, v185\n"
        "v_xor_b32 v188, v188, v186\n"
        "v_lshl_add_u32 v248, v188, 4, v187\n"
        "s_lshl_b32 s35, s31, 2\n"
        "s_add_u32 s35, s35, 129024\n"
        "v_mov_b32 v237, s35\n"
        "s_mov_b32 s60, 0x0c0c0400\n"
        "s_mov_b32 s61, 0x0c0c0500\n"
        "s_mov_b32 s62, 0x0c0c0600\n"
        "s_mov_b32 s63, 0x0c0c0700\n"
        "global_load_dwordx4 v[238:241], v108, s[26:27]\n"
        "global_load_dwordx2 v[242:243], v110, s[26:27] offset:128\n"
        "global_load_dwordx2 v[244:245], v109, s[26:27] offset:192\n"
        "s_cmp_ge_u32 s30, 2\n"
        "s_cbranch_scc1 .Ln_none\n"
        "s_waitcnt vmcnt(3)\n"
        "v_lshl_add_u64 v[140:141], v[140:141], 4, v[136:137]\n"
        "v_lshl_add_u64 v[142:143], v[142:143], 4, v[138:139]\n"
        "v_lshl_add_u64 v[140:141], v[140:141], 4, v[132:133]\n"
        "v_lshl_add_u64 v[142:143], v[142:143], 4, v[134:135]\n"
        "v_lshl_add_u64 v[140:141], v[140:141], 4, v[128:129]\n"
        "v_lshl_add_u64 v[142:143], v[142:143], 4, v[130:131]\n"
        "v_lshl_add_u64 v[140:141], v[140:141], 4, v[124:125]\n"
        "v_lshl_add_u64 v[142:143], v[142:143], 4, v[126:127]\n"
        "v_lshl_add_u64 v[140:141], v[140:141], 4, v[120:121]\n"
        "v_lshl_add_u64 v[142:143], v[142:143], 4, v[122:123]\n"
        "v_lshl_add_u64 v[140:141], v[140:141], 4, v[116:117]\n"
        "v_lshl_add_u64 v[142:143], v[142:143], 4, v[118:119]\n"
        "v_lshl_add_u64 v[176:177], v[140:141], 4, v[112:113]\n"
        "v_lshl_add_u64 v[180:181], v[142:143], 4, v[114:115]\n"
        "s_nop 0\n"
        "v_permlane32_swap_b32 v176, v177\n"
        "v_permlane32_swap_b32 v180, v181\n"
        "buffer_store_dwordx4 v[176:179], v250, s[16:19], s75 offen sc1\n"
        "buffer_store_dwordx4 v[180:183], v251, s[16:19], s75 offen offset:1024 sc1\n"
        ".Ln_none:\n"
        "s_waitcnt lgkmcnt(0)\n"
        "s_barrier\n"
        "s_sleep 12\n"
        "buffer_load_dwordx4 v[144:147], v232, s[16:19], s64 offen sc1\n"
        "buffer_load_dwordx4 v[148:151], v233, s[16:19], s64 offen sc1\n"
        "buffer_load_dwordx4 v[152:155], v234, s[16:19], s64 offen sc1\n"
        "buffer_load_dwordx4 v[156:159], v235, s[16:19], s64 offen sc1\n"
        "buffer_load_dwordx4 v[208:211], v232, s[16:19], s65 offen sc1\n"
        "buffer_load_dwordx4 v[212:215], v233, s[16:19], s65 offen sc1\n"
        "buffer_load_dwordx4 v[216:219], v234, s[16:19], s65 offen sc1\n"
        "buffer_load_dwordx4 v[220:223], v235, s[16:19], s65 offen sc1\n"
        "s_branch .Ln_go\n"
        ".Ln_helper:\n"
        "s_lshl_b32 s35, s32, 2\n"
        "s_add_u32 s35, s35, s31\n"
        "s_lshl_b32 s35, s35, 16\n"
        "v_lshrrev_b32 v80, 4, v252\n"
        "v_xor_b32 v81, v80, v2\n"
        "v_lshl_add_u32 v82, v1, 11, s35\n"
        "v_lshl_add_u32 v8, v81, 4, v82\n"
        "v_xor_b32 v9, 64, v8\n"
        "v_add_u32 v9, 0x3c00, v9\n"
        "v_add_u32 v10, 0x7800, v8\n"
        "v_add_u32 v11, 0x7800, v9\n"
        "s_lshl_b32 s58, s31, 12\n"
        "s_add_u32 s58, s58, 65536\n"
        "s_waitcnt lgkmcnt(0)\n"
        "s_add_u32 m0, s58, 0\n"
        "s_movk_i32 s59, 0\n"
        "buffer_load_dwordx4 v8, s[12:15], s59 offen offset:0 lds\n"
        "buffer_load_dwordx4 v9, s[12:15], s59 offen offset:1024 lds\n"
        "buffer_load_dwordx4 v10, s[12:15], s59 offen offset:2048 lds\n"
        "buffer_load_dwordx4 v11, s[12:15], s59 offen offset:3072 lds\n"
        "s_mul_i32 s34, s4, 0x7f4a7c15\n"
        "s_mul_hi_u32 s39, s4, 0x7f4a7c15\n"
        "s_mul_i32 s35, s4, 0x9e3779b9\n"
        "s_mul_i32 s36, s5, 0x7f4a7c15\n"
        "s_add_u32 s39, s39, s35\n"
        "s_add_u32 s39, s39, s36\n"
        "s_xor_b32 s34, s34, s0\n"
        "s_xor_b32 s39, s39, s1\n"
        "s_xor_b32 s34, s34, 0x5bd1e995\n"
        "s_cmp_lt_u32 s31, 3\n"
        "s_cselect_b32 s35, 0, 0x7f000000\n"
        "s_lshl_b32 s64, s33, 18\n"
        "s_add_u32 s36, s58, 49152\n"
        "v_lshl_add_u32 v12, v252, 4, s36\n"
        "v_or_b32 v13, s35, v12\n"
        "v_and_b32 v83, 3, v1\n"
        "v_lshlrev_b32 v83, 8, v83\n"
        "v_lshl_add_u32 v83, v2, 4, v83\n"
        "v_lshrrev_b32 v250, 5, v252\n"
        "v_lshl_add_u32 v250, v250, 7, v83\n"
        "v_xor_b32 v251, 0xc0, v250\n"
        "v_mov_b32 v74, s34\n"
        "v_mov_b32 v75, s39\n"
        "v_mov_b32 v78, s34\n"
        "v_mov_b32 v79, s39\n"
        "v_mov_b32 v83, 0\n"
        "v_mov_b32 v84, 129024\n"
        "v_lshl_add_u32 v84, v252, 2, v84\n"
        "ds_write_b32 v84, v83\n"
        "v_and_b32 v81, 15, v252\n"
        "s_lshr_b32 s66, s31, 1\n"
        "s_and_b32 s67, s31, 1\n"
        "v_xor_b32 v82, v80, v81\n"
        "v_lshlrev_b32 v82, 4, v82\n"
        "v_lshl_add_u32 v82, v80, 8, v82\n"
        "s_lshl_b32 s35, s31, 11\n"
        "v_add_u32 v88, s35, v82\n"
        "v_xor_b32 v89, 64, v88\n"
        "s_lshl_b32 s35, s32, 4\n"
        "s_add_u32 s35, s35, s67\n"
        "s_mul_i32 s35, s35, 57344\n"
        "s_mul_i32 s36, s33, 448\n"
        "s_add_u32 s35, s35, s36\n"
        "s_mul_i32 s36, s66, 192\n"
        "s_add_u32 s35, s35, s36\n"
        "v_mul_u32_u24 v82, 0x1c00, v80\n"
        "v_lshl_add_u32 v82, v81, 4, v82\n"
        "v_add_u32 v90, s35, v82\n"
        "s_lshl_b32 s36, s66, 2\n"
        "v_subrev_u32 v81, s36, v81\n"
        "v_cmp_gt_u32 vcc, 12, v81\n"
        "v_mov_b32 v82, 0x7f000000\n"
        "s_lshl_b32 s35, s66, 3\n"
        "v_cndmask_b32 v91, v82, v90, vcc\n"
        "s_add_u32 s88, s35, 129024\n"
        "s_add_u32 s89, s88, 4\n"
        "v_mov_b32 v92, s88\n"
        "s_mul_i32 s84, s66, 458752\n"
        "s_sub_u32 s85, 458752, s84\n"
        "s_cmp_eq_u32 s31, 1\n"
        "s_cbranch_scc1 .Ln_h1\n"
        "s_cmp_eq_u32 s31, 2\n"
        "s_cbranch_scc1 .Ln_h2\n"
        "s_cmp_eq_u32 s31, 3\n"
        "s_cbranch_scc1 .Ln_h3\n"
        ".Ln_h0:\n"
        "s_add_u32 s72, s32, 32\n"
        "s_mul_i32 s35, s72, 9363\n"
        "s_lshr_b32 s35, s35, 16\n"
        "s_mul_i32 s36, s35, 7\n"
        "s_sub_u32 s36, s72, s36\n"
        "s_mul_i32 s40, s35, 917504\n"
        "s_mul_i32 s37, s33, 896\n"
        "s_lshl_b32 s38, s36, 7\n"
        "s_add_u32 s40, s40, s37\n"
        "s_add_u32 s40, s40, s38\n"
        "s_add_u32 s41, s40, 14336\n"
        "s_add_u32 s42, s41, 14336\n"
        "s_add_u32 s43, s42, 14336\n"
        "s_add_u32 s44, s43, 14336\n"
        "s_add_u32 s45, s44, 14336\n"
        "s_add_u32 s46, s45, 14336\n"
        "s_add_u32 s47, s46, 14336\n"
        "s_lshl_b32 s75, s33, 4\n"
        "s_add_u32 s75, s75, s35\n"
        "s_lshl_b32 s75, s75, 3\n"
        "s_add_u32 s75, s75, s36\n"
        "s_lshl_b32 s75, s75, 11\n"
        "s_add_u32 s75, s75, 114688\n"
        "buffer_load_dwordx4 v[40:43], v249, s[8:11], s40 offen\n"
        "buffer_load_dwordx4 v[44:47], v249, s[8:11], s41 offen\n"
        "buffer_load_dwordx4 v[48:51], v249, s[8:11], s42 offen\n"
        "buffer_load_dwordx4 v[52:55], v249, s[8:11], s43 offen\n"
        "buffer_load_dwordx4 v[56:59], v249, s[8:11], s44 offen\n"
        "buffer_load_dwordx4 v[60:63], v249, s[8:11], s45 offen\n"
        "buffer_load_dwordx4 v[64:67], v249, s[8:11], s46 offen\n"
        "buffer_load_dwordx4 v[68:71], v249, s[8:11], s47 offen\n"
        "s_waitcnt vmcnt(8)\n"
        "s_waitcnt lgkmcnt(0)\n"
        "s_barrier\n"
        "s_sleep 16\n"
        "s_add_u32 m0, s58, 16384\n"
        "s_movk_i32 s59, 128\n"
        "buffer_load_dwordx4 v8, s[12:15], s59 offen offset:0 lds\n"
        "buffer_load_dwordx4 v9, s[12:15], s59 offen offset:1024 lds\n"
        "buffer_load_dwordx4 v10, s[12:15], s59 offen offset:2048 lds\n"
        "buffer_load_dwordx4 v11, s[12:15], s59 offen offset:3072 lds\n"
        "s_add_u32 m0, s58, 81920\n"
        "s_add_u32 s76, s64, 32768\n"
        "buffer_load_dwordx4 v12, s[16:19], s76 offen offset:0 sc1 lds\n"
        "buffer_load_dwordx4 v12, s[16:19], s76 offen offset:1024 sc1 lds\n"
        "buffer_load_dwordx4 v12, s[16:19], s76 offen offset:2048 sc1 lds\n"
        "buffer_load_dwordx4 v12, s[16:19], s76 offen offset:3072 sc1 lds\n"
        "s_waitcnt vmcnt(8)\n"
        "v_lshl_add_u64 v[68:69], v[68:69], 4, v[64:65]\n"
        "v_lshl_add_u64 v[70:71], v[70:71], 4, v[66:67]\n"
        "v_lshl_add_u64 v[68:69], v[68:69], 4, v[60:61]\n"
        "v_lshl_add_u64 v[70:71], v[70:71], 4, v[62:63]\n"
        "v_lshl_add_u64 v[68:69], v[68:69], 4, v[56:57]\n"
        "v_lshl_add_u64 v[70:71], v[70:71], 4, v[58:59]\n"
        "v_lshl_add_u64 v[68:69], v[68:69], 4, v[52:53]\n"
        "v_lshl_add_u64 v[70:71], v[70:71], 4, v[54:55]\n"
        "v_lshl_add_u64 v[68:69], v[68:69], 4, v[48:49]\n"
        "v_lshl_add_u64 v[70:71], v[70:71], 4, v[50:51]\n"
        "v_lshl_add_u64 v[68:69], v[68:69], 4, v[44:45]\n"
        "v_lshl_add_u64 v[70:71], v[70:71], 4, v[46:47]\n"
        "v_lshl_add_u64 v[72:73], v[68:69], 4, v[40:41]\n"
        "v_lshl_add_u64 v[76:77], v[70:71], 4, v[42:43]\n"
        "s_nop 0\n"
        "v_permlane32_swap_b32 v72, v73\n"
        "v_permlane32_swap_b32 v76, v77\n"
        "buffer_store_dwordx4 v[72:75], v250, s[16:19], s75 offen sc1\n"
        "buffer_store_dwordx4 v[76:79], v251, s[16:19], s75 offen offset:1024 sc1\n"
        "s_add_u32 s72, s32, 96\n"
        "s_mul_i32 s35, s72, 9363\n"
        "s_lshr_b32 s35, s35, 16\n"
        "s_mul_i32 s36, s35, 7\n"
        "s_sub_u32 s36, s72, s36\n"
        "s_mul_i32 s40, s35, 917504\n"
        "s_mul_i32 s37, s33, 896\n"
        "s_lshl_b32 s38, s36, 7\n"
        "s_add_u32 s40, s40, s37\n"
        "s_add_u32 s40, s40, s38\n"
        "s_add_u32 s41, s40, 14336\n"
        "s_add_u32 s42, s41, 14336\n"
        "s_add_u32 s43, s42, 14336\n"
        "s_add_u32 s44, s43, 14336\n"
        "s_add_u32 s45, s44, 14336\n"
        "s_add_u32 s46, s45, 14336\n"
        "s_add_u32 s47, s46, 14336\n"
        "s_lshl_b32 s75, s33, 4\n"
        "s_add_u32 s75, s75, s35\n"
        "s_lshl_b32 s75, s75, 3\n"
        "s_add_u32 s75, s75, s36\n"
        "s_lshl_b32 s75, s75, 11\n"
        "s_add_u32 s75, s75, 114688\n"
        "v_mov_b32 v84, 129024\n"
        "s_movk_i32 s73, 64\n"
        "1:\n"
        "s_sub_u32 s73, s73, 1\n"
        "s_cbranch_scc1 2f\n"
        "s_sleep 1\n"
        "ds_read_b128 v[96:99], v84 offset:128\n"
        "s_waitcnt lgkmcnt(0)\n"
        "v_min3_u32 v96, v96, v97, v98\n"
        "v_min_u32 v96, v96, v99\n"
        "v_cmp_eq_u32 vcc, 0, v96\n"
        "s_cbranch_vccnz 1b\n"
        "2:\n"
        "s_add_u32 m0, s58, 32768\n"
        "s_movk_i32 s59, 256\n"
        "buffer_load_dwordx4 v8, s[12:15], s59 offen offset:0 lds\n"
        "buffer_load_dwordx4 v9, s[12:15], s59 offen offset:1024 lds\n"
        "buffer_load_dwordx4 v10, s[12:15], s59 offen offset:2048 lds\n"
        "buffer_load_dwordx4 v11, s[12:15], s59 offen offset:3072 lds\n"
        "s_add_u32 m0, s58, 49152\n"
        "s_add_u32 s76, s64, 49152\n"
        "buffer_load_dwordx4 v12, s[16:19], s76 offen offset:0 sc1 lds\n"
        "buffer_load_dwordx4 v12, s[16:19], s76 offen offset:1024 sc1 lds\n"
        "buffer_load_dwordx4 v12, s[16:19], s76 offen offset:2048 sc1 lds\n"
        "buffer_load_dwordx4 v12, s[16:19], s76 offen offset:3072 sc1 lds\n"
        "s_mov_b64 exec, 0\n"
        "s_add_u32 s94, s86, .Ln_tp0-.Ln_here\n"
        "s_addc_u32 s95, s87, 0\n"
        "s_swappc_b64 s[70:71], s[94:95]\n"
        "s_mov_b64 exec, -1\n"
        "s_waitcnt vmcnt(10)\n"
        "s_barrier\n"
        "s_add_u32 m0, s58, 0\n"
        "s_movk_i32 s59, 384\n"
        "buffer_load_dwordx4 v8, s[12:15], s59 offen offset:0 lds\n"
        "buffer_load_dwordx4 v9, s[12:15], s59 offen offset:1024 lds\n"
        "buffer_load_dwordx4 v10, s[12:15], s59 offen offset:2048 lds\n"
        "buffer_load_dwordx4 v11, s[12:15], s59 offen offset:3072 lds\n"
        "s_add_u32 m0, s58, 65536\n"
        "s_add_u32 s76, s64, 65536\n"
        "buffer_load_dwordx4 v12, s[16:19], s76 offen offset:0 sc1 lds\n"
        "buffer_load_dwordx4 v12, s[16:19], s76 offen offset:1024 sc1 lds\n"
        "buffer_load_dwordx4 v12, s[16:19], s76 offen offset:2048 sc1 lds\n"
        "buffer_load_dwordx4 v12, s[16:19], s76 offen offset:3072 sc1 lds\n"
        "s_waitcnt vmcnt(8)\n"
        "s_barrier\n"
        "s_add_u32 m0, s58, 81920\n"
        "s_add_u32 s76, s64, 81920\n"
        "buffer_load_dwordx4 v12, s[16:19], s76 offen offset:0 sc1 lds\n"
        "buffer_load_dwordx4 v12, s[16:19], s76 offen offset:1024 sc1 lds\n"
        "buffer_load_dwordx4 v12, s[16:19], s76 offen offset:2048 sc1 lds\n"
        "buffer_load_dwordx4 v12, s[16:19], s76 offen offset:3072 sc1 lds\n"
        "s_add_u32 m0, s58, 16384\n"
        "s_movk_i32 s59, 512\n"
        "buffer_load_dwordx4 v8, s[12:15], s59 offen offset:0 lds\n"
        "buffer_load_dwordx4 v9, s[12:15], s59 offen offset:1024 lds\n"
        "buffer_load_dwordx4 v10, s[12:15], s59 offen offset:2048 lds\n"
        "buffer_load_dwordx4 v11, s[12:15], s59 offen offset:3072 lds\n"
        "s_waitcnt vmcnt(8)\n"
        "s_barrier\n"
        "s_add_u32 m0, s58, 49152\n"
        "s_add_u32 s76, s64, 98304\n"
        "buffer_load_dwordx4 v12, s[16:19], s76 offen offset:0 sc1 lds\n"
        "buffer_load_dwordx4 v12, s[16:19], s76 offen offset:1024 sc1 lds\n"
        "buffer_load_dwordx4 v12, s[16:19], s76 offen offset:2048 sc1 lds\n"
        "buffer_load_dwordx4 v12, s[16:19], s76 offen offset:3072 sc1 lds\n"
        "s_add_u32 m0, s58, 32768\n"
        "s_movk_i32 s59, 640\n"
        "buffer_load_dwordx4 v8, s[12:15], s59 offen offset:0 lds\n"
        "buffer_load_dwordx4 v9, s[12:15], s59 offen offset:1024 lds\n"
        "buffer_load_dwordx4 v10, s[12:15], s59 offen offset:2048 lds\n"
        "buffer_load_dwordx4 v11, s[12:15], s59 offen offset:3072 lds\n"
        "buffer_load_dwordx4 v[40:43], v249, s[8:11], s40 offen\n"
        "buffer_load_dwordx4 v[44:47], v249, s[8:11], s41 offen\n"
        "buffer_load_dwordx4 v[48:51], v249, s[8:11], s42 offen\n"
        "buffer_load_dwordx4 v[52:55], v249, s[8:11], s43 offen\n"
        "buffer_load_dwordx4 v[56:59], v249, s[8:11], s44 offen\n"
        "buffer_load_dwordx4 v[60:63], v249, s[8:11], s45 offen\n"
        "buffer_load_dwordx4 v[64:67], v249, s[8:11], s46 offen\n"
        "buffer_load_dwordx4 v[68:71], v249, s[8:11], s47 offen\n"
        "s_waitcnt vmcnt(16)\n"
        "s_barrier\n"
        "s_add_u32 m0, s58, 65536\n"
        "s_add_u32 s76, s64, 114688\n"
        "buffer_load_dwordx4 v12, s[16:19], s76 offen offset:0 sc1 lds\n"
        "buffer_load_dwordx4 v12, s[16:19], s76 offen offset:1024 sc1 lds\n"
        "buffer_load_dwordx4 v12, s[16:19], s76 offen offset:2048 sc1 lds\n"
        "buffer_load_dwordx4 v12, s[16:19], s76 offen offset:3072 sc1 lds\n"
        "s_add_u32 m0, s58, 0\n"
        "s_movk_i32 s59, 768\n"
        "buffer_load_dwordx4 v8, s[12:15], s59 offen offset:0 lds\n"
        "buffer_load_dwordx4 v9, s[12:15], s59 offen offset:1024 lds\n"
        "buffer_load_dwordx4 v10, s[12:15], s59 offen offset:2048 lds\n"
        "buffer_load_dwordx4 v11, s[12:15], s59 offen offset:3072 lds\n"
        "s_waitcnt vmcnt(16)\n"
        "s_barrier\n"
        "s_add_u32 m0, s58, 81920\n"
        "s_add_u32 s76, s64, 131072\n"
        "buffer_load_dwordx4 v12, s[16:19], s76 offen offset:0 sc1 lds\n"
        "buffer_load_dwordx4 v12, s[16:19], s76 offen offset:1024 sc1 lds\n"
        "buffer_load_dwordx4 v12, s[16:19], s76 offen offset:2048 sc1 lds\n"
        "buffer_load_dwordx4 v12, s[16:19], s76 offen offset:3072 sc1 lds\n"
        "s_add_u32 m0, s58, 16384\n"
        "s_movk_i32 s59, 896\n"
        "buffer_load_dwordx4 v8, s[12:15], s59 offen offset:0 lds\n"
        "buffer_load_dwordx4 v9, s[12:15], s59 offen offset:1024 lds\n"
        "buffer_load_dwordx4 v10, s[12:15], s59 offen offset:2048 lds\n"
        "buffer_load_dwordx4 v11, s[12:15], s59 offen offset:3072 lds\n"
        "s_waitcnt vmcnt(16)\n"
        "v_lshl_add_u64 v[68:69], v[68:69], 4, v[64:65]\n"
        "v_lshl_add_u64 v[70:71], v[70:71], 4, v[66:67]\n"
        "v_lshl_add_u64 v[68:69], v[68:69], 4, v[60:61]\n"
        "v_lshl_add_u64 v[70:71], v[70:71], 4, v[62:63]\n"
        "v_lshl_add_u64 v[68:69], v[68:69], 4, v[56:57]\n"
        "v_lshl_add_u64 v[70:71], v[70:71], 4, v[58:59]\n"
        "v_lshl_add_u64 v[68:69], v[68:69], 4, v[52:53]\n"
        "v_lshl_add_u64 v[70:71], v[70:71], 4, v[54:55]\n"
        "v_lshl_add_u64 v[68:69], v[68:69], 4, v[48:49]\n"
        "v_lshl_add_u64 v[70:71], v[70:71], 4, v[50:51]\n"
        "v_lshl_add_u64 v[68:69], v[68:69], 4, v[44:45]\n"
        "v_lshl_add_u64 v[70:71], v[70:71], 4, v[46:47]\n"
        "v_lshl_add_u64 v[72:73], v[68:69], 4, v[40:41]\n"
        "v_lshl_add_u64 v[76:77], v[70:71], 4, v[42:43]\n"
        "s_nop 0\n"
        "v_permlane32_swap_b32 v72, v73\n"
        "v_permlane32_swap_b32 v76, v77\n"
        "buffer_store_dwordx4 v[72:75], v250, s[16:19], s75 offen sc1\n"
        "buffer_store_dwordx4 v[76:79], v251, s[16:19], s75 offen offset:1024 sc1\n"
        "s_waitcnt vmcnt(10)\n"
        "s_barrier\n"
        "s_add_u32 m0, s58, 49152\n"
        "s_add_u32 s76, s64, 147456\n"
        "buffer_load_dwordx4 v12, s[16:19], s76 offen offset:0 sc1 lds\n"
        "buffer_load_dwordx4 v12, s[16:19], s76 offen offset:1024 sc1 lds\n"
        "buffer_load_dwordx4 v12, s[16:19], s76 offen offset:2048 sc1 lds\n"
        "buffer_load_dwordx4 v12, s[16:19], s76 offen offset:3072 sc1 lds\n"
        "s_add_u32 m0, s58, 32768\n"
        "s_movk_i32 s59, 1024\n"
        "buffer_load_dwordx4 v8, s[12:15], s59 offen offset:0 lds\n"
        "buffer_load_dwordx4 v9, s[12:15], s59 offen offset:1024 lds\n"
        "buffer_load_dwordx4 v10, s[12:15], s59 offen offset:2048 lds\n"
        "buffer_load_dwordx4 v11, s[12:15], s59 offen offset:3072 lds\n"
        "s_waitcnt vmcnt(10)\n"
        "s_barrier\n"
        "s_add_u32 m0, s58, 65536\n"
        "s_add_u32 s76, s64, 163840\n"
        "buffer_load_dwordx4 v12, s[16:19], s76 offen offset:0 sc1 lds\n"
        "buffer_load_dwordx4 v12, s[16:19], s76 offen offset:1024 sc1 lds\n"
        "buffer_load_dwordx4 v12, s[16:19], s76 offen offset:2048 sc1 lds\n"
        "buffer_load_dwordx4 v12, s[16:19], s76 offen offset:3072 sc1 lds\n"
        "s_add_u32 m0, s58, 0\n"
        "s_movk_i32 s59, 1152\n"
        "buffer_load_dwordx4 v8, s[12:15], s59 offen offset:0 lds\n"
        "buffer_load_dwordx4 v9, s[12:15], s59 offen offset:1024 lds\n"
        "buffer_load_dwordx4 v10, s[12:15], s59 offen offset:2048 lds\n"
        "buffer_load_dwordx4 v11, s[12:15], s59 offen offset:3072 lds\n"
        "s_waitcnt vmcnt(8)\n"
        "s_barrier\n"
        "s_add_u32 m0, s58, 81920\n"
        "s_add_u32 s76, s64, 180224\n"
        "buffer_load_dwordx4 v12, s[16:19], s76 offen offset:0 sc1 lds\n"
        "buffer_load_dwordx4 v12, s[16:19], s76 offen offset:1024 sc1 lds\n"
        "buffer_load_dwordx4 v12, s[16:19], s76 offen offset:2048 sc1 lds\n"
        "buffer_load_dwordx4 v12, s[16:19], s76 offen offset:3072 sc1 lds\n"
        "s_add_u32 m0, s58, 16384\n"
        "s_movk_i32 s59, 1280\n"
        "buffer_load_dwordx4 v8, s[12:15], s59 offen offset:0 lds\n"
        "buffer_load_dwordx4 v9, s[12:15], s59 offen offset:1024 lds\n"
        "buffer_load_dwordx4 v10, s[12:15], s59 offen offset:2048 lds\n"
        "buffer_load_dwordx4 v11, s[12:15], s59 offen offset:3072 lds\n"
        "s_waitcnt vmcnt(8)\n"
        "s_barrier\n"
        "s_add_u32 m0, s58, 49152\n"
        "s_add_u32 s76, s64, 196608\n"
        "buffer_load_dwordx4 v12, s[16:19], s76 offen offset:0 sc1 lds\n"
        "buffer_load_dwordx4 v12, s[16:19], s76 offen offset:1024 sc1 lds\n"
        "buffer_load_dwordx4 v12, s[16:19], s76 offen offset:2048 sc1 lds\n"
        "buffer_load_dwordx4 v12, s[16:19], s76 offen offset:3072 sc1 lds\n"
        "s_add_u32 m0, s58, 32768\n"
        "s_movk_i32 s59, 1408\n"
        "buffer_load_dwordx4 v8, s[12:15], s59 offen offset:0 lds\n"
        "buffer_load_dwordx4 v9, s[12:15], s59 offen offset:1024 lds\n"
        "buffer_load_dwordx4 v10, s[12:15], s59 offen offset:2048 lds\n"
        "buffer_load_dwordx4 v11, s[12:15], s59 offen offset:3072 lds\n"
        "s_waitcnt vmcnt(8)\n"
        "s_barrier\n"
        "s_add_u32 m0, s58, 65536\n"
        "s_add_u32 s76, s64, 212992\n"
        "buffer_load_dwordx4 v12, s[16:19], s76 offen offset:0 sc1 lds\n"
        "buffer_load_dwordx4 v12, s[16:19], s76 offen offset:1024 sc1 lds\n"
        "buffer_load_dwordx4 v12, s[16:19], s76 offen offset:2048 sc1 lds\n"
        "buffer_load_dwordx4 v12, s[16:19], s76 offen offset:3072 sc1 lds\n"
        "s_add_u32 m0, s58, 0\n"
        "s_movk_i32 s59, 1536\n"
        "buffer_load_dwordx4 v8, s[12:15], s59 offen offset:0 lds\n"
        "buffer_load_dwordx4 v9, s[12:15], s59 offen offset:1024 lds\n"
        "buffer_load_dwordx4 v10, s[12:15], s59 offen offset:2048 lds\n"
        "buffer_load_dwordx4 v11, s[12:15], s59 offen offset:3072 lds\n"
        "s_waitcnt vmcnt(8)\n"
        "s_barrier\n"
        "s_add_u32 m0, s58, 81920\n"
        "s_add_u32 s76, s64, 229376\n"
        "buffer_load_dwordx4 v12, s[16:19], s76 offen offset:0 sc1 lds\n"
        "buffer_load_dwordx4 v12, s[16:19], s76 offen offset:1024 sc1 lds\n"
        "buffer_load_dwordx4 v12, s[16:19], s76 offen offset:2048 sc1 lds\n"
        "buffer_load_dwordx4 v12, s[16:19], s76 offen offset:3072 sc1 lds\n"
        "s_add_u32 m0, s58, 16384\n"
        "s_movk_i32 s59, 1664\n"
        "buffer_load_dwordx4 v8, s[12:15], s59 offen offset:0 lds\n"
        "buffer_load_dwordx4 v9, s[12:15], s59 offen offset:1024 lds\n"
        "buffer_load_dwordx4 v10, s[12:15], s59 offen offset:2048 lds\n"
        "buffer_load_dwordx4 v11, s[12:15], s59 offen offset:3072 lds\n"
        "s_waitcnt vmcnt(8)\n"
        "s_barrier\n"
        "s_add_u32 m0, s58, 49152\n"
        "s_add_u32 s76, s64, 245760\n"
        "buffer_load_dwordx4 v12, s[16:19], s76 offen offset:0 sc1 lds\n"
        "buffer_load_dwordx4 v12, s[16:19], s76 offen offset:1024 sc1 lds\n"
        "buffer_load_dwordx4 v12, s[16:19], s76 offen offset:2048 sc1 lds\n"
        "buffer_load_dwordx4 v12, s[16:19], s76 offen offset:3072 sc1 lds\n"
        "s_add_u32 m0, s58, 32768\n"
        "s_movk_i32 s59, 1792\n"
        "buffer_load_dwordx4 v8, s[12:15], s59 offen offset:0 lds\n"
        "buffer_load_dwordx4 v9, s[12:15], s59 offen offset:1024 lds\n"
        "buffer_load_dwordx4 v10, s[12:15], s59 offen offset:2048 lds\n"
        "buffer_load_dwordx4 v11, s[12:15], s59 offen offset:3072 lds\n"
        "s_waitcnt vmcnt(8)\n"
        "s_barrier\n"
        "s_add_u32 m0, s58, 0\n"
        "s_movk_i32 s59, 1920\n"
        "buffer_load_dwordx4 v8, s[12:15], s59 offen offset:0 lds\n"
        "buffer_load_dwordx4 v9, s[12:15], s59 offen offset:1024 lds\n"
        "buffer_load_dwordx4 v10, s[12:15], s59 offen offset:2048 lds\n"
        "buffer_load_dwordx4 v11, s[12:15], s59 offen offset:3072 lds\n"
        "s_waitcnt vmcnt(0)\n"
        "s_barrier\n"
        "s_branch .Ln_ht\n"
        ".Ln_h1:\n"
        "s_add_u32 s72, s32, 48\n"
        "s_mul_i32 s35, s72, 9363\n"
        "s_lshr_b32 s35, s35, 16\n"
        "s_mul_i32 s36, s35, 7\n"
        "s_sub_u32 s36, s72, s36\n"
        "s_mul_i32 s40, s35, 917504\n"
        "s_mul_i32 s37, s33, 896\n"
        "s_lshl_b32 s38, s36, 7\n"
        "s_add_u32 s40, s40, s37\n"
        "s_add_u32 s40, s40, s38\n"
        "s_add_u32 s41, s40, 14336\n"
        "s_add_u32 s42, s41, 14336\n"
        "s_add_u32 s43, s42, 14336\n"
        "s_add_u32 s44, s43, 14336\n"
        "s_add_u32 s45, s44, 14336\n"
        "s_add_u32 s46, s45, 14336\n"
        "s_add_u32 s47, s46, 14336\n"
        "s_lshl_b32 s75, s33, 4\n"
        "s_add_u32 s75, s75, s35\n"
        "s_lshl_b32 s75, s75, 3\n"
        "s_add_u32 s75, s75, s36\n"
        "s_lshl_b32 s75, s75, 11\n"
        "s_add_u32 s75, s75, 114688\n"
        "s_waitcnt vmcnt(0)\n"
        "s_waitcnt lgkmcnt(0)\n"
        "s_barrier\n"
        "s_sleep 16\n"
        "s_add_u32 m0, s58, 16384\n"
        "s_movk_i32 s59, 128\n"
        "buffer_load_dwordx4 v8, s[12:15], s59 offen offset:0 lds\n"
        "buffer_load_dwordx4 v9, s[12:15], s59 offen offset:1024 lds\n"
        "buffer_load_dwordx4 v10, s[12:15], s59 offen offset:2048 lds\n"
        "buffer_load_dwordx4 v11, s[12:15], s59 offen offset:3072 lds\n"
        "s_add_u32 m0, s58, 81920\n"
        "s_add_u32 s76, s64, 32768\n"
        "buffer_load_dwordx4 v12, s[16:19], s76 offen offset:0 sc1 lds\n"
        "buffer_load_dwordx4 v12, s[16:19], s76 offen offset:1024 sc1 lds\n"
        "buffer_load_dwordx4 v12, s[16:19], s76 offen offset:2048 sc1 lds\n"
        "buffer_load_dwordx4 v12, s[16:19], s76 offen offset:3072 sc1 lds\n"
        "buffer_load_dwordx4 v[40:43], v249, s[8:11], s40 offen\n"
        "buffer_load_dwordx4 v[44:47], v249, s[8:11], s41 offen\n"
        "buffer_load_dwordx4 v[48:51], v249, s[8:11], s42 offen\n"
        "buffer_load_dwordx4 v[52:55], v249, s[8:11], s43 offen\n"
        "buffer_load_dwordx4 v[56:59], v249, s[8:11], s44 offen\n"
        "buffer_load_dwordx4 v[60:63], v249, s[8:11], s45 offen\n"
        "buffer_load_dwordx4 v[64:67], v249, s[8:11], s46 offen\n"
        "buffer_load_dwordx4 v[68:71], v249, s[8:11], s47 offen\n"
        "v_mov_b32 v84, 129024\n"
        "s_movk_i32 s73, 64\n"
        "1:\n"
        "s_sub_u32 s73, s73, 1\n"
        "s_cbranch_scc1 2f\n"
        "s_sleep 1\n"
        "ds_read_b128 v[96:99], v84 offset:128\n"
        "s_waitcnt lgkmcnt(0)\n"
        "v_min3_u32 v96, v96, v97, v98\n"
        "v_min_u32 v96, v96, v99\n"
        "v_cmp_eq_u32 vcc, 0, v96\n"
        "s_cbranch_vccnz 1b\n"
        "2:\n"
        "s_add_u32 m0, s58, 32768\n"
        "s_movk_i32 s59, 256\n"
        "buffer_load_dwordx4 v8, s[12:15], s59 offen offset:0 lds\n"
        "buffer_load_dwordx4 v9, s[12:15], s59 offen offset:1024 lds\n"
        "buffer_load_dwordx4 v10, s[12:15], s59 offen offset:2048 lds\n"
        "buffer_load_dwordx4 v11, s[12:15], s59 offen offset:3072 lds\n"
        "s_add_u32 m0, s58, 49152\n"
        "s_add_u32 s76, s64, 49152\n"
        "buffer_load_dwordx4 v12, s[16:19], s76 offen offset:0 sc1 lds\n"
        "buffer_load_dwordx4 v12, s[16:19], s76 offen offset:1024 sc1 lds\n"
        "buffer_load_dwordx4 v12, s[16:19], s76 offen offset:2048 sc1 lds\n"
        "buffer_load_dwordx4 v12, s[16:19], s76 offen offset:3072 sc1 lds\n"
        "s_mov_b64 exec, 0\n"
        "s_add_u32 s94, s86, .Ln_tp1-.Ln_here\n"
        "s_addc_u32 s95, s87, 0\n"
        "s_swappc_b64 s[70:71], s[94:95]\n"
        "s_mov_b64 exec, -1\n"
        "s_waitcnt vmcnt(16)\n"
        "s_barrier\n"
        "s_add_u32 m0, s58, 0\n"
        "s_movk_i32 s59, 384\n"
        "buffer_load_dwordx4 v8, s[12:15], s59 offen offset:0 lds\n"
        "buffer_load_dwordx4 v9, s[12:15], s59 offen offset:1024 lds\n"
        "buffer_load_dwordx4 v10, s[12:15], s59 offen offset:2048 lds\n"
        "buffer_load_dwordx4 v11, s[12:15], s59 offen offset:3072 lds\n"
        "s_add_u32 m0, s58, 65536\n"
        "s_add_u32 s76, s64, 65536\n"
        "buffer_load_dwordx4 v12, s[16:19], s76 offen offset:0 sc1 lds\n"
        "buffer_load_dwordx4 v12, s[16:19], s76 offen offset:1024 sc1 lds\n"
        "buffer_load_dwordx4 v12, s[16:19], s76 offen offset:2048 sc1 lds\n"
        "buffer_load_dwordx4 v12, s[16:19], s76 offen offset:3072 sc1 lds\n"
        "s_waitcnt vmcnt(16)\n"
        "v_lshl_add_u64 v[68:69], v[68:69], 4, v[64:65]\n"
        "v_lshl_add_u64 v[70:71], v[70:71], 4, v[66:67]\n"
        "v_lshl_add_u64 v[68:69], v[68:69], 4, v[60:61]\n"
        "v_lshl_add_u64 v[70:71], v[70:71], 4, v[62:63]\n"
        "v_lshl_add_u64 v[68:69], v[68:69], 4, v[56:57]\n"
        "v_lshl_add_u64 v[70:71], v[70:71], 4, v[58:59]\n"
        "v_lshl_add_u64 v[68:69], v[68:69], 4, v[52:53]\n"
        "v_lshl_add_u64 v[70:71], v[70:71], 4, v[54:55]\n"
        "v_lshl_add_u64 v[68:69], v[68:69], 4, v[48:49]\n"
        "v_lshl_add_u64 v[70:71], v[70:71], 4, v[50:51]\n"
        "v_lshl_add_u64 v[68:69], v[68:69], 4, v[44:45]\n"
        "v_lshl_add_u64 v[70:71], v[70:71], 4, v[46:47]\n"
        "v_lshl_add_u64 v[72:73], v[68:69], 4, v[40:41]\n"
        "v_lshl_add_u64 v[76:77], v[70:71], 4, v[42:43]\n"
        "s_nop 0\n"
        "v_permlane32_swap_b32 v72, v73\n"
        "v_permlane32_swap_b32 v76, v77\n"
        "buffer_store_dwordx4 v[72:75], v250, s[16:19], s75 offen sc1\n"
        "buffer_store_dwordx4 v[76:79], v251, s[16:19], s75 offen offset:1024 sc1\n"
        "s_waitcnt vmcnt(10)\n"
        "s_barrier\n"
        "s_add_u32 m0, s58, 81920\n"
        "s_add_u32 s76, s64, 81920\n"
        "buffer_load_dwordx4 v12, s[16:19], s76 offen offset:0 sc1 lds\n"
        "buffer_load_dwordx4 v12, s[16:19], s76 offen offset:1024 sc1 lds\n"
        "buffer_load_dwordx4 v12, s[16:19], s76 offen offset:2048 sc1 lds\n"
        "buffer_load_dwordx4 v12, s[16:19], s76 offen offset:3072 sc1 lds\n"
        "s_add_u32 m0, s58, 16384\n"
        "s_movk_i32 s59, 512\n"
        "buffer_load_dwordx4 v8, s[12:15], s59 offen offset:0 lds\n"
        "buffer_load_dwordx4 v9, s[12:15], s59 offen offset:1024 lds\n"
        "buffer_load_dwordx4 v10, s[12:15], s59 offen offset:2048 lds\n"
        "buffer_load_dwordx4 v11, s[12:15], s59 offen offset:3072 lds\n"
        "s_waitcnt vmcnt(10)\n"
        "s_barrier\n"
        "s_add_u32 m0, s58, 49152\n"
        "s_add_u32 s76, s64, 98304\n"
        "buffer_load_dwordx4 v12, s[16:19], s76 offen offset:0 sc1 lds\n"
        "buffer_load_dwordx4 v12, s[16:19], s76 offen offset:1024 sc1 lds\n"
        "buffer_load_dwordx4 v12, s[16:19], s76 offen offset:2048 sc1 lds\n"
        "buffer_load_dwordx4 v12, s[16:19], s76 offen offset:3072 sc1 lds\n"
        "s_add_u32 m0, s58, 32768\n"
        "s_movk_i32 s59, 640\n"
        "buffer_load_dwordx4 v8, s[12:15], s59 offen offset:0 lds\n"
        "buffer_load_dwordx4 v9, s[12:15], s59 offen offset:1024 lds\n"
        "buffer_load_dwordx4 v10, s[12:15], s59 offen offset:2048 lds\n"
        "buffer_load_dwordx4 v11, s[12:15], s59 offen offset:3072 lds\n"
        "s_waitcnt vmcnt(8)\n"
        "s_barrier\n"
        "s_add_u32 m0, s58, 65536\n"
        "s_add_u32 s76, s64, 114688\n"
        "buffer_load_dwordx4 v12, s[16:19], s76 offen offset:0 sc1 lds\n"
        "buffer_load_dwordx4 v12, s[16:19], s76 offen offset:1024 sc1 lds\n"
        "buffer_load_dwordx4 v12, s[16:19], s76 offen offset:2048 sc1 lds\n"
        "buffer_load_dwordx4 v12, s[16:19], s76 offen offset:3072 sc1 lds\n"
        "s_add_u32 m0, s58, 0\n"
        "s_movk_i32 s59, 768\n"
        "buffer_load_dwordx4 v8, s[12:15], s59 offen offset:0 lds\n"
        "buffer_load_dwordx4 v9, s[12:15], s59 offen offset:1024 lds\n"
        "buffer_load_dwordx4 v10, s[12:15], s59 offen offset:2048 lds\n"
        "buffer_load_dwordx4 v11, s[12:15], s59 offen offset:3072 lds\n"
        "s_waitcnt vmcnt(8)\n"
        "s_barrier\n"
        "s_add_u32 m0, s58, 81920\n"
        "s_add_u32 s76, s64, 131072\n"
        "buffer_load_dwordx4 v12, s[16:19], s76 offen offset:0 sc1 lds\n"
        "buffer_load_dwordx4 v12, s[16:19], s76 offen offset:1024 sc1 lds\n"
        "buffer_load_dwordx4 v12, s[16:19], s76 offen offset:2048 sc1 lds\n"
        "buffer_load_dwordx4 v12, s[16:19], s76 offen offset:3072 sc1 lds\n"
        "s_add_u32 m0, s58, 16384\n"
        "s_movk_i32 s59, 896\n"
        "buffer_load_dwordx4 v8, s[12:15], s59 offen offset:0 lds\n"
        "buffer_load_dwordx4 v9, s[12:15], s59 offen offset:1024 lds\n"
        "buffer_load_dwordx4 v10, s[12:15], s59 offen offset:2048 lds\n"
        "buffer_load_dwordx4 v11, s[12:15], s59 offen offset:3072 lds\n"
        "s_waitcnt vmcnt(8)\n"
        "s_barrier\n"
        "s_add_u32 m0, s58, 49152\n"
        "s_add_u32 s76, s64, 147456\n"
        "buffer_load_dwordx4 v12, s[16:19], s76 offen offset:0 sc1 lds\n"
        "buffer_load_dwordx4 v12, s[16:19], s76 offen offset:1024 sc1 lds\n"
        "buffer_load_dwordx4 v12, s[16:19], s76 offen offset:2048 sc1 lds\n"
        "buffer_load_dwordx4 v12, s[16:19], s76 offen offset:3072 sc1 lds\n"
        "s_add_u32 m0, s58, 32768\n"
        "s_movk_i32 s59, 1024\n"
        "buffer_load_dwordx4 v8, s[12:15], s59 offen offset:0 lds\n"
        "buffer_load_dwordx4 v9, s[12:15], s59 offen offset:1024 lds\n"
        "buffer_load_dwordx4 v10, s[12:15], s59 offen offset:2048 lds\n"
        "buffer_load_dwordx4 v11, s[12:15], s59 offen offset:3072 lds\n"
        "s_waitcnt vmcnt(8)\n"
        "s_barrier\n"
        "s_add_u32 m0, s58, 65536\n"
        "s_add_u32 s76, s64, 163840\n"
        "buffer_load_dwordx4 v12, s[16:19], s76 offen offset:0 sc1 lds\n"
        "buffer_load_dwordx4 v12, s[16:19], s76 offen offset:1024 sc1 lds\n"
        "buffer_load_dwordx4 v12, s[16:19], s76 offen offset:2048 sc1 lds\n"
        "buffer_load_dwordx4 v12, s[16:19], s76 offen offset:3072 sc1 lds\n"
        "s_add_u32 m0, s58, 0\n"
        "s_movk_i32 s59, 1152\n"
        "buffer_load_dwordx4 v8, s[12:15], s59 offen offset:0 lds\n"
        "buffer_load_dwordx4 v9, s[12:15], s59 offen offset:1024 lds\n"
        "buffer_load_dwordx4 v10, s[12:15], s59 offen offset:2048 lds\n"
        "buffer_load_dwordx4 v11, s[12:15], s59 offen offset:3072 lds\n"
        "s_waitcnt vmcnt(8)\n"
        "s_barrier\n"
        "s_add_u32 m0, s58, 81920\n"
        "s_add_u32 s76, s64, 180224\n"
        "buffer_load_dwordx4 v12, s[16:19], s76 offen offset:0 sc1 lds\n"
        "buffer_load_dwordx4 v12, s[16:19], s76 offen offset:1024 sc1 lds\n"
        "buffer_load_dwordx4 v12, s[16:19], s76 offen offset:2048 sc1 lds\n"
        "buffer_load_dwordx4 v12, s[16:19], s76 offen offset:3072 sc1 lds\n"
        "s_add_u32 m0, s58, 16384\n"
        "s_movk_i32 s59, 1280\n"
        "buffer_load_dwordx4 v8, s[12:15], s59 offen offset:0 lds\n"
        "buffer_load_dwordx4 v9, s[12:15], s59 offen offset:1024 lds\n"
        "buffer_load_dwordx4 v10, s[12:15], s59 offen offset:2048 lds\n"
        "buffer_load_dwordx4 v11, s[12:15], s59 offen offset:3072 lds\n"
        "s_waitcnt vmcnt(8)\n"
        "s_barrier\n"
        "s_add_u32 m0, s58, 49152\n"
        "s_add_u32 s76, s64, 196608\n"
        "buffer_load_dwordx4 v12, s[16:19], s76 offen offset:0 sc1 lds\n"
        "buffer_load_dwordx4 v12, s[16:19], s76 offen offset:1024 sc1 lds\n"
        "buffer_load_dwordx4 v12, s[16:19], s76 offen offset:2048 sc1 lds\n"
        "buffer_load_dwordx4 v12, s[16:19], s76 offen offset:3072 sc1 lds\n"
        "s_add_u32 m0, s58, 32768\n"
        "s_movk_i32 s59, 1408\n"
        "buffer_load_dwordx4 v8, s[12:15], s59 offen offset:0 lds\n"
        "buffer_load_dwordx4 v9, s[12:15], s59 offen offset:1024 lds\n"
        "buffer_load_dwordx4 v10, s[12:15], s59 offen offset:2048 lds\n"
        "buffer_load_dwordx4 v11, s[12:15], s59 offen offset:3072 lds\n"
        "s_waitcnt vmcnt(8)\n"
        "s_barrier\n"
        "s_add_u32 m0, s58, 65536\n"
        "s_add_u32 s76, s64, 212992\n"
        "buffer_load_dwordx4 v12, s[16:19], s76 offen offset:0 sc1 lds\n"
        "buffer_load_dwordx4 v12, s[16:19], s76 offen offset:1024 sc1 lds\n"
        "buffer_load_dwordx4 v12, s[16:19], s76 offen offset:2048 sc1 lds\n"
        "buffer_load_dwordx4 v12, s[16:19], s76 offen offset:3072 sc1 lds\n"
        "s_add_u32 m0, s58, 0\n"
        "s_movk_i32 s59, 1536\n"
        "buffer_load_dwordx4 v8, s[12:15], s59 offen offset:0 lds\n"
        "buffer_load_dwordx4 v9, s[12:15], s59 offen offset:1024 lds\n"
        "buffer_load_dwordx4 v10, s[12:15], s59 offen offset:2048 lds\n"
        "buffer_load_dwordx4 v11, s[12:15], s59 offen offset:3072 lds\n"
        "s_waitcnt vmcnt(8)\n"
        "s_barrier\n"
        "s_add_u32 m0, s58, 81920\n"
        "s_add_u32 s76, s64, 229376\n"
        "buffer_load_dwordx4 v12, s[16:19], s76 offen offset:0 sc1 lds\n"
        "buffer_load_dwordx4 v12, s[16:19], s76 offen offset:1024 sc1 lds\n"
        "buffer_load_dwordx4 v12, s[16:19], s76 offen offset:2048 sc1 lds\n"
        "buffer_load_dwordx4 v12, s[16:19], s76 offen offset:3072 sc1 lds\n"
        "s_add_u32 m0, s58, 16384\n"
        "s_movk_i32 s59, 1664\n"
        "buffer_load_dwordx4 v8, s[12:15], s59 offen offset:0 lds\n"
        "buffer_load_dwordx4 v9, s[12:15], s59 offen offset:1024 lds\n"
        "buffer_load_dwordx4 v10, s[12:15], s59 offen offset:2048 lds\n"
        "buffer_load_dwordx4 v11, s[12:15], s59 offen offset:3072 lds\n"
        "s_waitcnt vmcnt(8)\n"
        "s_barrier\n"
        "s_add_u32 m0, s58, 49152\n"
        "s_add_u32 s76, s64, 245760\n"
        "buffer_load_dwordx4 v12, s[16:19], s76 offen offset:0 sc1 lds\n"
        "buffer_load_dwordx4 v12, s[16:19], s76 offen offset:1024 sc1 lds\n"
        "buffer_load_dwordx4 v12, s[16:19], s76 offen offset:2048 sc1 lds\n"
        "buffer_load_dwordx4 v12, s[16:19], s76 offen offset:3072 sc1 lds\n"
        "s_add_u32 m0, s58, 32768\n"
        "s_movk_i32 s59, 1792\n"
        "buffer_load_dwordx4 v8, s[12:15], s59 offen offset:0 lds\n"
        "buffer_load_dwordx4 v9, s[12:15], s59 offen offset:1024 lds\n"
        "buffer_load_dwordx4 v10, s[12:15], s59 offen offset:2048 lds\n"
        "buffer_load_dwordx4 v11, s[12:15], s59 offen offset:3072 lds\n"
        "s_waitcnt vmcnt(8)\n"
        "s_barrier\n"
        "s_add_u32 m0, s58, 0\n"
        "s_movk_i32 s59, 1920\n"
        "buffer_load_dwordx4 v8, s[12:15], s59 offen offset:0 lds\n"
        "buffer_load_dwordx4 v9, s[12:15], s59 offen offset:1024 lds\n"
        "buffer_load_dwordx4 v10, s[12:15], s59 offen offset:2048 lds\n"
        "buffer_load_dwordx4 v11, s[12:15], s59 offen offset:3072 lds\n"
        "s_waitcnt vmcnt(0)\n"
        "s_barrier\n"
        "s_branch .Ln_ht\n"
        ".Ln_h2:\n"
        "s_add_u32 s72, s32, 64\n"
        "s_mul_i32 s35, s72, 9363\n"
        "s_lshr_b32 s35, s35, 16\n"
        "s_mul_i32 s36, s35, 7\n"
        "s_sub_u32 s36, s72, s36\n"
        "s_mul_i32 s40, s35, 917504\n"
        "s_mul_i32 s37, s33, 896\n"
        "s_lshl_b32 s38, s36, 7\n"
        "s_add_u32 s40, s40, s37\n"
        "s_add_u32 s40, s40, s38\n"
        "s_add_u32 s41, s40, 14336\n"
        "s_add_u32 s42, s41, 14336\n"
        "s_add_u32 s43, s42, 14336\n"
        "s_add_u32 s44, s43, 14336\n"
        "s_add_u32 s45, s44, 14336\n"
        "s_add_u32 s46, s45, 14336\n"
        "s_add_u32 s47, s46, 14336\n"
        "s_lshl_b32 s75, s33, 4\n"
        "s_add_u32 s75, s75, s35\n"
        "s_lshl_b32 s75, s75, 3\n"
        "s_add_u32 s75, s75, s36\n"
        "s_lshl_b32 s75, s75, 11\n"
        "s_add_u32 s75, s75, 114688\n"
        "s_waitcnt vmcnt(0)\n"
        "s_waitcnt lgkmcnt(0)\n"
        "s_barrier\n"
        "s_sleep 16\n"
        "s_add_u32 m0, s58, 16384\n"
        "s_movk_i32 s59, 128\n"
        "buffer_load_dwordx4 v8, s[12:15], s59 offen offset:0 lds\n"
        "buffer_load_dwordx4 v9, s[12:15], s59 offen offset:1024 lds\n"
        "buffer_load_dwordx4 v10, s[12:15], s59 offen offset:2048 lds\n"
        "buffer_load_dwordx4 v11, s[12:15], s59 offen offset:3072 lds\n"
        "s_add_u32 m0, s58, 81920\n"
        "s_add_u32 s76, s64, 32768\n"
        "buffer_load_dwordx4 v12, s[16:19], s76 offen offset:0 sc1 lds\n"
        "buffer_load_dwordx4 v12, s[16:19], s76 offen offset:1024 sc1 lds\n"
        "buffer_load_dwordx4 v12, s[16:19], s76 offen offset:2048 sc1 lds\n"
        "buffer_load_dwordx4 v12, s[16:19], s76 offen offset:3072 sc1 lds\n"
        "v_mov_b32 v84, 129024\n"
        "s_movk_i32 s73, 64\n"
        "1:\n"
        "s_sub_u32 s73, s73, 1\n"
        "s_cbranch_scc1 2f\n"
        "s_sleep 1\n"
        "ds_read_b128 v[96:99], v84 offset:128\n"
        "s_waitcnt lgkmcnt(0)\n"
        "v_min3_u32 v96, v96, v97, v98\n"
        "v_min_u32 v96, v96, v99\n"
        "v_cmp_eq_u32 vcc, 0, v96\n"
        "s_cbranch_vccnz 1b\n"
        "2:\n"
        "s_add_u32 m0, s58, 32768\n"
        "s_movk_i32 s59, 256\n"
        "buffer_load_dwordx4 v8, s[12:15], s59 offen offset:0 lds\n"
        "buffer_load_dwordx4 v9, s[12:15], s59 offen offset:1024 lds\n"
        "buffer_load_dwordx4 v10, s[12:15], s59 offen offset:2048 lds\n"
        "buffer_load_dwordx4 v11, s[12:15], s59 offen offset:3072 lds\n"
        "s_add_u32 m0, s58, 49152\n"
        "s_add_u32 s76, s64, 49152\n"
        "buffer_load_dwordx4 v12, s[16:19], s76 offen offset:0 sc1 lds\n"
        "buffer_load_dwordx4 v12, s[16:19], s76 offen offset:1024 sc1 lds\n"
        "buffer_load_dwordx4 v12, s[16:19], s76 offen offset:2048 sc1 lds\n"
        "buffer_load_dwordx4 v12, s[16:19], s76 offen offset:3072 sc1 lds\n"
        "s_mov_b64 exec, 0\n"
        "s_add_u32 s94, s86, .Ln_tp2-.Ln_here\n"
        "s_addc_u32 s95, s87, 0\n"
        "s_swappc_b64 s[70:71], s[94:95]\n"
        "s_mov_b64 exec, -1\n"
        "s_waitcnt vmcnt(8)\n"
        "s_barrier\n"
        "s_add_u32 m0, s58, 0\n"
        "s_movk_i32 s59, 384\n"
        "buffer_load_dwordx4 v8, s[12:15], s59 offen offset:0 lds\n"
        "buffer_load_dwordx4 v9, s[12:15], s59 offen offset:1024 lds\n"
        "buffer_load_dwordx4 v10, s[12:15], s59 offen offset:2048 lds\n"
        "buffer_load_dwordx4 v11, s[12:15], s59 offen offset:3072 lds\n"
        "s_add_u32 m0, s58, 65536\n"
        "s_add_u32 s76, s64, 65536\n"
        "buffer_load_dwordx4 v12, s[16:19], s76 offen offset:0 sc1 lds\n"
        "buffer_load_dwordx4 v12, s[16:19], s76 offen offset:1024 sc1 lds\n"
        "buffer_load_dwordx4 v12, s[16:19], s76 offen offset:2048 sc1 lds\n"
        "buffer_load_dwordx4 v12, s[16:19], s76 offen offset:3072 sc1 lds\n"
        "buffer_load_dwordx4 v[40:43], v249, s[8:11], s40 offen\n"
        "buffer_load_dwordx4 v[44:47], v249, s[8:11], s41 offen\n"
        "buffer_load_dwordx4 v[48:51], v249, s[8:11], s42 offen\n"
        "buffer_load_dwordx4 v[52:55], v249, s[8:11], s43 offen\n"
        "buffer_load_dwordx4 v[56:59], v249, s[8:11], s44 offen\n"
        "buffer_load_dwordx4 v[60:63], v249, s[8:11], s45 offen\n"
        "buffer_load_dwordx4 v[64:67], v249, s[8:11], s46 offen\n"
        "buffer_load_dwordx4 v[68:71], v249, s[8:11], s47 offen\n"
        "s_waitcnt vmcnt(16)\n"
        "s_barrier\n"
        "s_add_u32 m0, s58, 81920\n"
        "s_add_u32 s76, s64, 81920\n"
        "buffer_load_dwordx4 v12, s[16:19], s76 offen offset:0 sc1 lds\n"
        "buffer_load_dwordx4 v12, s[16:19], s76 offen offset:1024 sc1 lds\n"
        "buffer_load_dwordx4 v12, s[16:19], s76 offen offset:2048 sc1 lds\n"
        "buffer_load_dwordx4 v12, s[16:19], s76 offen offset:3072 sc1 lds\n"
        "s_add_u32 m0, s58, 16384\n"
        "s_movk_i32 s59, 512\n"
        "buffer_load_dwordx4 v8, s[12:15], s59 offen offset:0 lds\n"
        "buffer_load_dwordx4 v9, s[12:15], s59 offen offset:1024 lds\n"
        "buffer_load_dwordx4 v10, s[12:15], s59 offen offset:2048 lds\n"
        "buffer_load_dwordx4 v11, s[12:15], s59 offen offset:3072 lds\n"
        "s_waitcnt vmcnt(16)\n"
        "s_barrier\n"
        "s_add_u32 m0, s58, 49152\n"
        "s_add_u32 s76, s64, 98304\n"
        "buffer_load_dwordx4 v12, s[16:19], s76 offen offset:0 sc1 lds\n"
        "buffer_load_dwordx4 v12, s[16:19], s76 offen offset:1024 sc1 lds\n"
        "buffer_load_dwordx4 v12, s[16:19], s76 offen offset:2048 sc1 lds\n"
        "buffer_load_dwordx4 v12, s[16:19], s76 offen offset:3072 sc1 lds\n"
        "s_add_u32 m0, s58, 32768\n"
        "s_movk_i32 s59, 640\n"
        "buffer_load_dwordx4 v8, s[12:15], s59 offen offset:0 lds\n"
        "buffer_load_dwordx4 v9, s[12:15], s59 offen offset:1024 lds\n"
        "buffer_load_dwordx4 v10, s[12:15], s59 offen offset:2048 lds\n"
        "buffer_load_dwordx4 v11, s[12:15], s59 offen offset:3072 lds\n"
        "s_waitcnt vmcnt(16)\n"
        "v_lshl_add_u64 v[68:69], v[68:69], 4, v[64:65]\n"
        "v_lshl_add_u64 v[70:71], v[70:71], 4, v[66:67]\n"
        "v_lshl_add_u64 v[68:69], v[68:69], 4, v[60:61]\n"
        "v_lshl_add_u64 v[70:71], v[70:71], 4, v[62:63]\n"
        "v_lshl_add_u64 v[68:69], v[68:69], 4, v[56:57]\n"
        "v_lshl_add_u64 v[70:71], v[70:71], 4, v[58:59]\n"
        "v_lshl_add_u64 v[68:69], v[68:69], 4, v[52:53]\n"
        "v_lshl_add_u64 v[70:71], v[70:71], 4, v[54:55]\n"
        "v_lshl_add_u64 v[68:69], v[68:69], 4, v[48:49]\n"
        "v_lshl_add_u64 v[70:71], v[70:71], 4, v[50:51]\n"
        "v_lshl_add_u64 v[68:69], v[68:69], 4, v[44:45]\n"
        "v_lshl_add_u64 v[70:71], v[70:71], 4, v[46:47]\n"
        "v_lshl_add_u64 v[72:73], v[68:69], 4, v[40:41]\n"
        "v_lshl_add_u64 v[76:77], v[70:71], 4, v[42:43]\n"
        "s_nop 0\n"
        "v_permlane32_swap_b32 v72, v73\n"
        "v_permlane32_swap_b32 v76, v77\n"
        "buffer_store_dwordx4 v[72:75], v250, s[16:19], s75 offen sc1\n"
        "buffer_store_dwordx4 v[76:79], v251, s[16:19], s75 offen offset:1024 sc1\n"
        "s_waitcnt vmcnt(10)\n"
        "s_barrier\n"
        "s_add_u32 m0, s58, 65536\n"
        "s_add_u32 s76, s64, 114688\n"
        "buffer_load_dwordx4 v12, s[16:19], s76 offen offset:0 sc1 lds\n"
        "buffer_load_dwordx4 v12, s[16:19], s76 offen offset:1024 sc1 lds\n"
        "buffer_load_dwordx4 v12, s[16:19], s76 offen offset:2048 sc1 lds\n"
        "buffer_load_dwordx4 v12, s[16:19], s76 offen offset:3072 sc1 lds\n"
        "s_add_u32 m0, s58, 0\n"
        "s_movk_i32 s59, 768\n"
        "buffer_load_dwordx4 v8, s[12:15], s59 offen offset:0 lds\n"
        "buffer_load_dwordx4 v9, s[12:15], s59 offen offset:1024 lds\n"
        "buffer_load_dwordx4 v10, s[12:15], s59 offen offset:2048 lds\n"
        "buffer_load_dwordx4 v11, s[12:15], s59 offen offset:3072 lds\n"
        "s_waitcnt vmcnt(10)\n"
        "s_barrier\n"
        "s_add_u32 m0, s58, 81920\n"
        "s_add_u32 s76, s64, 131072\n"
        "buffer_load_dwordx4 v12, s[16:19], s76 offen offset:0 sc1 lds\n"
        "buffer_load_dwordx4 v12, s[16:19], s76 offen offset:1024 sc1 lds\n"
        "buffer_load_dwordx4 v12, s[16:19], s76 offen offset:2048 sc1 lds\n"
        "buffer_load_dwordx4 v12, s[16:19], s76 offen offset:3072 sc1 lds\n"
        "s_add_u32 m0, s58, 16384\n"
        "s_movk_i32 s59, 896\n"
        "buffer_load_dwordx4 v8, s[12:15], s59 offen offset:0 lds\n"
        "buffer_load_dwordx4 v9, s[12:15], s59 offen offset:1024 lds\n"
        "buffer_load_dwordx4 v10, s[12:15], s59 offen offset:2048 lds\n"
        "buffer_load_dwordx4 v11, s[12:15], s59 offen offset:3072 lds\n"
        "s_waitcnt vmcnt(8)\n"
        "s_barrier\n"
        "s_add_u32 m0, s58, 49152\n"
        "s_add_u32 s76, s64, 147456\n"
        "buffer_load_dwordx4 v12, s[16:19], s76 offen offset:0 sc1 lds\n"
        "buffer_load_dwordx4 v12, s[16:19], s76 offen offset:1024 sc1 lds\n"
        "buffer_load_dwordx4 v12, s[16:19], s76 offen offset:2048 sc1 lds\n"
        "buffer_load_dwordx4 v12, s[16:19], s76 offen offset:3072 sc1 lds\n"
        "s_add_u32 m0, s58, 32768\n"
        "s_movk_i32 s59, 1024\n"
        "buffer_load_dwordx4 v8, s[12:15], s59 offen offset:0 lds\n"
        "buffer_load_dwordx4 v9, s[12:15], s59 offen offset:1024 lds\n"
        "buffer_load_dwordx4 v10, s[12:15], s59 offen offset:2048 lds\n"
        "buffer_load_dwordx4 v11, s[12:15], s59 offen offset:3072 lds\n"
        "s_waitcnt vmcnt(8)\n"
        "s_barrier\n"
        "s_add_u32 m0, s58, 65536\n"
        "s_add_u32 s76, s64, 163840\n"
        "buffer_load_dwordx4 v12, s[16:19], s76 offen offset:0 sc1 lds\n"
        "buffer_load_dwordx4 v12, s[16:19], s76 offen offset:1024 sc1 lds\n"
        "buffer_load_dwordx4 v12, s[16:19], s76 offen offset:2048 sc1 lds\n"
        "buffer_load_dwordx4 v12, s[16:19], s76 offen offset:3072 sc1 lds\n"
        "s_add_u32 m0, s58, 0\n"
        "s_movk_i32 s59, 1152\n"
        "buffer_load_dwordx4 v8, s[12:15], s59 offen offset:0 lds\n"
        "buffer_load_dwordx4 v9, s[12:15], s59 offen offset:1024 lds\n"
        "buffer_load_dwordx4 v10, s[12:15], s59 offen offset:2048 lds\n"
        "buffer_load_dwordx4 v11, s[12:15], s59 offen offset:3072 lds\n"
        "s_waitcnt vmcnt(8)\n"
        "s_barrier\n"
        "s_add_u32 m0, s58, 81920\n"
        "s_add_u32 s76, s64, 180224\n"
        "buffer_load_dwordx4 v12, s[16:19], s76 offen offset:0 sc1 lds\n"
        "buffer_load_dwordx4 v12, s[16:19], s76 offen offset:1024 sc1 lds\n"
        "buffer_load_dwordx4 v12, s[16:19], s76 offen offset:2048 sc1 lds\n"
        "buffer_load_dwordx4 v12, s[16:19], s76 offen offset:3072 sc1 lds\n"
        "s_add_u32 m0, s58, 16384\n"
        "s_movk_i32 s59, 1280\n"
        "buffer_load_dwordx4 v8, s[12:15], s59 offen offset:0 lds\n"
        "buffer_load_dwordx4 v9, s[12:15], s59 offen offset:1024 lds\n"
        "buffer_load_dwordx4 v10, s[12:15], s59 offen offset:2048 lds\n"
        "buffer_load_dwordx4 v11, s[12:15], s59 offen offset:3072 lds\n"
        "s_waitcnt vmcnt(8)\n"
        "s_barrier\n"
        "s_add_u32 m0, s58, 49152\n"
        "s_add_u32 s76, s64, 196608\n"
        "buffer_load_dwordx4 v12, s[16:19], s76 offen offset:0 sc1 lds\n"
        "buffer_load_dwordx4 v12, s[16:19], s76 offen offset:1024 sc1 lds\n"
        "buffer_load_dwordx4 v12, s[16:19], s76 offen offset:2048 sc1 lds\n"
        "buffer_load_dwordx4 v12, s[16:19], s76 offen offset:3072 sc1 lds\n"
        "s_add_u32 m0, s58, 32768\n"
        "s_movk_i32 s59, 1408\n"
        "buffer_load_dwordx4 v8, s[12:15], s59 offen offset:0 lds\n"
        "buffer_load_dwordx4 v9, s[12:15], s59 offen offset:1024 lds\n"
        "buffer_load_dwordx4 v10, s[12:15], s59 offen offset:2048 lds\n"
        "buffer_load_dwordx4 v11, s[12:15], s59 offen offset:3072 lds\n"
        "s_waitcnt vmcnt(8)\n"
        "s_barrier\n"
        "s_add_u32 m0, s58, 65536\n"
        "s_add_u32 s76, s64, 212992\n"
        "buffer_load_dwordx4 v12, s[16:19], s76 offen offset:0 sc1 lds\n"
        "buffer_load_dwordx4 v12, s[16:19], s76 offen offset:1024 sc1 lds\n"
        "buffer_load_dwordx4 v12, s[16:19], s76 offen offset:2048 sc1 lds\n"
        "buffer_load_dwordx4 v12, s[16:19], s76 offen offset:3072 sc1 lds\n"
        "s_add_u32 m0, s58, 0\n"
        "s_movk_i32 s59, 1536\n"
        "buffer_load_dwordx4 v8, s[12:15], s59 offen offset:0 lds\n"
        "buffer_load_dwordx4 v9, s[12:15], s59 offen offset:1024 lds\n"
        "buffer_load_dwordx4 v10, s[12:15], s59 offen offset:2048 lds\n"
        "buffer_load_dwordx4 v11, s[12:15], s59 offen offset:3072 lds\n"
        "s_waitcnt vmcnt(8)\n"
        "s_barrier\n"
        "s_add_u32 m0, s58, 81920\n"
        "s_add_u32 s76, s64, 229376\n"
        "buffer_load_dwordx4 v12, s[16:19], s76 offen offset:0 sc1 lds\n"
        "buffer_load_dwordx4 v12, s[16:19], s76 offen offset:1024 sc1 lds\n"
        "buffer_load_dwordx4 v12, s[16:19], s76 offen offset:2048 sc1 lds\n"
        "buffer_load_dwordx4 v12, s[16:19], s76 offen offset:3072 sc1 lds\n"
        "s_add_u32 m0, s58, 16384\n"
        "s_movk_i32 s59, 1664\n"
        "buffer_load_dwordx4 v8, s[12:15], s59 offen offset:0 lds\n"
        "buffer_load_dwordx4 v9, s[12:15], s59 offen offset:1024 lds\n"
        "buffer_load_dwordx4 v10, s[12:15], s59 offen offset:2048 lds\n"
        "buffer_load_dwordx4 v11, s[12:15], s59 offen offset:3072 lds\n"
        "s_waitcnt vmcnt(8)\n"
        "s_barrier\n"
        "s_add_u32 m0, s58, 49152\n"
        "s_add_u32 s76, s64, 245760\n"
        "buffer_load_dwordx4 v12, s[16:19], s76 offen offset:0 sc1 lds\n"
        "buffer_load_dwordx4 v12, s[16:19], s76 offen offset:1024 sc1 lds\n"
        "buffer_load_dwordx4 v12, s[16:19], s76 offen offset:2048 sc1 lds\n"
        "buffer_load_dwordx4 v12, s[16:19], s76 offen offset:3072 sc1 lds\n"
        "s_add_u32 m0, s58, 32768\n"
        "s_movk_i32 s59, 1792\n"
        "buffer_load_dwordx4 v8, s[12:15], s59 offen offset:0 lds\n"
        "buffer_load_dwordx4 v9, s[12:15], s59 offen offset:1024 lds\n"
        "buffer_load_dwordx4 v10, s[12:15], s59 offen offset:2048 lds\n"
        "buffer_load_dwordx4 v11, s[12:15], s59 offen offset:3072 lds\n"
        "s_waitcnt vmcnt(8)\n"
        "s_barrier\n"
        "s_add_u32 m0, s58, 0\n"
        "s_movk_i32 s59, 1920\n"
        "buffer_load_dwordx4 v8, s[12:15], s59 offen offset:0 lds\n"
        "buffer_load_dwordx4 v9, s[12:15], s59 offen offset:1024 lds\n"
        "buffer_load_dwordx4 v10, s[12:15], s59 offen offset:2048 lds\n"
        "buffer_load_dwordx4 v11, s[12:15], s59 offen offset:3072 lds\n"
        "s_waitcnt vmcnt(0)\n"
        "s_barrier\n"
        "s_branch .Ln_ht\n"
        ".Ln_h3:\n"
        "s_add_u32 s72, s32, 80\n"
        "s_mul_i32 s35, s72, 9363\n"
        "s_lshr_b32 s35, s35, 16\n"
        "s_mul_i32 s36, s35, 7\n"
        "s_sub_u32 s36, s72, s36\n"
        "s_mul_i32 s40, s35, 917504\n"
        "s_mul_i32 s37, s33, 896\n"
        "s_lshl_b32 s38, s36, 7\n"
        "s_add_u32 s40, s40, s37\n"
        "s_add_u32 s40, s40, s38\n"
        "s_add_u32 s41, s40, 14336\n"
        "s_add_u32 s42, s41, 14336\n"
        "s_add_u32 s43, s42, 14336\n"
        "s_add_u32 s44, s43, 14336\n"
        "s_add_u32 s45, s44, 14336\n"
        "s_add_u32 s46, s45, 14336\n"
        "s_add_u32 s47, s46, 14336\n"
        "s_lshl_b32 s75, s33, 4\n"
        "s_add_u32 s75, s75, s35\n"
        "s_lshl_b32 s75, s75, 3\n"
        "s_add_u32 s75, s75, s36\n"
        "s_lshl_b32 s75, s75, 11\n"
        "s_add_u32 s75, s75, 114688\n"
        "s_waitcnt vmcnt(0)\n"
        "s_waitcnt lgkmcnt(0)\n"
        "s_barrier\n"
        "s_sleep 16\n"
        "s_add_u32 m0, s58, 16384\n"
        "s_movk_i32 s59, 128\n"
        "buffer_load_dwordx4 v8, s[12:15], s59 offen offset:0 lds\n"
        "buffer_load_dwordx4 v9, s[12:15], s59 offen offset:1024 lds\n"
        "buffer_load_dwordx4 v10, s[12:15], s59 offen offset:2048 lds\n"
        "buffer_load_dwordx4 v11, s[12:15], s59 offen offset:3072 lds\n"
        "s_add_u32 m0, s58, 81920\n"
        "s_add_u32 s76, s64, 32768\n"
        "buffer_load_dwordx4 v12, s[16:19], s76 offen offset:0 sc1 lds\n"
        "buffer_load_dwordx4 v12, s[16:19], s76 offen offset:1024 sc1 lds\n"
        "v_mov_b32 v84, 129024\n"
        "s_movk_i32 s73, 64\n"
        "1:\n"
        "s_sub_u32 s73, s73, 1\n"
        "s_cbranch_scc1 2f\n"
        "s_sleep 1\n"
        "ds_read_b128 v[96:99], v84 offset:128\n"
        "s_waitcnt lgkmcnt(0)\n"
        "v_min3_u32 v96, v96, v97, v98\n"
        "v_min_u32 v96, v96, v99\n"
        "v_cmp_eq_u32 vcc, 0, v96\n"
        "s_cbranch_vccnz 1b\n"
        "2:\n"
        "s_add_u32 m0, s58, 32768\n"
        "s_movk_i32 s59, 256\n"
        "buffer_load_dwordx4 v8, s[12:15], s59 offen offset:0 lds\n"
        "buffer_load_dwordx4 v9, s[12:15], s59 offen offset:1024 lds\n"
        "buffer_load_dwordx4 v10, s[12:15], s59 offen offset:2048 lds\n"
        "buffer_load_dwordx4 v11, s[12:15], s59 offen offset:3072 lds\n"
        "s_add_u32 m0, s58, 49152\n"
        "s_add_u32 s76, s64, 49152\n"
        "buffer_load_dwordx4 v12, s[16:19], s76 offen offset:0 sc1 lds\n"
        "buffer_load_dwordx4 v12, s[16:19], s76 offen offset:1024 sc1 lds\n"
        "s_mov_b64 exec, 0\n"
        "s_add_u32 s94, s86, .Ln_tp3-.Ln_here\n"
        "s_addc_u32 s95, s87, 0\n"
        "s_swappc_b64 s[70:71], s[94:95]\n"
        "s_mov_b64 exec, -1\n"
        "s_waitcnt vmcnt(6)\n"
        "s_barrier\n"
        "s_add_u32 m0, s58, 0\n"
        "s_movk_i32 s59, 384\n"
        "buffer_load_dwordx4 v8, s[12:15], s59 offen offset:0 lds\n"
        "buffer_load_dwordx4 v9, s[12:15], s59 offen offset:1024 lds\n"
        "buffer_load_dwordx4 v10, s[12:15], s59 offen offset:2048 lds\n"
        "buffer_load_dwordx4 v11, s[12:15], s59 offen offset:3072 lds\n"
        "s_add_u32 m0, s58, 65536\n"
        "s_add_u32 s76, s64, 65536\n"
        "buffer_load_dwordx4 v12, s[16:19], s76 offen offset:0 sc1 lds\n"
        "buffer_load_dwordx4 v12, s[16:19], s76 offen offset:1024 sc1 lds\n"
        "s_waitcnt vmcnt(6)\n"
        "s_barrier\n"
        "s_add_u32 m0, s58, 81920\n"
        "s_add_u32 s76, s64, 81920\n"
        "buffer_load_dwordx4 v12, s[16:19], s76 offen offset:0 sc1 lds\n"
        "buffer_load_dwordx4 v12, s[16:19], s76 offen offset:1024 sc1 lds\n"
        "s_add_u32 m0, s58, 16384\n"
        "s_movk_i32 s59, 512\n"
        "buffer_load_dwordx4 v8, s[12:15], s59 offen offset:0 lds\n"
        "buffer_load_dwordx4 v9, s[12:15], s59 offen offset:1024 lds\n"
        "buffer_load_dwordx4 v10, s[12:15], s59 offen offset:2048 lds\n"
        "buffer_load_dwordx4 v11, s[12:15], s59 offen offset:3072 lds\n"
        "buffer_load_dwordx4 v[40:43], v249, s[8:11], s40 offen\n"
        "buffer_load_dwordx4 v[44:47], v249, s[8:11], s41 offen\n"
        "buffer_load_dwordx4 v[48:51], v249, s[8:11], s42 offen\n"
        "buffer_load_dwordx4 v[52:55], v249, s[8:11], s43 offen\n"
        "buffer_load_dwordx4 v[56:59], v249, s[8:11], s44 offen\n"
        "buffer_load_dwordx4 v[60:63], v249, s[8:11], s45 offen\n"
        "buffer_load_dwordx4 v[64:67], v249, s[8:11], s46 offen\n"
        "buffer_load_dwordx4 v[68:71], v249, s[8:11], s47 offen\n"
        "s_waitcnt vmcnt(14)\n"
        "s_barrier\n"
        "s_add_u32 m0, s58, 49152\n"
        "s_add_u32 s76, s64, 98304\n"
        "buffer_load_dwordx4 v12, s[16:19], s76 offen offset:0 sc1 lds\n"
        "buffer_load_dwordx4 v12, s[16:19], s76 offen offset:1024 sc1 lds\n"
        "s_add_u32 m0, s58, 32768\n"
        "s_movk_i32 s59, 640\n"
        "buffer_load_dwordx4 v8, s[12:15], s59 offen offset:0 lds\n"
        "buffer_load_dwordx4 v9, s[12:15], s59 offen offset:1024 lds\n"
        "buffer_load_dwordx4 v10, s[12:15], s59 offen offset:2048 lds\n"
        "buffer_load_dwordx4 v11, s[12:15], s59 offen offset:3072 lds\n"
        "s_waitcnt vmcnt(14)\n"
        "s_barrier\n"
        "s_add_u32 m0, s58, 65536\n"
        "s_add_u32 s76, s64, 114688\n"
        "buffer_load_dwordx4 v12, s[16:19], s76 offen offset:0 sc1 lds\n"
        "buffer_load_dwordx4 v12, s[16:19], s76 offen offset:1024 sc1 lds\n"
        "s_add_u32 m0, s58, 0\n"
        "s_movk_i32 s59, 768\n"
        "buffer_load_dwordx4 v8, s[12:15], s59 offen offset:0 lds\n"
        "buffer_load_dwordx4 v9, s[12:15], s59 offen offset:1024 lds\n"
        "buffer_load_dwordx4 v10, s[12:15], s59 offen offset:2048 lds\n"
        "buffer_load_dwordx4 v11, s[12:15], s59 offen offset:3072 lds\n"
        "s_waitcnt vmcnt(12)\n"
        "v_lshl_add_u64 v[68:69], v[68:69], 4, v[64:65]\n"
        "v_lshl_add_u64 v[70:71], v[70:71], 4, v[66:67]\n"
        "v_lshl_add_u64 v[68:69], v[68:69], 4, v[60:61]\n"
        "v_lshl_add_u64 v[70:71], v[70:71], 4, v[62:63]\n"
        "v_lshl_add_u64 v[68:69], v[68:69], 4, v[56:57]\n"
        "v_lshl_add_u64 v[70:71], v[70:71], 4, v[58:59]\n"
        "v_lshl_add_u64 v[68:69], v[68:69], 4, v[52:53]\n"
        "v_lshl_add_u64 v[70:71], v[70:71], 4, v[54:55]\n"
        "v_lshl_add_u64 v[68:69], v[68:69], 4, v[48:49]\n"
        "v_lshl_add_u64 v[70:71], v[70:71], 4, v[50:51]\n"
        "v_lshl_add_u64 v[68:69], v[68:69], 4, v[44:45]\n"
        "v_lshl_add_u64 v[70:71], v[70:71], 4, v[46:47]\n"
        "v_lshl_add_u64 v[72:73], v[68:69], 4, v[40:41]\n"
        "v_lshl_add_u64 v[76:77], v[70:71], 4, v[42:43]\n"
        "s_nop 0\n"
        "v_permlane32_swap_b32 v72, v73\n"
        "v_permlane32_swap_b32 v76, v77\n"
        "buffer_store_dwordx4 v[72:75], v250, s[16:19], s75 offen sc1\n"
        "buffer_store_dwordx4 v[76:79], v251, s[16:19], s75 offen offset:1024 sc1\n"
        "s_waitcnt vmcnt(8)\n"
        "s_barrier\n"
        "s_add_u32 m0, s58, 81920\n"
        "s_add_u32 s76, s64, 131072\n"
        "buffer_load_dwordx4 v12, s[16:19], s76 offen offset:0 sc1 lds\n"
        "buffer_load_dwordx4 v12, s[16:19], s76 offen offset:1024 sc1 lds\n"
        "s_add_u32 m0, s58, 16384\n"
        "s_movk_i32 s59, 896\n"
        "buffer_load_dwordx4 v8, s[12:15], s59 offen offset:0 lds\n"
        "buffer_load_dwordx4 v9, s[12:15], s59 offen offset:1024 lds\n"
        "buffer_load_dwordx4 v10, s[12:15], s59 offen offset:2048 lds\n"
        "buffer_load_dwordx4 v11, s[12:15], s59 offen offset:3072 lds\n"
        "s_waitcnt vmcnt(8)\n"
        "s_barrier\n"
        "s_add_u32 m0, s58, 49152\n"
        "s_add_u32 s76, s64, 147456\n"
        "buffer_load_dwordx4 v12, s[16:19], s76 offen offset:0 sc1 lds\n"
        "buffer_load_dwordx4 v12, s[16:19], s76 offen offset:1024 sc1 lds\n"
        "s_add_u32 m0, s58, 32768\n"
        "s_movk_i32 s59, 1024\n"
        "buffer_load_dwordx4 v8, s[12:15], s59 offen offset:0 lds\n"
        "buffer_load_dwordx4 v9, s[12:15], s59 offen offset:1024 lds\n"
        "buffer_load_dwordx4 v10, s[12:15], s59 offen offset:2048 lds\n"
        "buffer_load_dwordx4 v11, s[12:15], s59 offen offset:3072 lds\n"
        "s_waitcnt vmcnt(6)\n"
        "s_barrier\n"
        "s_add_u32 m0, s58, 65536\n"
        "s_add_u32 s76, s64, 163840\n"
        "buffer_load_dwordx4 v12, s[16:19], s76 offen offset:0 sc1 lds\n"
        "buffer_load_dwordx4 v12, s[16:19], s76 offen offset:1024 sc1 lds\n"
        "s_add_u32 m0, s58, 0\n"
        "s_movk_i32 s59, 1152\n"
        "buffer_load_dwordx4 v8, s[12:15], s59 offen offset:0 lds\n"
        "buffer_load_dwordx4 v9, s[12:15], s59 offen offset:1024 lds\n"
        "buffer_load_dwordx4 v10, s[12:15], s59 offen offset:2048 lds\n"
        "buffer_load_dwordx4 v11, s[12:15], s59 offen offset:3072 lds\n"
        "s_waitcnt vmcnt(6)\n"
        "s_barrier\n"
        "s_add_u32 m0, s58, 81920\n"
        "s_add_u32 s76, s64, 180224\n"
        "buffer_load_dwordx4 v12, s[16:19], s76 offen offset:0 sc1 lds\n"
        "buffer_load_dwordx4 v12, s[16:19], s76 offen offset:1024 sc1 lds\n"
        "s_add_u32 m0, s58, 16384\n"
        "s_movk_i32 s59, 1280\n"
        "buffer_load_dwordx4 v8, s[12:15], s59 offen offset:0 lds\n"
        "buffer_load_dwordx4 v9, s[12:15], s59 offen offset:1024 lds\n"
        "buffer_load_dwordx4 v10, s[12:15], s59 offen offset:2048 lds\n"
        "buffer_load_dwordx4 v11, s[12:15], s59 offen offset:3072 lds\n"
        "s_waitcnt vmcnt(6)\n"
        "s_barrier\n"
        "s_add_u32 m0, s58, 49152\n"
        "s_add_u32 s76, s64, 196608\n"
        "buffer_load_dwordx4 v12, s[16:19], s76 offen offset:0 sc1 lds\n"
        "buffer_load_dwordx4 v12, s[16:19], s76 offen offset:1024 sc1 lds\n"
        "s_add_u32 m0, s58, 32768\n"
        "s_movk_i32 s59, 1408\n"
        "buffer_load_dwordx4 v8, s[12:15], s59 offen offset:0 lds\n"
        "buffer_load_dwordx4 v9, s[12:15], s59 offen offset:1024 lds\n"
        "buffer_load_dwordx4 v10, s[12:15], s59 offen offset:2048 lds\n"
        "buffer_load_dwordx4 v11, s[12:15], s59 offen offset:3072 lds\n"
        "s_waitcnt vmcnt(6)\n"
        "s_barrier\n"
        "s_add_u32 m0, s58, 65536\n"
        "s_add_u32 s76, s64, 212992\n"
        "buffer_load_dwordx4 v12, s[16:19], s76 offen offset:0 sc1 lds\n"
        "buffer_load_dwordx4 v12, s[16:19], s76 offen offset:1024 sc1 lds\n"
        "s_add_u32 m0, s58, 0\n"
        "s_movk_i32 s59, 1536\n"
        "buffer_load_dwordx4 v8, s[12:15], s59 offen offset:0 lds\n"
        "buffer_load_dwordx4 v9, s[12:15], s59 offen offset:1024 lds\n"
        "buffer_load_dwordx4 v10, s[12:15], s59 offen offset:2048 lds\n"
        "buffer_load_dwordx4 v11, s[12:15], s59 offen offset:3072 lds\n"
        "s_waitcnt vmcnt(6)\n"
        "s_barrier\n"
        "s_add_u32 m0, s58, 81920\n"
        "s_add_u32 s76, s64, 229376\n"
        "buffer_load_dwordx4 v12, s[16:19], s76 offen offset:0 sc1 lds\n"
        "buffer_load_dwordx4 v12, s[16:19], s76 offen offset:1024 sc1 lds\n"
        "s_add_u32 m0, s58, 16384\n"
        "s_movk_i32 s59, 1664\n"
        "buffer_load_dwordx4 v8, s[12:15], s59 offen offset:0 lds\n"
        "buffer_load_dwordx4 v9, s[12:15], s59 offen offset:1024 lds\n"
        "buffer_load_dwordx4 v10, s[12:15], s59 offen offset:2048 lds\n"
        "buffer_load_dwordx4 v11, s[12:15], s59 offen offset:3072 lds\n"
        "s_waitcnt vmcnt(6)\n"
        "s_barrier\n"
        "s_add_u32 m0, s58, 49152\n"
        "s_add_u32 s76, s64, 245760\n"
        "buffer_load_dwordx4 v12, s[16:19], s76 offen offset:0 sc1 lds\n"
        "buffer_load_dwordx4 v12, s[16:19], s76 offen offset:1024 sc1 lds\n"
        "s_add_u32 m0, s58, 32768\n"
        "s_movk_i32 s59, 1792\n"
        "buffer_load_dwordx4 v8, s[12:15], s59 offen offset:0 lds\n"
        "buffer_load_dwordx4 v9, s[12:15], s59 offen offset:1024 lds\n"
        "buffer_load_dwordx4 v10, s[12:15], s59 offen offset:2048 lds\n"
        "buffer_load_dwordx4 v11, s[12:15], s59 offen offset:3072 lds\n"
        "s_waitcnt vmcnt(6)\n"
        "s_barrier\n"
        "s_add_u32 m0, s58, 0\n"
        "s_movk_i32 s59, 1920\n"
        "buffer_load_dwordx4 v8, s[12:15], s59 offen offset:0 lds\n"
        "buffer_load_dwordx4 v9, s[12:15], s59 offen offset:1024 lds\n"
        "buffer_load_dwordx4 v10, s[12:15], s59 offen offset:2048 lds\n"
        "buffer_load_dwordx4 v11, s[12:15], s59 offen offset:3072 lds\n"
        "s_waitcnt vmcnt(0)\n"
        "s_barrier\n"
        ".Ln_ht:\n"
        "s_barrier\n"
        "1:\n"
        "s_sleep 1\n"
        "ds_read_b64 v[80:81], v92 offset:0\n"
        "s_waitcnt lgkmcnt(0)\n"
        "v_cmp_ne_u64 vcc, s[88:89], v[80:81]\n"
        "s_cbranch_vccnz 1b\n"
        "ds_read_b128 v[184:187], v88 offset:0\n"
        "ds_read_b128 v[188:191], v89 offset:1024\n"
        "s_add_u32 s35, s84, 0\n"
        "s_add_u32 s36, s35, 28672\n"
        "s_waitcnt lgkmcnt(1)\n"
        "buffer_store_dwordx4 v[184:187], v90, s[20:23], s35 offen nt\n"
        "s_waitcnt lgkmcnt(0)\n"
        "buffer_store_dwordx4 v[188:191], v90, s[20:23], s36 offen nt\n"
        "1:\n"
        "s_sleep 1\n"
        "ds_read_b64 v[80:81], v92 offset:16\n"
        "s_waitcnt lgkmcnt(0)\n"
        "v_cmp_ne_u64 vcc, s[88:89], v[80:81]\n"
        "s_cbranch_vccnz 1b\n"
        "ds_read_b128 v[192:195], v88 offset:8192\n"
        "ds_read_b128 v[196:199], v89 offset:9216\n"
        "s_add_u32 s35, s84, 114688\n"
        "s_add_u32 s36, s35, 28672\n"
        "s_waitcnt lgkmcnt(1)\n"
        "buffer_store_dwordx4 v[192:195], v90, s[20:23], s35 offen nt\n"
        "s_waitcnt lgkmcnt(0)\n"
        "buffer_store_dwordx4 v[196:199], v90, s[20:23], s36 offen nt\n"
        "1:\n"
        "s_sleep 1\n"
        "ds_read_b64 v[80:81], v92 offset:32\n"
        "s_waitcnt lgkmcnt(0)\n"
        "v_cmp_ne_u64 vcc, s[88:89], v[80:81]\n"
        "s_cbranch_vccnz 1b\n"
        "ds_read_b128 v[200:203], v88 offset:16384\n"
        "ds_read_b128 v[204:207], v89 offset:17408\n"
        "s_add_u32 s35, s84, 229376\n"
        "s_add_u32 s36, s35, 28672\n"
        "s_waitcnt lgkmcnt(1)\n"
        "buffer_store_dwordx4 v[200:203], v90, s[20:23], s35 offen nt\n"
        "s_waitcnt lgkmcnt(0)\n"
        "buffer_store_dwordx4 v[204:207], v90, s[20:23], s36 offen nt\n"
        "1:\n"
        "s_sleep 1\n"
        "ds_read_b64 v[80:81], v92 offset:48\n"
        "s_waitcnt lgkmcnt(0)\n"
        "v_cmp_ne_u64 vcc, s[88:89], v[80:81]\n"
        "s_cbranch_vccnz 1b\n"
        "ds_read_b128 v[208:211], v88 offset:24576\n"
        "ds_read_b128 v[212:215], v89 offset:25600\n"
        "s_add_u32 s35, s84, 344064\n"
        "s_add_u32 s36, s35, 28672\n"
        "s_waitcnt lgkmcnt(1)\n"
        "buffer_store_dwordx4 v[208:211], v90, s[20:23], s35 offen nt\n"
        "s_waitcnt lgkmcnt(0)\n"
        "buffer_store_dwordx4 v[212:215], v90, s[20:23], s36 offen nt\n"
        "1:\n"
        "s_sleep 1\n"
        "ds_read_b64 v[80:81], v92 offset:64\n"
        "s_waitcnt lgkmcnt(0)\n"
        "v_cmp_ne_u64 vcc, s[88:89], v[80:81]\n"
        "s_cbranch_vccnz 1b\n"
        "ds_read_b128 v[216:219], v88 offset:32768\n"
        "ds_read_b128 v[220:223], v89 offset:33792\n"
        "s_add_u32 s35, s85, 0\n"
        "s_add_u32 s36, s35, 28672\n"
        "s_waitcnt lgkmcnt(1)\n"
        "buffer_store_dwordx4 v[216:219], v91, s[20:23], s35 offen nt\n"
        "s_waitcnt lgkmcnt(0)\n"
        "buffer_store_dwordx4 v[220:223], v91, s[20:23], s36 offen nt\n"
        "1:\n"
        "s_sleep 1\n"
        "ds_read_b64 v[80:81], v92 offset:80\n"
        "s_waitcnt lgkmcnt(0)\n"
        "v_cmp_ne_u64 vcc, s[88:89], v[80:81]\n"
        "s_cbranch_vccnz 1b\n"
        "ds_read_b128 v[224:227], v88 offset:40960\n"
        "ds_read_b128 v[228:231], v89 offset:41984\n"
        "s_add_u32 s35, s85, 114688\n"
        "s_add_u32 s36, s35, 28672\n"
        "s_waitcnt lgkmcnt(1)\n"
        "buffer_store_dwordx4 v[224:227], v91, s[20:23], s35 offen nt\n"
        "s_waitcnt lgkmcnt(0)\n"
        "buffer_store_dwordx4 v[228:231], v91, s[20:23], s36 offen nt\n"
        "1:\n"
        "s_sleep 1\n"
        "ds_read_b64 v[80:81], v92 offset:96\n"
        "s_waitcnt lgkmcnt(0)\n"
        "v_cmp_ne_u64 vcc, s[88:89], v[80:81]\n"
        "s_cbranch_vccnz 1b\n"
        "ds_read_b128 v[232:235], v88 offset:49152\n"
        "ds_read_b128 v[236:239], v89 offset:50176\n"
        "s_add_u32 s35, s85, 229376\n"
        "s_add_u32 s36, s35, 28672\n"
        "s_waitcnt lgkmcnt(1)\n"
        "buffer_store_dwordx4 v[232:235], v91, s[20:23], s35 offen nt\n"
        "s_waitcnt lgkmcnt(0)\n"
        "buffer_store_dwordx4 v[236:239], v91, s[20:23], s36 offen nt\n"
        "1:\n"
        "s_sleep 1\n"
        "ds_read_b64 v[80:81], v92 offset:112\n"
        "s_waitcnt lgkmcnt(0)\n"
        "v_cmp_ne_u64 vcc, s[88:89], v[80:81]\n"
        "s_cbranch_vccnz 1b\n"
        "ds_read_b128 v[240:243], v88 offset:57344\n"
        "ds_read_b128 v[244:247], v89 offset:58368\n"
        "s_add_u32 s35, s85, 344064\n"
        "s_add_u32 s36, s35, 28672\n"
        "s_waitcnt lgkmcnt(1)\n"
        "buffer_store_dwordx4 v[240:243], v91, s[20:23], s35 offen nt\n"
        "s_waitcnt lgkmcnt(0)\n"
        "buffer_store_dwordx4 v[244:247], v91, s[20:23], s36 offen nt\n"
        "s_endpgm\n"
        ".p2align 12\n"
        ".Ln_r1:\n"
        "s_add_u32 s73, s73, 1\n"
        "s_cmp_lt_u32 s73, s74\n"
        "s_cbranch_scc0 .Ln_f1\n"
        "buffer_load_dwordx4 v[208:211], v232, s[16:19], s65 offen sc1\n"
        "buffer_load_dwordx4 v[212:215], v233, s[16:19], s65 offen sc1\n"
        "buffer_load_dwordx4 v[216:219], v234, s[16:19], s65 offen sc1\n"
        "buffer_load_dwordx4 v[220:223], v235, s[16:19], s65 offen sc1\n"
        "s_waitcnt vmcnt(0)\n"
        "s_branch .Ln_c1\n"
        ".Ln_r0:\n"
        "s_add_u32 s73, s73, 1\n"
        "s_cmp_lt_u32 s73, s74\n"
        "s_cbranch_scc0 .Ln_f0\n"
        "buffer_load_dwordx4 v[144:147], v232, s[16:19], s64 offen sc1\n"
        "buffer_load_dwordx4 v[148:151], v233, s[16:19], s64 offen sc1\n"
        "buffer_load_dwordx4 v[152:155], v234, s[16:19], s64 offen sc1\n"
        "buffer_load_dwordx4 v[156:159], v235, s[16:19], s64 offen sc1\n"
        "buffer_load_dwordx4 v[208:211], v232, s[16:19], s65 offen sc1\n"
        "buffer_load_dwordx4 v[212:215], v233, s[16:19], s65 offen sc1\n"
        "buffer_load_dwordx4 v[216:219], v234, s[16:19], s65 offen sc1\n"
        "buffer_load_dwordx4 v[220:223], v235, s[16:19], s65 offen sc1\n"
        "s_waitcnt vmcnt(0)\n"
        ".Ln_go:\n"
        "s_waitcnt vmcnt(4)\n"
        "v_bitop3_b32 v146, v146, v150, s34 bitop3:0x7e\n"
        "v_bitop3_b32 v147, v147, v151, s39 bitop3:0x7e\n"
        "v_bitop3_b32 v154, v154, v158, s34 bitop3:0x7e\n"
        "v_bitop3_b32 v155, v155, v159, s39 bitop3:0x7e\n"
        "v_or3_b32 v146, v146, v147, v154\n"
        "v_or_b32 v146, v146, v155\n"
        "v_cmp_ne_u32 vcc, 0, v146\n"
        "s_cbranch_vccnz .Ln_r0\n"
        ".Ln_b0:\n"
        "ds_read_b128 v[176:179], v228 offset:0\n"
        "ds_read_b128 v[180:183], v229 offset:0\n"
        "ds_read_b128 v[184:187], v228 offset:2048\n"
        "ds_read_b128 v[188:191], v229 offset:2048\n"
        "s_waitcnt vmcnt(0)\n"
        ".Ln_c1:\n"
        "v_bitop3_b32 v210, v210, v214, s34 bitop3:0x7e\n"
        "v_bitop3_b32 v211, v211, v215, s39 bitop3:0x7e\n"
        "v_bitop3_b32 v218, v218, v222, s34 bitop3:0x7e\n"
        "v_bitop3_b32 v219, v219, v223, s39 bitop3:0x7e\n"
        "v_or3_b32 v210, v210, v211, v218\n"
        "v_or_b32 v210, v210, v219\n"
        "v_cmp_ne_u32 vcc, 0, v210\n"
        "s_cbranch_vccnz .Ln_r1\n"
        ".Ln_b1:\n"
        "ds_write_b32 v237, v237 offset:128\n"
        "v_perm_b32 v224, v144, v236, s60\n"
        "ds_read_b32 v112, v224\n"
        "v_perm_b32 v225, v144, v236, s61\n"
        "ds_read_b32 v113, v225\n"
        "v_perm_b32 v226, v144, v236, s62\n"
        "ds_read_b32 v114, v226\n"
        "v_perm_b32 v227, v144, v236, s63\n"
        "ds_read_b32 v115, v227\n"
        "v_perm_b32 v224, v145, v236, s60\n"
        "ds_read_b32 v116, v224\n"
        "v_perm_b32 v225, v145, v236, s61\n"
        "ds_read_b32 v117, v225\n"
        "v_perm_b32 v226, v145, v236, s62\n"
        "ds_read_b32 v118, v226\n"
        "v_perm_b32 v227, v145, v236, s63\n"
        "ds_read_b32 v119, v227\n"
        "v_perm_b32 v224, v148, v236, s60\n"
        "ds_read_b32 v120, v224\n"
        "v_perm_b32 v225, v148, v236, s61\n"
        "ds_read_b32 v121, v225\n"
        "v_perm_b32 v226, v148, v236, s62\n"
        "ds_read_b32 v122, v226\n"
        "v_perm_b32 v227, v148, v236, s63\n"
        "ds_read_b32 v123, v227\n"
        "v_perm_b32 v224, v149, v236, s60\n"
        "ds_read_b32 v124, v224\n"
        "v_perm_b32 v225, v149, v236, s61\n"
        "ds_read_b32 v125, v225\n"
        "v_perm_b32 v226, v149, v236, s62\n"
        "ds_read_b32 v126, v226\n"
        "v_perm_b32 v227, v149, v236, s63\n"
        "ds_read_b32 v127, v227\n"
        "v_perm_b32 v224, v152, v236, s60\n"
        "ds_read_b32 v128, v224\n"
        "v_perm_b32 v225, v152, v236, s61\n"
        "ds_read_b32 v129, v225\n"
        "v_perm_b32 v226, v152, v236, s62\n"
        "ds_read_b32 v130, v226\n"
        "v_perm_b32 v227, v152, v236, s63\n"
        "ds_read_b32 v131, v227\n"
        "v_perm_b32 v224, v153, v236, s60\n"
        "ds_read_b32 v132, v224\n"
        "v_perm_b32 v225, v153, v236, s61\n"
        "ds_read_b32 v133, v225\n"
        "v_perm_b32 v226, v153, v236, s62\n"
        "ds_read_b32 v134, v226\n"
        "v_perm_b32 v227, v153, v236, s63\n"
        "ds_read_b32 v135, v227\n"
        "v_perm_b32 v224, v156, v236, s60\n"
        "ds_read_b32 v136, v224\n"
        "v_perm_b32 v225, v156, v236, s61\n"
        "ds_read_b32 v137, v225\n"
        "v_perm_b32 v226, v156, v236, s62\n"
        "ds_read_b32 v138, v226\n"
        "v_perm_b32 v227, v156, v236, s63\n"
        "ds_read_b32 v139, v227\n"
        "v_perm_b32 v224, v157, v236, s60\n"
        "ds_read_b32 v140, v224\n"
        "v_perm_b32 v225, v157, v236, s61\n"
        "ds_read_b32 v141, v225\n"
        "v_perm_b32 v226, v157, v236, s62\n"
        "ds_read_b32 v142, v226\n"
        "v_perm_b32 v227, v157, v236, s63\n"
        "ds_read_b32 v143, v227\n"
        "s_waitcnt lgkmcnt(0)\n"
        "v_mfma_f32_16x16x32_bf16 v[0:3], v[112:115], v[176:179], 0\n"
        "ds_read_b128 v[192:195], v228 offset:4096\n"
        "v_mfma_f32_16x16x32_bf16 v[0:3], v[116:119], v[180:183], v[0:3]\n"
        "ds_read_b128 v[196:199], v229 offset:4096\n"
        "v_mfma_f32_16x16x32_bf16 v[4:7], v[120:123], v[176:179], 0\n"
        "ds_read_b128 v[200:203], v228 offset:6144\n"
        "v_mfma_f32_16x16x32_bf16 v[4:7], v[124:127], v[180:183], v[4:7]\n"
        "ds_read_b128 v[204:207], v229 offset:6144\n"
        "v_perm_b32 v224, v208, v236, s60\n"
        "v_mfma_f32_16x16x32_bf16 v[8:11], v[128:131], v[176:179], 0\n"
        "ds_read_b32 v144, v224\n"
        "v_perm_b32 v225, v208, v236, s61\n"
        "v_mfma_f32_16x16x32_bf16 v[8:11], v[132:135], v[180:183], v[8:11]\n"
        "ds_read_b32 v145, v225\n"
        "v_perm_b32 v226, v208, v236, s62\n"
        "v_mfma_f32_16x16x32_bf16 v[12:15], v[136:139], v[176:179], 0\n"
        "ds_read_b32 v146, v226\n"
        "v_perm_b32 v227, v208, v236, s63\n"
        "v_mfma_f32_16x16x32_bf16 v[12:15], v[140:143], v[180:183], v[12:15]\n"
        "ds_read_b32 v147, v227\n"
        "v_mfma_f32_16x16x32_bf16 v[16:19], v[112:115], v[184:187], 0\n"
        "ds_read_b128 v[176:179], v230 offset:0\n"
        "v_mfma_f32_16x16x32_bf16 v[16:19], v[116:119], v[188:191], v[16:19]\n"
        "ds_read_b128 v[180:183], v231 offset:0\n"
        "v_perm_b32 v224, v209, v236, s60\n"
        "v_mfma_f32_16x16x32_bf16 v[20:23], v[120:123], v[184:187], 0\n"
        "ds_read_b32 v148, v224\n"
        "v_perm_b32 v225, v209, v236, s61\n"
        "v_mfma_f32_16x16x32_bf16 v[20:23], v[124:127], v[188:191], v[20:23]\n"
        "ds_read_b32 v149, v225\n"
        "v_perm_b32 v226, v209, v236, s62\n"
        "v_mfma_f32_16x16x32_bf16 v[24:27], v[128:131], v[184:187], 0\n"
        "ds_read_b32 v150, v226\n"
        "v_perm_b32 v227, v209, v236, s63\n"
        "v_mfma_f32_16x16x32_bf16 v[24:27], v[132:135], v[188:191], v[24:27]\n"
        "ds_read_b32 v151, v227\n"
        "v_perm_b32 v224, v212, v236, s60\n"
        "v_mfma_f32_16x16x32_bf16 v[28:31], v[136:139], v[184:187], 0\n"
        "ds_read_b32 v152, v224\n"
        "v_perm_b32 v225, v212, v236, s61\n"
        "v_mfma_f32_16x16x32_bf16 v[28:31], v[140:143], v[188:191], v[28:31]\n"
        "ds_read_b32 v153, v225\n"
        "s_waitcnt lgkmcnt(14)\n"
        "v_mfma_f32_16x16x32_bf16 v[32:35], v[112:115], v[192:195], 0\n"
        "ds_read_b128 v[184:187], v230 offset:2048\n"
        "v_mfma_f32_16x16x32_bf16 v[32:35], v[116:119], v[196:199], v[32:35]\n"
        "ds_read_b128 v[188:191], v231 offset:2048\n"
        "v_perm_b32 v226, v212, v236, s62\n"
        "v_mfma_f32_16x16x32_bf16 v[36:39], v[120:123], v[192:195], 0\n"
        "ds_read_b32 v154, v226\n"
        "v_perm_b32 v227, v212, v236, s63\n"
        "v_mfma_f32_16x16x32_bf16 v[36:39], v[124:127], v[196:199], v[36:39]\n"
        "ds_read_b32 v155, v227\n"
        "v_perm_b32 v224, v213, v236, s60\n"
        "v_mfma_f32_16x16x32_bf16 v[40:43], v[128:131], v[192:195], 0\n"
        "ds_read_b32 v156, v224\n"
        "v_perm_b32 v225, v213, v236, s61\n"
        "v_mfma_f32_16x16x32_bf16 v[40:43], v[132:135], v[196:199], v[40:43]\n"
        "ds_read_b32 v157, v225\n"
        "v_perm_b32 v226, v213, v236, s62\n"
        "v_mfma_f32_16x16x32_bf16 v[44:47], v[136:139], v[192:195], 0\n"
        "ds_read_b32 v158, v226\n"
        "v_perm_b32 v227, v213, v236, s63\n"
        "v_mfma_f32_16x16x32_bf16 v[44:47], v[140:143], v[196:199], v[44:47]\n"
        "ds_read_b32 v159, v227\n"
        "s_waitcnt lgkmcnt(15)\n"
        "v_mfma_f32_16x16x32_bf16 v[48:51], v[112:115], v[200:203], 0\n"
        "ds_read_b128 v[192:195], v230 offset:4096\n"
        "v_mfma_f32_16x16x32_bf16 v[48:51], v[116:119], v[204:207], v[48:51]\n"
        "ds_read_b128 v[196:199], v231 offset:4096\n"
        "v_perm_b32 v224, v216, v236, s60\n"
        "v_mfma_f32_16x16x32_bf16 v[52:55], v[120:123], v[200:203], 0\n"
        "ds_read_b32 v160, v224\n"
        "v_perm_b32 v225, v216, v236, s61\n"
        "v_mfma_f32_16x16x32_bf16 v[52:55], v[124:127], v[204:207], v[52:55]\n"
        "ds_read_b32 v161, v225\n"
        "v_perm_b32 v226, v216, v236, s62\n"
        "v_mfma_f32_16x16x32_bf16 v[56:59], v[128:131], v[200:203], 0\n"
        "ds_read_b32 v162, v226\n"
        "v_perm_b32 v227, v216, v236, s63\n"
        "v_mfma_f32_16x16x32_bf16 v[56:59], v[132:135], v[204:207], v[56:59]\n"
        "ds_read_b32 v163, v227\n"
        "v_perm_b32 v224, v217, v236, s60\n"
        "v_mfma_f32_16x16x32_bf16 v[60:63], v[136:139], v[200:203], 0\n"
        "ds_read_b32 v164, v224\n"
        "v_perm_b32 v225, v217, v236, s61\n"
        "v_mfma_f32_16x16x32_bf16 v[60:63], v[140:143], v[204:207], v[60:63]\n"
        "ds_read_b32 v165, v225\n"
        "s_waitcnt lgkmcnt(15)\n"
        "v_mfma_f32_16x16x32_bf16 v[64:67], v[112:115], v[176:179], 0\n"
        "ds_read_b128 v[200:203], v230 offset:6144\n"
        "v_mfma_f32_16x16x32_bf16 v[64:67], v[116:119], v[180:183], v[64:67]\n"
        "ds_read_b128 v[204:207], v231 offset:6144\n"
        "v_perm_b32 v226, v217, v236, s62\n"
        "v_mfma_f32_16x16x32_bf16 v[68:71], v[120:123], v[176:179], 0\n"
        "ds_read_b32 v166, v226\n"
        "v_perm_b32 v227, v217, v236, s63\n"
        "v_mfma_f32_16x16x32_bf16 v[68:71], v[124:127], v[180:183], v[68:71]\n"
        "ds_read_b32 v167, v227\n"
        "v_perm_b32 v224, v220, v236, s60\n"
        "v_mfma_f32_16x16x32_bf16 v[72:75], v[128:131], v[176:179], 0\n"
        "ds_read_b32 v168, v224\n"
        "v_perm_b32 v225, v220, v236, s61\n"
        "v_mfma_f32_16x16x32_bf16 v[72:75], v[132:135], v[180:183], v[72:75]\n"
        "ds_read_b32 v169, v225\n"
        "v_perm_b32 v226, v220, v236, s62\n"
        "s_waitcnt lgkmcnt(15)\n"
        "v_mfma_f32_16x16x32_bf16 v[76:79], v[112:115], v[184:187], 0\n"
        "ds_read_b32 v170, v226\n"
        "v_perm_b32 v227, v220, v236, s63\n"
        "v_mfma_f32_16x16x32_bf16 v[76:79], v[116:119], v[188:191], v[76:79]\n"
        "ds_read_b32 v171, v227\n"
        "v_perm_b32 v224, v221, v236, s60\n"
        "v_mfma_f32_16x16x32_bf16 v[80:83], v[120:123], v[184:187], 0\n"
        "ds_read_b32 v172, v224\n"
        "v_perm_b32 v225, v221, v236, s61\n"
        "v_mfma_f32_16x16x32_bf16 v[80:83], v[124:127], v[188:191], v[80:83]\n"
        "ds_read_b32 v173, v225\n"
        "v_perm_b32 v226, v221, v236, s62\n"
        "v_mfma_f32_16x16x32_bf16 v[84:87], v[128:131], v[184:187], 0\n"
        "ds_read_b32 v174, v226\n"
        "v_perm_b32 v227, v221, v236, s63\n"
        "v_mfma_f32_16x16x32_bf16 v[84:87], v[132:135], v[188:191], v[84:87]\n"
        "ds_read_b32 v175, v227\n"
        "s_waitcnt lgkmcnt(10)\n"
        "s_barrier\n"
        "v_mfma_f32_16x16x32_bf16 v[88:91], v[112:115], v[192:195], 0\n"
        "ds_read_b128 v[208:211], v232 offset:32768\n"
        "v_mfma_f32_16x16x32_bf16 v[88:91], v[116:119], v[196:199], v[88:91]\n"
        "ds_read_b128 v[212:215], v233 offset:32768\n"
        "v_mfma_f32_16x16x32_bf16 v[92:95], v[120:123], v[192:195], 0\n"
        "v_mfma_f32_16x16x32_bf16 v[92:95], v[124:127], v[196:199], v[92:95]\n"
        "ds_read_b128 v[216:219], v234 offset:32768\n"
        "v_mfma_f32_16x16x32_bf16 v[96:99], v[128:131], v[192:195], 0\n"
        "ds_read_b128 v[220:223], v235 offset:32768\n"
        "v_mfma_f32_16x16x32_bf16 v[96:99], v[132:135], v[196:199], v[96:99]\n"
        "v_mfma_f32_16x16x32_bf16 v[100:103], v[112:115], v[200:203], 0\n"
        "ds_read_b128 v[176:179], v228 offset:16384\n"
        "v_mfma_f32_16x16x32_bf16 v[100:103], v[116:119], v[204:207], v[100:103]\n"
        "ds_read_b128 v[180:183], v229 offset:16384\n"
        "v_mfma_f32_16x16x32_bf16 v[104:107], v[120:123], v[200:203], 0\n"
        "v_mfma_f32_16x16x32_bf16 v[104:107], v[124:127], v[204:207], v[104:107]\n"
        "ds_read_b128 v[184:187], v228 offset:18432\n"
        "v_mfma_f32_16x16x32_bf16 v[108:111], v[128:131], v[200:203], 0\n"
        "ds_read_b128 v[188:191], v229 offset:18432\n"
        "v_mfma_f32_16x16x32_bf16 v[108:111], v[132:135], v[204:207], v[108:111]\n"
        "s_waitcnt lgkmcnt(2)\n"
        "v_mfma_f32_16x16x32_bf16 v[0:3], v[144:147], v[176:179], v[0:3]\n"
        "ds_read_b128 v[192:195], v228 offset:20480\n"
        "v_bitop3_b32 v210, v210, v214, s34 bitop3:0x7e\n"
        "v_mfma_f32_16x16x32_bf16 v[0:3], v[148:151], v[180:183], v[0:3]\n"
        "ds_read_b128 v[196:199], v229 offset:20480\n"
        "v_bitop3_b32 v211, v211, v215, s39 bitop3:0x7e\n"
        "v_mfma_f32_16x16x32_bf16 v[4:7], v[152:155], v[176:179], v[4:7]\n"
        "ds_read_b128 v[200:203], v228 offset:22528\n"
        "v_bitop3_b32 v218, v218, v222, s34 bitop3:0x7e\n"
        "v_mfma_f32_16x16x32_bf16 v[4:7], v[156:159], v[180:183], v[4:7]\n"
        "ds_read_b128 v[204:207], v229 offset:22528\n"
        "v_perm_b32 v224, v208, v236, s60\n"
        "v_mfma_f32_16x16x32_bf16 v[8:11], v[160:163], v[176:179], v[8:11]\n"
        "ds_read_b32 v112, v224\n"
        "v_perm_b32 v225, v208, v236, s61\n"
        "v_mfma_f32_16x16x32_bf16 v[8:11], v[164:167], v[180:183], v[8:11]\n"
        "ds_read_b32 v113, v225\n"
        "v_perm_b32 v226, v208, v236, s62\n"
        "v_mfma_f32_16x16x32_bf16 v[12:15], v[168:171], v[176:179], v[12:15]\n"
        "ds_read_b32 v114, v226\n"
        "v_perm_b32 v227, v208, v236, s63\n"
        "v_mfma_f32_16x16x32_bf16 v[12:15], v[172:175], v[180:183], v[12:15]\n"
        "ds_read_b32 v115, v227\n"
        "v_bitop3_b32 v219, v219, v223, s39 bitop3:0x7e\n"
        "s_waitcnt lgkmcnt(8)\n"
        "v_mfma_f32_16x16x32_bf16 v[16:19], v[144:147], v[184:187], v[16:19]\n"
        "ds_read_b128 v[176:179], v230 offset:16384\n"
        "v_or3_b32 v210, v210, v211, v218\n"
        "v_mfma_f32_16x16x32_bf16 v[16:19], v[148:151], v[188:191], v[16:19]\n"
        "ds_read_b128 v[180:183], v231 offset:16384\n"
        "v_perm_b32 v224, v209, v236, s60\n"
        "v_mfma_f32_16x16x32_bf16 v[20:23], v[152:155], v[184:187], v[20:23]\n"
        "ds_read_b32 v116, v224\n"
        "v_perm_b32 v225, v209, v236, s61\n"
        "v_mfma_f32_16x16x32_bf16 v[20:23], v[156:159], v[188:191], v[20:23]\n"
        "ds_read_b32 v117, v225\n"
        "v_perm_b32 v226, v209, v236, s62\n"
        "v_mfma_f32_16x16x32_bf16 v[24:27], v[160:163], v[184:187], v[24:27]\n"
        "ds_read_b32 v118, v226\n"
        "v_perm_b32 v227, v209, v236, s63\n"
        "v_mfma_f32_16x16x32_bf16 v[24:27], v[164:167], v[188:191], v[24:27]\n"
        "ds_read_b32 v119, v227\n"
        "v_perm_b32 v224, v212, v236, s60\n"
        "v_mfma_f32_16x16x32_bf16 v[28:31], v[168:171], v[184:187], v[28:31]\n"
        "ds_read_b32 v120, v224\n"
        "v_perm_b32 v225, v212, v236, s61\n"
        "v_mfma_f32_16x16x32_bf16 v[28:31], v[172:175], v[188:191], v[28:31]\n"
        "ds_read_b32 v121, v225\n"
        "v_or_b32 v210, v210, v219\n"
        "s_waitcnt lgkmcnt(14)\n"
        "v_mfma_f32_16x16x32_bf16 v[32:35], v[144:147], v[192:195], v[32:35]\n"
        "ds_read_b128 v[184:187], v230 offset:18432\n"
        "v_cmp_ne_u32 vcc, 0, v210\n"
        "v_mfma_f32_16x16x32_bf16 v[32:35], v[148:151], v[196:199], v[32:35]\n"
        "ds_read_b128 v[188:191], v231 offset:18432\n"
        "v_perm_b32 v226, v212, v236, s62\n"
        "v_mfma_f32_16x16x32_bf16 v[36:39], v[152:155], v[192:195], v[36:39]\n"
        "ds_read_b32 v122, v226\n"
        "v_perm_b32 v227, v212, v236, s63\n"
        "v_mfma_f32_16x16x32_bf16 v[36:39], v[156:159], v[196:199], v[36:39]\n"
        "ds_read_b32 v123, v227\n"
        "v_perm_b32 v224, v213, v236, s60\n"
        "v_mfma_f32_16x16x32_bf16 v[40:43], v[160:163], v[192:195], v[40:43]\n"
        "ds_read_b32 v124, v224\n"
        "v_perm_b32 v225, v213, v236, s61\n"
        "v_mfma_f32_16x16x32_bf16 v[40:43], v[164:167], v[196:199], v[40:43]\n"
        "ds_read_b32 v125, v225\n"
        "v_perm_b32 v226, v213, v236, s62\n"
        "v_mfma_f32_16x16x32_bf16 v[44:47], v[168:171], v[192:195], v[44:47]\n"
        "ds_read_b32 v126, v226\n"
        "v_perm_b32 v227, v213, v236, s63\n"
        "v_mfma_f32_16x16x32_bf16 v[44:47], v[172:175], v[196:199], v[44:47]\n"
        "ds_read_b32 v127, v227\n"
        "s_waitcnt lgkmcnt(15)\n"
        "v_mfma_f32_16x16x32_bf16 v[48:51], v[144:147], v[200:203], v[48:51]\n"
        "ds_read_b128 v[192:195], v230 offset:20480\n"
        "s_cbranch_vccnz .Ln_f2\n"
        ".Ln_b2:\n"
        "v_mfma_f32_16x16x32_bf16 v[48:51], v[148:151], v[204:207], v[48:51]\n"
        "ds_read_b128 v[196:199], v231 offset:20480\n"
        "v_perm_b32 v224, v216, v236, s60\n"
        "v_mfma_f32_16x16x32_bf16 v[52:55], v[152:155], v[200:203], v[52:55]\n"
        "ds_read_b32 v128, v224\n"
        "v_perm_b32 v225, v216, v236, s61\n"
        "v_mfma_f32_16x16x32_bf16 v[52:55], v[156:159], v[204:207], v[52:55]\n"
        "ds_read_b32 v129, v225\n"
        "v_perm_b32 v226, v216, v236, s62\n"
        "v_mfma_f32_16x16x32_bf16 v[56:59], v[160:163], v[200:203], v[56:59]\n"
        "ds_read_b32 v130, v226\n"
        "v_perm_b32 v227, v216, v236, s63\n"
        "v_mfma_f32_16x16x32_bf16 v[56:59], v[164:167], v[204:207], v[56:59]\n"
        "ds_read_b32 v131, v227\n"
        "v_perm_b32 v224, v217, v236, s60\n"
        "v_mfma_f32_16x16x32_bf16 v[60:63], v[168:171], v[200:203], v[60:63]\n"
        "ds_read_b32 v132, v224\n"
        "v_perm_b32 v225, v217, v236, s61\n"
        "v_mfma_f32_16x16x32_bf16 v[60:63], v[172:175], v[204:207], v[60:63]\n"
        "ds_read_b32 v133, v225\n"
        "s_waitcnt lgkmcnt(15)\n"
        "v_mfma_f32_16x16x32_bf16 v[64:67], v[144:147], v[176:179], v[64:67]\n"
        "ds_read_b128 v[200:203], v230 offset:22528\n"
        "v_mfma_f32_16x16x32_bf16 v[64:67], v[148:151], v[180:183], v[64:67]\n"
        "ds_read_b128 v[204:207], v231 offset:22528\n"
        "v_perm_b32 v226, v217, v236, s62\n"
        "v_mfma_f32_16x16x32_bf16 v[68:71], v[152:155], v[176:179], v[68:71]\n"
        "ds_read_b32 v134, v226\n"
        "v_perm_b32 v227, v217, v236, s63\n"
        "v_mfma_f32_16x16x32_bf16 v[68:71], v[156:159], v[180:183], v[68:71]\n"
        "ds_read_b32 v135, v227\n"
        "v_perm_b32 v224, v220, v236, s60\n"
        "v_mfma_f32_16x16x32_bf16 v[72:75], v[160:163], v[176:179], v[72:75]\n"
        "ds_read_b32 v136, v224\n"
        "v_perm_b32 v225, v220, v236, s61\n"
        "v_mfma_f32_16x16x32_bf16 v[72:75], v[164:167], v[180:183], v[72:75]\n"
        "ds_read_b32 v137, v225\n"
        "v_perm_b32 v226, v220, v236, s62\n"
        "s_waitcnt lgkmcnt(15)\n"
        "v_mfma_f32_16x16x32_bf16 v[76:79], v[144:147], v[184:187], v[76:79]\n"
        "ds_read_b32 v138, v226\n"
        "v_perm_b32 v227, v220, v236, s63\n"
        "v_mfma_f32_16x16x32_bf16 v[76:79], v[148:151], v[188:191], v[76:79]\n"
        "ds_read_b32 v139, v227\n"
        "v_perm_b32 v224, v221, v236, s60\n"
        "v_mfma_f32_16x16x32_bf16 v[80:83], v[152:155], v[184:187], v[80:83]\n"
        "ds_read_b32 v140, v224\n"
        "v_perm_b32 v225, v221, v236, s61\n"
        "v_mfma_f32_16x16x32_bf16 v[80:83], v[156:159], v[188:191], v[80:83]\n"
        "ds_read_b32 v141, v225\n"
        "v_perm_b32 v226, v221, v236, s62\n"
        "v_mfma_f32_16x16x32_bf16 v[84:87], v[160:163], v[184:187], v[84:87]\n"
        "ds_read_b32 v142, v226\n"
        "v_perm_b32 v227, v221, v236, s63\n"
        "v_mfma_f32_16x16x32_bf16 v[84:87], v[164:167], v[188:191], v[84:87]\n"
        "ds_read_b32 v143, v227\n"
        "s_waitcnt lgkmcnt(10)\n"
        "s_barrier\n"
        "v_mfma_f32_16x16x32_bf16 v[88:91], v[144:147], v[192:195], v[88:91]\n"
        "ds_read_b128 v[208:211], v232 offset:0\n"
        "v_mfma_f32_16x16x32_bf16 v[88:91], v[148:151], v[196:199], v[88:91]\n"
        "ds_read_b128 v[212:215], v233 offset:0\n"
        "v_mfma_f32_16x16x32_bf16 v[92:95], v[152:155], v[192:195], v[92:95]\n"
        "v_mfma_f32_16x16x32_bf16 v[92:95], v[156:159], v[196:199], v[92:95]\n"
        "ds_read_b128 v[216:219], v234 offset:0\n"
        "v_mfma_f32_16x16x32_bf16 v[96:99], v[160:163], v[192:195], v[96:99]\n"
        "ds_read_b128 v[220:223], v235 offset:0\n"
        "v_mfma_f32_16x16x32_bf16 v[96:99], v[164:167], v[196:199], v[96:99]\n"
        "v_mfma_f32_16x16x32_bf16 v[100:103], v[144:147], v[200:203], v[100:103]\n"
        "ds_read_b128 v[176:179], v228 offset:32768\n"
        "v_mfma_f32_16x16x32_bf16 v[100:103], v[148:151], v[204:207], v[100:103]\n"
        "ds_read_b128 v[180:183], v229 offset:32768\n"
        "v_mfma_f32_16x16x32_bf16 v[104:107], v[152:155], v[200:203], v[104:107]\n"
        "v_mfma_f32_16x16x32_bf16 v[104:107], v[156:159], v[204:207], v[104:107]\n"
        "ds_read_b128 v[184:187], v228 offset:34816\n"
        "v_mfma_f32_16x16x32_bf16 v[108:111], v[160:163], v[200:203], v[108:111]\n"
        "ds_read_b128 v[188:191], v229 offset:34816\n"
        "v_mfma_f32_16x16x32_bf16 v[108:111], v[164:167], v[204:207], v[108:111]\n"
        "s_waitcnt lgkmcnt(2)\n"
        "v_mfma_f32_16x16x32_bf16 v[0:3], v[112:115], v[176:179], v[0:3]\n"
        "ds_read_b128 v[192:195], v228 offset:36864\n"
        "v_bitop3_b32 v210, v210, v214, s34 bitop3:0x7e\n"
        "v_mfma_f32_16x16x32_bf16 v[0:3], v[116:119], v[180:183], v[0:3]\n"
        "ds_read_b128 v[196:199], v229 offset:36864\n"
        "v_bitop3_b32 v211, v211, v215, s39 bitop3:0x7e\n"
        "v_mfma_f32_16x16x32_bf16 v[4:7], v[120:123], v[176:179], v[4:7]\n"
        "ds_read_b128 v[200:203], v228 offset:38912\n"
        "v_bitop3_b32 v218, v218, v222, s34 bitop3:0x7e\n"
        "v_mfma_f32_16x16x32_bf16 v[4:7], v[124:127], v[180:183], v[4:7]\n"
        "ds_read_b128 v[204:207], v229 offset:38912\n"
        "v_perm_b32 v224, v208, v236, s60\n"
        "v_mfma_f32_16x16x32_bf16 v[8:11], v[128:131], v[176:179], v[8:11]\n"
        "ds_read_b32 v144, v224\n"
        "v_perm_b32 v225, v208, v236, s61\n"
        "v_mfma_f32_16x16x32_bf16 v[8:11], v[132:135], v[180:183], v[8:11]\n"
        "ds_read_b32 v145, v225\n"
        "v_perm_b32 v226, v208, v236, s62\n"
        "v_mfma_f32_16x16x32_bf16 v[12:15], v[136:139], v[176:179], v[12:15]\n"
        "ds_read_b32 v146, v226\n"
        "v_perm_b32 v227, v208, v236, s63\n"
        "v_mfma_f32_16x16x32_bf16 v[12:15], v[140:143], v[180:183], v[12:15]\n"
        "ds_read_b32 v147, v227\n"
        "v_bitop3_b32 v219, v219, v223, s39 bitop3:0x7e\n"
        "s_waitcnt lgkmcnt(8)\n"
        "v_mfma_f32_16x16x32_bf16 v[16:19], v[112:115], v[184:187], v[16:19]\n"
        "ds_read_b128 v[176:179], v230 offset:32768\n"
        "v_or3_b32 v210, v210, v211, v218\n"
        "v_mfma_f32_16x16x32_bf16 v[16:19], v[116:119], v[188:191], v[16:19]\n"
        "ds_read_b128 v[180:183], v231 offset:32768\n"
        "v_perm_b32 v224, v209, v236, s60\n"
        "v_mfma_f32_16x16x32_bf16 v[20:23], v[120:123], v[184:187], v[20:23]\n"
        "ds_read_b32 v148, v224\n"
        "v_perm_b32 v225, v209, v236, s61\n"
        "v_mfma_f32_16x16x32_bf16 v[20:23], v[124:127], v[188:191], v[20:23]\n"
        "ds_read_b32 v149, v225\n"
        "v_perm_b32 v226, v209, v236, s62\n"
        "v_mfma_f32_16x16x32_bf16 v[24:27], v[128:131], v[184:187], v[24:27]\n"
        "ds_read_b32 v150, v226\n"
        "v_perm_b32 v227, v209, v236, s63\n"
        "v_mfma_f32_16x16x32_bf16 v[24:27], v[132:135], v[188:191], v[24:27]\n"
        "ds_read_b32 v151, v227\n"
        "v_perm_b32 v224, v212, v236, s60\n"
        "v_mfma_f32_16x16x32_bf16 v[28:31], v[136:139], v[184:187], v[28:31]\n"
        "ds_read_b32 v152, v224\n"
        "v_perm_b32 v225, v212, v236, s61\n"
        "v_mfma_f32_16x16x32_bf16 v[28:31], v[140:143], v[188:191], v[28:31]\n"
        "ds_read_b32 v153, v225\n"
        "v_or_b32 v210, v210, v219\n"
        "s_waitcnt lgkmcnt(14)\n"
        "v_mfma_f32_16x16x32_bf16 v[32:35], v[112:115], v[192:195], v[32:35]\n"
        "ds_read_b128 v[184:187], v230 offset:34816\n"
        "v_cmp_ne_u32 vcc, 0, v210\n"
        "v_mfma_f32_16x16x32_bf16 v[32:35], v[116:119], v[196:199], v[32:35]\n"
        "ds_read_b128 v[188:191], v231 offset:34816\n"
        "v_perm_b32 v226, v212, v236, s62\n"
        "v_mfma_f32_16x16x32_bf16 v[36:39], v[120:123], v[192:195], v[36:39]\n"
        "ds_read_b32 v154, v226\n"
        "v_perm_b32 v227, v212, v236, s63\n"
        "v_mfma_f32_16x16x32_bf16 v[36:39], v[124:127], v[196:199], v[36:39]\n"
        "ds_read_b32 v155, v227\n"
        "v_perm_b32 v224, v213, v236, s60\n"
        "v_mfma_f32_16x16x32_bf16 v[40:43], v[128:131], v[192:195], v[40:43]\n"
        "ds_read_b32 v156, v224\n"
        "v_perm_b32 v225, v213, v236, s61\n"
        "v_mfma_f32_16x16x32_bf16 v[40:43], v[132:135], v[196:199], v[40:43]\n"
        "ds_read_b32 v157, v225\n"
        "v_perm_b32 v226, v213, v236, s62\n"
        "v_mfma_f32_16x16x32_bf16 v[44:47], v[136:139], v[192:195], v[44:47]\n"
        "ds_read_b32 v158, v226\n"
        "v_perm_b32 v227, v213, v236, s63\n"
        "v_mfma_f32_16x16x32_bf16 v[44:47], v[140:143], v[196:199], v[44:47]\n"
        "ds_read_b32 v159, v227\n"
        "s_waitcnt lgkmcnt(15)\n"
        "v_mfma_f32_16x16x32_bf16 v[48:51], v[112:115], v[200:203], v[48:51]\n"
        "ds_read_b128 v[192:195], v230 offset:36864\n"
        "s_cbranch_vccnz .Ln_f3\n"
        ".Ln_b3:\n"
        "v_mfma_f32_16x16x32_bf16 v[48:51], v[116:119], v[204:207], v[48:51]\n"
        "ds_read_b128 v[196:199], v231 offset:36864\n"
        "v_perm_b32 v224, v216, v236, s60\n"
        "v_mfma_f32_16x16x32_bf16 v[52:55], v[120:123], v[200:203], v[52:55]\n"
        "ds_read_b32 v160, v224\n"
        "v_perm_b32 v225, v216, v236, s61\n"
        "v_mfma_f32_16x16x32_bf16 v[52:55], v[124:127], v[204:207], v[52:55]\n"
        "ds_read_b32 v161, v225\n"
        "v_perm_b32 v226, v216, v236, s62\n"
        "v_mfma_f32_16x16x32_bf16 v[56:59], v[128:131], v[200:203], v[56:59]\n"
        "ds_read_b32 v162, v226\n"
        "v_perm_b32 v227, v216, v236, s63\n"
        "v_mfma_f32_16x16x32_bf16 v[56:59], v[132:135], v[204:207], v[56:59]\n"
        "ds_read_b32 v163, v227\n"
        "v_perm_b32 v224, v217, v236, s60\n"
        "v_mfma_f32_16x16x32_bf16 v[60:63], v[136:139], v[200:203], v[60:63]\n"
        "ds_read_b32 v164, v224\n"
        "v_perm_b32 v225, v217, v236, s61\n"
        "v_mfma_f32_16x16x32_bf16 v[60:63], v[140:143], v[204:207], v[60:63]\n"
        "ds_read_b32 v165, v225\n"
        "s_waitcnt lgkmcnt(15)\n"
        "v_mfma_f32_16x16x32_bf16 v[64:67], v[112:115], v[176:179], v[64:67]\n"
        "ds_read_b128 v[200:203], v230 offset:38912\n"
        "v_mfma_f32_16x16x32_bf16 v[64:67], v[116:119], v[180:183], v[64:67]\n"
        "ds_read_b128 v[204:207], v231 offset:38912\n"
        "v_perm_b32 v226, v217, v236, s62\n"
        "v_mfma_f32_16x16x32_bf16 v[68:71], v[120:123], v[176:179], v[68:71]\n"
        "ds_read_b32 v166, v226\n"
        "v_perm_b32 v227, v217, v236, s63\n"
        "v_mfma_f32_16x16x32_bf16 v[68:71], v[124:127], v[180:183], v[68:71]\n"
        "ds_read_b32 v167, v227\n"
        "v_perm_b32 v224, v220, v236, s60\n"
        "v_mfma_f32_16x16x32_bf16 v[72:75], v[128:131], v[176:179], v[72:75]\n"
        "ds_read_b32 v168, v224\n"
        "v_perm_b32 v225, v220, v236, s61\n"
        "v_mfma_f32_16x16x32_bf16 v[72:75], v[132:135], v[180:183], v[72:75]\n"
        "ds_read_b32 v169, v225\n"
        "v_perm_b32 v226, v220, v236, s62\n"
        "s_waitcnt lgkmcnt(15)\n"
        "v_mfma_f32_16x16x32_bf16 v[76:79], v[112:115], v[184:187], v[76:79]\n"
        "ds_read_b32 v170, v226\n"
        "v_perm_b32 v227, v220, v236, s63\n"
        "v_mfma_f32_16x16x32_bf16 v[76:79], v[116:119], v[188:191], v[76:79]\n"
        "ds_read_b32 v171, v227\n"
        "v_perm_b32 v224, v221, v236, s60\n"
        ".Ln_tp0:\n"
        "s_cbranch_execz .Ln_ret\n"
        "v_mfma_f32_16x16x32_bf16 v[80:83], v[120:123], v[184:187], v[80:83]\n"
        "ds_read_b32 v172, v224\n"
        "v_perm_b32 v225, v221, v236, s61\n"
        "v_mfma_f32_16x16x32_bf16 v[80:83], v[124:127], v[188:191], v[80:83]\n"
        "ds_read_b32 v173, v225\n"
        "v_perm_b32 v226, v221, v236, s62\n"
        "v_mfma_f32_16x16x32_bf16 v[84:87], v[128:131], v[184:187], v[84:87]\n"
        "ds_read_b32 v174, v226\n"
        "v_perm_b32 v227, v221, v236, s63\n"
        "v_mfma_f32_16x16x32_bf16 v[84:87], v[132:135], v[188:191], v[84:87]\n"
        "ds_read_b32 v175, v227\n"
        "s_waitcnt lgkmcnt(10)\n"
        "s_barrier\n"
        "v_mfma_f32_16x16x32_bf16 v[88:91], v[112:115], v[192:195], v[88:91]\n"
        "ds_read_b128 v[208:211], v232 offset:16384\n"
        "v_mfma_f32_16x16x32_bf16 v[88:91], v[116:119], v[196:199], v[88:91]\n"
        "ds_read_b128 v[212:215], v233 offset:16384\n"
        "v_mfma_f32_16x16x32_bf16 v[92:95], v[120:123], v[192:195], v[92:95]\n"
        "v_mfma_f32_16x16x32_bf16 v[92:95], v[124:127], v[196:199], v[92:95]\n"
        "ds_read_b128 v[216:219], v234 offset:16384\n"
        "v_mfma_f32_16x16x32_bf16 v[96:99], v[128:131], v[192:195], v[96:99]\n"
        "ds_read_b128 v[220:223], v235 offset:16384\n"
        "v_mfma_f32_16x16x32_bf16 v[96:99], v[132:135], v[196:199], v[96:99]\n"
        "v_mfma_f32_16x16x32_bf16 v[100:103], v[112:115], v[200:203], v[100:103]\n"
        "ds_read_b128 v[176:179], v228 offset:0\n"
        "v_mfma_f32_16x16x32_bf16 v[100:103], v[116:119], v[204:207], v[100:103]\n"
        "ds_read_b128 v[180:183], v229 offset:0\n"
        "v_mfma_f32_16x16x32_bf16 v[104:107], v[120:123], v[200:203], v[104:107]\n"
        "v_mfma_f32_16x16x32_bf16 v[104:107], v[124:127], v[204:207], v[104:107]\n"
        "ds_read_b128 v[184:187], v228 offset:2048\n"
        "v_mfma_f32_16x16x32_bf16 v[108:111], v[128:131], v[200:203], v[108:111]\n"
        "ds_read_b128 v[188:191], v229 offset:2048\n"
        "v_mfma_f32_16x16x32_bf16 v[108:111], v[132:135], v[204:207], v[108:111]\n"
        "s_waitcnt lgkmcnt(2)\n"
        "v_mfma_f32_16x16x32_bf16 v[0:3], v[144:147], v[176:179], v[0:3]\n"
        "ds_read_b128 v[192:195], v228 offset:4096\n"
        "v_bitop3_b32 v210, v210, v214, s34 bitop3:0x7e\n"
        "v_mfma_f32_16x16x32_bf16 v[0:3], v[148:151], v[180:183], v[0:3]\n"
        "ds_read_b128 v[196:199], v229 offset:4096\n"
        "v_bitop3_b32 v211, v211, v215, s39 bitop3:0x7e\n"
        "v_mfma_f32_16x16x32_bf16 v[4:7], v[152:155], v[176:179], v[4:7]\n"
        "ds_read_b128 v[200:203], v228 offset:6144\n"
        "v_bitop3_b32 v218, v218, v222, s34 bitop3:0x7e\n"
        "v_mfma_f32_16x16x32_bf16 v[4:7], v[156:159], v[180:183], v[4:7]\n"
        "ds_read_b128 v[204:207], v229 offset:6144\n"
        "v_perm_b32 v224, v208, v236, s60\n"
        "v_mfma_f32_16x16x32_bf16 v[8:11], v[160:163], v[176:179], v[8:11]\n"
        "ds_read_b32 v112, v224\n"
        "v_perm_b32 v225, v208, v236, s61\n"
        "v_mfma_f32_16x16x32_bf16 v[8:11], v[164:167], v[180:183], v[8:11]\n"
        "ds_read_b32 v113, v225\n"
        "v_perm_b32 v226, v208, v236, s62\n"
        "v_mfma_f32_16x16x32_bf16 v[12:15], v[168:171], v[176:179], v[12:15]\n"
        "ds_read_b32 v114, v226\n"
        "v_perm_b32 v227, v208, v236, s63\n"
        "v_mfma_f32_16x16x32_bf16 v[12:15], v[172:175], v[180:183], v[12:15]\n"
        "ds_read_b32 v115, v227\n"
        "v_bitop3_b32 v219, v219, v223, s39 bitop3:0x7e\n"
        "s_waitcnt lgkmcnt(8)\n"
        "v_mfma_f32_16x16x32_bf16 v[16:19], v[144:147], v[184:187], v[16:19]\n"
        "ds_read_b128 v[176:179], v230 offset:0\n"
        "v_or3_b32 v210, v210, v211, v218\n"
        "v_mfma_f32_16x16x32_bf16 v[16:19], v[148:151], v[188:191], v[16:19]\n"
        "ds_read_b128 v[180:183], v231 offset:0\n"
        "v_perm_b32 v224, v209, v236, s60\n"
        "v_mfma_f32_16x16x32_bf16 v[20:23], v[152:155], v[184:187], v[20:23]\n"
        "ds_read_b32 v116, v224\n"
        "v_perm_b32 v225, v209, v236, s61\n"
        "v_mfma_f32_16x16x32_bf16 v[20:23], v[156:159], v[188:191], v[20:23]\n"
        "ds_read_b32 v117, v225\n"
        "v_perm_b32 v226, v209, v236, s62\n"
        "v_mfma_f32_16x16x32_bf16 v[24:27], v[160:163], v[184:187], v[24:27]\n"
        "ds_read_b32 v118, v226\n"
        "v_perm_b32 v227, v209, v236, s63\n"
        "v_mfma_f32_16x16x32_bf16 v[24:27], v[164:167], v[188:191], v[24:27]\n"
        "ds_read_b32 v119, v227\n"
        "v_perm_b32 v224, v212, v236, s60\n"
        "v_mfma_f32_16x16x32_bf16 v[28:31], v[168:171], v[184:187], v[28:31]\n"
        "ds_read_b32 v120, v224\n"
        "v_perm_b32 v225, v212, v236, s61\n"
        "v_mfma_f32_16x16x32_bf16 v[28:31], v[172:175], v[188:191], v[28:31]\n"
        "ds_read_b32 v121, v225\n"
        "v_or_b32 v210, v210, v219\n"
        "s_waitcnt lgkmcnt(14)\n"
        "v_mfma_f32_16x16x32_bf16 v[32:35], v[144:147], v[192:195], v[32:35]\n"
        "ds_read_b128 v[184:187], v230 offset:2048\n"
        "v_cmp_ne_u32 vcc, 0, v210\n"
        "v_mfma_f32_16x16x32_bf16 v[32:35], v[148:151], v[196:199], v[32:35]\n"
        "ds_read_b128 v[188:191], v231 offset:2048\n"
        "v_perm_b32 v226, v212, v236, s62\n"
        "v_mfma_f32_16x16x32_bf16 v[36:39], v[152:155], v[192:195], v[36:39]\n"
        "ds_read_b32 v122, v226\n"
        "v_perm_b32 v227, v212, v236, s63\n"
        "v_mfma_f32_16x16x32_bf16 v[36:39], v[156:159], v[196:199], v[36:39]\n"
        "ds_read_b32 v123, v227\n"
        "v_perm_b32 v224, v213, v236, s60\n"
        "v_mfma_f32_16x16x32_bf16 v[40:43], v[160:163], v[192:195], v[40:43]\n"
        "ds_read_b32 v124, v224\n"
        "v_perm_b32 v225, v213, v236, s61\n"
        "v_mfma_f32_16x16x32_bf16 v[40:43], v[164:167], v[196:199], v[40:43]\n"
        "ds_read_b32 v125, v225\n"
        "v_perm_b32 v226, v213, v236, s62\n"
        "v_mfma_f32_16x16x32_bf16 v[44:47], v[168:171], v[192:195], v[44:47]\n"
        "ds_read_b32 v126, v226\n"
        "v_perm_b32 v227, v213, v236, s63\n"
        "v_mfma_f32_16x16x32_bf16 v[44:47], v[172:175], v[196:199], v[44:47]\n"
        "ds_read_b32 v127, v227\n"
        "s_waitcnt lgkmcnt(15)\n"
        "v_mfma_f32_16x16x32_bf16 v[48:51], v[144:147], v[200:203], v[48:51]\n"
        "ds_read_b128 v[192:195], v230 offset:4096\n"
        "s_cbranch_vccnz .Ln_f4\n"
        ".Ln_b4:\n"
        "v_mfma_f32_16x16x32_bf16 v[48:51], v[148:151], v[204:207], v[48:51]\n"
        "ds_read_b128 v[196:199], v231 offset:4096\n"
        "v_perm_b32 v224, v216, v236, s60\n"
        "v_mfma_f32_16x16x32_bf16 v[52:55], v[152:155], v[200:203], v[52:55]\n"
        "ds_read_b32 v128, v224\n"
        "v_perm_b32 v225, v216, v236, s61\n"
        "v_mfma_f32_16x16x32_bf16 v[52:55], v[156:159], v[204:207], v[52:55]\n"
        "ds_read_b32 v129, v225\n"
        "v_perm_b32 v226, v216, v236, s62\n"
        "v_mfma_f32_16x16x32_bf16 v[56:59], v[160:163], v[200:203], v[56:59]\n"
        "ds_read_b32 v130, v226\n"
        "v_perm_b32 v227, v216, v236, s63\n"
        "v_mfma_f32_16x16x32_bf16 v[56:59], v[164:167], v[204:207], v[56:59]\n"
        "ds_read_b32 v131, v227\n"
        "v_perm_b32 v224, v217, v236, s60\n"
        "v_mfma_f32_16x16x32_bf16 v[60:63], v[168:171], v[200:203], v[60:63]\n"
        "ds_read_b32 v132, v224\n"
        "v_perm_b32 v225, v217, v236, s61\n"
        "v_mfma_f32_16x16x32_bf16 v[60:63], v[172:175], v[204:207], v[60:63]\n"
        "ds_read_b32 v133, v225\n"
        "s_waitcnt lgkmcnt(15)\n"
        "v_mfma_f32_16x16x32_bf16 v[64:67], v[144:147], v[176:179], v[64:67]\n"
        "ds_read_b128 v[200:203], v230 offset:6144\n"
        "v_mfma_f32_16x16x32_bf16 v[64:67], v[148:151], v[180:183], v[64:67]\n"
        "ds_read_b128 v[204:207], v231 offset:6144\n"
        "v_perm_b32 v226, v217, v236, s62\n"
        "v_mfma_f32_16x16x32_bf16 v[68:71], v[152:155], v[176:179], v[68:71]\n"
        "ds_read_b32 v134, v226\n"
        "v_perm_b32 v227, v217, v236, s63\n"
        "v_mfma_f32_16x16x32_bf16 v[68:71], v[156:159], v[180:183], v[68:71]\n"
        "ds_read_b32 v135, v227\n"
        "v_perm_b32 v224, v220, v236, s60\n"
        "v_mfma_f32_16x16x32_bf16 v[72:75], v[160:163], v[176:179], v[72:75]\n"
        "ds_read_b32 v136, v224\n"
        "v_perm_b32 v225, v220, v236, s61\n"
        "v_mfma_f32_16x16x32_bf16 v[72:75], v[164:167], v[180:183], v[72:75]\n"
        "ds_read_b32 v137, v225\n"
        "v_perm_b32 v226, v220, v236, s62\n"
        "s_waitcnt lgkmcnt(15)\n"
        "v_mfma_f32_16x16x32_bf16 v[76:79], v[144:147], v[184:187], v[76:79]\n"
        "ds_read_b32 v138, v226\n"
        "v_perm_b32 v227, v220, v236, s63\n"
        "v_mfma_f32_16x16x32_bf16 v[76:79], v[148:151], v[188:191], v[76:79]\n"
        "ds_read_b32 v139, v227\n"
        "v_perm_b32 v224, v221, v236, s60\n"
        "v_mfma_f32_16x16x32_bf16 v[80:83], v[152:155], v[184:187], v[80:83]\n"
        "ds_read_b32 v140, v224\n"
        "v_perm_b32 v225, v221, v236, s61\n"
        "v_mfma_f32_16x16x32_bf16 v[80:83], v[156:159], v[188:191], v[80:83]\n"
        "ds_read_b32 v141, v225\n"
        "v_perm_b32 v226, v221, v236, s62\n"
        "v_mfma_f32_16x16x32_bf16 v[84:87], v[160:163], v[184:187], v[84:87]\n"
        "ds_read_b32 v142, v226\n"
        "v_perm_b32 v227, v221, v236, s63\n"
        "v_mfma_f32_16x16x32_bf16 v[84:87], v[164:167], v[188:191], v[84:87]\n"
        "ds_read_b32 v143, v227\n"
        "s_waitcnt lgkmcnt(10)\n"
        "s_barrier\n"
        "v_mfma_f32_16x16x32_bf16 v[88:91], v[144:147], v[192:195], v[88:91]\n"
        "ds_read_b128 v[208:211], v232 offset:32768\n"
        "v_mfma_f32_16x16x32_bf16 v[88:91], v[148:151], v[196:199], v[88:91]\n"
        "ds_read_b128 v[212:215], v233 offset:32768\n"
        "v_mfma_f32_16x16x32_bf16 v[92:95], v[152:155], v[192:195], v[92:95]\n"
        "v_mfma_f32_16x16x32_bf16 v[92:95], v[156:159], v[196:199], v[92:95]\n"
        "ds_read_b128 v[216:219], v234 offset:32768\n"
        "v_mfma_f32_16x16x32_bf16 v[96:99], v[160:163], v[192:195], v[96:99]\n"
        "ds_read_b128 v[220:223], v235 offset:32768\n"
        "v_mfma_f32_16x16x32_bf16 v[96:99], v[164:167], v[196:199], v[96:99]\n"
        "v_mfma_f32_16x16x32_bf16 v[100:103], v[144:147], v[200:203], v[100:103]\n"
        "ds_read_b128 v[176:179], v228 offset:16384\n"
        "v_mfma_f32_16x16x32_bf16 v[100:103], v[148:151], v[204:207], v[100:103]\n"
        "ds_read_b128 v[180:183], v229 offset:16384\n"
        "v_mfma_f32_16x16x32_bf16 v[104:107], v[152:155], v[200:203], v[104:107]\n"
        "v_mfma_f32_16x16x32_bf16 v[104:107], v[156:159], v[204:207], v[104:107]\n"
        "ds_read_b128 v[184:187], v228 offset:18432\n"
        "v_mfma_f32_16x16x32_bf16 v[108:111], v[160:163], v[200:203], v[108:111]\n"
        "ds_read_b128 v[188:191], v229 offset:18432\n"
        "v_mfma_f32_16x16x32_bf16 v[108:111], v[164:167], v[204:207], v[108:111]\n"
        "s_waitcnt lgkmcnt(2)\n"
        "v_mfma_f32_16x16x32_bf16 v[0:3], v[112:115], v[176:179], v[0:3]\n"
        "ds_read_b128 v[192:195], v228 offset:20480\n"
        "v_bitop3_b32 v210, v210, v214, s34 bitop3:0x7e\n"
        "v_mfma_f32_16x16x32_bf16 v[0:3], v[116:119], v[180:183], v[0:3]\n"
        "ds_read_b128 v[196:199], v229 offset:20480\n"
        "v_bitop3_b32 v211, v211, v215, s39 bitop3:0x7e\n"
        "v_mfma_f32_16x16x32_bf16 v[4:7], v[120:123], v[176:179], v[4:7]\n"
        "ds_read_b128 v[200:203], v228 offset:22528\n"
        "v_bitop3_b32 v218, v218, v222, s34 bitop3:0x7e\n"
        "v_mfma_f32_16x16x32_bf16 v[4:7], v[124:127], v[180:183], v[4:7]\n"
        "ds_read_b128 v[204:207], v229 offset:22528\n"
        "v_perm_b32 v224, v208, v236, s60\n"
        "v_mfma_f32_16x16x32_bf16 v[8:11], v[128:131], v[176:179], v[8:11]\n"
        "ds_read_b32 v144, v224\n"
        "v_perm_b32 v225, v208, v236, s61\n"
        "v_mfma_f32_16x16x32_bf16 v[8:11], v[132:135], v[180:183], v[8:11]\n"
        "ds_read_b32 v145, v225\n"
        "v_perm_b32 v226, v208, v236, s62\n"
        "v_mfma_f32_16x16x32_bf16 v[12:15], v[136:139], v[176:179], v[12:15]\n"
        "ds_read_b32 v146, v226\n"
        "v_perm_b32 v227, v208, v236, s63\n"
        "v_mfma_f32_16x16x32_bf16 v[12:15], v[140:143], v[180:183], v[12:15]\n"
        "ds_read_b32 v147, v227\n"
        "v_bitop3_b32 v219, v219, v223, s39 bitop3:0x7e\n"
        "s_waitcnt lgkmcnt(8)\n"
        "v_mfma_f32_16x16x32_bf16 v[16:19], v[112:115], v[184:187], v[16:19]\n"
        "ds_read_b128 v[176:179], v230 offset:16384\n"
        "v_or3_b32 v210, v210, v211, v218\n"
        "v_mfma_f32_16x16x32_bf16 v[16:19], v[116:119], v[188:191], v[16:19]\n"
        "ds_read_b128 v[180:183], v231 offset:16384\n"
        "v_perm_b32 v224, v209, v236, s60\n"
        "v_mfma_f32_16x16x32_bf16 v[20:23], v[120:123], v[184:187], v[20:23]\n"
        "ds_read_b32 v148, v224\n"
        "v_perm_b32 v225, v209, v236, s61\n"
        "v_mfma_f32_16x16x32_bf16 v[20:23], v[124:127], v[188:191], v[20:23]\n"
        "ds_read_b32 v149, v225\n"
        "v_perm_b32 v226, v209, v236, s62\n"
        "v_mfma_f32_16x16x32_bf16 v[24:27], v[128:131], v[184:187], v[24:27]\n"
        "ds_read_b32 v150, v226\n"
        "v_perm_b32 v227, v209, v236, s63\n"
        "v_mfma_f32_16x16x32_bf16 v[24:27], v[132:135], v[188:191], v[24:27]\n"
        "ds_read_b32 v151, v227\n"
        "v_perm_b32 v224, v212, v236, s60\n"
        "v_mfma_f32_16x16x32_bf16 v[28:31], v[136:139], v[184:187], v[28:31]\n"
        "ds_read_b32 v152, v224\n"
        "v_perm_b32 v225, v212, v236, s61\n"
        "v_mfma_f32_16x16x32_bf16 v[28:31], v[140:143], v[188:191], v[28:31]\n"
        "ds_read_b32 v153, v225\n"
        "v_or_b32 v210, v210, v219\n"
        "s_waitcnt lgkmcnt(14)\n"
        "v_mfma_f32_16x16x32_bf16 v[32:35], v[112:115], v[192:195], v[32:35]\n"
        "ds_read_b128 v[184:187], v230 offset:18432\n"
        "v_cmp_ne_u32 vcc, 0, v210\n"
        "v_mfma_f32_16x16x32_bf16 v[32:35], v[116:119], v[196:199], v[32:35]\n"
        "ds_read_b128 v[188:191], v231 offset:18432\n"
        "v_perm_b32 v226, v212, v236, s62\n"
        "v_mfma_f32_16x16x32_bf16 v[36:39], v[120:123], v[192:195], v[36:39]\n"
        "ds_read_b32 v154, v226\n"
        "v_perm_b32 v227, v212, v236, s63\n"
        "v_mfma_f32_16x16x32_bf16 v[36:39], v[124:127], v[196:199], v[36:39]\n"
        "ds_read_b32 v155, v227\n"
        "v_perm_b32 v224, v213, v236, s60\n"
        "v_mfma_f32_16x16x32_bf16 v[40:43], v[128:131], v[192:195], v[40:43]\n"
        "ds_read_b32 v156, v224\n"
        "v_perm_b32 v225, v213, v236, s61\n"
        "v_mfma_f32_16x16x32_bf16 v[40:43], v[132:135], v[196:199], v[40:43]\n"
        "ds_read_b32 v157, v225\n"
        "v_perm_b32 v226, v213, v236, s62\n"
        "v_mfma_f32_16x16x32_bf16 v[44:47], v[136:139], v[192:195], v[44:47]\n"
        "ds_read_b32 v158, v226\n"
        "v_perm_b32 v227, v213, v236, s63\n"
        "v_mfma_f32_16x16x32_bf16 v[44:47], v[140:143], v[196:199], v[44:47]\n"
        "ds_read_b32 v159, v227\n"
        "s_waitcnt lgkmcnt(15)\n"
        "v_mfma_f32_16x16x32_bf16 v[48:51], v[112:115], v[200:203], v[48:51]\n"
        "ds_read_b128 v[192:195], v230 offset:20480\n"
        "s_cbranch_vccnz .Ln_f5\n"
        ".Ln_b5:\n"
        "v_mfma_f32_16x16x32_bf16 v[48:51], v[116:119], v[204:207], v[48:51]\n"
        "ds_read_b128 v[196:199], v231 offset:20480\n"
        "v_perm_b32 v224, v216, v236, s60\n"
        "v_mfma_f32_16x16x32_bf16 v[52:55], v[120:123], v[200:203], v[52:55]\n"
        "ds_read_b32 v160, v224\n"
        "v_perm_b32 v225, v216, v236, s61\n"
        "v_mfma_f32_16x16x32_bf16 v[52:55], v[124:127], v[204:207], v[52:55]\n"
        "ds_read_b32 v161, v225\n"
        "v_perm_b32 v226, v216, v236, s62\n"
        "v_mfma_f32_16x16x32_bf16 v[56:59], v[128:131], v[200:203], v[56:59]\n"
        "ds_read_b32 v162, v226\n"
        "v_perm_b32 v227, v216, v236, s63\n"
        "v_mfma_f32_16x16x32_bf16 v[56:59], v[132:135], v[204:207], v[56:59]\n"
        "ds_read_b32 v163, v227\n"
        "v_perm_b32 v224, v217, v236, s60\n"
        "v_mfma_f32_16x16x32_bf16 v[60:63], v[136:139], v[200:203], v[60:63]\n"
        "ds_read_b32 v164, v224\n"
        "v_perm_b32 v225, v217, v236, s61\n"
        "v_mfma_f32_16x16x32_bf16 v[60:63], v[140:143], v[204:207], v[60:63]\n"
        "ds_read_b32 v165, v225\n"
        "s_waitcnt lgkmcnt(15)\n"
        "v_mfma_f32_16x16x32_bf16 v[64:67], v[112:115], v[176:179], v[64:67]\n"
        "ds_read_b128 v[200:203], v230 offset:22528\n"
        "v_mfma_f32_16x16x32_bf16 v[64:67], v[116:119], v[180:183], v[64:67]\n"
        "ds_read_b128 v[204:207], v231 offset:22528\n"
        "v_perm_b32 v226, v217, v236, s62\n"
        "v_mfma_f32_16x16x32_bf16 v[68:71], v[120:123], v[176:179], v[68:71]\n"
        "ds_read_b32 v166, v226\n"
        "v_perm_b32 v227, v217, v236, s63\n"
        "v_mfma_f32_16x16x32_bf16 v[68:71], v[124:127], v[180:183], v[68:71]\n"
        "ds_read_b32 v167, v227\n"
        "v_perm_b32 v224, v220, v236, s60\n"
        "v_mfma_f32_16x16x32_bf16 v[72:75], v[128:131], v[176:179], v[72:75]\n"
        "ds_read_b32 v168, v224\n"
        "v_perm_b32 v225, v220, v236, s61\n"
        "v_mfma_f32_16x16x32_bf16 v[72:75], v[132:135], v[180:183], v[72:75]\n"
        "ds_read_b32 v169, v225\n"
        "v_perm_b32 v226, v220, v236, s62\n"
        "s_waitcnt lgkmcnt(15)\n"
        "v_mfma_f32_16x16x32_bf16 v[76:79], v[112:115], v[184:187], v[76:79]\n"
        "ds_read_b32 v170, v226\n"
        "v_perm_b32 v227, v220, v236, s63\n"
        "v_mfma_f32_16x16x32_bf16 v[76:79], v[116:119], v[188:191], v[76:79]\n"
        "ds_read_b32 v171, v227\n"
        "v_perm_b32 v224, v221, v236, s60\n"
        "v_mfma_f32_16x16x32_bf16 v[80:83], v[120:123], v[184:187], v[80:83]\n"
        "ds_read_b32 v172, v224\n"
        "v_perm_b32 v225, v221, v236, s61\n"
        "v_mfma_f32_16x16x32_bf16 v[80:83], v[124:127], v[188:191], v[80:83]\n"
        "ds_read_b32 v173, v225\n"
        "v_perm_b32 v226, v221, v236, s62\n"
        "v_mfma_f32_16x16x32_bf16 v[84:87], v[128:131], v[184:187], v[84:87]\n"
        "ds_read_b32 v174, v226\n"
        "v_perm_b32 v227, v221, v236, s63\n"
        "v_mfma_f32_16x16x32_bf16 v[84:87], v[132:135], v[188:191], v[84:87]\n"
        "ds_read_b32 v175, v227\n"
        "s_waitcnt lgkmcnt(10)\n"
        "s_barrier\n"
        "v_mfma_f32_16x16x32_bf16 v[88:91], v[112:115], v[192:195], v[88:91]\n"
        "ds_read_b128 v[208:211], v232 offset:0\n"
        "v_mfma_f32_16x16x32_bf16 v[88:91], v[116:119], v[196:199], v[88:91]\n"
        "ds_read_b128 v[212:215], v233 offset:0\n"
        "v_mfma_f32_16x16x32_bf16 v[92:95], v[120:123], v[192:195], v[92:95]\n"
        "v_mfma_f32_16x16x32_bf16 v[92:95], v[124:127], v[196:199], v[92:95]\n"
        "ds_read_b128 v[216:219], v234 offset:0\n"
        "v_mfma_f32_16x16x32_bf16 v[96:99], v[128:131], v[192:195], v[96:99]\n"
        "ds_read_b128 v[220:223], v235 offset:0\n"
        "v_mfma_f32_16x16x32_bf16 v[96:99], v[132:135], v[196:199], v[96:99]\n"
        "v_mfma_f32_16x16x32_bf16 v[100:103], v[112:115], v[200:203], v[100:103]\n"
        "ds_read_b128 v[176:179], v228 offset:32768\n"
        "v_mfma_f32_16x16x32_bf16 v[100:103], v[116:119], v[204:207], v[100:103]\n"
        "ds_read_b128 v[180:183], v229 offset:32768\n"
        "v_mfma_f32_16x16x32_bf16 v[104:107], v[120:123], v[200:203], v[104:107]\n"
        "v_mfma_f32_16x16x32_bf16 v[104:107], v[124:127], v[204:207], v[104:107]\n"
        "ds_read_b128 v[184:187], v228 offset:34816\n"
        "v_mfma_f32_16x16x32_bf16 v[108:111], v[128:131], v[200:203], v[108:111]\n"
        "ds_read_b128 v[188:191], v229 offset:34816\n"
        "v_mfma_f32_16x16x32_bf16 v[108:111], v[132:135], v[204:207], v[108:111]\n"
        "s_waitcnt lgkmcnt(2)\n"
        "v_mfma_f32_16x16x32_bf16 v[0:3], v[144:147], v[176:179], v[0:3]\n"
        "ds_read_b128 v[192:195], v228 offset:36864\n"
        "v_bitop3_b32 v210, v210, v214, s34 bitop3:0x7e\n"
        "v_mfma_f32_16x16x32_bf16 v[0:3], v[148:151], v[180:183], v[0:3]\n"
        "ds_read_b128 v[196:199], v229 offset:36864\n"
        "v_bitop3_b32 v211, v211, v215, s39 bitop3:0x7e\n"
        "v_mfma_f32_16x16x32_bf16 v[4:7], v[152:155], v[176:179], v[4:7]\n"
        "ds_read_b128 v[200:203], v228 offset:38912\n"
        "v_bitop3_b32 v218, v218, v222, s34 bitop3:0x7e\n"
        "v_mfma_f32_16x16x32_bf16 v[4:7], v[156:159], v[180:183], v[4:7]\n"
        "ds_read_b128 v[204:207], v229 offset:38912\n"
        "v_perm_b32 v224, v208, v236, s60\n"
        "v_mfma_f32_16x16x32_bf16 v[8:11], v[160:163], v[176:179], v[8:11]\n"
        "ds_read_b32 v112, v224\n"
        "v_perm_b32 v225, v208, v236, s61\n"
        "v_mfma_f32_16x16x32_bf16 v[8:11], v[164:167], v[180:183], v[8:11]\n"
        "ds_read_b32 v113, v225\n"
        "v_perm_b32 v226, v208, v236, s62\n"
        "v_mfma_f32_16x16x32_bf16 v[12:15], v[168:171], v[176:179], v[12:15]\n"
        "ds_read_b32 v114, v226\n"
        "v_perm_b32 v227, v208, v236, s63\n"
        "v_mfma_f32_16x16x32_bf16 v[12:15], v[172:175], v[180:183], v[12:15]\n"
        "ds_read_b32 v115, v227\n"
        "v_bitop3_b32 v219, v219, v223, s39 bitop3:0x7e\n"
        "s_waitcnt lgkmcnt(8)\n"
        "v_mfma_f32_16x16x32_bf16 v[16:19], v[144:147], v[184:187], v[16:19]\n"
        "ds_read_b128 v[176:179], v230 offset:32768\n"
        "v_or3_b32 v210, v210, v211, v218\n"
        "v_mfma_f32_16x16x32_bf16 v[16:19], v[148:151], v[188:191], v[16:19]\n"
        "ds_read_b128 v[180:183], v231 offset:32768\n"
        "v_perm_b32 v224, v209, v236, s60\n"
        "v_mfma_f32_16x16x32_bf16 v[20:23], v[152:155], v[184:187], v[20:23]\n"
        "ds_read_b32 v116, v224\n"
        "v_perm_b32 v225, v209, v236, s61\n"
        "v_mfma_f32_16x16x32_bf16 v[20:23], v[156:159], v[188:191], v[20:23]\n"
        "ds_read_b32 v117, v225\n"
        "v_perm_b32 v226, v209, v236, s62\n"
        "v_mfma_f32_16x16x32_bf16 v[24:27], v[160:163], v[184:187], v[24:27]\n"
        "ds_read_b32 v118, v226\n"
        "v_perm_b32 v227, v209, v236, s63\n"
        "v_mfma_f32_16x16x32_bf16 v[24:27], v[164:167], v[188:191], v[24:27]\n"
        "ds_read_b32 v119, v227\n"
        "v_perm_b32 v224, v212, v236, s60\n"
        "v_mfma_f32_16x16x32_bf16 v[28:31], v[168:171], v[184:187], v[28:31]\n"
        "ds_read_b32 v120, v224\n"
        "v_perm_b32 v225, v212, v236, s61\n"
        "v_mfma_f32_16x16x32_bf16 v[28:31], v[172:175], v[188:191], v[28:31]\n"
        "ds_read_b32 v121, v225\n"
        "v_or_b32 v210, v210, v219\n"
        "s_waitcnt lgkmcnt(14)\n"
        "v_mfma_f32_16x16x32_bf16 v[32:35], v[144:147], v[192:195], v[32:35]\n"
        "ds_read_b128 v[184:187], v230 offset:34816\n"
        "v_cmp_ne_u32 vcc, 0, v210\n"
        "v_mfma_f32_16x16x32_bf16 v[32:35], v[148:151], v[196:199], v[32:35]\n"
        "ds_read_b128 v[188:191], v231 offset:34816\n"
        "v_perm_b32 v226, v212, v236, s62\n"
        "v_mfma_f32_16x16x32_bf16 v[36:39], v[152:155], v[192:195], v[36:39]\n"
        "ds_read_b32 v122, v226\n"
        "v_perm_b32 v227, v212, v236, s63\n"
        "v_mfma_f32_16x16x32_bf16 v[36:39], v[156:159], v[196:199], v[36:39]\n"
        "ds_read_b32 v123, v227\n"
        "v_perm_b32 v224, v213, v236, s60\n"
        "v_mfma_f32_16x16x32_bf16 v[40:43], v[160:163], v[192:195], v[40:43]\n"
        "ds_read_b32 v124, v224\n"
        "v_perm_b32 v225, v213, v236, s61\n"
        "v_mfma_f32_16x16x32_bf16 v[40:43], v[164:167], v[196:199], v[40:43]\n"
        "ds_read_b32 v125, v225\n"
        "v_perm_b32 v226, v213, v236, s62\n"
        "v_mfma_f32_16x16x32_bf16 v[44:47], v[168:171], v[192:195], v[44:47]\n"
        "ds_read_b32 v126, v226\n"
        "v_perm_b32 v227, v213, v236, s63\n"
        "v_mfma_f32_16x16x32_bf16 v[44:47], v[172:175], v[196:199], v[44:47]\n"
        "ds_read_b32 v127, v227\n"
        "s_waitcnt lgkmcnt(15)\n"
        "v_mfma_f32_16x16x32_bf16 v[48:51], v[144:147], v[200:203], v[48:51]\n"
        "ds_read_b128 v[192:195], v230 offset:36864\n"
        "s_cbranch_vccnz .Ln_f6\n"
        ".Ln_b6:\n"
        "v_mfma_f32_16x16x32_bf16 v[48:51], v[148:151], v[204:207], v[48:51]\n"
        "ds_read_b128 v[196:199], v231 offset:36864\n"
        "v_perm_b32 v224, v216, v236, s60\n"
        "v_mfma_f32_16x16x32_bf16 v[52:55], v[152:155], v[200:203], v[52:55]\n"
        "ds_read_b32 v128, v224\n"
        "v_perm_b32 v225, v216, v236, s61\n"
        "v_mfma_f32_16x16x32_bf16 v[52:55], v[156:159], v[204:207], v[52:55]\n"
        "ds_read_b32 v129, v225\n"
        "v_perm_b32 v226, v216, v236, s62\n"
        "v_mfma_f32_16x16x32_bf16 v[56:59], v[160:163], v[200:203], v[56:59]\n"
        "ds_read_b32 v130, v226\n"
        "v_perm_b32 v227, v216, v236, s63\n"
        "v_mfma_f32_16x16x32_bf16 v[56:59], v[164:167], v[204:207], v[56:59]\n"
        "ds_read_b32 v131, v227\n"
        "v_perm_b32 v224, v217, v236, s60\n"
        "v_mfma_f32_16x16x32_bf16 v[60:63], v[168:171], v[200:203], v[60:63]\n"
        "ds_read_b32 v132, v224\n"
        "v_perm_b32 v225, v217, v236, s61\n"
        "v_mfma_f32_16x16x32_bf16 v[60:63], v[172:175], v[204:207], v[60:63]\n"
        "ds_read_b32 v133, v225\n"
        "s_waitcnt lgkmcnt(15)\n"
        "v_mfma_f32_16x16x32_bf16 v[64:67], v[144:147], v[176:179], v[64:67]\n"
        "ds_read_b128 v[200:203], v230 offset:38912\n"
        "v_mfma_f32_16x16x32_bf16 v[64:67], v[148:151], v[180:183], v[64:67]\n"
        "ds_read_b128 v[204:207], v231 offset:38912\n"
        "v_perm_b32 v226, v217, v236, s62\n"
        "v_mfma_f32_16x16x32_bf16 v[68:71], v[152:155], v[176:179], v[68:71]\n"
        "ds_read_b32 v134, v226\n"
        "v_perm_b32 v227, v217, v236, s63\n"
        "v_mfma_f32_16x16x32_bf16 v[68:71], v[156:159], v[180:183], v[68:71]\n"
        "ds_read_b32 v135, v227\n"
        "v_perm_b32 v224, v220, v236, s60\n"
        "v_mfma_f32_16x16x32_bf16 v[72:75], v[160:163], v[176:179], v[72:75]\n"
        "ds_read_b32 v136, v224\n"
        "v_perm_b32 v225, v220, v236, s61\n"
        "v_mfma_f32_16x16x32_bf16 v[72:75], v[164:167], v[180:183], v[72:75]\n"
        "ds_read_b32 v137, v225\n"
        "v_perm_b32 v226, v220, v236, s62\n"
        "s_waitcnt lgkmcnt(15)\n"
        "v_mfma_f32_16x16x32_bf16 v[76:79], v[144:147], v[184:187], v[76:79]\n"
        "ds_read_b32 v138, v226\n"
        "v_perm_b32 v227, v220, v236, s63\n"
        "v_mfma_f32_16x16x32_bf16 v[76:79], v[148:151], v[188:191], v[76:79]\n"
        "ds_read_b32 v139, v227\n"
        "v_perm_b32 v224, v221, v236, s60\n"
        "v_mfma_f32_16x16x32_bf16 v[80:83], v[152:155], v[184:187], v[80:83]\n"
        "ds_read_b32 v140, v224\n"
        "v_perm_b32 v225, v221, v236, s61\n"
        "v_mfma_f32_16x16x32_bf16 v[80:83], v[156:159], v[188:191], v[80:83]\n"
        "ds_read_b32 v141, v225\n"
        "v_perm_b32 v226, v221, v236, s62\n"
        "v_mfma_f32_16x16x32_bf16 v[84:87], v[160:163], v[184:187], v[84:87]\n"
        "ds_read_b32 v142, v226\n"
        "v_perm_b32 v227, v221, v236, s63\n"
        "v_mfma_f32_16x16x32_bf16 v[84:87], v[164:167], v[188:191], v[84:87]\n"
        "ds_read_b32 v143, v227\n"
        "s_waitcnt lgkmcnt(10)\n"
        "s_barrier\n"
        "v_mfma_f32_16x16x32_bf16 v[88:91], v[144:147], v[192:195], v[88:91]\n"
        "ds_read_b128 v[208:211], v232 offset:16384\n"
        "v_mfma_f32_16x16x32_bf16 v[88:91], v[148:151], v[196:199], v[88:91]\n"
        "ds_read_b128 v[212:215], v233 offset:16384\n"
        "v_mfma_f32_16x16x32_bf16 v[92:95], v[152:155], v[192:195], v[92:95]\n"
        "v_mfma_f32_16x16x32_bf16 v[92:95], v[156:159], v[196:199], v[92:95]\n"
        "ds_read_b128 v[216:219], v234 offset:16384\n"
        "v_mfma_f32_16x16x32_bf16 v[96:99], v[160:163], v[192:195], v[96:99]\n"
        "ds_read_b128 v[220:223], v235 offset:16384\n"
        "v_mfma_f32_16x16x32_bf16 v[96:99], v[164:167], v[196:199], v[96:99]\n"
        "v_mfma_f32_16x16x32_bf16 v[100:103], v[144:147], v[200:203], v[100:103]\n"
        "ds_read_b128 v[176:179], v228 offset:0\n"
        "v_mfma_f32_16x16x32_bf16 v[100:103], v[148:151], v[204:207], v[100:103]\n"
        "ds_read_b128 v[180:183], v229 offset:0\n"
        "v_mfma_f32_16x16x32_bf16 v[104:107], v[152:155], v[200:203], v[104:107]\n"
        "v_mfma_f32_16x16x32_bf16 v[104:107], v[156:159], v[204:207], v[104:107]\n"
        "ds_read_b128 v[184:187], v228 offset:2048\n"
        "v_mfma_f32_16x16x32_bf16 v[108:111], v[160:163], v[200:203], v[108:111]\n"
        "ds_read_b128 v[188:191], v229 offset:2048\n"
        "v_mfma_f32_16x16x32_bf16 v[108:111], v[164:167], v[204:207], v[108:111]\n"
        "s_waitcnt lgkmcnt(2)\n"
        "v_mfma_f32_16x16x32_bf16 v[0:3], v[112:115], v[176:179], v[0:3]\n"
        "ds_read_b128 v[192:195], v228 offset:4096\n"
        "v_bitop3_b32 v210, v210, v214, s34 bitop3:0x7e\n"
        "v_mfma_f32_16x16x32_bf16 v[0:3], v[116:119], v[180:183], v[0:3]\n"
        "ds_read_b128 v[196:199], v229 offset:4096\n"
        "v_bitop3_b32 v211, v211, v215, s39 bitop3:0x7e\n"
        "v_mfma_f32_16x16x32_bf16 v[4:7], v[120:123], v[176:179], v[4:7]\n"
        "ds_read_b128 v[200:203], v228 offset:6144\n"
        "v_bitop3_b32 v218, v218, v222, s34 bitop3:0x7e\n"
        "v_mfma_f32_16x16x32_bf16 v[4:7], v[124:127], v[180:183], v[4:7]\n"
        "ds_read_b128 v[204:207], v229 offset:6144\n"
        "v_perm_b32 v224, v208, v236, s60\n"
        "v_mfma_f32_16x16x32_bf16 v[8:11], v[128:131], v[176:179], v[8:11]\n"
        "ds_read_b32 v144, v224\n"
        "v_perm_b32 v225, v208, v236, s61\n"
        "v_mfma_f32_16x16x32_bf16 v[8:11], v[132:135], v[180:183], v[8:11]\n"
        "ds_read_b32 v145, v225\n"
        "v_perm_b32 v226, v208, v236, s62\n"
        "v_mfma_f32_16x16x32_bf16 v[12:15], v[136:139], v[176:179], v[12:15]\n"
        "ds_read_b32 v146, v226\n"
        "v_perm_b32 v227, v208, v236, s63\n"
        "v_mfma_f32_16x16x32_bf16 v[12:15], v[140:143], v[180:183], v[12:15]\n"
        "ds_read_b32 v147, v227\n"
        "v_bitop3_b32 v219, v219, v223, s39 bitop3:0x7e\n"
        "s_waitcnt lgkmcnt(8)\n"
        "v_mfma_f32_16x16x32_bf16 v[16:19], v[112:115], v[184:187], v[16:19]\n"
        "ds_read_b128 v[176:179], v230 offset:0\n"
        "v_or3_b32 v210, v210, v211, v218\n"
        "v_mfma_f32_16x16x32_bf16 v[16:19], v[116:119], v[188:191], v[16:19]\n"
        "ds_read_b128 v[180:183], v231 offset:0\n"
        ".Ln_tp1:\n"
        "s_cbranch_execz .Ln_ret\n"
        "v_perm_b32 v224, v209, v236, s60\n"
        "v_mfma_f32_16x16x32_bf16 v[20:23], v[120:123], v[184:187], v[20:23]\n"
        "ds_read_b32 v148, v224\n"
        "v_perm_b32 v225, v209, v236, s61\n"
        "v_mfma_f32_16x16x32_bf16 v[20:23], v[124:127], v[188:191], v[20:23]\n"
        "ds_read_b32 v149, v225\n"
        "v_perm_b32 v226, v209, v236, s62\n"
        "v_mfma_f32_16x16x32_bf16 v[24:27], v[128:131], v[184:187], v[24:27]\n"
        "ds_read_b32 v150, v226\n"
        "v_perm_b32 v227, v209, v236, s63\n"
        "v_mfma_f32_16x16x32_bf16 v[24:27], v[132:135], v[188:191], v[24:27]\n"
        "ds_read_b32 v151, v227\n"
        "v_perm_b32 v224, v212, v236, s60\n"
        "v_mfma_f32_16x16x32_bf16 v[28:31], v[136:139], v[184:187], v[28:31]\n"
        "ds_read_b32 v152, v224\n"
        "v_perm_b32 v225, v212, v236, s61\n"
        "v_mfma_f32_16x16x32_bf16 v[28:31], v[140:143], v[188:191], v[28:31]\n"
        "ds_read_b32 v153, v225\n"
        "v_or_b32 v210, v210, v219\n"
        "s_waitcnt lgkmcnt(14)\n"
        "v_mfma_f32_16x16x32_bf16 v[32:35], v[112:115], v[192:195], v[32:35]\n"
        "ds_read_b128 v[184:187], v230 offset:2048\n"
        "v_cmp_ne_u32 vcc, 0, v210\n"
        "v_mfma_f32_16x16x32_bf16 v[32:35], v[116:119], v[196:199], v[32:35]\n"
        "ds_read_b128 v[188:191], v231 offset:2048\n"
        "v_perm_b32 v226, v212, v236, s62\n"
        "v_mfma_f32_16x16x32_bf16 v[36:39], v[120:123], v[192:195], v[36:39]\n"
        "ds_read_b32 v154, v226\n"
        "v_perm_b32 v227, v212, v236, s63\n"
        "v_mfma_f32_16x16x32_bf16 v[36:39], v[124:127], v[196:199], v[36:39]\n"
        "ds_read_b32 v155, v227\n"
        "v_perm_b32 v224, v213, v236, s60\n"
        "v_mfma_f32_16x16x32_bf16 v[40:43], v[128:131], v[192:195], v[40:43]\n"
        "ds_read_b32 v156, v224\n"
        "v_perm_b32 v225, v213, v236, s61\n"
        "v_mfma_f32_16x16x32_bf16 v[40:43], v[132:135], v[196:199], v[40:43]\n"
        "ds_read_b32 v157, v225\n"
        "v_perm_b32 v226, v213, v236, s62\n"
        "v_mfma_f32_16x16x32_bf16 v[44:47], v[136:139], v[192:195], v[44:47]\n"
        "ds_read_b32 v158, v226\n"
        "v_perm_b32 v227, v213, v236, s63\n"
        "v_mfma_f32_16x16x32_bf16 v[44:47], v[140:143], v[196:199], v[44:47]\n"
        "ds_read_b32 v159, v227\n"
        "s_waitcnt lgkmcnt(15)\n"
        "v_mfma_f32_16x16x32_bf16 v[48:51], v[112:115], v[200:203], v[48:51]\n"
        "ds_read_b128 v[192:195], v230 offset:4096\n"
        "s_cbranch_vccnz .Ln_f7\n"
        ".Ln_b7:\n"
        "v_mfma_f32_16x16x32_bf16 v[48:51], v[116:119], v[204:207], v[48:51]\n"
        "ds_read_b128 v[196:199], v231 offset:4096\n"
        "v_perm_b32 v224, v216, v236, s60\n"
        "v_mfma_f32_16x16x32_bf16 v[52:55], v[120:123], v[200:203], v[52:55]\n"
        "ds_read_b32 v160, v224\n"
        "v_perm_b32 v225, v216, v236, s61\n"
        "v_mfma_f32_16x16x32_bf16 v[52:55], v[124:127], v[204:207], v[52:55]\n"
        "ds_read_b32 v161, v225\n"
        "v_perm_b32 v226, v216, v236, s62\n"
        "v_mfma_f32_16x16x32_bf16 v[56:59], v[128:131], v[200:203], v[56:59]\n"
        "ds_read_b32 v162, v226\n"
        "v_perm_b32 v227, v216, v236, s63\n"
        "v_mfma_f32_16x16x32_bf16 v[56:59], v[132:135], v[204:207], v[56:59]\n"
        "ds_read_b32 v163, v227\n"
        "v_perm_b32 v224, v217, v236, s60\n"
        "v_mfma_f32_16x16x32_bf16 v[60:63], v[136:139], v[200:203], v[60:63]\n"
        "ds_read_b32 v164, v224\n"
        "v_perm_b32 v225, v217, v236, s61\n"
        "v_mfma_f32_16x16x32_bf16 v[60:63], v[140:143], v[204:207], v[60:63]\n"
        "ds_read_b32 v165, v225\n"
        "s_waitcnt lgkmcnt(15)\n"
        "v_mfma_f32_16x16x32_bf16 v[64:67], v[112:115], v[176:179], v[64:67]\n"
        "ds_read_b128 v[200:203], v230 offset:6144\n"
        "v_mfma_f32_16x16x32_bf16 v[64:67], v[116:119], v[180:183], v[64:67]\n"
        "ds_read_b128 v[204:207], v231 offset:6144\n"
        "v_perm_b32 v226, v217, v236, s62\n"
        "v_mfma_f32_16x16x32_bf16 v[68:71], v[120:123], v[176:179], v[68:71]\n"
        "ds_read_b32 v166, v226\n"
        "v_perm_b32 v227, v217, v236, s63\n"
        "v_mfma_f32_16x16x32_bf16 v[68:71], v[124:127], v[180:183], v[68:71]\n"
        "ds_read_b32 v167, v227\n"
        "v_perm_b32 v224, v220, v236, s60\n"
        "v_mfma_f32_16x16x32_bf16 v[72:75], v[128:131], v[176:179], v[72:75]\n"
        "ds_read_b32 v168, v224\n"
        "v_perm_b32 v225, v220, v236, s61\n"
        "v_mfma_f32_16x16x32_bf16 v[72:75], v[132:135], v[180:183], v[72:75]\n"
        "ds_read_b32 v169, v225\n"
        "v_perm_b32 v226, v220, v236, s62\n"
        "s_waitcnt lgkmcnt(15)\n"
        "v_mfma_f32_16x16x32_bf16 v[76:79], v[112:115], v[184:187], v[76:79]\n"
        "ds_read_b32 v170, v226\n"
        "v_perm_b32 v227, v220, v236, s63\n"
        "v_mfma_f32_16x16x32_bf16 v[76:79], v[116:119], v[188:191], v[76:79]\n"
        "ds_read_b32 v171, v227\n"
        "v_perm_b32 v224, v221, v236, s60\n"
        "v_mfma_f32_16x16x32_bf16 v[80:83], v[120:123], v[184:187], v[80:83]\n"
        "ds_read_b32 v172, v224\n"
        "v_perm_b32 v225, v221, v236, s61\n"
        "v_mfma_f32_16x16x32_bf16 v[80:83], v[124:127], v[188:191], v[80:83]\n"
        "ds_read_b32 v173, v225\n"
        "v_perm_b32 v226, v221, v236, s62\n"
        "v_mfma_f32_16x16x32_bf16 v[84:87], v[128:131], v[184:187], v[84:87]\n"
        "ds_read_b32 v174, v226\n"
        "v_perm_b32 v227, v221, v236, s63\n"
        "v_mfma_f32_16x16x32_bf16 v[84:87], v[132:135], v[188:191], v[84:87]\n"
        "ds_read_b32 v175, v227\n"
        "s_waitcnt lgkmcnt(10)\n"
        "s_barrier\n"
        "v_mfma_f32_16x16x32_bf16 v[88:91], v[112:115], v[192:195], v[88:91]\n"
        "ds_read_b128 v[208:211], v232 offset:32768\n"
        "v_mfma_f32_16x16x32_bf16 v[88:91], v[116:119], v[196:199], v[88:91]\n"
        "ds_read_b128 v[212:215], v233 offset:32768\n"
        "v_mfma_f32_16x16x32_bf16 v[92:95], v[120:123], v[192:195], v[92:95]\n"
        "v_mfma_f32_16x16x32_bf16 v[92:95], v[124:127], v[196:199], v[92:95]\n"
        "ds_read_b128 v[216:219], v234 offset:32768\n"
        "v_mfma_f32_16x16x32_bf16 v[96:99], v[128:131], v[192:195], v[96:99]\n"
        "ds_read_b128 v[220:223], v235 offset:32768\n"
        "v_mfma_f32_16x16x32_bf16 v[96:99], v[132:135], v[196:199], v[96:99]\n"
        "v_mfma_f32_16x16x32_bf16 v[100:103], v[112:115], v[200:203], v[100:103]\n"
        "ds_read_b128 v[176:179], v228 offset:16384\n"
        "v_mfma_f32_16x16x32_bf16 v[100:103], v[116:119], v[204:207], v[100:103]\n"
        "ds_read_b128 v[180:183], v229 offset:16384\n"
        "v_mfma_f32_16x16x32_bf16 v[104:107], v[120:123], v[200:203], v[104:107]\n"
        "v_mfma_f32_16x16x32_bf16 v[104:107], v[124:127], v[204:207], v[104:107]\n"
        "ds_read_b128 v[184:187], v228 offset:18432\n"
        "v_mfma_f32_16x16x32_bf16 v[108:111], v[128:131], v[200:203], v[108:111]\n"
        "ds_read_b128 v[188:191], v229 offset:18432\n"
        "v_mfma_f32_16x16x32_bf16 v[108:111], v[132:135], v[204:207], v[108:111]\n"
        "s_waitcnt lgkmcnt(2)\n"
        "v_mfma_f32_16x16x32_bf16 v[0:3], v[144:147], v[176:179], v[0:3]\n"
        "ds_read_b128 v[192:195], v228 offset:20480\n"
        "v_bitop3_b32 v210, v210, v214, s34 bitop3:0x7e\n"
        "v_mfma_f32_16x16x32_bf16 v[0:3], v[148:151], v[180:183], v[0:3]\n"
        "ds_read_b128 v[196:199], v229 offset:20480\n"
        "v_bitop3_b32 v211, v211, v215, s39 bitop3:0x7e\n"
        "v_mfma_f32_16x16x32_bf16 v[4:7], v[152:155], v[176:179], v[4:7]\n"
        "ds_read_b128 v[200:203], v228 offset:22528\n"
        "v_bitop3_b32 v218, v218, v222, s34 bitop3:0x7e\n"
        "v_mfma_f32_16x16x32_bf16 v[4:7], v[156:159], v[180:183], v[4:7]\n"
        "ds_read_b128 v[204:207], v229 offset:22528\n"
        "v_perm_b32 v224, v208, v236, s60\n"
        "v_mfma_f32_16x16x32_bf16 v[8:11], v[160:163], v[176:179], v[8:11]\n"
        "ds_read_b32 v112, v224\n"
        "v_perm_b32 v225, v208, v236, s61\n"
        "v_mfma_f32_16x16x32_bf16 v[8:11], v[164:167], v[180:183], v[8:11]\n"
        "ds_read_b32 v113, v225\n"
        "v_perm_b32 v226, v208, v236, s62\n"
        "v_mfma_f32_16x16x32_bf16 v[12:15], v[168:171], v[176:179], v[12:15]\n"
        "ds_read_b32 v114, v226\n"
        "v_perm_b32 v227, v208, v236, s63\n"
        "v_mfma_f32_16x16x32_bf16 v[12:15], v[172:175], v[180:183], v[12:15]\n"
        "ds_read_b32 v115, v227\n"
        "v_bitop3_b32 v219, v219, v223, s39 bitop3:0x7e\n"
        "s_waitcnt lgkmcnt(8)\n"
        "v_mfma_f32_16x16x32_bf16 v[16:19], v[144:147], v[184:187], v[16:19]\n"
        "ds_read_b128 v[176:179], v230 offset:16384\n"
        "v_or3_b32 v210, v210, v211, v218\n"
        "v_mfma_f32_16x16x32_bf16 v[16:19], v[148:151], v[188:191], v[16:19]\n"
        "ds_read_b128 v[180:183], v231 offset:16384\n"
        "v_perm_b32 v224, v209, v236, s60\n"
        "v_mfma_f32_16x16x32_bf16 v[20:23], v[152:155], v[184:187], v[20:23]\n"
        "ds_read_b32 v116, v224\n"
        "v_perm_b32 v225, v209, v236, s61\n"
        "v_mfma_f32_16x16x32_bf16 v[20:23], v[156:159], v[188:191], v[20:23]\n"
        "ds_read_b32 v117, v225\n"
        "v_perm_b32 v226, v209, v236, s62\n"
        "v_mfma_f32_16x16x32_bf16 v[24:27], v[160:163], v[184:187], v[24:27]\n"
        "ds_read_b32 v118, v226\n"
        "v_perm_b32 v227, v209, v236, s63\n"
        "v_mfma_f32_16x16x32_bf16 v[24:27], v[164:167], v[188:191], v[24:27]\n"
        "ds_read_b32 v119, v227\n"
        "v_perm_b32 v224, v212, v236, s60\n"
        "v_mfma_f32_16x16x32_bf16 v[28:31], v[168:171], v[184:187], v[28:31]\n"
        "ds_read_b32 v120, v224\n"
        "v_perm_b32 v225, v212, v236, s61\n"
        "v_mfma_f32_16x16x32_bf16 v[28:31], v[172:175], v[188:191], v[28:31]\n"
        "ds_read_b32 v121, v225\n"
        "v_or_b32 v210, v210, v219\n"
        "s_waitcnt lgkmcnt(14)\n"
        "v_mfma_f32_16x16x32_bf16 v[32:35], v[144:147], v[192:195], v[32:35]\n"
        "ds_read_b128 v[184:187], v230 offset:18432\n"
        "v_cmp_ne_u32 vcc, 0, v210\n"
        "v_mfma_f32_16x16x32_bf16 v[32:35], v[148:151], v[196:199], v[32:35]\n"
        "ds_read_b128 v[188:191], v231 offset:18432\n"
        "v_perm_b32 v226, v212, v236, s62\n"
        "v_mfma_f32_16x16x32_bf16 v[36:39], v[152:155], v[192:195], v[36:39]\n"
        "ds_read_b32 v122, v226\n"
        "v_perm_b32 v227, v212, v236, s63\n"
        "v_mfma_f32_16x16x32_bf16 v[36:39], v[156:159], v[196:199], v[36:39]\n"
        "ds_read_b32 v123, v227\n"
        "v_perm_b32 v224, v213, v236, s60\n"
        "v_mfma_f32_16x16x32_bf16 v[40:43], v[160:163], v[192:195], v[40:43]\n"
        "ds_read_b32 v124, v224\n"
        "v_perm_b32 v225, v213, v236, s61\n"
        "v_mfma_f32_16x16x32_bf16 v[40:43], v[164:167], v[196:199], v[40:43]\n"
        "ds_read_b32 v125, v225\n"
        "v_perm_b32 v226, v213, v236, s62\n"
        "v_mfma_f32_16x16x32_bf16 v[44:47], v[168:171], v[192:195], v[44:47]\n"
        "ds_read_b32 v126, v226\n"
        "v_perm_b32 v227, v213, v236, s63\n"
        "v_mfma_f32_16x16x32_bf16 v[44:47], v[172:175], v[196:199], v[44:47]\n"
        "ds_read_b32 v127, v227\n"
        "s_waitcnt lgkmcnt(15)\n"
        "v_mfma_f32_16x16x32_bf16 v[48:51], v[144:147], v[200:203], v[48:51]\n"
        "ds_read_b128 v[192:195], v230 offset:20480\n"
        "s_cbranch_vccnz .Ln_f8\n"
        ".Ln_b8:\n"
        "v_mfma_f32_16x16x32_bf16 v[48:51], v[148:151], v[204:207], v[48:51]\n"
        "ds_read_b128 v[196:199], v231 offset:20480\n"
        "v_perm_b32 v224, v216, v236, s60\n"
        "v_mfma_f32_16x16x32_bf16 v[52:55], v[152:155], v[200:203], v[52:55]\n"
        "ds_read_b32 v128, v224\n"
        "v_perm_b32 v225, v216, v236, s61\n"
        "v_mfma_f32_16x16x32_bf16 v[52:55], v[156:159], v[204:207], v[52:55]\n"
        "ds_read_b32 v129, v225\n"
        "v_perm_b32 v226, v216, v236, s62\n"
        "v_mfma_f32_16x16x32_bf16 v[56:59], v[160:163], v[200:203], v[56:59]\n"
        "ds_read_b32 v130, v226\n"
        "v_perm_b32 v227, v216, v236, s63\n"
        "v_mfma_f32_16x16x32_bf16 v[56:59], v[164:167], v[204:207], v[56:59]\n"
        "ds_read_b32 v131, v227\n"
        "v_perm_b32 v224, v217, v236, s60\n"
        "v_mfma_f32_16x16x32_bf16 v[60:63], v[168:171], v[200:203], v[60:63]\n"
        "ds_read_b32 v132, v224\n"
        "v_perm_b32 v225, v217, v236, s61\n"
        "v_mfma_f32_16x16x32_bf16 v[60:63], v[172:175], v[204:207], v[60:63]\n"
        "ds_read_b32 v133, v225\n"
        "s_waitcnt lgkmcnt(15)\n"
        "v_mfma_f32_16x16x32_bf16 v[64:67], v[144:147], v[176:179], v[64:67]\n"
        "ds_read_b128 v[200:203], v230 offset:22528\n"
        "v_mfma_f32_16x16x32_bf16 v[64:67], v[148:151], v[180:183], v[64:67]\n"
        "ds_read_b128 v[204:207], v231 offset:22528\n"
        "v_perm_b32 v226, v217, v236, s62\n"
        "v_mfma_f32_16x16x32_bf16 v[68:71], v[152:155], v[176:179], v[68:71]\n"
        "ds_read_b32 v134, v226\n"
        "v_perm_b32 v227, v217, v236, s63\n"
        "v_mfma_f32_16x16x32_bf16 v[68:71], v[156:159], v[180:183], v[68:71]\n"
        "ds_read_b32 v135, v227\n"
        "v_perm_b32 v224, v220, v236, s60\n"
        "v_mfma_f32_16x16x32_bf16 v[72:75], v[160:163], v[176:179], v[72:75]\n"
        "ds_read_b32 v136, v224\n"
        "v_perm_b32 v225, v220, v236, s61\n"
        "v_mfma_f32_16x16x32_bf16 v[72:75], v[164:167], v[180:183], v[72:75]\n"
        "ds_read_b32 v137, v225\n"
        "v_perm_b32 v226, v220, v236, s62\n"
        "s_waitcnt lgkmcnt(15)\n"
        "v_mfma_f32_16x16x32_bf16 v[76:79], v[144:147], v[184:187], v[76:79]\n"
        "ds_read_b32 v138, v226\n"
        "v_perm_b32 v227, v220, v236, s63\n"
        "v_mfma_f32_16x16x32_bf16 v[76:79], v[148:151], v[188:191], v[76:79]\n"
        "ds_read_b32 v139, v227\n"
        "v_perm_b32 v224, v221, v236, s60\n"
        "v_mfma_f32_16x16x32_bf16 v[80:83], v[152:155], v[184:187], v[80:83]\n"
        "ds_read_b32 v140, v224\n"
        "v_perm_b32 v225, v221, v236, s61\n"
        "v_mfma_f32_16x16x32_bf16 v[80:83], v[156:159], v[188:191], v[80:83]\n"
        "ds_read_b32 v141, v225\n"
        "v_perm_b32 v226, v221, v236, s62\n"
        "v_mfma_f32_16x16x32_bf16 v[84:87], v[160:163], v[184:187], v[84:87]\n"
        "ds_read_b32 v142, v226\n"
        "v_perm_b32 v227, v221, v236, s63\n"
        "v_mfma_f32_16x16x32_bf16 v[84:87], v[164:167], v[188:191], v[84:87]\n"
        "ds_read_b32 v143, v227\n"
        "s_waitcnt lgkmcnt(10)\n"
        "s_barrier\n"
        "v_mfma_f32_16x16x32_bf16 v[88:91], v[144:147], v[192:195], v[88:91]\n"
        "ds_read_b128 v[208:211], v232 offset:0\n"
        "v_mfma_f32_16x16x32_bf16 v[88:91], v[148:151], v[196:199], v[88:91]\n"
        "ds_read_b128 v[212:215], v233 offset:0\n"
        "v_mfma_f32_16x16x32_bf16 v[92:95], v[152:155], v[192:195], v[92:95]\n"
        "v_mfma_f32_16x16x32_bf16 v[92:95], v[156:159], v[196:199], v[92:95]\n"
        "ds_read_b128 v[216:219], v234 offset:0\n"
        "v_mfma_f32_16x16x32_bf16 v[96:99], v[160:163], v[192:195], v[96:99]\n"
        "ds_read_b128 v[220:223], v235 offset:0\n"
        "v_mfma_f32_16x16x32_bf16 v[96:99], v[164:167], v[196:199], v[96:99]\n"
        "v_mfma_f32_16x16x32_bf16 v[100:103], v[144:147], v[200:203], v[100:103]\n"
        "ds_read_b128 v[176:179], v228 offset:32768\n"
        "v_mfma_f32_16x16x32_bf16 v[100:103], v[148:151], v[204:207], v[100:103]\n"
        "ds_read_b128 v[180:183], v229 offset:32768\n"
        "v_mfma_f32_16x16x32_bf16 v[104:107], v[152:155], v[200:203], v[104:107]\n"
        "v_mfma_f32_16x16x32_bf16 v[104:107], v[156:159], v[204:207], v[104:107]\n"
        "ds_read_b128 v[184:187], v228 offset:34816\n"
        "v_mfma_f32_16x16x32_bf16 v[108:111], v[160:163], v[200:203], v[108:111]\n"
        "ds_read_b128 v[188:191], v229 offset:34816\n"
        "v_mfma_f32_16x16x32_bf16 v[108:111], v[164:167], v[204:207], v[108:111]\n"
        "s_waitcnt lgkmcnt(2)\n"
        "v_mfma_f32_16x16x32_bf16 v[0:3], v[112:115], v[176:179], v[0:3]\n"
        "ds_read_b128 v[192:195], v228 offset:36864\n"
        "v_bitop3_b32 v210, v210, v214, s34 bitop3:0x7e\n"
        "v_mfma_f32_16x16x32_bf16 v[0:3], v[116:119], v[180:183], v[0:3]\n"
        "ds_read_b128 v[196:199], v229 offset:36864\n"
        "v_bitop3_b32 v211, v211, v215, s39 bitop3:0x7e\n"
        "v_mfma_f32_16x16x32_bf16 v[4:7], v[120:123], v[176:179], v[4:7]\n"
        "ds_read_b128 v[200:203], v228 offset:38912\n"
        "v_bitop3_b32 v218, v218, v222, s34 bitop3:0x7e\n"
        "v_mfma_f32_16x16x32_bf16 v[4:7], v[124:127], v[180:183], v[4:7]\n"
        "ds_read_b128 v[204:207], v229 offset:38912\n"
        "v_perm_b32 v224, v208, v236, s60\n"
        "v_mfma_f32_16x16x32_bf16 v[8:11], v[128:131], v[176:179], v[8:11]\n"
        "ds_read_b32 v144, v224\n"
        "v_perm_b32 v225, v208, v236, s61\n"
        "v_mfma_f32_16x16x32_bf16 v[8:11], v[132:135], v[180:183], v[8:11]\n"
        "ds_read_b32 v145, v225\n"
        "v_perm_b32 v226, v208, v236, s62\n"
        "v_mfma_f32_16x16x32_bf16 v[12:15], v[136:139], v[176:179], v[12:15]\n"
        "ds_read_b32 v146, v226\n"
        "v_perm_b32 v227, v208, v236, s63\n"
        "v_mfma_f32_16x16x32_bf16 v[12:15], v[140:143], v[180:183], v[12:15]\n"
        "ds_read_b32 v147, v227\n"
        "v_bitop3_b32 v219, v219, v223, s39 bitop3:0x7e\n"
        "s_waitcnt lgkmcnt(8)\n"
        "v_mfma_f32_16x16x32_bf16 v[16:19], v[112:115], v[184:187], v[16:19]\n"
        "ds_read_b128 v[176:179], v230 offset:32768\n"
        "v_or3_b32 v210, v210, v211, v218\n"
        "v_mfma_f32_16x16x32_bf16 v[16:19], v[116:119], v[188:191], v[16:19]\n"
        "ds_read_b128 v[180:183], v231 offset:32768\n"
        "v_perm_b32 v224, v209, v236, s60\n"
        "v_mfma_f32_16x16x32_bf16 v[20:23], v[120:123], v[184:187], v[20:23]\n"
        "ds_read_b32 v148, v224\n"
        "v_perm_b32 v225, v209, v236, s61\n"
        "v_mfma_f32_16x16x32_bf16 v[20:23], v[124:127], v[188:191], v[20:23]\n"
        "ds_read_b32 v149, v225\n"
        "v_perm_b32 v226, v209, v236, s62\n"
        "v_mfma_f32_16x16x32_bf16 v[24:27], v[128:131], v[184:187], v[24:27]\n"
        "ds_read_b32 v150, v226\n"
        "v_perm_b32 v227, v209, v236, s63\n"
        "v_mfma_f32_16x16x32_bf16 v[24:27], v[132:135], v[188:191], v[24:27]\n"
        "ds_read_b32 v151, v227\n"
        "v_perm_b32 v224, v212, v236, s60\n"
        "v_mfma_f32_16x16x32_bf16 v[28:31], v[136:139], v[184:187], v[28:31]\n"
        "ds_read_b32 v152, v224\n"
        "v_perm_b32 v225, v212, v236, s61\n"
        "v_mfma_f32_16x16x32_bf16 v[28:31], v[140:143], v[188:191], v[28:31]\n"
        "ds_read_b32 v153, v225\n"
        "v_or_b32 v210, v210, v219\n"
        "s_waitcnt lgkmcnt(14)\n"
        "v_mfma_f32_16x16x32_bf16 v[32:35], v[112:115], v[192:195], v[32:35]\n"
        "ds_read_b128 v[184:187], v230 offset:34816\n"
        "v_cmp_ne_u32 vcc, 0, v210\n"
        "v_mfma_f32_16x16x32_bf16 v[32:35], v[116:119], v[196:199], v[32:35]\n"
        "ds_read_b128 v[188:191], v231 offset:34816\n"
        "v_perm_b32 v226, v212, v236, s62\n"
        "v_mfma_f32_16x16x32_bf16 v[36:39], v[120:123], v[192:195], v[36:39]\n"
        "ds_read_b32 v154, v226\n"
        "v_perm_b32 v227, v212, v236, s63\n"
        "v_mfma_f32_16x16x32_bf16 v[36:39], v[124:127], v[196:199], v[36:39]\n"
        "ds_read_b32 v155, v227\n"
        "v_perm_b32 v224, v213, v236, s60\n"
        "v_mfma_f32_16x16x32_bf16 v[40:43], v[128:131], v[192:195], v[40:43]\n"
        "ds_read_b32 v156, v224\n"
        "v_perm_b32 v225, v213, v236, s61\n"
        "v_mfma_f32_16x16x32_bf16 v[40:43], v[132:135], v[196:199], v[40:43]\n"
        "ds_read_b32 v157, v225\n"
        "v_perm_b32 v226, v213, v236, s62\n"
        "v_mfma_f32_16x16x32_bf16 v[44:47], v[136:139], v[192:195], v[44:47]\n"
        "ds_read_b32 v158, v226\n"
        "v_perm_b32 v227, v213, v236, s63\n"
        "v_mfma_f32_16x16x32_bf16 v[44:47], v[140:143], v[196:199], v[44:47]\n"
        "ds_read_b32 v159, v227\n"
        "s_waitcnt lgkmcnt(15)\n"
        "v_mfma_f32_16x16x32_bf16 v[48:51], v[112:115], v[200:203], v[48:51]\n"
        "ds_read_b128 v[192:195], v230 offset:36864\n"
        "s_cbranch_vccnz .Ln_f9\n"
        ".Ln_b9:\n"
        "v_mfma_f32_16x16x32_bf16 v[48:51], v[116:119], v[204:207], v[48:51]\n"
        "ds_read_b128 v[196:199], v231 offset:36864\n"
        "v_perm_b32 v224, v216, v236, s60\n"
        "v_mfma_f32_16x16x32_bf16 v[52:55], v[120:123], v[200:203], v[52:55]\n"
        "ds_read_b32 v160, v224\n"
        "v_perm_b32 v225, v216, v236, s61\n"
        "v_mfma_f32_16x16x32_bf16 v[52:55], v[124:127], v[204:207], v[52:55]\n"
        "ds_read_b32 v161, v225\n"
        "v_perm_b32 v226, v216, v236, s62\n"
        "v_mfma_f32_16x16x32_bf16 v[56:59], v[128:131], v[200:203], v[56:59]\n"
        "ds_read_b32 v162, v226\n"
        "v_perm_b32 v227, v216, v236, s63\n"
        "v_mfma_f32_16x16x32_bf16 v[56:59], v[132:135], v[204:207], v[56:59]\n"
        "ds_read_b32 v163, v227\n"
        "v_perm_b32 v224, v217, v236, s60\n"
        "v_mfma_f32_16x16x32_bf16 v[60:63], v[136:139], v[200:203], v[60:63]\n"
        "ds_read_b32 v164, v224\n"
        "v_perm_b32 v225, v217, v236, s61\n"
        "v_mfma_f32_16x16x32_bf16 v[60:63], v[140:143], v[204:207], v[60:63]\n"
        "ds_read_b32 v165, v225\n"
        "s_waitcnt lgkmcnt(15)\n"
        "v_mfma_f32_16x16x32_bf16 v[64:67], v[112:115], v[176:179], v[64:67]\n"
        "ds_read_b128 v[200:203], v230 offset:38912\n"
        "v_mfma_f32_16x16x32_bf16 v[64:67], v[116:119], v[180:183], v[64:67]\n"
        "ds_read_b128 v[204:207], v231 offset:38912\n"
        "v_perm_b32 v226, v217, v236, s62\n"
        "v_mfma_f32_16x16x32_bf16 v[68:71], v[120:123], v[176:179], v[68:71]\n"
        "ds_read_b32 v166, v226\n"
        "v_perm_b32 v227, v217, v236, s63\n"
        "v_mfma_f32_16x16x32_bf16 v[68:71], v[124:127], v[180:183], v[68:71]\n"
        "ds_read_b32 v167, v227\n"
        "v_perm_b32 v224, v220, v236, s60\n"
        "v_mfma_f32_16x16x32_bf16 v[72:75], v[128:131], v[176:179], v[72:75]\n"
        "ds_read_b32 v168, v224\n"
        "v_perm_b32 v225, v220, v236, s61\n"
        "v_mfma_f32_16x16x32_bf16 v[72:75], v[132:135], v[180:183], v[72:75]\n"
        "ds_read_b32 v169, v225\n"
        "v_perm_b32 v226, v220, v236, s62\n"
        "s_waitcnt lgkmcnt(15)\n"
        "v_mfma_f32_16x16x32_bf16 v[76:79], v[112:115], v[184:187], v[76:79]\n"
        "ds_read_b32 v170, v226\n"
        "v_perm_b32 v227, v220, v236, s63\n"
        "v_mfma_f32_16x16x32_bf16 v[76:79], v[116:119], v[188:191], v[76:79]\n"
        "ds_read_b32 v171, v227\n"
        "v_perm_b32 v224, v221, v236, s60\n"
        "v_mfma_f32_16x16x32_bf16 v[80:83], v[120:123], v[184:187], v[80:83]\n"
        "ds_read_b32 v172, v224\n"
        "v_perm_b32 v225, v221, v236, s61\n"
        "v_mfma_f32_16x16x32_bf16 v[80:83], v[124:127], v[188:191], v[80:83]\n"
        "ds_read_b32 v173, v225\n"
        "v_perm_b32 v226, v221, v236, s62\n"
        "v_mfma_f32_16x16x32_bf16 v[84:87], v[128:131], v[184:187], v[84:87]\n"
        "ds_read_b32 v174, v226\n"
        "v_perm_b32 v227, v221, v236, s63\n"
        "v_mfma_f32_16x16x32_bf16 v[84:87], v[132:135], v[188:191], v[84:87]\n"
        "ds_read_b32 v175, v227\n"
        "s_waitcnt lgkmcnt(10)\n"
        "s_barrier\n"
        "v_mfma_f32_16x16x32_bf16 v[88:91], v[112:115], v[192:195], v[88:91]\n"
        "ds_read_b128 v[208:211], v232 offset:16384\n"
        "v_mfma_f32_16x16x32_bf16 v[88:91], v[116:119], v[196:199], v[88:91]\n"
        "ds_read_b128 v[212:215], v233 offset:16384\n"
        "v_mfma_f32_16x16x32_bf16 v[92:95], v[120:123], v[192:195], v[92:95]\n"
        "v_mfma_f32_16x16x32_bf16 v[92:95], v[124:127], v[196:199], v[92:95]\n"
        "ds_read_b128 v[216:219], v234 offset:16384\n"
        "v_mfma_f32_16x16x32_bf16 v[96:99], v[128:131], v[192:195], v[96:99]\n"
        "ds_read_b128 v[220:223], v235 offset:16384\n"
        "v_mfma_f32_16x16x32_bf16 v[96:99], v[132:135], v[196:199], v[96:99]\n"
        "v_mfma_f32_16x16x32_bf16 v[100:103], v[112:115], v[200:203], v[100:103]\n"
        "ds_read_b128 v[176:179], v228 offset:0\n"
        "v_mfma_f32_16x16x32_bf16 v[100:103], v[116:119], v[204:207], v[100:103]\n"
        "ds_read_b128 v[180:183], v229 offset:0\n"
        "v_mfma_f32_16x16x32_bf16 v[104:107], v[120:123], v[200:203], v[104:107]\n"
        "v_mfma_f32_16x16x32_bf16 v[104:107], v[124:127], v[204:207], v[104:107]\n"
        "ds_read_b128 v[184:187], v228 offset:2048\n"
        "v_mfma_f32_16x16x32_bf16 v[108:111], v[128:131], v[200:203], v[108:111]\n"
        "ds_read_b128 v[188:191], v229 offset:2048\n"
        "v_mfma_f32_16x16x32_bf16 v[108:111], v[132:135], v[204:207], v[108:111]\n"
        "s_waitcnt lgkmcnt(2)\n"
        "v_mfma_f32_16x16x32_bf16 v[0:3], v[144:147], v[176:179], v[0:3]\n"
        "ds_read_b128 v[192:195], v228 offset:4096\n"
        "v_bitop3_b32 v210, v210, v214, s34 bitop3:0x7e\n"
        "v_mfma_f32_16x16x32_bf16 v[0:3], v[148:151], v[180:183], v[0:3]\n"
        "ds_read_b128 v[196:199], v229 offset:4096\n"
        "v_bitop3_b32 v211, v211, v215, s39 bitop3:0x7e\n"
        "v_mfma_f32_16x16x32_bf16 v[4:7], v[152:155], v[176:179], v[4:7]\n"
        "ds_read_b128 v[200:203], v228 offset:6144\n"
        "v_bitop3_b32 v218, v218, v222, s34 bitop3:0x7e\n"
        "v_mfma_f32_16x16x32_bf16 v[4:7], v[156:159], v[180:183], v[4:7]\n"
        "ds_read_b128 v[204:207], v229 offset:6144\n"
        "v_perm_b32 v224, v208, v236, s60\n"
        "v_mfma_f32_16x16x32_bf16 v[8:11], v[160:163], v[176:179], v[8:11]\n"
        "ds_read_b32 v112, v224\n"
        "v_perm_b32 v225, v208, v236, s61\n"
        "v_mfma_f32_16x16x32_bf16 v[8:11], v[164:167], v[180:183], v[8:11]\n"
        "ds_read_b32 v113, v225\n"
        "v_perm_b32 v226, v208, v236, s62\n"
        "v_mfma_f32_16x16x32_bf16 v[12:15], v[168:171], v[176:179], v[12:15]\n"
        "ds_read_b32 v114, v226\n"
        "v_perm_b32 v227, v208, v236, s63\n"
        "v_mfma_f32_16x16x32_bf16 v[12:15], v[172:175], v[180:183], v[12:15]\n"
        "ds_read_b32 v115, v227\n"
        "v_bitop3_b32 v219, v219, v223, s39 bitop3:0x7e\n"
        "s_waitcnt lgkmcnt(8)\n"
        "v_mfma_f32_16x16x32_bf16 v[16:19], v[144:147], v[184:187], v[16:19]\n"
        "ds_read_b128 v[176:179], v230 offset:0\n"
        "v_or3_b32 v210, v210, v211, v218\n"
        "v_mfma_f32_16x16x32_bf16 v[16:19], v[148:151], v[188:191], v[16:19]\n"
        "ds_read_b128 v[180:183], v231 offset:0\n"
        "v_perm_b32 v224, v209, v236, s60\n"
        "v_mfma_f32_16x16x32_bf16 v[20:23], v[152:155], v[184:187], v[20:23]\n"
        "ds_read_b32 v116, v224\n"
        "v_perm_b32 v225, v209, v236, s61\n"
        "v_mfma_f32_16x16x32_bf16 v[20:23], v[156:159], v[188:191], v[20:23]\n"
        "ds_read_b32 v117, v225\n"
        "v_perm_b32 v226, v209, v236, s62\n"
        "v_mfma_f32_16x16x32_bf16 v[24:27], v[160:163], v[184:187], v[24:27]\n"
        "ds_read_b32 v118, v226\n"
        "v_perm_b32 v227, v209, v236, s63\n"
        "v_mfma_f32_16x16x32_bf16 v[24:27], v[164:167], v[188:191], v[24:27]\n"
        "ds_read_b32 v119, v227\n"
        "v_perm_b32 v224, v212, v236, s60\n"
        "v_mfma_f32_16x16x32_bf16 v[28:31], v[168:171], v[184:187], v[28:31]\n"
        "ds_read_b32 v120, v224\n"
        "v_perm_b32 v225, v212, v236, s61\n"
        "v_mfma_f32_16x16x32_bf16 v[28:31], v[172:175], v[188:191], v[28:31]\n"
        "ds_read_b32 v121, v225\n"
        "v_or_b32 v210, v210, v219\n"
        "s_waitcnt lgkmcnt(14)\n"
        "v_mfma_f32_16x16x32_bf16 v[32:35], v[144:147], v[192:195], v[32:35]\n"
        "ds_read_b128 v[184:187], v230 offset:2048\n"
        "v_cmp_ne_u32 vcc, 0, v210\n"
        "v_mfma_f32_16x16x32_bf16 v[32:35], v[148:151], v[196:199], v[32:35]\n"
        "ds_read_b128 v[188:191], v231 offset:2048\n"
        "v_perm_b32 v226, v212, v236, s62\n"
        "v_mfma_f32_16x16x32_bf16 v[36:39], v[152:155], v[192:195], v[36:39]\n"
        "ds_read_b32 v122, v226\n"
        "v_perm_b32 v227, v212, v236, s63\n"
        "v_mfma_f32_16x16x32_bf16 v[36:39], v[156:159], v[196:199], v[36:39]\n"
        "ds_read_b32 v123, v227\n"
        "v_perm_b32 v224, v213, v236, s60\n"
        "v_mfma_f32_16x16x32_bf16 v[40:43], v[160:163], v[192:195], v[40:43]\n"
        "ds_read_b32 v124, v224\n"
        "v_perm_b32 v225, v213, v236, s61\n"
        "v_mfma_f32_16x16x32_bf16 v[40:43], v[164:167], v[196:199], v[40:43]\n"
        "ds_read_b32 v125, v225\n"
        "v_perm_b32 v226, v213, v236, s62\n"
        "v_mfma_f32_16x16x32_bf16 v[44:47], v[168:171], v[192:195], v[44:47]\n"
        "ds_read_b32 v126, v226\n"
        "v_perm_b32 v227, v213, v236, s63\n"
        "v_mfma_f32_16x16x32_bf16 v[44:47], v[172:175], v[196:199], v[44:47]\n"
        "ds_read_b32 v127, v227\n"
        "s_waitcnt lgkmcnt(15)\n"
        "v_mfma_f32_16x16x32_bf16 v[48:51], v[144:147], v[200:203], v[48:51]\n"
        "ds_read_b128 v[192:195], v230 offset:4096\n"
        "s_cbranch_vccnz .Ln_f10\n"
        ".Ln_b10:\n"
        "v_mfma_f32_16x16x32_bf16 v[48:51], v[148:151], v[204:207], v[48:51]\n"
        "ds_read_b128 v[196:199], v231 offset:4096\n"
        "v_perm_b32 v224, v216, v236, s60\n"
        "v_mfma_f32_16x16x32_bf16 v[52:55], v[152:155], v[200:203], v[52:55]\n"
        "ds_read_b32 v128, v224\n"
        "v_perm_b32 v225, v216, v236, s61\n"
        "v_mfma_f32_16x16x32_bf16 v[52:55], v[156:159], v[204:207], v[52:55]\n"
        "ds_read_b32 v129, v225\n"
        "v_perm_b32 v226, v216, v236, s62\n"
        "v_mfma_f32_16x16x32_bf16 v[56:59], v[160:163], v[200:203], v[56:59]\n"
        "ds_read_b32 v130, v226\n"
        "v_perm_b32 v227, v216, v236, s63\n"
        "v_mfma_f32_16x16x32_bf16 v[56:59], v[164:167], v[204:207], v[56:59]\n"
        "ds_read_b32 v131, v227\n"
        ".Ln_tp2:\n"
        "s_cbranch_execz .Ln_ret\n"
        "v_perm_b32 v224, v217, v236, s60\n"
        "v_mfma_f32_16x16x32_bf16 v[60:63], v[168:171], v[200:203], v[60:63]\n"
        "ds_read_b32 v132, v224\n"
        "v_perm_b32 v225, v217, v236, s61\n"
        "v_mfma_f32_16x16x32_bf16 v[60:63], v[172:175], v[204:207], v[60:63]\n"
        "ds_read_b32 v133, v225\n"
        "s_waitcnt lgkmcnt(15)\n"
        "v_mfma_f32_16x16x32_bf16 v[64:67], v[144:147], v[176:179], v[64:67]\n"
        "ds_read_b128 v[200:203], v230 offset:6144\n"
        "v_mfma_f32_16x16x32_bf16 v[64:67], v[148:151], v[180:183], v[64:67]\n"
        "ds_read_b128 v[204:207], v231 offset:6144\n"
        "v_perm_b32 v226, v217, v236, s62\n"
        "v_mfma_f32_16x16x32_bf16 v[68:71], v[152:155], v[176:179], v[68:71]\n"
        "ds_read_b32 v134, v226\n"
        "v_perm_b32 v227, v217, v236, s63\n"
        "v_mfma_f32_16x16x32_bf16 v[68:71], v[156:159], v[180:183], v[68:71]\n"
        "ds_read_b32 v135, v227\n"
        "v_perm_b32 v224, v220, v236, s60\n"
        "v_mfma_f32_16x16x32_bf16 v[72:75], v[160:163], v[176:179], v[72:75]\n"
        "ds_read_b32 v136, v224\n"
        "v_perm_b32 v225, v220, v236, s61\n"
        "v_mfma_f32_16x16x32_bf16 v[72:75], v[164:167], v[180:183], v[72:75]\n"
        "ds_read_b32 v137, v225\n"
        "v_perm_b32 v226, v220, v236, s62\n"
        "s_waitcnt lgkmcnt(15)\n"
        "v_mfma_f32_16x16x32_bf16 v[76:79], v[144:147], v[184:187], v[76:79]\n"
        "ds_read_b32 v138, v226\n"
        "v_perm_b32 v227, v220, v236, s63\n"
        "v_mfma_f32_16x16x32_bf16 v[76:79], v[148:151], v[188:191], v[76:79]\n"
        "ds_read_b32 v139, v227\n"
        "v_perm_b32 v224, v221, v236, s60\n"
        "v_mfma_f32_16x16x32_bf16 v[80:83], v[152:155], v[184:187], v[80:83]\n"
        "ds_read_b32 v140, v224\n"
        "v_perm_b32 v225, v221, v236, s61\n"
        "v_mfma_f32_16x16x32_bf16 v[80:83], v[156:159], v[188:191], v[80:83]\n"
        "ds_read_b32 v141, v225\n"
        "v_perm_b32 v226, v221, v236, s62\n"
        "v_mfma_f32_16x16x32_bf16 v[84:87], v[160:163], v[184:187], v[84:87]\n"
        "ds_read_b32 v142, v226\n"
        "v_perm_b32 v227, v221, v236, s63\n"
        "v_mfma_f32_16x16x32_bf16 v[84:87], v[164:167], v[188:191], v[84:87]\n"
        "ds_read_b32 v143, v227\n"
        "s_waitcnt lgkmcnt(10)\n"
        "s_barrier\n"
        "v_mfma_f32_16x16x32_bf16 v[88:91], v[144:147], v[192:195], v[88:91]\n"
        "ds_read_b128 v[208:211], v232 offset:32768\n"
        "v_mfma_f32_16x16x32_bf16 v[88:91], v[148:151], v[196:199], v[88:91]\n"
        "ds_read_b128 v[212:215], v233 offset:32768\n"
        "v_mfma_f32_16x16x32_bf16 v[92:95], v[152:155], v[192:195], v[92:95]\n"
        "v_mfma_f32_16x16x32_bf16 v[92:95], v[156:159], v[196:199], v[92:95]\n"
        "ds_read_b128 v[216:219], v234 offset:32768\n"
        "v_mfma_f32_16x16x32_bf16 v[96:99], v[160:163], v[192:195], v[96:99]\n"
        "ds_read_b128 v[220:223], v235 offset:32768\n"
        "v_mfma_f32_16x16x32_bf16 v[96:99], v[164:167], v[196:199], v[96:99]\n"
        "v_mfma_f32_16x16x32_bf16 v[100:103], v[144:147], v[200:203], v[100:103]\n"
        "ds_read_b128 v[176:179], v228 offset:16384\n"
        "v_mfma_f32_16x16x32_bf16 v[100:103], v[148:151], v[204:207], v[100:103]\n"
        "ds_read_b128 v[180:183], v229 offset:16384\n"
        "v_mfma_f32_16x16x32_bf16 v[104:107], v[152:155], v[200:203], v[104:107]\n"
        "v_mfma_f32_16x16x32_bf16 v[104:107], v[156:159], v[204:207], v[104:107]\n"
        "ds_read_b128 v[184:187], v228 offset:18432\n"
        "v_mfma_f32_16x16x32_bf16 v[108:111], v[160:163], v[200:203], v[108:111]\n"
        "ds_read_b128 v[188:191], v229 offset:18432\n"
        "v_mfma_f32_16x16x32_bf16 v[108:111], v[164:167], v[204:207], v[108:111]\n"
        "s_waitcnt lgkmcnt(2)\n"
        "v_mfma_f32_16x16x32_bf16 v[0:3], v[112:115], v[176:179], v[0:3]\n"
        "ds_read_b128 v[192:195], v228 offset:20480\n"
        "v_bitop3_b32 v210, v210, v214, s34 bitop3:0x7e\n"
        "v_mfma_f32_16x16x32_bf16 v[0:3], v[116:119], v[180:183], v[0:3]\n"
        "ds_read_b128 v[196:199], v229 offset:20480\n"
        "v_bitop3_b32 v211, v211, v215, s39 bitop3:0x7e\n"
        "v_mfma_f32_16x16x32_bf16 v[4:7], v[120:123], v[176:179], v[4:7]\n"
        "ds_read_b128 v[200:203], v228 offset:22528\n"
        "v_bitop3_b32 v218, v218, v222, s34 bitop3:0x7e\n"
        "v_mfma_f32_16x16x32_bf16 v[4:7], v[124:127], v[180:183], v[4:7]\n"
        "ds_read_b128 v[204:207], v229 offset:22528\n"
        "v_perm_b32 v224, v208, v236, s60\n"
        "v_mfma_f32_16x16x32_bf16 v[8:11], v[128:131], v[176:179], v[8:11]\n"
        "ds_read_b32 v144, v224\n"
        "v_perm_b32 v225, v208, v236, s61\n"
        "v_mfma_f32_16x16x32_bf16 v[8:11], v[132:135], v[180:183], v[8:11]\n"
        "ds_read_b32 v145, v225\n"
        "v_perm_b32 v226, v208, v236, s62\n"
        "v_mfma_f32_16x16x32_bf16 v[12:15], v[136:139], v[176:179], v[12:15]\n"
        "ds_read_b32 v146, v226\n"
        "v_perm_b32 v227, v208, v236, s63\n"
        "v_mfma_f32_16x16x32_bf16 v[12:15], v[140:143], v[180:183], v[12:15]\n"
        "ds_read_b32 v147, v227\n"
        "v_bitop3_b32 v219, v219, v223, s39 bitop3:0x7e\n"
        "s_waitcnt lgkmcnt(8)\n"
        "v_mfma_f32_16x16x32_bf16 v[16:19], v[112:115], v[184:187], v[16:19]\n"
        "ds_read_b128 v[176:179], v230 offset:16384\n"
        "v_or3_b32 v210, v210, v211, v218\n"
        "v_mfma_f32_16x16x32_bf16 v[16:19], v[116:119], v[188:191], v[16:19]\n"
        "ds_read_b128 v[180:183], v231 offset:16384\n"
        "v_perm_b32 v224, v209, v236, s60\n"
        "v_mfma_f32_16x16x32_bf16 v[20:23], v[120:123], v[184:187], v[20:23]\n"
        "ds_read_b32 v148, v224\n"
        "v_perm_b32 v225, v209, v236, s61\n"
        "v_mfma_f32_16x16x32_bf16 v[20:23], v[124:127], v[188:191], v[20:23]\n"
        "ds_read_b32 v149, v225\n"
        "v_perm_b32 v226, v209, v236, s62\n"
        "v_mfma_f32_16x16x32_bf16 v[24:27], v[128:131], v[184:187], v[24:27]\n"
        "ds_read_b32 v150, v226\n"
        "v_perm_b32 v227, v209, v236, s63\n"
        "v_mfma_f32_16x16x32_bf16 v[24:27], v[132:135], v[188:191], v[24:27]\n"
        "ds_read_b32 v151, v227\n"
        "v_perm_b32 v224, v212, v236, s60\n"
        "v_mfma_f32_16x16x32_bf16 v[28:31], v[136:139], v[184:187], v[28:31]\n"
        "ds_read_b32 v152, v224\n"
        "v_perm_b32 v225, v212, v236, s61\n"
        "v_mfma_f32_16x16x32_bf16 v[28:31], v[140:143], v[188:191], v[28:31]\n"
        "ds_read_b32 v153, v225\n"
        "v_or_b32 v210, v210, v219\n"
        "s_waitcnt lgkmcnt(14)\n"
        "v_mfma_f32_16x16x32_bf16 v[32:35], v[112:115], v[192:195], v[32:35]\n"
        "ds_read_b128 v[184:187], v230 offset:18432\n"
        "v_cmp_ne_u32 vcc, 0, v210\n"
        "v_mfma_f32_16x16x32_bf16 v[32:35], v[116:119], v[196:199], v[32:35]\n"
        "ds_read_b128 v[188:191], v231 offset:18432\n"
        "v_perm_b32 v226, v212, v236, s62\n"
        "v_mfma_f32_16x16x32_bf16 v[36:39], v[120:123], v[192:195], v[36:39]\n"
        "ds_read_b32 v154, v226\n"
        "v_perm_b32 v227, v212, v236, s63\n"
        "v_mfma_f32_16x16x32_bf16 v[36:39], v[124:127], v[196:199], v[36:39]\n"
        "ds_read_b32 v155, v227\n"
        "v_perm_b32 v224, v213, v236, s60\n"
        "v_mfma_f32_16x16x32_bf16 v[40:43], v[128:131], v[192:195], v[40:43]\n"
        "ds_read_b32 v156, v224\n"
        "v_perm_b32 v225, v213, v236, s61\n"
        "v_mfma_f32_16x16x32_bf16 v[40:43], v[132:135], v[196:199], v[40:43]\n"
        "ds_read_b32 v157, v225\n"
        "v_perm_b32 v226, v213, v236, s62\n"
        "v_mfma_f32_16x16x32_bf16 v[44:47], v[136:139], v[192:195], v[44:47]\n"
        "ds_read_b32 v158, v226\n"
        "v_perm_b32 v227, v213, v236, s63\n"
        "v_mfma_f32_16x16x32_bf16 v[44:47], v[140:143], v[196:199], v[44:47]\n"
        "ds_read_b32 v159, v227\n"
        "s_waitcnt lgkmcnt(15)\n"
        "v_mfma_f32_16x16x32_bf16 v[48:51], v[112:115], v[200:203], v[48:51]\n"
        "ds_read_b128 v[192:195], v230 offset:20480\n"
        "s_cbranch_vccnz .Ln_f11\n"
        ".Ln_b11:\n"
        "v_mfma_f32_16x16x32_bf16 v[48:51], v[116:119], v[204:207], v[48:51]\n"
        "ds_read_b128 v[196:199], v231 offset:20480\n"
        "v_perm_b32 v224, v216, v236, s60\n"
        "v_mfma_f32_16x16x32_bf16 v[52:55], v[120:123], v[200:203], v[52:55]\n"
        "ds_read_b32 v160, v224\n"
        "v_perm_b32 v225, v216, v236, s61\n"
        "v_mfma_f32_16x16x32_bf16 v[52:55], v[124:127], v[204:207], v[52:55]\n"
        "ds_read_b32 v161, v225\n"
        "v_perm_b32 v226, v216, v236, s62\n"
        "v_mfma_f32_16x16x32_bf16 v[56:59], v[128:131], v[200:203], v[56:59]\n"
        "ds_read_b32 v162, v226\n"
        "v_perm_b32 v227, v216, v236, s63\n"
        "v_mfma_f32_16x16x32_bf16 v[56:59], v[132:135], v[204:207], v[56:59]\n"
        "ds_read_b32 v163, v227\n"
        "v_perm_b32 v224, v217, v236, s60\n"
        "v_mfma_f32_16x16x32_bf16 v[60:63], v[136:139], v[200:203], v[60:63]\n"
        "ds_read_b32 v164, v224\n"
        "v_perm_b32 v225, v217, v236, s61\n"
        "v_mfma_f32_16x16x32_bf16 v[60:63], v[140:143], v[204:207], v[60:63]\n"
        "ds_read_b32 v165, v225\n"
        "s_waitcnt lgkmcnt(15)\n"
        "v_mfma_f32_16x16x32_bf16 v[64:67], v[112:115], v[176:179], v[64:67]\n"
        "ds_read_b128 v[200:203], v230 offset:22528\n"
        "v_mfma_f32_16x16x32_bf16 v[64:67], v[116:119], v[180:183], v[64:67]\n"
        "ds_read_b128 v[204:207], v231 offset:22528\n"
        "v_perm_b32 v226, v217, v236, s62\n"
        "v_mfma_f32_16x16x32_bf16 v[68:71], v[120:123], v[176:179], v[68:71]\n"
        "ds_read_b32 v166, v226\n"
        "v_perm_b32 v227, v217, v236, s63\n"
        "v_mfma_f32_16x16x32_bf16 v[68:71], v[124:127], v[180:183], v[68:71]\n"
        "ds_read_b32 v167, v227\n"
        "v_perm_b32 v224, v220, v236, s60\n"
        "v_mfma_f32_16x16x32_bf16 v[72:75], v[128:131], v[176:179], v[72:75]\n"
        "ds_read_b32 v168, v224\n"
        "v_perm_b32 v225, v220, v236, s61\n"
        "v_mfma_f32_16x16x32_bf16 v[72:75], v[132:135], v[180:183], v[72:75]\n"
        "ds_read_b32 v169, v225\n"
        "v_perm_b32 v226, v220, v236, s62\n"
        "s_waitcnt lgkmcnt(15)\n"
        "v_mfma_f32_16x16x32_bf16 v[76:79], v[112:115], v[184:187], v[76:79]\n"
        "ds_read_b32 v170, v226\n"
        "v_perm_b32 v227, v220, v236, s63\n"
        "v_mfma_f32_16x16x32_bf16 v[76:79], v[116:119], v[188:191], v[76:79]\n"
        "ds_read_b32 v171, v227\n"
        "v_perm_b32 v224, v221, v236, s60\n"
        "v_mfma_f32_16x16x32_bf16 v[80:83], v[120:123], v[184:187], v[80:83]\n"
        "ds_read_b32 v172, v224\n"
        "v_perm_b32 v225, v221, v236, s61\n"
        "v_mfma_f32_16x16x32_bf16 v[80:83], v[124:127], v[188:191], v[80:83]\n"
        "ds_read_b32 v173, v225\n"
        "v_perm_b32 v226, v221, v236, s62\n"
        "v_mfma_f32_16x16x32_bf16 v[84:87], v[128:131], v[184:187], v[84:87]\n"
        "ds_read_b32 v174, v226\n"
        "v_perm_b32 v227, v221, v236, s63\n"
        "v_mfma_f32_16x16x32_bf16 v[84:87], v[132:135], v[188:191], v[84:87]\n"
        "ds_read_b32 v175, v227\n"
        "s_waitcnt lgkmcnt(10)\n"
        "s_barrier\n"
        "v_mfma_f32_16x16x32_bf16 v[88:91], v[112:115], v[192:195], v[88:91]\n"
        "ds_read_b128 v[208:211], v232 offset:0\n"
        "v_mfma_f32_16x16x32_bf16 v[88:91], v[116:119], v[196:199], v[88:91]\n"
        "ds_read_b128 v[212:215], v233 offset:0\n"
        "v_mfma_f32_16x16x32_bf16 v[92:95], v[120:123], v[192:195], v[92:95]\n"
        "v_mfma_f32_16x16x32_bf16 v[92:95], v[124:127], v[196:199], v[92:95]\n"
        "ds_read_b128 v[216:219], v234 offset:0\n"
        "v_mfma_f32_16x16x32_bf16 v[96:99], v[128:131], v[192:195], v[96:99]\n"
        "ds_read_b128 v[220:223], v235 offset:0\n"
        "v_mfma_f32_16x16x32_bf16 v[96:99], v[132:135], v[196:199], v[96:99]\n"
        "v_mfma_f32_16x16x32_bf16 v[100:103], v[112:115], v[200:203], v[100:103]\n"
        "ds_read_b128 v[176:179], v228 offset:32768\n"
        "v_mfma_f32_16x16x32_bf16 v[100:103], v[116:119], v[204:207], v[100:103]\n"
        "ds_read_b128 v[180:183], v229 offset:32768\n"
        "v_mfma_f32_16x16x32_bf16 v[104:107], v[120:123], v[200:203], v[104:107]\n"
        "v_mfma_f32_16x16x32_bf16 v[104:107], v[124:127], v[204:207], v[104:107]\n"
        "ds_read_b128 v[184:187], v228 offset:34816\n"
        "v_mfma_f32_16x16x32_bf16 v[108:111], v[128:131], v[200:203], v[108:111]\n"
        "ds_read_b128 v[188:191], v229 offset:34816\n"
        "v_mfma_f32_16x16x32_bf16 v[108:111], v[132:135], v[204:207], v[108:111]\n"
        "s_waitcnt lgkmcnt(2)\n"
        "v_mfma_f32_16x16x32_bf16 v[0:3], v[144:147], v[176:179], v[0:3]\n"
        "ds_read_b128 v[192:195], v228 offset:36864\n"
        "v_bitop3_b32 v210, v210, v214, s34 bitop3:0x7e\n"
        "v_mfma_f32_16x16x32_bf16 v[0:3], v[148:151], v[180:183], v[0:3]\n"
        "ds_read_b128 v[196:199], v229 offset:36864\n"
        "v_bitop3_b32 v211, v211, v215, s39 bitop3:0x7e\n"
        "v_mfma_f32_16x16x32_bf16 v[4:7], v[152:155], v[176:179], v[4:7]\n"
        "ds_read_b128 v[200:203], v228 offset:38912\n"
        "v_bitop3_b32 v218, v218, v222, s34 bitop3:0x7e\n"
        "v_mfma_f32_16x16x32_bf16 v[4:7], v[156:159], v[180:183], v[4:7]\n"
        "ds_read_b128 v[204:207], v229 offset:38912\n"
        "v_perm_b32 v224, v208, v236, s60\n"
        "v_mfma_f32_16x16x32_bf16 v[8:11], v[160:163], v[176:179], v[8:11]\n"
        "ds_read_b32 v112, v224\n"
        "v_perm_b32 v225, v208, v236, s61\n"
        "v_mfma_f32_16x16x32_bf16 v[8:11], v[164:167], v[180:183], v[8:11]\n"
        "ds_read_b32 v113, v225\n"
        "v_perm_b32 v226, v208, v236, s62\n"
        "v_mfma_f32_16x16x32_bf16 v[12:15], v[168:171], v[176:179], v[12:15]\n"
        "ds_read_b32 v114, v226\n"
        "v_perm_b32 v227, v208, v236, s63\n"
        "v_mfma_f32_16x16x32_bf16 v[12:15], v[172:175], v[180:183], v[12:15]\n"
        "ds_read_b32 v115, v227\n"
        "v_bitop3_b32 v219, v219, v223, s39 bitop3:0x7e\n"
        "s_waitcnt lgkmcnt(8)\n"
        "v_mfma_f32_16x16x32_bf16 v[16:19], v[144:147], v[184:187], v[16:19]\n"
        "ds_read_b128 v[176:179], v230 offset:32768\n"
        "v_or3_b32 v210, v210, v211, v218\n"
        "v_mfma_f32_16x16x32_bf16 v[16:19], v[148:151], v[188:191], v[16:19]\n"
        "ds_read_b128 v[180:183], v231 offset:32768\n"
        "v_perm_b32 v224, v209, v236, s60\n"
        "v_mfma_f32_16x16x32_bf16 v[20:23], v[152:155], v[184:187], v[20:23]\n"
        "ds_read_b32 v116, v224\n"
        "v_perm_b32 v225, v209, v236, s61\n"
        "v_mfma_f32_16x16x32_bf16 v[20:23], v[156:159], v[188:191], v[20:23]\n"
        "ds_read_b32 v117, v225\n"
        "v_perm_b32 v226, v209, v236, s62\n"
        "v_mfma_f32_16x16x32_bf16 v[24:27], v[160:163], v[184:187], v[24:27]\n"
        "ds_read_b32 v118, v226\n"
        "v_perm_b32 v227, v209, v236, s63\n"
        "v_mfma_f32_16x16x32_bf16 v[24:27], v[164:167], v[188:191], v[24:27]\n"
        "ds_read_b32 v119, v227\n"
        "v_perm_b32 v224, v212, v236, s60\n"
        "v_mfma_f32_16x16x32_bf16 v[28:31], v[168:171], v[184:187], v[28:31]\n"
        "ds_read_b32 v120, v224\n"
        "v_perm_b32 v225, v212, v236, s61\n"
        "v_mfma_f32_16x16x32_bf16 v[28:31], v[172:175], v[188:191], v[28:31]\n"
        "ds_read_b32 v121, v225\n"
        "v_or_b32 v210, v210, v219\n"
        "s_waitcnt lgkmcnt(14)\n"
        "v_mfma_f32_16x16x32_bf16 v[32:35], v[144:147], v[192:195], v[32:35]\n"
        "ds_read_b128 v[184:187], v230 offset:34816\n"
        "v_cmp_ne_u32 vcc, 0, v210\n"
        "v_mfma_f32_16x16x32_bf16 v[32:35], v[148:151], v[196:199], v[32:35]\n"
        "ds_read_b128 v[188:191], v231 offset:34816\n"
        "v_perm_b32 v226, v212, v236, s62\n"
        "v_mfma_f32_16x16x32_bf16 v[36:39], v[152:155], v[192:195], v[36:39]\n"
        "ds_read_b32 v122, v226\n"
        "v_perm_b32 v227, v212, v236, s63\n"
        "v_mfma_f32_16x16x32_bf16 v[36:39], v[156:159], v[196:199], v[36:39]\n"
        "ds_read_b32 v123, v227\n"
        "v_perm_b32 v224, v213, v236, s60\n"
        "v_mfma_f32_16x16x32_bf16 v[40:43], v[160:163], v[192:195], v[40:43]\n"
        "ds_read_b32 v124, v224\n"
        "v_perm_b32 v225, v213, v236, s61\n"
        "v_mfma_f32_16x16x32_bf16 v[40:43], v[164:167], v[196:199], v[40:43]\n"
        "ds_read_b32 v125, v225\n"
        "v_perm_b32 v226, v213, v236, s62\n"
        "v_mfma_f32_16x16x32_bf16 v[44:47], v[168:171], v[192:195], v[44:47]\n"
        "ds_read_b32 v126, v226\n"
        "v_perm_b32 v227, v213, v236, s63\n"
        "v_mfma_f32_16x16x32_bf16 v[44:47], v[172:175], v[196:199], v[44:47]\n"
        "ds_read_b32 v127, v227\n"
        "s_waitcnt lgkmcnt(15)\n"
        "v_mfma_f32_16x16x32_bf16 v[48:51], v[144:147], v[200:203], v[48:51]\n"
        "ds_read_b128 v[192:195], v230 offset:36864\n"
        "s_cbranch_vccnz .Ln_f12\n"
        ".Ln_b12:\n"
        "v_mfma_f32_16x16x32_bf16 v[48:51], v[148:151], v[204:207], v[48:51]\n"
        "ds_read_b128 v[196:199], v231 offset:36864\n"
        "v_perm_b32 v224, v216, v236, s60\n"
        "v_mfma_f32_16x16x32_bf16 v[52:55], v[152:155], v[200:203], v[52:55]\n"
        "ds_read_b32 v128, v224\n"
        "v_perm_b32 v225, v216, v236, s61\n"
        "v_mfma_f32_16x16x32_bf16 v[52:55], v[156:159], v[204:207], v[52:55]\n"
        "ds_read_b32 v129, v225\n"
        "v_perm_b32 v226, v216, v236, s62\n"
        "v_mfma_f32_16x16x32_bf16 v[56:59], v[160:163], v[200:203], v[56:59]\n"
        "ds_read_b32 v130, v226\n"
        "v_perm_b32 v227, v216, v236, s63\n"
        "v_mfma_f32_16x16x32_bf16 v[56:59], v[164:167], v[204:207], v[56:59]\n"
        "ds_read_b32 v131, v227\n"
        "v_perm_b32 v224, v217, v236, s60\n"
        "v_mfma_f32_16x16x32_bf16 v[60:63], v[168:171], v[200:203], v[60:63]\n"
        "ds_read_b32 v132, v224\n"
        "v_perm_b32 v225, v217, v236, s61\n"
        "v_mfma_f32_16x16x32_bf16 v[60:63], v[172:175], v[204:207], v[60:63]\n"
        "ds_read_b32 v133, v225\n"
        "s_waitcnt lgkmcnt(15)\n"
        "v_mfma_f32_16x16x32_bf16 v[64:67], v[144:147], v[176:179], v[64:67]\n"
        "ds_read_b128 v[200:203], v230 offset:38912\n"
        "v_mfma_f32_16x16x32_bf16 v[64:67], v[148:151], v[180:183], v[64:67]\n"
        "ds_read_b128 v[204:207], v231 offset:38912\n"
        "v_perm_b32 v226, v217, v236, s62\n"
        "v_mfma_f32_16x16x32_bf16 v[68:71], v[152:155], v[176:179], v[68:71]\n"
        "ds_read_b32 v134, v226\n"
        "v_perm_b32 v227, v217, v236, s63\n"
        "v_mfma_f32_16x16x32_bf16 v[68:71], v[156:159], v[180:183], v[68:71]\n"
        "ds_read_b32 v135, v227\n"
        "v_perm_b32 v224, v220, v236, s60\n"
        "v_mfma_f32_16x16x32_bf16 v[72:75], v[160:163], v[176:179], v[72:75]\n"
        "ds_read_b32 v136, v224\n"
        "v_perm_b32 v225, v220, v236, s61\n"
        "v_mfma_f32_16x16x32_bf16 v[72:75], v[164:167], v[180:183], v[72:75]\n"
        "ds_read_b32 v137, v225\n"
        "v_perm_b32 v226, v220, v236, s62\n"
        "s_waitcnt lgkmcnt(15)\n"
        "v_mfma_f32_16x16x32_bf16 v[76:79], v[144:147], v[184:187], v[76:79]\n"
        "ds_read_b32 v138, v226\n"
        "v_perm_b32 v227, v220, v236, s63\n"
        "v_mfma_f32_16x16x32_bf16 v[76:79], v[148:151], v[188:191], v[76:79]\n"
        "ds_read_b32 v139, v227\n"
        "v_perm_b32 v224, v221, v236, s60\n"
        "v_mfma_f32_16x16x32_bf16 v[80:83], v[152:155], v[184:187], v[80:83]\n"
        "ds_read_b32 v140, v224\n"
        "v_perm_b32 v225, v221, v236, s61\n"
        "v_mfma_f32_16x16x32_bf16 v[80:83], v[156:159], v[188:191], v[80:83]\n"
        "ds_read_b32 v141, v225\n"
        "v_perm_b32 v226, v221, v236, s62\n"
        "v_mfma_f32_16x16x32_bf16 v[84:87], v[160:163], v[184:187], v[84:87]\n"
        "ds_read_b32 v142, v226\n"
        "v_perm_b32 v227, v221, v236, s63\n"
        "v_mfma_f32_16x16x32_bf16 v[84:87], v[164:167], v[188:191], v[84:87]\n"
        "ds_read_b32 v143, v227\n"
        "s_waitcnt lgkmcnt(10)\n"
        "s_barrier\n"
        "v_mfma_f32_16x16x32_bf16 v[88:91], v[144:147], v[192:195], v[88:91]\n"
        "ds_read_b128 v[208:211], v232 offset:16384\n"
        "v_mfma_f32_16x16x32_bf16 v[88:91], v[148:151], v[196:199], v[88:91]\n"
        "ds_read_b128 v[212:215], v233 offset:16384\n"
        "v_mfma_f32_16x16x32_bf16 v[92:95], v[152:155], v[192:195], v[92:95]\n"
        "v_mfma_f32_16x16x32_bf16 v[92:95], v[156:159], v[196:199], v[92:95]\n"
        "ds_read_b128 v[216:219], v234 offset:16384\n"
        "v_mfma_f32_16x16x32_bf16 v[96:99], v[160:163], v[192:195], v[96:99]\n"
        "ds_read_b128 v[220:223], v235 offset:16384\n"
        "v_mfma_f32_16x16x32_bf16 v[96:99], v[164:167], v[196:199], v[96:99]\n"
        "v_mfma_f32_16x16x32_bf16 v[100:103], v[144:147], v[200:203], v[100:103]\n"
        "ds_read_b128 v[176:179], v228 offset:0\n"
        "v_mfma_f32_16x16x32_bf16 v[100:103], v[148:151], v[204:207], v[100:103]\n"
        "ds_read_b128 v[180:183], v229 offset:0\n"
        "v_mfma_f32_16x16x32_bf16 v[104:107], v[152:155], v[200:203], v[104:107]\n"
        "v_mfma_f32_16x16x32_bf16 v[104:107], v[156:159], v[204:207], v[104:107]\n"
        "ds_read_b128 v[184:187], v228 offset:2048\n"
        "v_mfma_f32_16x16x32_bf16 v[108:111], v[160:163], v[200:203], v[108:111]\n"
        "ds_read_b128 v[188:191], v229 offset:2048\n"
        "v_mfma_f32_16x16x32_bf16 v[108:111], v[164:167], v[204:207], v[108:111]\n"
        "s_waitcnt lgkmcnt(2)\n"
        "v_mfma_f32_16x16x32_bf16 v[0:3], v[112:115], v[176:179], v[0:3]\n"
        "ds_read_b128 v[192:195], v228 offset:4096\n"
        "v_bitop3_b32 v210, v210, v214, s34 bitop3:0x7e\n"
        "v_mfma_f32_16x16x32_bf16 v[0:3], v[116:119], v[180:183], v[0:3]\n"
        "ds_read_b128 v[196:199], v229 offset:4096\n"
        "v_bitop3_b32 v211, v211, v215, s39 bitop3:0x7e\n"
        "v_mfma_f32_16x16x32_bf16 v[4:7], v[120:123], v[176:179], v[4:7]\n"
        "ds_read_b128 v[200:203], v228 offset:6144\n"
        "v_bitop3_b32 v218, v218, v222, s34 bitop3:0x7e\n"
        "v_mfma_f32_16x16x32_bf16 v[4:7], v[124:127], v[180:183], v[4:7]\n"
        "ds_read_b128 v[204:207], v229 offset:6144\n"
        "v_perm_b32 v224, v208, v236, s60\n"
        "v_mfma_f32_16x16x32_bf16 v[8:11], v[128:131], v[176:179], v[8:11]\n"
        "ds_read_b32 v144, v224\n"
        "v_perm_b32 v225, v208, v236, s61\n"
        "v_mfma_f32_16x16x32_bf16 v[8:11], v[132:135], v[180:183], v[8:11]\n"
        "ds_read_b32 v145, v225\n"
        "v_perm_b32 v226, v208, v236, s62\n"
        "v_mfma_f32_16x16x32_bf16 v[12:15], v[136:139], v[176:179], v[12:15]\n"
        "ds_read_b32 v146, v226\n"
        "v_perm_b32 v227, v208, v236, s63\n"
        "v_mfma_f32_16x16x32_bf16 v[12:15], v[140:143], v[180:183], v[12:15]\n"
        "ds_read_b32 v147, v227\n"
        "v_bitop3_b32 v219, v219, v223, s39 bitop3:0x7e\n"
        "s_waitcnt lgkmcnt(8)\n"
        "v_mfma_f32_16x16x32_bf16 v[16:19], v[112:115], v[184:187], v[16:19]\n"
        "ds_read_b128 v[176:179], v230 offset:0\n"
        "v_or3_b32 v210, v210, v211, v218\n"
        "v_mfma_f32_16x16x32_bf16 v[16:19], v[116:119], v[188:191], v[16:19]\n"
        "ds_read_b128 v[180:183], v231 offset:0\n"
        "v_perm_b32 v224, v209, v236, s60\n"
        "v_mfma_f32_16x16x32_bf16 v[20:23], v[120:123], v[184:187], v[20:23]\n"
        "ds_read_b32 v148, v224\n"
        "v_perm_b32 v225, v209, v236, s61\n"
        "v_mfma_f32_16x16x32_bf16 v[20:23], v[124:127], v[188:191], v[20:23]\n"
        "ds_read_b32 v149, v225\n"
        "v_perm_b32 v226, v209, v236, s62\n"
        "v_mfma_f32_16x16x32_bf16 v[24:27], v[128:131], v[184:187], v[24:27]\n"
        "ds_read_b32 v150, v226\n"
        "v_perm_b32 v227, v209, v236, s63\n"
        "v_mfma_f32_16x16x32_bf16 v[24:27], v[132:135], v[188:191], v[24:27]\n"
        "ds_read_b32 v151, v227\n"
        "v_perm_b32 v224, v212, v236, s60\n"
        "v_mfma_f32_16x16x32_bf16 v[28:31], v[136:139], v[184:187], v[28:31]\n"
        "ds_read_b32 v152, v224\n"
        "v_perm_b32 v225, v212, v236, s61\n"
        "v_mfma_f32_16x16x32_bf16 v[28:31], v[140:143], v[188:191], v[28:31]\n"
        "ds_read_b32 v153, v225\n"
        "v_or_b32 v210, v210, v219\n"
        "s_waitcnt lgkmcnt(14)\n"
        "v_mfma_f32_16x16x32_bf16 v[32:35], v[112:115], v[192:195], v[32:35]\n"
        "ds_read_b128 v[184:187], v230 offset:2048\n"
        "v_cmp_ne_u32 vcc, 0, v210\n"
        "v_mfma_f32_16x16x32_bf16 v[32:35], v[116:119], v[196:199], v[32:35]\n"
        "ds_read_b128 v[188:191], v231 offset:2048\n"
        "v_perm_b32 v226, v212, v236, s62\n"
        "v_mfma_f32_16x16x32_bf16 v[36:39], v[120:123], v[192:195], v[36:39]\n"
        "ds_read_b32 v154, v226\n"
        "v_perm_b32 v227, v212, v236, s63\n"
        "v_mfma_f32_16x16x32_bf16 v[36:39], v[124:127], v[196:199], v[36:39]\n"
        "ds_read_b32 v155, v227\n"
        "v_perm_b32 v224, v213, v236, s60\n"
        "v_mfma_f32_16x16x32_bf16 v[40:43], v[128:131], v[192:195], v[40:43]\n"
        "ds_read_b32 v156, v224\n"
        "v_perm_b32 v225, v213, v236, s61\n"
        "v_mfma_f32_16x16x32_bf16 v[40:43], v[132:135], v[196:199], v[40:43]\n"
        "ds_read_b32 v157, v225\n"
        "v_perm_b32 v226, v213, v236, s62\n"
        "v_mfma_f32_16x16x32_bf16 v[44:47], v[136:139], v[192:195], v[44:47]\n"
        "ds_read_b32 v158, v226\n"
        "v_perm_b32 v227, v213, v236, s63\n"
        "v_mfma_f32_16x16x32_bf16 v[44:47], v[140:143], v[196:199], v[44:47]\n"
        "ds_read_b32 v159, v227\n"
        "s_waitcnt lgkmcnt(15)\n"
        "v_mfma_f32_16x16x32_bf16 v[48:51], v[112:115], v[200:203], v[48:51]\n"
        "ds_read_b128 v[192:195], v230 offset:4096\n"
        "s_cbranch_vccnz .Ln_f13\n"
        ".Ln_b13:\n"
        "v_mfma_f32_16x16x32_bf16 v[48:51], v[116:119], v[204:207], v[48:51]\n"
        "ds_read_b128 v[196:199], v231 offset:4096\n"
        "v_perm_b32 v224, v216, v236, s60\n"
        "v_mfma_f32_16x16x32_bf16 v[52:55], v[120:123], v[200:203], v[52:55]\n"
        "ds_read_b32 v160, v224\n"
        "v_perm_b32 v225, v216, v236, s61\n"
        "v_mfma_f32_16x16x32_bf16 v[52:55], v[124:127], v[204:207], v[52:55]\n"
        "ds_read_b32 v161, v225\n"
        "v_perm_b32 v226, v216, v236, s62\n"
        "v_mfma_f32_16x16x32_bf16 v[56:59], v[128:131], v[200:203], v[56:59]\n"
        "ds_read_b32 v162, v226\n"
        "v_perm_b32 v227, v216, v236, s63\n"
        "v_mfma_f32_16x16x32_bf16 v[56:59], v[132:135], v[204:207], v[56:59]\n"
        "ds_read_b32 v163, v227\n"
        "v_perm_b32 v224, v217, v236, s60\n"
        "v_mfma_f32_16x16x32_bf16 v[60:63], v[136:139], v[200:203], v[60:63]\n"
        "ds_read_b32 v164, v224\n"
        "v_perm_b32 v225, v217, v236, s61\n"
        "v_mfma_f32_16x16x32_bf16 v[60:63], v[140:143], v[204:207], v[60:63]\n"
        "ds_read_b32 v165, v225\n"
        "s_waitcnt lgkmcnt(15)\n"
        "v_mfma_f32_16x16x32_bf16 v[64:67], v[112:115], v[176:179], v[64:67]\n"
        "ds_read_b128 v[200:203], v230 offset:6144\n"
        "v_mfma_f32_16x16x32_bf16 v[64:67], v[116:119], v[180:183], v[64:67]\n"
        "ds_read_b128 v[204:207], v231 offset:6144\n"
        "v_perm_b32 v226, v217, v236, s62\n"
        "v_mfma_f32_16x16x32_bf16 v[68:71], v[120:123], v[176:179], v[68:71]\n"
        "ds_read_b32 v166, v226\n"
        "v_perm_b32 v227, v217, v236, s63\n"
        "v_mfma_f32_16x16x32_bf16 v[68:71], v[124:127], v[180:183], v[68:71]\n"
        "ds_read_b32 v167, v227\n"
        "v_perm_b32 v224, v220, v236, s60\n"
        "v_mfma_f32_16x16x32_bf16 v[72:75], v[128:131], v[176:179], v[72:75]\n"
        "ds_read_b32 v168, v224\n"
        "v_perm_b32 v225, v220, v236, s61\n"
        "v_mfma_f32_16x16x32_bf16 v[72:75], v[132:135], v[180:183], v[72:75]\n"
        "ds_read_b32 v169, v225\n"
        "v_perm_b32 v226, v220, v236, s62\n"
        "s_waitcnt lgkmcnt(15)\n"
        "v_mfma_f32_16x16x32_bf16 v[76:79], v[112:115], v[184:187], v[76:79]\n"
        "ds_read_b32 v170, v226\n"
        "v_perm_b32 v227, v220, v236, s63\n"
        "v_mfma_f32_16x16x32_bf16 v[76:79], v[116:119], v[188:191], v[76:79]\n"
        "ds_read_b32 v171, v227\n"
        "v_perm_b32 v224, v221, v236, s60\n"
        "v_mfma_f32_16x16x32_bf16 v[80:83], v[120:123], v[184:187], v[80:83]\n"
        "ds_read_b32 v172, v224\n"
        "v_perm_b32 v225, v221, v236, s61\n"
        "v_mfma_f32_16x16x32_bf16 v[80:83], v[124:127], v[188:191], v[80:83]\n"
        "ds_read_b32 v173, v225\n"
        "v_perm_b32 v226, v221, v236, s62\n"
        "v_mfma_f32_16x16x32_bf16 v[84:87], v[128:131], v[184:187], v[84:87]\n"
        "ds_read_b32 v174, v226\n"
        "v_perm_b32 v227, v221, v236, s63\n"
        "v_mfma_f32_16x16x32_bf16 v[84:87], v[132:135], v[188:191], v[84:87]\n"
        "ds_read_b32 v175, v227\n"
        "s_waitcnt lgkmcnt(10)\n"
        "s_barrier\n"
        "v_mfma_f32_16x16x32_bf16 v[88:91], v[112:115], v[192:195], v[88:91]\n"
        "ds_read_b128 v[208:211], v232 offset:32768\n"
        "v_mfma_f32_16x16x32_bf16 v[88:91], v[116:119], v[196:199], v[88:91]\n"
        "ds_read_b128 v[212:215], v233 offset:32768\n"
        "v_mfma_f32_16x16x32_bf16 v[92:95], v[120:123], v[192:195], v[92:95]\n"
        "v_mfma_f32_16x16x32_bf16 v[92:95], v[124:127], v[196:199], v[92:95]\n"
        "ds_read_b128 v[216:219], v234 offset:32768\n"
        "v_mfma_f32_16x16x32_bf16 v[96:99], v[128:131], v[192:195], v[96:99]\n"
        "ds_read_b128 v[220:223], v235 offset:32768\n"
        "v_mfma_f32_16x16x32_bf16 v[96:99], v[132:135], v[196:199], v[96:99]\n"
        "v_mfma_f32_16x16x32_bf16 v[100:103], v[112:115], v[200:203], v[100:103]\n"
        "ds_read_b128 v[176:179], v228 offset:16384\n"
        "v_mfma_f32_16x16x32_bf16 v[100:103], v[116:119], v[204:207], v[100:103]\n"
        "ds_read_b128 v[180:183], v229 offset:16384\n"
        "v_mfma_f32_16x16x32_bf16 v[104:107], v[120:123], v[200:203], v[104:107]\n"
        "v_mfma_f32_16x16x32_bf16 v[104:107], v[124:127], v[204:207], v[104:107]\n"
        "ds_read_b128 v[184:187], v228 offset:18432\n"
        "v_mfma_f32_16x16x32_bf16 v[108:111], v[128:131], v[200:203], v[108:111]\n"
        "ds_read_b128 v[188:191], v229 offset:18432\n"
        ".Ln_tp3:\n"
        "s_cbranch_execz .Ln_ret\n"
        "v_mfma_f32_16x16x32_bf16 v[108:111], v[132:135], v[204:207], v[108:111]\n"
        "s_waitcnt lgkmcnt(2)\n"
        "v_mfma_f32_16x16x32_bf16 v[0:3], v[144:147], v[176:179], v[0:3]\n"
        "ds_read_b128 v[192:195], v228 offset:20480\n"
        "v_bitop3_b32 v210, v210, v214, s34 bitop3:0x7e\n"
        "v_mfma_f32_16x16x32_bf16 v[0:3], v[148:151], v[180:183], v[0:3]\n"
        "ds_read_b128 v[196:199], v229 offset:20480\n"
        "v_bitop3_b32 v211, v211, v215, s39 bitop3:0x7e\n"
        "v_mfma_f32_16x16x32_bf16 v[4:7], v[152:155], v[176:179], v[4:7]\n"
        "ds_read_b128 v[200:203], v228 offset:22528\n"
        "v_bitop3_b32 v218, v218, v222, s34 bitop3:0x7e\n"
        "v_mfma_f32_16x16x32_bf16 v[4:7], v[156:159], v[180:183], v[4:7]\n"
        "ds_read_b128 v[204:207], v229 offset:22528\n"
        "v_perm_b32 v224, v208, v236, s60\n"
        "v_mfma_f32_16x16x32_bf16 v[8:11], v[160:163], v[176:179], v[8:11]\n"
        "ds_read_b32 v112, v224\n"
        "v_perm_b32 v225, v208, v236, s61\n"
        "v_mfma_f32_16x16x32_bf16 v[8:11], v[164:167], v[180:183], v[8:11]\n"
        "ds_read_b32 v113, v225\n"
        "v_perm_b32 v226, v208, v236, s62\n"
        "v_mfma_f32_16x16x32_bf16 v[12:15], v[168:171], v[176:179], v[12:15]\n"
        "ds_read_b32 v114, v226\n"
        "v_perm_b32 v227, v208, v236, s63\n"
        "v_mfma_f32_16x16x32_bf16 v[12:15], v[172:175], v[180:183], v[12:15]\n"
        "ds_read_b32 v115, v227\n"
        "v_bitop3_b32 v219, v219, v223, s39 bitop3:0x7e\n"
        "s_waitcnt lgkmcnt(8)\n"
        "v_mfma_f32_16x16x32_bf16 v[16:19], v[144:147], v[184:187], v[16:19]\n"
        "ds_read_b128 v[176:179], v230 offset:16384\n"
        "v_or3_b32 v210, v210, v211, v218\n"
        "v_mfma_f32_16x16x32_bf16 v[16:19], v[148:151], v[188:191], v[16:19]\n"
        "ds_read_b128 v[180:183], v231 offset:16384\n"
        "v_perm_b32 v224, v209, v236, s60\n"
        "v_mfma_f32_16x16x32_bf16 v[20:23], v[152:155], v[184:187], v[20:23]\n"
        "ds_read_b32 v116, v224\n"
        "v_perm_b32 v225, v209, v236, s61\n"
        "v_mfma_f32_16x16x32_bf16 v[20:23], v[156:159], v[188:191], v[20:23]\n"
        "ds_read_b32 v117, v225\n"
        "v_perm_b32 v226, v209, v236, s62\n"
        "v_mfma_f32_16x16x32_bf16 v[24:27], v[160:163], v[184:187], v[24:27]\n"
        "ds_read_b32 v118, v226\n"
        "v_perm_b32 v227, v209, v236, s63\n"
        "v_mfma_f32_16x16x32_bf16 v[24:27], v[164:167], v[188:191], v[24:27]\n"
        "ds_read_b32 v119, v227\n"
        "v_perm_b32 v224, v212, v236, s60\n"
        "v_mfma_f32_16x16x32_bf16 v[28:31], v[168:171], v[184:187], v[28:31]\n"
        "ds_read_b32 v120, v224\n"
        "v_perm_b32 v225, v212, v236, s61\n"
        "v_mfma_f32_16x16x32_bf16 v[28:31], v[172:175], v[188:191], v[28:31]\n"
        "ds_read_b32 v121, v225\n"
        "v_or_b32 v210, v210, v219\n"
        "s_waitcnt lgkmcnt(14)\n"
        "v_mfma_f32_16x16x32_bf16 v[32:35], v[144:147], v[192:195], v[32:35]\n"
        "ds_read_b128 v[184:187], v230 offset:18432\n"
        "v_cmp_ne_u32 vcc, 0, v210\n"
        "v_mfma_f32_16x16x32_bf16 v[32:35], v[148:151], v[196:199], v[32:35]\n"
        "ds_read_b128 v[188:191], v231 offset:18432\n"
        "v_perm_b32 v226, v212, v236, s62\n"
        "v_mfma_f32_16x16x32_bf16 v[36:39], v[152:155], v[192:195], v[36:39]\n"
        "ds_read_b32 v122, v226\n"
        "v_perm_b32 v227, v212, v236, s63\n"
        "v_mfma_f32_16x16x32_bf16 v[36:39], v[156:159], v[196:199], v[36:39]\n"
        "ds_read_b32 v123, v227\n"
        "v_perm_b32 v224, v213, v236, s60\n"
        "v_mfma_f32_16x16x32_bf16 v[40:43], v[160:163], v[192:195], v[40:43]\n"
        "ds_read_b32 v124, v224\n"
        "v_perm_b32 v225, v213, v236, s61\n"
        "v_mfma_f32_16x16x32_bf16 v[40:43], v[164:167], v[196:199], v[40:43]\n"
        "ds_read_b32 v125, v225\n"
        "v_perm_b32 v226, v213, v236, s62\n"
        "v_mfma_f32_16x16x32_bf16 v[44:47], v[168:171], v[192:195], v[44:47]\n"
        "ds_read_b32 v126, v226\n"
        "v_perm_b32 v227, v213, v236, s63\n"
        "v_mfma_f32_16x16x32_bf16 v[44:47], v[172:175], v[196:199], v[44:47]\n"
        "ds_read_b32 v127, v227\n"
        "s_waitcnt lgkmcnt(15)\n"
        "v_mfma_f32_16x16x32_bf16 v[48:51], v[144:147], v[200:203], v[48:51]\n"
        "ds_read_b128 v[192:195], v230 offset:20480\n"
        "s_cbranch_vccnz .Ln_f14\n"
        ".Ln_b14:\n"
        "v_mfma_f32_16x16x32_bf16 v[48:51], v[148:151], v[204:207], v[48:51]\n"
        "ds_read_b128 v[196:199], v231 offset:20480\n"
        "v_perm_b32 v224, v216, v236, s60\n"
        "v_mfma_f32_16x16x32_bf16 v[52:55], v[152:155], v[200:203], v[52:55]\n"
        "ds_read_b32 v128, v224\n"
        "v_perm_b32 v225, v216, v236, s61\n"
        "v_mfma_f32_16x16x32_bf16 v[52:55], v[156:159], v[204:207], v[52:55]\n"
        "ds_read_b32 v129, v225\n"
        "v_perm_b32 v226, v216, v236, s62\n"
        "v_mfma_f32_16x16x32_bf16 v[56:59], v[160:163], v[200:203], v[56:59]\n"
        "ds_read_b32 v130, v226\n"
        "v_perm_b32 v227, v216, v236, s63\n"
        "v_mfma_f32_16x16x32_bf16 v[56:59], v[164:167], v[204:207], v[56:59]\n"
        "ds_read_b32 v131, v227\n"
        "v_perm_b32 v224, v217, v236, s60\n"
        "v_mfma_f32_16x16x32_bf16 v[60:63], v[168:171], v[200:203], v[60:63]\n"
        "ds_read_b32 v132, v224\n"
        "v_perm_b32 v225, v217, v236, s61\n"
        "v_mfma_f32_16x16x32_bf16 v[60:63], v[172:175], v[204:207], v[60:63]\n"
        "ds_read_b32 v133, v225\n"
        "s_waitcnt lgkmcnt(15)\n"
        "v_mfma_f32_16x16x32_bf16 v[64:67], v[144:147], v[176:179], v[64:67]\n"
        "ds_read_b128 v[200:203], v230 offset:22528\n"
        "v_mfma_f32_16x16x32_bf16 v[64:67], v[148:151], v[180:183], v[64:67]\n"
        "ds_read_b128 v[204:207], v231 offset:22528\n"
        "v_perm_b32 v226, v217, v236, s62\n"
        "v_mfma_f32_16x16x32_bf16 v[68:71], v[152:155], v[176:179], v[68:71]\n"
        "ds_read_b32 v134, v226\n"
        "v_perm_b32 v227, v217, v236, s63\n"
        "v_mfma_f32_16x16x32_bf16 v[68:71], v[156:159], v[180:183], v[68:71]\n"
        "ds_read_b32 v135, v227\n"
        "v_perm_b32 v224, v220, v236, s60\n"
        "v_mfma_f32_16x16x32_bf16 v[72:75], v[160:163], v[176:179], v[72:75]\n"
        "ds_read_b32 v136, v224\n"
        "v_perm_b32 v225, v220, v236, s61\n"
        "v_mfma_f32_16x16x32_bf16 v[72:75], v[164:167], v[180:183], v[72:75]\n"
        "ds_read_b32 v137, v225\n"
        "v_perm_b32 v226, v220, v236, s62\n"
        "s_waitcnt lgkmcnt(15)\n"
        "v_mfma_f32_16x16x32_bf16 v[76:79], v[144:147], v[184:187], v[76:79]\n"
        "ds_read_b32 v138, v226\n"
        "v_perm_b32 v227, v220, v236, s63\n"
        "v_mfma_f32_16x16x32_bf16 v[76:79], v[148:151], v[188:191], v[76:79]\n"
        "ds_read_b32 v139, v227\n"
        "v_perm_b32 v224, v221, v236, s60\n"
        "v_mfma_f32_16x16x32_bf16 v[80:83], v[152:155], v[184:187], v[80:83]\n"
        "ds_read_b32 v140, v224\n"
        "v_perm_b32 v225, v221, v236, s61\n"
        "v_mfma_f32_16x16x32_bf16 v[80:83], v[156:159], v[188:191], v[80:83]\n"
        "ds_read_b32 v141, v225\n"
        "v_perm_b32 v226, v221, v236, s62\n"
        "v_mfma_f32_16x16x32_bf16 v[84:87], v[160:163], v[184:187], v[84:87]\n"
        "ds_read_b32 v142, v226\n"
        "v_perm_b32 v227, v221, v236, s63\n"
        "v_mfma_f32_16x16x32_bf16 v[84:87], v[164:167], v[188:191], v[84:87]\n"
        "ds_read_b32 v143, v227\n"
        "s_waitcnt lgkmcnt(10)\n"
        "s_barrier\n"
        "v_mfma_f32_16x16x32_bf16 v[88:91], v[144:147], v[192:195], v[88:91]\n"
        "ds_read_b128 v[208:211], v232 offset:0\n"
        "v_mfma_f32_16x16x32_bf16 v[88:91], v[148:151], v[196:199], v[88:91]\n"
        "ds_read_b128 v[212:215], v233 offset:0\n"
        "v_mfma_f32_16x16x32_bf16 v[92:95], v[152:155], v[192:195], v[92:95]\n"
        "v_mfma_f32_16x16x32_bf16 v[92:95], v[156:159], v[196:199], v[92:95]\n"
        "ds_read_b128 v[216:219], v234 offset:0\n"
        "v_mfma_f32_16x16x32_bf16 v[96:99], v[160:163], v[192:195], v[96:99]\n"
        "ds_read_b128 v[220:223], v235 offset:0\n"
        "v_mfma_f32_16x16x32_bf16 v[96:99], v[164:167], v[196:199], v[96:99]\n"
        "v_mfma_f32_16x16x32_bf16 v[100:103], v[144:147], v[200:203], v[100:103]\n"
        "ds_read_b128 v[176:179], v228 offset:32768\n"
        "v_mfma_f32_16x16x32_bf16 v[100:103], v[148:151], v[204:207], v[100:103]\n"
        "ds_read_b128 v[180:183], v229 offset:32768\n"
        "v_mfma_f32_16x16x32_bf16 v[104:107], v[152:155], v[200:203], v[104:107]\n"
        "v_mfma_f32_16x16x32_bf16 v[104:107], v[156:159], v[204:207], v[104:107]\n"
        "ds_read_b128 v[184:187], v228 offset:34816\n"
        "v_mfma_f32_16x16x32_bf16 v[108:111], v[160:163], v[200:203], v[108:111]\n"
        "ds_read_b128 v[188:191], v229 offset:34816\n"
        "v_mfma_f32_16x16x32_bf16 v[108:111], v[164:167], v[204:207], v[108:111]\n"
        "s_waitcnt lgkmcnt(2)\n"
        "v_perm_b32 v224, v208, v236, s60\n"
        "v_mfma_f32_16x16x32_bf16 v[0:3], v[112:115], v[176:179], v[0:3]\n"
        "ds_read_b32 v144, v224\n"
        "ds_read_b128 v[192:195], v228 offset:36864\n"
        "v_perm_b32 v225, v208, v236, s61\n"
        "v_mfma_f32_16x16x32_bf16 v[0:3], v[116:119], v[180:183], v[0:3]\n"
        "ds_read_b32 v145, v225\n"
        "ds_read_b128 v[196:199], v229 offset:36864\n"
        "v_perm_b32 v226, v208, v236, s62\n"
        "v_mfma_f32_16x16x32_bf16 v[4:7], v[120:123], v[176:179], v[4:7]\n"
        "ds_read_b32 v146, v226\n"
        "ds_read_b128 v[200:203], v228 offset:38912\n"
        "v_perm_b32 v227, v208, v236, s63\n"
        "v_mfma_f32_16x16x32_bf16 v[4:7], v[124:127], v[180:183], v[4:7]\n"
        "ds_read_b32 v147, v227\n"
        "ds_read_b128 v[204:207], v229 offset:38912\n"
        "v_perm_b32 v224, v209, v236, s60\n"
        "v_mfma_f32_16x16x32_bf16 v[8:11], v[128:131], v[176:179], v[8:11]\n"
        "ds_read_b32 v148, v224\n"
        "v_perm_b32 v225, v209, v236, s61\n"
        "v_bitop3_b32 v210, v210, v214, s34 bitop3:0x7e\n"
        "v_mfma_f32_16x16x32_bf16 v[8:11], v[132:135], v[180:183], v[8:11]\n"
        "ds_read_b32 v149, v225\n"
        "v_perm_b32 v226, v209, v236, s62\n"
        "v_bitop3_b32 v211, v211, v215, s39 bitop3:0x7e\n"
        "v_mfma_f32_16x16x32_bf16 v[12:15], v[136:139], v[176:179], v[12:15]\n"
        "ds_read_b32 v150, v226\n"
        "v_perm_b32 v227, v209, v236, s63\n"
        "v_bitop3_b32 v218, v218, v222, s34 bitop3:0x7e\n"
        "v_mfma_f32_16x16x32_bf16 v[12:15], v[140:143], v[180:183], v[12:15]\n"
        "ds_read_b32 v151, v227\n"
        "v_perm_b32 v224, v212, v236, s60\n"
        "v_bitop3_b32 v219, v219, v223, s39 bitop3:0x7e\n"
        "s_waitcnt lgkmcnt(12)\n"
        "v_mfma_f32_16x16x32_bf16 v[16:19], v[112:115], v[184:187], v[16:19]\n"
        "ds_read_b32 v152, v224\n"
        "ds_read_b128 v[176:179], v228 offset:0\n"
        "v_perm_b32 v225, v212, v236, s61\n"
        "v_mfma_f32_16x16x32_bf16 v[16:19], v[116:119], v[188:191], v[16:19]\n"
        "ds_read_b32 v153, v225\n"
        "ds_read_b128 v[180:183], v229 offset:0\n"
        "v_perm_b32 v226, v212, v236, s62\n"
        "v_mfma_f32_16x16x32_bf16 v[20:23], v[120:123], v[184:187], v[20:23]\n"
        "ds_read_b32 v154, v226\n"
        "v_perm_b32 v227, v212, v236, s63\n"
        "v_or3_b32 v210, v210, v211, v218\n"
        "v_mfma_f32_16x16x32_bf16 v[20:23], v[124:127], v[188:191], v[20:23]\n"
        "ds_read_b32 v155, v227\n"
        "v_perm_b32 v224, v213, v236, s60\n"
        "v_or_b32 v210, v210, v219\n"
        "v_mfma_f32_16x16x32_bf16 v[24:27], v[128:131], v[184:187], v[24:27]\n"
        "ds_read_b32 v156, v224\n"
        "v_perm_b32 v225, v213, v236, s61\n"
        "v_cmp_ne_u32 vcc, 0, v210\n"
        "v_mfma_f32_16x16x32_bf16 v[24:27], v[132:135], v[188:191], v[24:27]\n"
        "ds_read_b32 v157, v225\n"
        "v_perm_b32 v226, v213, v236, s62\n"
        "v_mfma_f32_16x16x32_bf16 v[28:31], v[136:139], v[184:187], v[28:31]\n"
        "ds_read_b32 v158, v226\n"
        "s_cbranch_vccnz .Ln_f15\n"
        ".Ln_b15:\n"
        "v_perm_b32 v227, v213, v236, s63\n"
        "v_mfma_f32_16x16x32_bf16 v[28:31], v[140:143], v[188:191], v[28:31]\n"
        "ds_read_b32 v159, v227\n"
        "v_perm_b32 v224, v216, v236, s60\n"
        "s_waitcnt lgkmcnt(15)\n"
        "v_mfma_f32_16x16x32_bf16 v[32:35], v[112:115], v[192:195], v[32:35]\n"
        "ds_read_b32 v160, v224\n"
        "ds_read_b128 v[184:187], v228 offset:2048\n"
        "v_perm_b32 v225, v216, v236, s61\n"
        "v_mfma_f32_16x16x32_bf16 v[32:35], v[116:119], v[196:199], v[32:35]\n"
        "ds_read_b32 v161, v225\n"
        "ds_read_b128 v[188:191], v229 offset:2048\n"
        "v_perm_b32 v226, v216, v236, s62\n"
        "v_mfma_f32_16x16x32_bf16 v[36:39], v[120:123], v[192:195], v[36:39]\n"
        "ds_read_b32 v162, v226\n"
        "v_perm_b32 v227, v216, v236, s63\n"
        "v_mfma_f32_16x16x32_bf16 v[36:39], v[124:127], v[196:199], v[36:39]\n"
        "ds_read_b32 v163, v227\n"
        "v_perm_b32 v224, v217, v236, s60\n"
        "v_mfma_f32_16x16x32_bf16 v[40:43], v[128:131], v[192:195], v[40:43]\n"
        "ds_read_b32 v164, v224\n"
        "v_perm_b32 v225, v217, v236, s61\n"
        "v_mfma_f32_16x16x32_bf16 v[40:43], v[132:135], v[196:199], v[40:43]\n"
        "ds_read_b32 v165, v225\n"
        "v_perm_b32 v226, v217, v236, s62\n"
        "v_mfma_f32_16x16x32_bf16 v[44:47], v[136:139], v[192:195], v[44:47]\n"
        "ds_read_b32 v166, v226\n"
        "v_perm_b32 v227, v217, v236, s63\n"
        "v_mfma_f32_16x16x32_bf16 v[44:47], v[140:143], v[196:199], v[44:47]\n"
        "ds_read_b32 v167, v227\n"
        "v_perm_b32 v224, v220, v236, s60\n"
        "s_waitcnt lgkmcnt(15)\n"
        "v_mfma_f32_16x16x32_bf16 v[48:51], v[112:115], v[200:203], v[48:51]\n"
        "ds_read_b32 v168, v224\n"
        "ds_read_b128 v[192:195], v228 offset:4096\n"
        "v_perm_b32 v225, v220, v236, s61\n"
        "v_mfma_f32_16x16x32_bf16 v[48:51], v[116:119], v[204:207], v[48:51]\n"
        "ds_read_b32 v169, v225\n"
        "ds_read_b128 v[196:199], v229 offset:4096\n"
        "v_perm_b32 v226, v220, v236, s62\n"
        "v_mfma_f32_16x16x32_bf16 v[52:55], v[120:123], v[200:203], v[52:55]\n"
        "ds_read_b32 v170, v226\n"
        "v_perm_b32 v227, v220, v236, s63\n"
        "v_mfma_f32_16x16x32_bf16 v[52:55], v[124:127], v[204:207], v[52:55]\n"
        "ds_read_b32 v171, v227\n"
        "v_perm_b32 v224, v221, v236, s60\n"
        "v_mfma_f32_16x16x32_bf16 v[56:59], v[128:131], v[200:203], v[56:59]\n"
        "ds_read_b32 v172, v224\n"
        "v_perm_b32 v225, v221, v236, s61\n"
        "v_mfma_f32_16x16x32_bf16 v[56:59], v[132:135], v[204:207], v[56:59]\n"
        "ds_read_b32 v173, v225\n"
        "v_perm_b32 v226, v221, v236, s62\n"
        "v_mfma_f32_16x16x32_bf16 v[60:63], v[136:139], v[200:203], v[60:63]\n"
        "ds_read_b32 v174, v226\n"
        "v_perm_b32 v227, v221, v236, s63\n"
        "s_waitcnt vmcnt(0)\n"
        "v_mfma_f32_16x16x32_bf16 v[60:63], v[140:143], v[204:207], v[60:63]\n"
        "ds_read_b32 v175, v227\n"
        "v_lshlrev_b32 v208, 16, v238\n"
        "v_and_b32 v209, 0xffff0000, v238\n"
        "v_mfma_f32_16x16x32_bf16 v[0:3], v[144:147], v[176:179], v[0:3]\n"
        "ds_read_b128 v[200:203], v228 offset:6144\n"
        "v_lshlrev_b32 v210, 16, v239\n"
        "v_and_b32 v211, 0xffff0000, v239\n"
        "v_mfma_f32_16x16x32_bf16 v[0:3], v[148:151], v[180:183], v[0:3]\n"
        "ds_read_b128 v[204:207], v229 offset:6144\n"
        "v_lshlrev_b32 v212, 16, v240\n"
        "v_and_b32 v213, 0xffff0000, v240\n"
        "s_waitcnt lgkmcnt(15)\n"
        "v_mfma_f32_16x16x32_bf16 v[4:7], v[152:155], v[176:179], v[4:7]\n"
        "v_lshlrev_b32 v214, 16, v241\n"
        "v_and_b32 v215, 0xffff0000, v241\n"
        "v_lshlrev_b32 v216, 16, v242\n"
        "v_mfma_f32_16x16x32_bf16 v[4:7], v[156:159], v[180:183], v[4:7]\n"
        "v_and_b32 v217, 0xffff0000, v242\n"
        "v_lshlrev_b32 v218, 16, v243\n"
        "v_and_b32 v219, 0xffff0000, v243\n"
        "v_mfma_f32_16x16x32_bf16 v[8:11], v[160:163], v[176:179], v[8:11]\n"
        "v_lshlrev_b32 v220, 16, v244\n"
        "v_and_b32 v221, 0xffff0000, v244\n"
        "v_lshlrev_b32 v222, 16, v245\n"
        "s_waitcnt lgkmcnt(12)\n"
        "v_mfma_f32_16x16x32_bf16 v[8:11], v[164:167], v[180:183], v[8:11]\n"
        "v_and_b32 v223, 0xffff0000, v245\n"
        "v_mul_f32 v0, v0, v208\n"
        "v_mul_f32 v4, v4, v209\n"
        "s_waitcnt lgkmcnt(6)\n"
        "s_waitcnt lgkmcnt(2)\n"
        "s_barrier\n"
        "v_mfma_f32_16x16x32_bf16 v[12:15], v[168:171], v[176:179], v[12:15]\n"
        "v_cvt_pk_bf16_f32 v0, v0, v4\n"
        "v_mul_f32 v1, v1, v210\n"
        "v_mul_f32 v5, v5, v211\n"
        "v_mfma_f32_16x16x32_bf16 v[12:15], v[172:175], v[180:183], v[12:15]\n"
        "v_cvt_pk_bf16_f32 v1, v1, v5\n"
        "v_mul_f32 v2, v2, v212\n"
        "v_mul_f32 v6, v6, v213\n"
        "v_mfma_f32_16x16x32_bf16 v[16:19], v[144:147], v[184:187], v[16:19]\n"
        "ds_read_b128 v[176:179], v230 offset:32768\n"
        "v_cvt_pk_bf16_f32 v2, v2, v6\n"
        "v_mul_f32 v3, v3, v214\n"
        "v_mfma_f32_16x16x32_bf16 v[16:19], v[148:151], v[188:191], v[16:19]\n"
        "ds_read_b128 v[180:183], v231 offset:32768\n"
        "v_mul_f32 v7, v7, v215\n"
        "v_cvt_pk_bf16_f32 v3, v3, v7\n"
        "v_mfma_f32_16x16x32_bf16 v[20:23], v[152:155], v[184:187], v[20:23]\n"
        "ds_write_b128 v246, v[0:3] offset:0\n"
        "v_mul_f32 v8, v8, v216\n"
        "v_mul_f32 v9, v9, v217\n"
        "v_mfma_f32_16x16x32_bf16 v[20:23], v[156:159], v[188:191], v[20:23]\n"
        "v_mul_f32 v10, v10, v218\n"
        "v_mul_f32 v11, v11, v219\n"
        "v_cvt_pk_bf16_f32 v8, v8, v9\n"
        "v_mfma_f32_16x16x32_bf16 v[24:27], v[160:163], v[184:187], v[24:27]\n"
        "v_cvt_pk_bf16_f32 v9, v10, v11\n"
        "v_mfma_f32_16x16x32_bf16 v[24:27], v[164:167], v[188:191], v[24:27]\n"
        "ds_write_b64 v247, v[8:9] offset:0\n"
        "v_mul_f32 v12, v12, v220\n"
        "v_mul_f32 v13, v13, v221\n"
        "v_mfma_f32_16x16x32_bf16 v[28:31], v[168:171], v[184:187], v[28:31]\n"
        "v_mul_f32 v14, v14, v222\n"
        "v_mul_f32 v15, v15, v223\n"
        "v_cvt_pk_bf16_f32 v12, v12, v13\n"
        "v_mfma_f32_16x16x32_bf16 v[28:31], v[172:175], v[188:191], v[28:31]\n"
        "v_cvt_pk_bf16_f32 v13, v14, v15\n"
        "v_mfma_f32_16x16x32_bf16 v[32:35], v[144:147], v[192:195], v[32:35]\n"
        "ds_read_b128 v[184:187], v230 offset:0\n"
        "ds_write_b64 v248, v[12:13] offset:0\n"
        "ds_write_b32 v237, v237 offset:0\n"
        "v_mfma_f32_16x16x32_bf16 v[32:35], v[148:151], v[196:199], v[32:35]\n"
        "ds_read_b128 v[188:191], v231 offset:0\n"
        "v_mul_f32 v16, v16, v208\n"
        "v_mul_f32 v20, v20, v209\n"
        "v_mfma_f32_16x16x32_bf16 v[36:39], v[152:155], v[192:195], v[36:39]\n"
        "v_cvt_pk_bf16_f32 v16, v16, v20\n"
        "v_mul_f32 v17, v17, v210\n"
        "v_mul_f32 v21, v21, v211\n"
        "v_mfma_f32_16x16x32_bf16 v[36:39], v[156:159], v[196:199], v[36:39]\n"
        "v_cvt_pk_bf16_f32 v17, v17, v21\n"
        "v_mul_f32 v18, v18, v212\n"
        "v_mul_f32 v22, v22, v213\n"
        "v_mfma_f32_16x16x32_bf16 v[40:43], v[160:163], v[192:195], v[40:43]\n"
        "v_cvt_pk_bf16_f32 v18, v18, v22\n"
        "v_mul_f32 v19, v19, v214\n"
        "v_mul_f32 v23, v23, v215\n"
        "v_mfma_f32_16x16x32_bf16 v[40:43], v[164:167], v[196:199], v[40:43]\n"
        "v_cvt_pk_bf16_f32 v19, v19, v23\n"
        "v_mfma_f32_16x16x32_bf16 v[44:47], v[168:171], v[192:195], v[44:47]\n"
        "ds_write_b128 v246, v[16:19] offset:8192\n"
        "v_mul_f32 v24, v24, v216\n"
        "v_mul_f32 v25, v25, v217\n"
        "v_mfma_f32_16x16x32_bf16 v[44:47], v[172:175], v[196:199], v[44:47]\n"
        "v_mul_f32 v26, v26, v218\n"
        "v_mul_f32 v27, v27, v219\n"
        "v_cvt_pk_bf16_f32 v24, v24, v25\n"
        "s_waitcnt lgkmcnt(9)\n"
        "v_mfma_f32_16x16x32_bf16 v[48:51], v[144:147], v[200:203], v[48:51]\n"
        "ds_read_b128 v[192:195], v230 offset:34816\n"
        "v_cvt_pk_bf16_f32 v25, v26, v27\n"
        "v_mfma_f32_16x16x32_bf16 v[48:51], v[148:151], v[204:207], v[48:51]\n"
        "ds_read_b128 v[196:199], v231 offset:34816\n"
        "ds_write_b64 v247, v[24:25] offset:8192\n"
        "v_mul_f32 v28, v28, v220\n"
        "v_mfma_f32_16x16x32_bf16 v[52:55], v[152:155], v[200:203], v[52:55]\n"
        "v_mul_f32 v29, v29, v221\n"
        "v_mul_f32 v30, v30, v222\n"
        "v_mul_f32 v31, v31, v223\n"
        "v_mfma_f32_16x16x32_bf16 v[52:55], v[156:159], v[204:207], v[52:55]\n"
        "v_cvt_pk_bf16_f32 v28, v28, v29\n"
        "v_cvt_pk_bf16_f32 v29, v30, v31\n"
        "v_mfma_f32_16x16x32_bf16 v[56:59], v[160:163], v[200:203], v[56:59]\n"
        "ds_write_b64 v248, v[28:29] offset:8192\n"
        "ds_write_b32 v237, v237 offset:16\n"
        "v_mul_f32 v32, v32, v208\n"
        "v_mfma_f32_16x16x32_bf16 v[56:59], v[164:167], v[204:207], v[56:59]\n"
        "v_mul_f32 v36, v36, v209\n"
        "v_cvt_pk_bf16_f32 v32, v32, v36\n"
        "v_mul_f32 v33, v33, v210\n"
        "v_mfma_f32_16x16x32_bf16 v[60:63], v[168:171], v[200:203], v[60:63]\n"
        "v_mul_f32 v37, v37, v211\n"
        "v_cvt_pk_bf16_f32 v33, v33, v37\n"
        "v_mul_f32 v34, v34, v212\n"
        "v_mfma_f32_16x16x32_bf16 v[60:63], v[172:175], v[204:207], v[60:63]\n"
        "v_mul_f32 v38, v38, v213\n"
        "v_cvt_pk_bf16_f32 v34, v34, v38\n"
        "v_mul_f32 v35, v35, v214\n"
        "s_waitcnt lgkmcnt(12)\n"
        "v_mfma_f32_16x16x32_bf16 v[64:67], v[112:115], v[176:179], v[64:67]\n"
        "ds_read_b128 v[200:203], v230 offset:2048\n"
        "v_mul_f32 v39, v39, v215\n"
        "v_cvt_pk_bf16_f32 v35, v35, v39\n"
        "v_mfma_f32_16x16x32_bf16 v[64:67], v[116:119], v[180:183], v[64:67]\n"
        "ds_read_b128 v[204:207], v231 offset:2048\n"
        "ds_write_b128 v246, v[32:35] offset:16384\n"
        "v_mul_f32 v40, v40, v216\n"
        "s_waitcnt lgkmcnt(9)\n"
        "v_mfma_f32_16x16x32_bf16 v[64:67], v[144:147], v[184:187], v[64:67]\n"
        "v_mul_f32 v41, v41, v217\n"
        "v_mul_f32 v42, v42, v218\n"
        "v_mul_f32 v43, v43, v219\n"
        "v_mfma_f32_16x16x32_bf16 v[64:67], v[148:151], v[188:191], v[64:67]\n"
        "v_cvt_pk_bf16_f32 v40, v40, v41\n"
        "v_cvt_pk_bf16_f32 v41, v42, v43\n"
        "v_mfma_f32_16x16x32_bf16 v[68:71], v[120:123], v[176:179], v[68:71]\n"
        "ds_write_b64 v247, v[40:41] offset:16384\n"
        "v_mul_f32 v44, v44, v220\n"
        "v_mul_f32 v45, v45, v221\n"
        "v_mfma_f32_16x16x32_bf16 v[68:71], v[124:127], v[180:183], v[68:71]\n"
        "v_mul_f32 v46, v46, v222\n"
        "v_mul_f32 v47, v47, v223\n"
        "v_cvt_pk_bf16_f32 v44, v44, v45\n"
        "v_mfma_f32_16x16x32_bf16 v[68:71], v[152:155], v[184:187], v[68:71]\n"
        "v_cvt_pk_bf16_f32 v45, v46, v47\n"
        "v_mfma_f32_16x16x32_bf16 v[68:71], v[156:159], v[188:191], v[68:71]\n"
        "ds_write_b64 v248, v[44:45] offset:16384\n"
        "ds_write_b32 v237, v237 offset:32\n"
        "v_mul_f32 v48, v48, v208\n"
        "v_mfma_f32_16x16x32_bf16 v[72:75], v[128:131], v[176:179], v[72:75]\n"
        "v_mul_f32 v52, v52, v209\n"
        "v_cvt_pk_bf16_f32 v48, v48, v52\n"
        "v_mul_f32 v49, v49, v210\n"
        "v_mfma_f32_16x16x32_bf16 v[72:75], v[132:135], v[180:183], v[72:75]\n"
        "v_mul_f32 v53, v53, v211\n"
        "v_cvt_pk_bf16_f32 v49, v49, v53\n"
        "v_mul_f32 v50, v50, v212\n"
        "v_mfma_f32_16x16x32_bf16 v[72:75], v[160:163], v[184:187], v[72:75]\n"
        "ds_read_b128 v[176:179], v230 offset:36864\n"
        "v_mul_f32 v54, v54, v213\n"
        "v_cvt_pk_bf16_f32 v50, v50, v54\n"
        "v_mfma_f32_16x16x32_bf16 v[72:75], v[164:167], v[188:191], v[72:75]\n"
        "ds_read_b128 v[180:183], v231 offset:36864\n"
        "v_mul_f32 v51, v51, v214\n"
        "v_mul_f32 v55, v55, v215\n"
        "s_waitcnt lgkmcnt(11)\n"
        "v_mfma_f32_16x16x32_bf16 v[76:79], v[112:115], v[192:195], v[76:79]\n"
        "ds_read_b128 v[184:187], v230 offset:4096\n"
        "v_cvt_pk_bf16_f32 v51, v51, v55\n"
        "v_mfma_f32_16x16x32_bf16 v[76:79], v[116:119], v[196:199], v[76:79]\n"
        "ds_read_b128 v[188:191], v231 offset:4096\n"
        "ds_write_b128 v246, v[48:51] offset:24576\n"
        "v_mul_f32 v56, v56, v216\n"
        "s_waitcnt lgkmcnt(9)\n"
        "v_mfma_f32_16x16x32_bf16 v[76:79], v[144:147], v[200:203], v[76:79]\n"
        "v_mul_f32 v57, v57, v217\n"
        "v_mul_f32 v58, v58, v218\n"
        "v_mul_f32 v59, v59, v219\n"
        "v_mfma_f32_16x16x32_bf16 v[76:79], v[148:151], v[204:207], v[76:79]\n"
        "v_cvt_pk_bf16_f32 v56, v56, v57\n"
        "v_cvt_pk_bf16_f32 v57, v58, v59\n"
        "v_mfma_f32_16x16x32_bf16 v[80:83], v[120:123], v[192:195], v[80:83]\n"
        "ds_write_b64 v247, v[56:57] offset:24576\n"
        "v_mul_f32 v60, v60, v220\n"
        "v_mul_f32 v61, v61, v221\n"
        "v_mfma_f32_16x16x32_bf16 v[80:83], v[124:127], v[196:199], v[80:83]\n"
        "v_mul_f32 v62, v62, v222\n"
        "v_mul_f32 v63, v63, v223\n"
        "v_cvt_pk_bf16_f32 v60, v60, v61\n"
        "v_mfma_f32_16x16x32_bf16 v[80:83], v[152:155], v[200:203], v[80:83]\n"
        "v_cvt_pk_bf16_f32 v61, v62, v63\n"
        "v_mfma_f32_16x16x32_bf16 v[80:83], v[156:159], v[204:207], v[80:83]\n"
        "ds_write_b64 v248, v[60:61] offset:24576\n"
        "ds_write_b32 v237, v237 offset:48\n"
        "v_mul_f32 v64, v64, v208\n"
        "v_mfma_f32_16x16x32_bf16 v[84:87], v[128:131], v[192:195], v[84:87]\n"
        "v_mul_f32 v68, v68, v209\n"
        "v_cvt_pk_bf16_f32 v64, v64, v68\n"
        "v_mul_f32 v65, v65, v210\n"
        "v_mfma_f32_16x16x32_bf16 v[84:87], v[132:135], v[196:199], v[84:87]\n"
        "v_mul_f32 v69, v69, v211\n"
        "v_cvt_pk_bf16_f32 v65, v65, v69\n"
        "v_mul_f32 v66, v66, v212\n"
        "v_mfma_f32_16x16x32_bf16 v[84:87], v[160:163], v[200:203], v[84:87]\n"
        "ds_read_b128 v[192:195], v230 offset:38912\n"
        "v_mul_f32 v70, v70, v213\n"
        "v_cvt_pk_bf16_f32 v66, v66, v70\n"
        "v_mfma_f32_16x16x32_bf16 v[84:87], v[164:167], v[204:207], v[84:87]\n"
        "ds_read_b128 v[196:199], v231 offset:38912\n"
        "v_mul_f32 v67, v67, v214\n"
        "v_mul_f32 v71, v71, v215\n"
        "s_waitcnt lgkmcnt(8)\n"
        "v_mfma_f32_16x16x32_bf16 v[88:91], v[112:115], v[176:179], v[88:91]\n"
        "ds_read_b128 v[200:203], v230 offset:6144\n"
        "v_cvt_pk_bf16_f32 v67, v67, v71\n"
        "v_mfma_f32_16x16x32_bf16 v[88:91], v[116:119], v[180:183], v[88:91]\n"
        "ds_read_b128 v[204:207], v231 offset:6144\n"
        "ds_write_b128 v246, v[64:67] offset:32768\n"
        "v_mul_f32 v72, v72, v216\n"
        "s_waitcnt lgkmcnt(9)\n"
        "v_mfma_f32_16x16x32_bf16 v[88:91], v[144:147], v[184:187], v[88:91]\n"
        "v_mul_f32 v73, v73, v217\n"
        "v_mul_f32 v74, v74, v218\n"
        "v_mul_f32 v75, v75, v219\n"
        "v_mfma_f32_16x16x32_bf16 v[88:91], v[148:151], v[188:191], v[88:91]\n"
        "v_cvt_pk_bf16_f32 v72, v72, v73\n"
        "v_cvt_pk_bf16_f32 v73, v74, v75\n"
        "v_mfma_f32_16x16x32_bf16 v[92:95], v[120:123], v[176:179], v[92:95]\n"
        "ds_write_b64 v247, v[72:73] offset:32768\n"
        "ds_write_b32 v237, v237 offset:64\n"
        "v_mul_f32 v76, v76, v208\n"
        "v_mfma_f32_16x16x32_bf16 v[92:95], v[124:127], v[180:183], v[92:95]\n"
        "v_mul_f32 v80, v80, v209\n"
        "v_cvt_pk_bf16_f32 v76, v76, v80\n"
        "v_mul_f32 v77, v77, v210\n"
        "v_mfma_f32_16x16x32_bf16 v[92:95], v[152:155], v[184:187], v[92:95]\n"
        "v_mul_f32 v81, v81, v211\n"
        "v_cvt_pk_bf16_f32 v77, v77, v81\n"
        "v_mul_f32 v78, v78, v212\n"
        "v_mfma_f32_16x16x32_bf16 v[92:95], v[156:159], v[188:191], v[92:95]\n"
        "v_mul_f32 v82, v82, v213\n"
        "v_cvt_pk_bf16_f32 v78, v78, v82\n"
        "v_mul_f32 v79, v79, v214\n"
        "v_mfma_f32_16x16x32_bf16 v[96:99], v[128:131], v[176:179], v[96:99]\n"
        "v_mul_f32 v83, v83, v215\n"
        "v_cvt_pk_bf16_f32 v79, v79, v83\n"
        "v_mfma_f32_16x16x32_bf16 v[96:99], v[132:135], v[180:183], v[96:99]\n"
        "ds_write_b128 v246, v[76:79] offset:40960\n"
        "v_mul_f32 v84, v84, v216\n"
        "v_mul_f32 v85, v85, v217\n"
        "v_mfma_f32_16x16x32_bf16 v[96:99], v[160:163], v[184:187], v[96:99]\n"
        "v_mul_f32 v86, v86, v218\n"
        "v_mul_f32 v87, v87, v219\n"
        "v_cvt_pk_bf16_f32 v84, v84, v85\n"
        "v_mfma_f32_16x16x32_bf16 v[96:99], v[164:167], v[188:191], v[96:99]\n"
        "v_cvt_pk_bf16_f32 v85, v86, v87\n"
        "s_waitcnt lgkmcnt(6)\n"
        "v_mfma_f32_16x16x32_bf16 v[100:103], v[112:115], v[192:195], v[100:103]\n"
        "ds_write_b64 v247, v[84:85] offset:40960\n"
        "ds_write_b32 v237, v237 offset:80\n"
        "v_mul_f32 v88, v88, v208\n"
        "v_mfma_f32_16x16x32_bf16 v[100:103], v[116:119], v[196:199], v[100:103]\n"
        "v_mul_f32 v92, v92, v209\n"
        "v_cvt_pk_bf16_f32 v88, v88, v92\n"
        "v_mul_f32 v89, v89, v210\n"
        "s_waitcnt lgkmcnt(6)\n"
        "v_mfma_f32_16x16x32_bf16 v[100:103], v[144:147], v[200:203], v[100:103]\n"
        "v_mul_f32 v93, v93, v211\n"
        "v_cvt_pk_bf16_f32 v89, v89, v93\n"
        "v_mul_f32 v90, v90, v212\n"
        "v_mfma_f32_16x16x32_bf16 v[100:103], v[148:151], v[204:207], v[100:103]\n"
        "v_mul_f32 v94, v94, v213\n"
        "v_cvt_pk_bf16_f32 v90, v90, v94\n"
        "v_mul_f32 v91, v91, v214\n"
        "v_mfma_f32_16x16x32_bf16 v[104:107], v[120:123], v[192:195], v[104:107]\n"
        "v_mul_f32 v95, v95, v215\n"
        "v_cvt_pk_bf16_f32 v91, v91, v95\n"
        "v_mfma_f32_16x16x32_bf16 v[104:107], v[124:127], v[196:199], v[104:107]\n"
        "ds_write_b128 v246, v[88:91] offset:49152\n"
        "v_mul_f32 v96, v96, v216\n"
        "v_mul_f32 v97, v97, v217\n"
        "v_mfma_f32_16x16x32_bf16 v[104:107], v[152:155], v[200:203], v[104:107]\n"
        "v_mul_f32 v98, v98, v218\n"
        "v_mul_f32 v99, v99, v219\n"
        "v_cvt_pk_bf16_f32 v96, v96, v97\n"
        "v_mfma_f32_16x16x32_bf16 v[104:107], v[156:159], v[204:207], v[104:107]\n"
        "v_cvt_pk_bf16_f32 v97, v98, v99\n"
        "v_mfma_f32_16x16x32_bf16 v[108:111], v[128:131], v[192:195], v[108:111]\n"
        "ds_write_b64 v247, v[96:97] offset:49152\n"
        "ds_write_b32 v237, v237 offset:96\n"
        "v_mfma_f32_16x16x32_bf16 v[108:111], v[132:135], v[196:199], v[108:111]\n"
        "v_mfma_f32_16x16x32_bf16 v[108:111], v[160:163], v[200:203], v[108:111]\n"
        "v_mfma_f32_16x16x32_bf16 v[108:111], v[164:167], v[204:207], v[108:111]\n"
        "s_nop 7\n"
        "v_mul_f32 v100, v100, v208\n"
        "v_mul_f32 v104, v104, v209\n"
        "v_cvt_pk_bf16_f32 v100, v100, v104\n"
        "v_mul_f32 v101, v101, v210\n"
        "v_mul_f32 v105, v105, v211\n"
        "v_cvt_pk_bf16_f32 v101, v101, v105\n"
        "v_mul_f32 v102, v102, v212\n"
        "v_mul_f32 v106, v106, v213\n"
        "v_cvt_pk_bf16_f32 v102, v102, v106\n"
        "v_mul_f32 v103, v103, v214\n"
        "v_mul_f32 v107, v107, v215\n"
        "v_cvt_pk_bf16_f32 v103, v103, v107\n"
        "ds_write_b128 v246, v[100:103] offset:57344\n"
        "v_mul_f32 v108, v108, v216\n"
        "v_mul_f32 v109, v109, v217\n"
        "v_mul_f32 v110, v110, v218\n"
        "v_mul_f32 v111, v111, v219\n"
        "v_cvt_pk_bf16_f32 v108, v108, v109\n"
        "v_cvt_pk_bf16_f32 v109, v110, v111\n"
        "ds_write_b64 v247, v[108:109] offset:57344\n"
        "ds_write_b32 v237, v237 offset:112\n"
        "s_endpgm\n"
        ".Ln_f1:\n"
        "s_movk_i32 s77, 1\n"
        "v_mov_b32 v192, v144\n"
        "v_mov_b32 v193, v145\n"
        "v_mov_b32 v194, v146\n"
        "v_mov_b32 v195, v147\n"
        "v_mov_b32 v196, v148\n"
        "v_mov_b32 v197, v149\n"
        "v_mov_b32 v198, v150\n"
        "v_mov_b32 v199, v151\n"
        "v_mov_b32 v200, v152\n"
        "v_mov_b32 v201, v153\n"
        "v_mov_b32 v202, v154\n"
        "v_mov_b32 v203, v155\n"
        "v_mov_b32 v204, v156\n"
        "v_mov_b32 v205, v157\n"
        "v_mov_b32 v206, v158\n"
        "v_mov_b32 v207, v159\n"
        "s_getpc_b64 s[70:71]\n"
        "s_branch .Ln_rec1\n"
        "v_mov_b32 v144, v192\n"
        "v_mov_b32 v145, v193\n"
        "v_mov_b32 v146, v194\n"
        "v_mov_b32 v147, v195\n"
        "v_mov_b32 v148, v196\n"
        "v_mov_b32 v149, v197\n"
        "v_mov_b32 v150, v198\n"
        "v_mov_b32 v151, v199\n"
        "v_mov_b32 v152, v200\n"
        "v_mov_b32 v153, v201\n"
        "v_mov_b32 v154, v202\n"
        "v_mov_b32 v155, v203\n"
        "v_mov_b32 v156, v204\n"
        "v_mov_b32 v157, v205\n"
        "v_mov_b32 v158, v206\n"
        "v_mov_b32 v159, v207\n"
        "s_branch .Ln_b1\n"
        ".Ln_f2:\n"
        "s_movk_i32 s77, 2\n"
        "s_getpc_b64 s[70:71]\n"
        "s_branch .Ln_rec0\n"
        "s_branch .Ln_b2\n"
        ".Ln_f3:\n"
        "s_movk_i32 s77, 3\n"
        "s_getpc_b64 s[70:71]\n"
        "s_branch .Ln_rec1\n"
        "s_branch .Ln_b3\n"
        ".Ln_f4:\n"
        "s_movk_i32 s77, 4\n"
        "s_getpc_b64 s[70:71]\n"
        "s_branch .Ln_rec0\n"
        "s_branch .Ln_b4\n"
        ".Ln_f5:\n"
        "s_movk_i32 s77, 5\n"
        "s_getpc_b64 s[70:71]\n"
        "s_branch .Ln_rec1\n"
        "s_branch .Ln_b5\n"
        ".Ln_f6:\n"
        "s_movk_i32 s77, 6\n"
        "s_getpc_b64 s[70:71]\n"
        "s_branch .Ln_rec0\n"
        "s_branch .Ln_b6\n"
        ".Ln_f7:\n"
        "s_movk_i32 s77, 7\n"
        "s_getpc_b64 s[70:71]\n"
        "s_branch .Ln_rec1\n"
        "s_branch .Ln_b7\n"
        ".Ln_f8:\n"
        "s_movk_i32 s77, 8\n"
        "s_getpc_b64 s[70:71]\n"
        "s_branch .Ln_rec0\n"
        "s_branch .Ln_b8\n"
        ".Ln_f9:\n"
        "s_movk_i32 s77, 9\n"
        "s_getpc_b64 s[70:71]\n"
        "s_branch .Ln_rec1\n"
        "s_branch .Ln_b9\n"
        ".Ln_f10:\n"
        "s_movk_i32 s77, 10\n"
        "s_getpc_b64 s[70:71]\n"
        "s_branch .Ln_rec0\n"
        "s_branch .Ln_b10\n"
        ".Ln_f11:\n"
        "s_movk_i32 s77, 11\n"
        "s_getpc_b64 s[70:71]\n"
        "s_branch .Ln_rec1\n"
        "s_branch .Ln_b11\n"
        ".Ln_f12:\n"
        "s_movk_i32 s77, 12\n"
        "s_getpc_b64 s[70:71]\n"
        "s_branch .Ln_rec0\n"
        "s_branch .Ln_b12\n"
        ".Ln_f13:\n"
        "s_movk_i32 s77, 13\n"
        "s_getpc_b64 s[70:71]\n"
        "s_branch .Ln_rec1\n"
        "s_branch .Ln_b13\n"
        ".Ln_f14:\n"
        "s_movk_i32 s77, 14\n"
        "s_getpc_b64 s[70:71]\n"
        "s_branch .Ln_rec0\n"
        "s_branch .Ln_b14\n"
        ".Ln_f15:\n"
        "s_movk_i32 s77, 15\n"
        "s_getpc_b64 s[70:71]\n"
        "s_branch .Ln_rec1\n"
        "s_branch .Ln_b15\n"
        ".Ln_f0:\n"
        "s_movk_i32 s77, 0\n"
        "s_waitcnt vmcnt(0)\n"
        "v_mov_b32 v192, v208\n"
        "v_mov_b32 v193, v209\n"
        "v_mov_b32 v194, v210\n"
        "v_mov_b32 v195, v211\n"
        "v_mov_b32 v196, v212\n"
        "v_mov_b32 v197, v213\n"
        "v_mov_b32 v198, v214\n"
        "v_mov_b32 v199, v215\n"
        "v_mov_b32 v200, v216\n"
        "v_mov_b32 v201, v217\n"
        "v_mov_b32 v202, v218\n"
        "v_mov_b32 v203, v219\n"
        "v_mov_b32 v204, v220\n"
        "v_mov_b32 v205, v221\n"
        "v_mov_b32 v206, v222\n"
        "v_mov_b32 v207, v223\n"
        "s_getpc_b64 s[70:71]\n"
        "s_branch .Ln_rec0\n"
        "v_mov_b32 v144, v208\n"
        "v_mov_b32 v208, v192\n"
        "v_mov_b32 v145, v209\n"
        "v_mov_b32 v209, v193\n"
        "v_mov_b32 v146, v210\n"
        "v_mov_b32 v210, v194\n"
        "v_mov_b32 v147, v211\n"
        "v_mov_b32 v211, v195\n"
        "v_mov_b32 v148, v212\n"
        "v_mov_b32 v212, v196\n"
        "v_mov_b32 v149, v213\n"
        "v_mov_b32 v213, v197\n"
        "v_mov_b32 v150, v214\n"
        "v_mov_b32 v214, v198\n"
        "v_mov_b32 v151, v215\n"
        "v_mov_b32 v215, v199\n"
        "v_mov_b32 v152, v216\n"
        "v_mov_b32 v216, v200\n"
        "v_mov_b32 v153, v217\n"
        "v_mov_b32 v217, v201\n"
        "v_mov_b32 v154, v218\n"
        "v_mov_b32 v218, v202\n"
        "v_mov_b32 v155, v219\n"
        "v_mov_b32 v219, v203\n"
        "v_mov_b32 v156, v220\n"
        "v_mov_b32 v220, v204\n"
        "v_mov_b32 v157, v221\n"
        "v_mov_b32 v221, v205\n"
        "v_mov_b32 v158, v222\n"
        "v_mov_b32 v222, v206\n"
        "v_mov_b32 v159, v223\n"
        "v_mov_b32 v223, v207\n"
        "s_branch .Ln_b0\n"
        ".Ln_rec0:\n"
        "s_waitcnt lgkmcnt(0)\n"
        "s_lshl_b32 s76, s77, 14\n"
        "s_add_u32 s76, s76, s64\n"
        "3:\n"
        "s_add_u32 s73, s73, 1\n"
        "s_cmp_lt_u32 s73, s74\n"
        "s_cbranch_scc1 2f\n"
        "s_mov_b32 s74, 0\n"
        "s_mov_b32 s78, 0\n"
        "1:\n"
        "s_lshl_b32 s72, s66, 2\n"
        "s_add_u32 s72, s72, 2\n"
        "s_cmp_eq_u32 s78, 0\n"
        "s_cselect_b32 s72, s68, s72\n"
        "s_cmp_eq_u32 s78, 2\n"
        "s_cselect_b32 s72, 3, s72\n"
        "s_mul_i32 s35, s77, 7\n"
        "s_add_u32 s72, s72, s35\n"
        "s_mul_i32 s35, s72, 9363\n"
        "s_lshr_b32 s35, s35, 16\n"
        "s_mul_i32 s36, s35, 7\n"
        "s_sub_u32 s36, s72, s36\n"
        "s_mul_i32 s40, s35, 917504\n"
        "s_mul_i32 s37, s33, 896\n"
        "s_lshl_b32 s38, s36, 7\n"
        "s_add_u32 s40, s40, s37\n"
        "s_add_u32 s40, s40, s38\n"
        "s_add_u32 s41, s40, 14336\n"
        "s_add_u32 s42, s41, 14336\n"
        "s_add_u32 s43, s42, 14336\n"
        "s_add_u32 s44, s43, 14336\n"
        "s_add_u32 s45, s44, 14336\n"
        "s_add_u32 s46, s45, 14336\n"
        "s_add_u32 s47, s46, 14336\n"
        "s_lshl_b32 s75, s33, 4\n"
        "s_add_u32 s75, s75, s35\n"
        "s_lshl_b32 s75, s75, 3\n"
        "s_add_u32 s75, s75, s36\n"
        "s_lshl_b32 s75, s75, 11\n"
        "s_add_u32 s75, s75, 114688\n"
        "buffer_load_dwordx4 v[112:115], v249, s[8:11], s40 offen\n"
        "buffer_load_dwordx4 v[116:119], v249, s[8:11], s41 offen\n"
        "buffer_load_dwordx4 v[120:123], v249, s[8:11], s42 offen\n"
        "buffer_load_dwordx4 v[124:127], v249, s[8:11], s43 offen\n"
        "buffer_load_dwordx4 v[128:131], v249, s[8:11], s44 offen\n"
        "buffer_load_dwordx4 v[132:135], v249, s[8:11], s45 offen\n"
        "buffer_load_dwordx4 v[136:139], v249, s[8:11], s46 offen\n"
        "buffer_load_dwordx4 v[140:143], v249, s[8:11], s47 offen\n"
        "v_mov_b32 v210, s34\n"
        "v_mov_b32 v211, s39\n"
        "v_mov_b32 v214, s34\n"
        "v_mov_b32 v215, s39\n"
        "s_waitcnt vmcnt(0)\n"
        "v_lshl_add_u64 v[140:141], v[140:141], 4, v[136:137]\n"
        "v_lshl_add_u64 v[142:143], v[142:143], 4, v[138:139]\n"
        "v_lshl_add_u64 v[140:141], v[140:141], 4, v[132:133]\n"
        "v_lshl_add_u64 v[142:143], v[142:143], 4, v[134:135]\n"
        "v_lshl_add_u64 v[140:141], v[140:141], 4, v[128:129]\n"
        "v_lshl_add_u64 v[142:143], v[142:143], 4, v[130:131]\n"
        "v_lshl_add_u64 v[140:141], v[140:141], 4, v[124:125]\n"
        "v_lshl_add_u64 v[142:143], v[142:143], 4, v[126:127]\n"
        "v_lshl_add_u64 v[140:141], v[140:141], 4, v[120:121]\n"
        "v_lshl_add_u64 v[142:143], v[142:143], 4, v[122:123]\n"
        "v_lshl_add_u64 v[140:141], v[140:141], 4, v[116:117]\n"
        "v_lshl_add_u64 v[142:143], v[142:143], 4, v[118:119]\n"
        "v_lshl_add_u64 v[208:209], v[140:141], 4, v[112:113]\n"
        "v_lshl_add_u64 v[212:213], v[142:143], 4, v[114:115]\n"
        "s_nop 0\n"
        "v_permlane32_swap_b32 v208, v209\n"
        "v_permlane32_swap_b32 v212, v213\n"
        "buffer_store_dwordx4 v[208:211], v250, s[16:19], s75 offen sc1\n"
        "buffer_store_dwordx4 v[212:215], v251, s[16:19], s75 offen offset:1024 sc1\n"
        "s_waitcnt vmcnt(0)\n"
        "s_add_u32 s78, s78, 1\n"
        "s_cmp_lt_u32 s78, 3\n"
        "s_cbranch_scc1 1b\n"
        "2:\n"
        "s_sleep 1\n"
        "buffer_load_dwordx4 v[208:211], v232, s[16:19], s76 offen sc1\n"
        "buffer_load_dwordx4 v[212:215], v233, s[16:19], s76 offen sc1\n"
        "buffer_load_dwordx4 v[216:219], v234, s[16:19], s76 offen sc1\n"
        "buffer_load_dwordx4 v[220:223], v235, s[16:19], s76 offen sc1\n"
        "s_waitcnt vmcnt(0)\n"
        "v_bitop3_b32 v210, v210, v214, s34 bitop3:0x7e\n"
        "v_bitop3_b32 v211, v211, v215, s39 bitop3:0x7e\n"
        "v_bitop3_b32 v218, v218, v222, s34 bitop3:0x7e\n"
        "v_bitop3_b32 v219, v219, v223, s39 bitop3:0x7e\n"
        "v_or3_b32 v210, v210, v211, v218\n"
        "v_or_b32 v210, v210, v219\n"
        "v_cmp_ne_u32 vcc, 0, v210\n"
        "s_cbranch_vccnz 3b\n"
        "4:\n"
        "v_perm_b32 v224, v208, v236, s60\n"
        "ds_read_b32 v112, v224\n"
        "v_perm_b32 v225, v208, v236, s61\n"
        "ds_read_b32 v113, v225\n"
        "v_perm_b32 v226, v208, v236, s62\n"
        "ds_read_b32 v114, v226\n"
        "v_perm_b32 v227, v208, v236, s63\n"
        "ds_read_b32 v115, v227\n"
        "v_perm_b32 v224, v209, v236, s60\n"
        "ds_read_b32 v116, v224\n"
        "v_perm_b32 v225, v209, v236, s61\n"
        "ds_read_b32 v117, v225\n"
        "v_perm_b32 v226, v209, v236, s62\n"
        "ds_read_b32 v118, v226\n"
        "v_perm_b32 v227, v209, v236, s63\n"
        "ds_read_b32 v119, v227\n"
        "v_perm_b32 v224, v212, v236, s60\n"
        "ds_read_b32 v120, v224\n"
        "v_perm_b32 v225, v212, v236, s61\n"
        "ds_read_b32 v121, v225\n"
        "v_perm_b32 v226, v212, v236, s62\n"
        "ds_read_b32 v122, v226\n"
        "v_perm_b32 v227, v212, v236, s63\n"
        "ds_read_b32 v123, v227\n"
        "v_perm_b32 v224, v213, v236, s60\n"
        "ds_read_b32 v124, v224\n"
        "v_perm_b32 v225, v213, v236, s61\n"
        "ds_read_b32 v125, v225\n"
        "v_perm_b32 v226, v213, v236, s62\n"
        "ds_read_b32 v126, v226\n"
        "v_perm_b32 v227, v213, v236, s63\n"
        "ds_read_b32 v127, v227\n"
        "v_perm_b32 v224, v216, v236, s60\n"
        "ds_read_b32 v128, v224\n"
        "v_perm_b32 v225, v216, v236, s61\n"
        "ds_read_b32 v129, v225\n"
        "v_perm_b32 v226, v216, v236, s62\n"
        "ds_read_b32 v130, v226\n"
        "v_perm_b32 v227, v216, v236, s63\n"
        "ds_read_b32 v131, v227\n"
        "v_perm_b32 v224, v217, v236, s60\n"
        "ds_read_b32 v132, v224\n"
        "v_perm_b32 v225, v217, v236, s61\n"
        "ds_read_b32 v133, v225\n"
        "v_perm_b32 v226, v217, v236, s62\n"
        "ds_read_b32 v134, v226\n"
        "v_perm_b32 v227, v217, v236, s63\n"
        "ds_read_b32 v135, v227\n"
        "v_perm_b32 v224, v220, v236, s60\n"
        "ds_read_b32 v136, v224\n"
        "v_perm_b32 v225, v220, v236, s61\n"
        "ds_read_b32 v137, v225\n"
        "v_perm_b32 v226, v220, v236, s62\n"
        "ds_read_b32 v138, v226\n"
        "v_perm_b32 v227, v220, v236, s63\n"
        "ds_read_b32 v139, v227\n"
        "v_perm_b32 v224, v221, v236, s60\n"
        "ds_read_b32 v140, v224\n"
        "v_perm_b32 v225, v221, v236, s61\n"
        "ds_read_b32 v141, v225\n"
        "v_perm_b32 v226, v221, v236, s62\n"
        "ds_read_b32 v142, v226\n"
        "v_perm_b32 v227, v221, v236, s63\n"
        "ds_read_b32 v143, v227\n"
        "s_waitcnt lgkmcnt(0)\n"
        "s_add_u32 s70, s70, 4\n"
        "s_addc_u32 s71, s71, 0\n"
        "s_setpc_b64 s[70:71]\n"
        ".Ln_rec1:\n"
        "s_waitcnt lgkmcnt(0)\n"
        "s_lshl_b32 s76, s77, 14\n"
        "s_add_u32 s76, s76, s64\n"
        "3:\n"
        "s_add_u32 s73, s73, 1\n"
        "s_cmp_lt_u32 s73, s74\n"
        "s_cbranch_scc1 2f\n"
        "s_mov_b32 s74, 0\n"
        "s_mov_b32 s78, 0\n"
        "1:\n"
        "s_lshl_b32 s72, s66, 2\n"
        "s_add_u32 s72, s72, 2\n"
        "s_cmp_eq_u32 s78, 0\n"
        "s_cselect_b32 s72, s68, s72\n"
        "s_cmp_eq_u32 s78, 2\n"
        "s_cselect_b32 s72, 3, s72\n"
        "s_mul_i32 s35, s77, 7\n"
        "s_add_u32 s72, s72, s35\n"
        "s_mul_i32 s35, s72, 9363\n"
        "s_lshr_b32 s35, s35, 16\n"
        "s_mul_i32 s36, s35, 7\n"
        "s_sub_u32 s36, s72, s36\n"
        "s_mul_i32 s40, s35, 917504\n"
        "s_mul_i32 s37, s33, 896\n"
        "s_lshl_b32 s38, s36, 7\n"
        "s_add_u32 s40, s40, s37\n"
        "s_add_u32 s40, s40, s38\n"
        "s_add_u32 s41, s40, 14336\n"
        "s_add_u32 s42, s41, 14336\n"
        "s_add_u32 s43, s42, 14336\n"
        "s_add_u32 s44, s43, 14336\n"
        "s_add_u32 s45, s44, 14336\n"
        "s_add_u32 s46, s45, 14336\n"
        "s_add_u32 s47, s46, 14336\n"
        "s_lshl_b32 s75, s33, 4\n"
        "s_add_u32 s75, s75, s35\n"
        "s_lshl_b32 s75, s75, 3\n"
        "s_add_u32 s75, s75, s36\n"
        "s_lshl_b32 s75, s75, 11\n"
        "s_add_u32 s75, s75, 114688\n"
        "buffer_load_dwordx4 v[144:147], v249, s[8:11], s40 offen\n"
        "buffer_load_dwordx4 v[148:151], v249, s[8:11], s41 offen\n"
        "buffer_load_dwordx4 v[152:155], v249, s[8:11], s42 offen\n"
        "buffer_load_dwordx4 v[156:159], v249, s[8:11], s43 offen\n"
        "buffer_load_dwordx4 v[160:163], v249, s[8:11], s44 offen\n"
        "buffer_load_dwordx4 v[164:167], v249, s[8:11], s45 offen\n"
        "buffer_load_dwordx4 v[168:171], v249, s[8:11], s46 offen\n"
        "buffer_load_dwordx4 v[172:175], v249, s[8:11], s47 offen\n"
        "v_mov_b32 v210, s34\n"
        "v_mov_b32 v211, s39\n"
        "v_mov_b32 v214, s34\n"
        "v_mov_b32 v215, s39\n"
        "s_waitcnt vmcnt(0)\n"
        "v_lshl_add_u64 v[172:173], v[172:173], 4, v[168:169]\n"
        "v_lshl_add_u64 v[174:175], v[174:175], 4, v[170:171]\n"
        "v_lshl_add_u64 v[172:173], v[172:173], 4, v[164:165]\n"
        "v_lshl_add_u64 v[174:175], v[174:175], 4, v[166:167]\n"
        "v_lshl_add_u64 v[172:173], v[172:173], 4, v[160:161]\n"
        "v_lshl_add_u64 v[174:175], v[174:175], 4, v[162:163]\n"
        "v_lshl_add_u64 v[172:173], v[172:173], 4, v[156:157]\n"
        "v_lshl_add_u64 v[174:175], v[174:175], 4, v[158:159]\n"
        "v_lshl_add_u64 v[172:173], v[172:173], 4, v[152:153]\n"
        "v_lshl_add_u64 v[174:175], v[174:175], 4, v[154:155]\n"
        "v_lshl_add_u64 v[172:173], v[172:173], 4, v[148:149]\n"
        "v_lshl_add_u64 v[174:175], v[174:175], 4, v[150:151]\n"
        "v_lshl_add_u64 v[208:209], v[172:173], 4, v[144:145]\n"
        "v_lshl_add_u64 v[212:213], v[174:175], 4, v[146:147]\n"
        "s_nop 0\n"
        "v_permlane32_swap_b32 v208, v209\n"
        "v_permlane32_swap_b32 v212, v213\n"
        "buffer_store_dwordx4 v[208:211], v250, s[16:19], s75 offen sc1\n"
        "buffer_store_dwordx4 v[212:215], v251, s[16:19], s75 offen offset:1024 sc1\n"
        "s_waitcnt vmcnt(0)\n"
        "s_add_u32 s78, s78, 1\n"
        "s_cmp_lt_u32 s78, 3\n"
        "s_cbranch_scc1 1b\n"
        "2:\n"
        "s_sleep 1\n"
        "buffer_load_dwordx4 v[208:211], v232, s[16:19], s76 offen sc1\n"
        "buffer_load_dwordx4 v[212:215], v233, s[16:19], s76 offen sc1\n"
        "buffer_load_dwordx4 v[216:219], v234, s[16:19], s76 offen sc1\n"
        "buffer_load_dwordx4 v[220:223], v235, s[16:19], s76 offen sc1\n"
        "s_waitcnt vmcnt(0)\n"
        "v_bitop3_b32 v210, v210, v214, s34 bitop3:0x7e\n"
        "v_bitop3_b32 v211, v211, v215, s39 bitop3:0x7e\n"
        "v_bitop3_b32 v218, v218, v222, s34 bitop3:0x7e\n"
        "v_bitop3_b32 v219, v219, v223, s39 bitop3:0x7e\n"
        "v_or3_b32 v210, v210, v211, v218\n"
        "v_or_b32 v210, v210, v219\n"
        "v_cmp_ne_u32 vcc, 0, v210\n"
        "s_cbranch_vccnz 3b\n"
        "4:\n"
        "v_perm_b32 v224, v208, v236, s60\n"
        "ds_read_b32 v144, v224\n"
        "v_perm_b32 v225, v208, v236, s61\n"
        "ds_read_b32 v145, v225\n"
        "v_perm_b32 v226, v208, v236, s62\n"
        "ds_read_b32 v146, v226\n"
        "v_perm_b32 v227, v208, v236, s63\n"
        "ds_read_b32 v147, v227\n"
        "v_perm_b32 v224, v209, v236, s60\n"
        "ds_read_b32 v148, v224\n"
        "v_perm_b32 v225, v209, v236, s61\n"
        "ds_read_b32 v149, v225\n"
        "v_perm_b32 v226, v209, v236, s62\n"
        "ds_read_b32 v150, v226\n"
        "v_perm_b32 v227, v209, v236, s63\n"
        "ds_read_b32 v151, v227\n"
        "v_perm_b32 v224, v212, v236, s60\n"
        "ds_read_b32 v152, v224\n"
        "v_perm_b32 v225, v212, v236, s61\n"
        "ds_read_b32 v153, v225\n"
        "v_perm_b32 v226, v212, v236, s62\n"
        "ds_read_b32 v154, v226\n"
        "v_perm_b32 v227, v212, v236, s63\n"
        "ds_read_b32 v155, v227\n"
        "v_perm_b32 v224, v213, v236, s60\n"
        "ds_read_b32 v156, v224\n"
        "v_perm_b32 v225, v213, v236, s61\n"
        "ds_read_b32 v157, v225\n"
        "v_perm_b32 v226, v213, v236, s62\n"
        "ds_read_b32 v158, v226\n"
        "v_perm_b32 v227, v213, v236, s63\n"
        "ds_read_b32 v159, v227\n"
        "v_perm_b32 v224, v216, v236, s60\n"
        "ds_read_b32 v160, v224\n"
        "v_perm_b32 v225, v216, v236, s61\n"
        "ds_read_b32 v161, v225\n"
        "v_perm_b32 v226, v216, v236, s62\n"
        "ds_read_b32 v162, v226\n"
        "v_perm_b32 v227, v216, v236, s63\n"
        "ds_read_b32 v163, v227\n"
        "v_perm_b32 v224, v217, v236, s60\n"
        "ds_read_b32 v164, v224\n"
        "v_perm_b32 v225, v217, v236, s61\n"
        "ds_read_b32 v165, v225\n"
        "v_perm_b32 v226, v217, v236, s62\n"
        "ds_read_b32 v166, v226\n"
        "v_perm_b32 v227, v217, v236, s63\n"
        "ds_read_b32 v167, v227\n"
        "v_perm_b32 v224, v220, v236, s60\n"
        "ds_read_b32 v168, v224\n"
        "v_perm_b32 v225, v220, v236, s61\n"
        "ds_read_b32 v169, v225\n"
        "v_perm_b32 v226, v220, v236, s62\n"
        "ds_read_b32 v170, v226\n"
        "v_perm_b32 v227, v220, v236, s63\n"
        "ds_read_b32 v171, v227\n"
        "v_perm_b32 v224, v221, v236, s60\n"
        "ds_read_b32 v172, v224\n"
        "v_perm_b32 v225, v221, v236, s61\n"
        "ds_read_b32 v173, v225\n"
        "v_perm_b32 v226, v221, v236, s62\n"
        "ds_read_b32 v174, v226\n"
        "v_perm_b32 v227, v221, v236, s63\n"
        "ds_read_b32 v175, v227\n"
        "s_waitcnt lgkmcnt(0)\n"
        "s_add_u32 s70, s70, 4\n"
        "s_addc_u32 s71, s71, 0\n"
        "s_setpc_b64 s[70:71]\n"
        ".Ln_ret:\n"
        "s_setpc_b64 s[70:71]\n"
        ".Ln_end:\n"
        :: "{s[0:1]}"((u64)__builtin_amdgcn_dispatch_ptr()), "{s[2:3]}"((u64)__builtin_amdgcn_kernarg_segment_ptr()), "{s[4:5]}"(llvm_dispatch_id()),
           "{s6}"(blockIdx.x), "{v0}"(threadIdx.x), "s"((unsigned)(size_t)(__attribute__((address_space(3))) unsigned char*)smem)
        : "v1", "v2", "v3", "v4", "v5", "v6", "v7", "v8", "v9", "v10", "v11", "v12", "v13", "v14", "v15", "v16", "v17", "v18", "v19", "v20", "v21", "v22", "v23", "v24", "v25", "v26", "v27", "v28", "v29", "v30", "v31", "v32", "v33", "v34", "v35", "v36", "v37", "v38", "v39", "v40", "v41", "v42", "v43", "v44", "v45", "v46", "v47", "v48", "v49", "v50", "v51", "v52", "v53", "v54", "v55", "v56", "v57", "v58", "v59", "v60", "v61", "v62", "v63", "v64", "v65", "v66", "v67", "v68", "v69", "v70", "v71", "v72", "v73", "v74", "v75", "v76", "v77", "v78", "v79", "v80", "v81", "v82", "v83", "v84", "v85", "v86", "v87", "v88", "v89", "v90", "v91", "v92", "v93", "v94", "v95", "v96", "v97", "v98", "v99", "v100", "v101", "v102", "v103", "v104", "v105", "v106", "v107", "v108", "v109", "v110", "v111", "v112", "v113", "v114", "v115", "v116", "v117", "v118", "v119", "v120", "v121", "v122", "v123", "v124", "v125", "v126", "v127", "v128", "v129", "v130", "v131", "v132", "v133", "v134", "v135", "v136", "v137", "v138", "v139", "v140", "v141", "v142", "v143", "v144", "v145", "v146", "v147", "v148", "v149", "v150", "v151", "v152", "v153", "v154", "v155", "v156", "v157", "v158", "v159", "v160", "v161", "v162", "v163", "v164", "v165", "v166", "v167", "v168", "v169", "v170", "v171", "v172", "v173", "v174", "v175", "v176", "v177", "v178", "v179", "v180", "v181", "v182", "v183", "v184", "v185", "v186", "v187", "v188", "v189", "v190", "v191", "v192", "v193", "v194", "v195", "v196", "v197", "v198", "v199", "v200", "v201", "v202", "v203", "v204", "v205", "v206", "v207", "v208", "v209", "v210", "v211", "v212", "v213", "v214", "v215", "v216", "v217", "v218", "v219", "v220", "v221", "v222", "v223", "v224", "v225", "v226", "v227", "v228", "v229", "v230", "v231", "v232", "v233", "v234", "v235", "v236", "v237", "v238", "v239", "v240", "v241", "v242", "v243", "v244", "v245", "v246", "v247", "v248", "v249", "v250", "v251", "v252", "v253", "v254", "v255", "s7", "s8", "s9", "s10", "s11", "s12", "s13", "s14", "s15", "s16", "s17", "s18", "s19", "s20", "s21", "s22", "s23", "s24", "s25", "s26", "s27", "s28", "s29", "s30", "s31", "s32", "s33", "s34", "s35", "s36", "s37", "s38", "s39", "s40", "s41", "s42", "s43", "s44", "s45", "s46", "s47", "s48", "s49", "s50", "s51", "s52", "s53", "s54", "s55", "s56", "s57", "s58", "s59", "s60", "s61", "s62", "s63", "s64", "s65", "s66", "s67", "s68", "s69", "s70", "s71", "s72", "s73", "s74", "s75", "s76", "s77", "s78", "s79", "s80", "s81", "s82", "s83", "s84", "s85", "s86", "s87", "s88", "s89", "s90", "s91", "s92", "s93", "s94", "s95", "vcc", "scc", "memory");
}
}

extern "C" void kernel_launch(void* const* d_in, const int* in_sizes, int n_in, void* d_out, int out_size, void* d_ws, size_t ws_size, hipStream_t stream) {
    const u64 fl = (u64)0x00020000 << 32;
    nf4_gemm_kernel<<<dim3(256), dim3(512), 0, stream>>>(d_in[1], fl | (1024u * 3584u * 4u), d_in[0], fl | (2048u * 1024u * 2u), (char*)d_ws + (32u << 20), fl | ((4u << 20) + 114688u),
                                                         d_out, fl | (2048u * 3584u * 2u), d_in[2], d_in[3], d_ws);
}
```

```cpp
#include <hip/hip_runtime.h>
#include <stdint.h>

extern "C" __device__ unsigned long long llvm_dispatch_id() __asm("llvm.amdgcn.dispatch.id");

namespace {
typedef unsigned long long u64;
__global__ __launch_bounds__(512, 2)
void nf4_gemm_kernel(const void* q, u64 qd, const void* x, u64 xd, void* w, u64 wd, void* o, u64 od, const void* lut, const void* scales, void* dbg) {
    __shared__ __attribute__((aligned(16))) unsigned char smem[163840];
    asm volatile(
        ".Ln_start:\n"
        "s_load_dwordx16 s[8:23], s[2:3], 0x0\n"
        "s_load_dwordx4 s[24:27], s[2:3], 0x40\n"
        "v_readfirstlane_b32 s30, v0\n"
        "v_and_b32 v252, 63, v0\n"
        "s_getpc_b64 s[86:87]\n"
        ".Ln_here:\n"
        "s_cmp_lt_u32 s30, 448\n"
        "s_cbranch_scc1 1f\n"
        "s_lshr_b32 s35, s6, 3\n"
        "s_lshl_b32 s35, s35, 7\n"
        "v_lshl_add_u32 v3, v252, 12, s35\n"
        "v_min_u32 v3, .Ln_end-.Ln_here, v3\n"
        "global_load_dword v110, v3, s[86:87]\n"
        "1:\n"
        "s_and_b32 s32, s6, 3\n"
        "s_lshl_b32 s32, s32, 2\n"
        "s_bfe_u32 s35, s6, 0x20003\n"
        "s_or_b32 s32, s32, s35\n"
        "s_and_b32 s33, s6, 4\n"
        "s_lshl_b32 s33, s33, 1\n"
        "s_lshr_b32 s35, s6, 5\n"
        "s_or_b32 s33, s33, s35\n"
        "s_lshr_b32 s30, s30, 6\n"
        "s_and_b32 s31, s30, 3\n"
        "v_lshrrev_b32 v1, 3, v252\n"
        "v_and_b32 v2, 7, v252\n"
        "v_mul_u32_u24 v4, 0x1c000, v1\n"
        "v_lshl_add_u32 v249, v2, 4, v4\n"
        "s_cmp_ge_u32 s30, 4\n"
        "s_cbranch_scc1 .Ln_helper\n"
        "s_lshl_b32 s72, s30, 4\n"
        "s_add_u32 s72, s72, s32\n"
        "s_mul_i32 s35, s72, 9363\n"
        "s_lshr_b32 s35, s35, 16\n"
        "s_mul_i32 s36, s35, 7\n"
        "s_sub_u32 s36, s72, s36\n"
        "s_mul_i32 s40, s35, 917504\n"
        "s_mul_i32 s37, s33, 896\n"
        "s_lshl_b32 s38, s36, 7\n"
        "s_add_u32 s40, s40, s37\n"
        "s_add_u32 s40, s40, s38\n"
        "s_add_u32 s41, s40, 14336\n"
        "s_add_u32 s42, s41, 14336\n"
        "s_add_u32 s43, s42, 14336\n"
        "s_add_u32 s44, s43, 14336\n"
        "s_add_u32 s45, s44, 14336\n"
        "s_add_u32 s46, s45, 14336\n"
        "s_add_u32 s47, s46, 14336\n"
        "s_cmp_ge_u32 s30, 2\n"
        "s_cbranch_scc1 2f\n"
        "s_waitcnt lgkmcnt(0)\n"
        "buffer_load_dwordx4 v[112:115], v249, s[8:11], s40 offen\n"
        "buffer_load_dwordx4 v[116:119], v249, s[8:11], s41 offen\n"
        "buffer_load_dwordx4 v[120:123], v249, s[8:11], s42 offen\n"
        "buffer_load_dwordx4 v[124:127], v249, s[8:11], s43 offen\n"
        "buffer_load_dwordx4 v[128:131], v249, s[8:11], s44 offen\n"
        "buffer_load_dwordx4 v[132:135], v249, s[8:11], s45 offen\n"
        "buffer_load_dwordx4 v[136:139], v249, s[8:11], s46 offen\n"
        "buffer_load_dwordx4 v[140:143], v249, s[8:11], s47 offen\n"
        "2:\n"
        "s_waitcnt lgkmcnt(0)\n"
        "s_lshl_b32 s37, s31, 3\n"
        "s_load_dwordx8 s[48:55], s[24:25], 0x0\n"
        "s_load_dwordx2 s[56:57], s[24:25], s37\n"
        "s_lshl_b32 s75, s33, 4\n"
        "s_add_u32 s75, s75, s35\n"
        "s_lshl_b32 s75, s75, 3\n"
        "s_add_u32 s75, s75, s36\n"
        "s_lshl_b32 s75, s75, 11\n"
        "s_add_u32 s75, s75, 114688\n"
        "s_mul_i32 s34, s4, 0x7f4a7c15\n"
        "s_mul_hi_u32 s39, s4, 0x7f4a7c15\n"
        "s_mul_i32 s35, s4, 0x9e3779b9\n"
        "s_mul_i32 s36, s5, 0x7f4a7c15\n"
        "s_add_u32 s39, s39, s35\n"
        "s_add_u32 s39, s39, s36\n"
        "s_xor_b32 s34, s34, s0\n"
        "s_xor_b32 s39, s39, s1\n"
        "s_xor_b32 s34, s34, 0x5bd1e995\n"
        "s_mov_b32 s73, 0\n"
        "s_mov_b32 s74, 20000\n"
        "s_lshr_b32 s66, s31, 1\n"
        "s_and_b32 s67, s31, 1\n"
        "s_lshl_b32 s68, s66, 2\n"
        "s_add_u32 s68, s68, s67\n"
        "s_lshl_b32 s64, s33, 18\n"
        "s_add_u32 s65, s64, 0x4000\n"
        "v_and_b32 v184, 15, v252\n"
        "v_lshrrev_b32 v185, 4, v252\n"
        "v_and_b32 v186, 3, v1\n"
        "v_lshlrev_b32 v186, 8, v186\n"
        "v_lshl_add_u32 v186, v2, 4, v186\n"
        "v_lshrrev_b32 v250, 5, v252\n"
        "v_lshl_add_u32 v250, v250, 7, v186\n"
        "v_xor_b32 v251, 0xc0, v250\n"
        "v_mov_b32 v178, s34\n"
        "v_mov_b32 v179, s39\n"
        "v_mov_b32 v182, s34\n"
        "v_mov_b32 v183, s39\n"
        "v_and_b32 v192, 3, v1\n"
        "v_mul_u32_u24 v193, 0x202, v192\n"
        "v_add_u32 v193, 0x0c0c0100, v193\n"
        "v_lshlrev_b32 v192, 8, v192\n"
        "v_lshrrev_b32 v144, 5, v252\n"
        "v_lshl_add_u32 v192, v144, 12, v192\n"
        "v_mul_u32_u24 v144, 0x202, v144\n"
        "v_lshlrev_b32 v144, 16, v144\n"
        "v_add_u32 v144, 0x01000c0c, v144\n"
        "v_lshl_add_u32 v192, v2, 4, v192\n"
        "s_lshl_b32 s35, s31, 14\n"
        "v_add_u32 v192, s35, v192\n"
        "v_and_b32 v186, 1, v184\n"
        "v_lshrrev_b32 v187, 1, v184\n"
        "v_lshl_add_u32 v187, v185, 4, v187\n"
        "v_mul_u32_u24 v188, 0x4c, v186\n"
        "v_xor_b32 v187, v187, v188\n"
        "s_lshl_b32 s36, s68, 11\n"
        "s_add_u32 s36, s36, 114688\n"
        "v_lshl_add_u32 v232, v187, 4, s36\n"
        "v_xor_b32 v233, 0x80, v232\n"
        "v_lshrrev_b32 v187, 2, v184\n"
        "v_lshl_add_u32 v187, v185, 4, v187\n"
        "v_lshl_add_u32 v187, v186, 3, v187\n"
        "v_bfe_u32 v188, v184, 1, 1\n"
        "v_mul_u32_u24 v188, 0x4c, v188\n"
        "s_lshl_b32 s36, s67, 2\n"
        "v_xor_b32 v188, s36, v188\n"
        "v_xor_b32 v187, v188, v187\n"
        "s_mov_b32 s36, 120832\n"
        "v_lshl_add_u32 v235, v187, 4, s36\n"
        "s_lshl_b32 s35, s66, 13\n"
        "s_sub_u32 s35, s35, 2048\n"
        "v_add_u32 v234, s35, v235\n"
        "v_and_b32 v236, 31, v252\n"
        "v_lshlrev_b32 v236, 2, v236\n"
        "v_bfe_u32 v186, v184, 1, 3\n"
        "v_xor_b32 v186, v186, v185\n"
        "v_lshlrev_b32 v186, 4, v186\n"
        "v_lshl_add_u32 v186, v184, 7, v186\n"
        "s_lshl_b32 s35, s66, 13\n"
        "s_add_u32 s35, s35, 65536\n"
        "v_add_u32 v228, s35, v186\n"
        "v_xor_b32 v229, 64, v228\n"
        "v_xor_b32 v230, 0x2000, v228\n"
        "v_xor_b32 v231, 0x2000, v229\n"
        "s_waitcnt lgkmcnt(0)\n"
        "v_mov_b32 v194, s48\n"
        "v_mov_b32 v195, s50\n"
        "v_mov_b32 v196, s52\n"
        "v_mov_b32 v197, s54\n"
        "v_perm_b32 v194, s49, v194, v193\n"
        "v_perm_b32 v195, s51, v195, v193\n"
        "v_perm_b32 v196, s53, v196, v193\n"
        "v_perm_b32 v197, s55, v197, v193\n"
        "v_perm_b32 v198, s56, s56, v144\n"
        "v_perm_b32 v199, s57, s57, v144\n"
        "v_or_b32 v144, v194, v198\n"
        "v_or_b32 v145, v194, v198\n"
        "v_or_b32 v146, v194, v198\n"
        "v_or_b32 v147, v194, v198\n"
        "ds_write_b128 v192, v[144:147] offset:0\n"
        "v_or_b32 v148, v195, v198\n"
        "v_or_b32 v149, v195, v198\n"
        "v_or_b32 v150, v195, v198\n"
        "v_or_b32 v151, v195, v198\n"
        "ds_write_b128 v192, v[148:151] offset:1024\n"
        "v_or_b32 v152, v196, v198\n"
        "v_or_b32 v153, v196, v198\n"
        "v_or_b32 v154, v196, v198\n"
        "v_or_b32 v155, v196, v198\n"
        "ds_write_b128 v192, v[152:155] offset:2048\n"
        "v_or_b32 v156, v197, v198\n"
        "v_or_b32 v157, v197, v198\n"
        "v_or_b32 v158, v197, v198\n"
        "v_or_b32 v159, v197, v198\n"
        "ds_write_b128 v192, v[156:159] offset:3072\n"
        "v_or_b32 v160, v194, v199\n"
        "v_or_b32 v161, v194, v199\n"
        "v_or_b32 v162, v194, v199\n"
        "v_or_b32 v163, v194, v199\n"
        "ds_write_b128 v192, v[160:163] offset:8192\n"
        "v_or_b32 v164, v195, v199\n"
        "v_or_b32 v165, v195, v199\n"
        "v_or_b32 v166, v195, v199\n"
        "v_or_b32 v167, v195, v199\n"
        "ds_write_b128 v192, v[164:167] offset:9216\n"
        "v_or_b32 v168, v196, v199\n"
        "v_or_b32 v169, v196, v199\n"
        "v_or_b32 v170, v196, v199\n"
        "v_or_b32 v171, v196, v199\n"
        "ds_write_b128 v192, v[168:171] offset:10240\n"
        "v_or_b32 v172, v197, v199\n"
        "v_or_b32 v173, v197, v199\n"
        "v_or_b32 v174, v197, v199\n"
        "v_or_b32 v175, v197, v199\n"
        "ds_write_b128 v192, v[172:175] offset:11264\n"
        "s_mul_i32 s35, s33, 448\n"
        "s_lshl_b32 s36, s68, 6\n"
        "s_add_u32 s36, s36, s35\n"
        "v_lshl_add_u32 v108, v185, 4, s36\n"
        "s_lshl_b32 s36, s67, 5\n"
        "s_add_u32 s35, s35, s36\n"
        "v_lshl_add_u32 v109, v185, 3, s35\n"
        "s_lshl_b32 s36, s66, 8\n"
        "v_add_u32 v110, s36, v109\n"
        "v_and_b32 v186, 7, v184\n"
        "s_lshl_b32 s35, s66, 12\n"
        "s_add_u32 s35, s35, 98304\n"
        "v_lshl_add_u32 v187, v184, 8, s35\n"
        "s_add_u32 s36, s66, s67\n"
        "s_lshl_b32 s36, s36, 2\n"
        "v_add_u32 v188, s36, v185\n"
        "v_xor_b32 v188, v188, v186\n"
        "v_lshl_add_u32 v246, v188, 4, v187\n"
        "v_and_b32 v188, 1, v185\n"
        "v_lshl_add_u32 v187, v188, 3, v187\n"
        "v_lshrrev_b32 v185, 1, v185\n"
        "s_lshl_b32 s35, s67, 1\n"
        "s_lshl_b32 s36, s66, 2\n"
        "s_add_u32 s36, s36, s35\n"
        "s_add_u32 s36, s36, 8\n"
        "v_add_u32 v188, s36, v185\n"
        "v_xor_b32 v188, v188, v186\n"
        "v_lshl_add_u32 v247, v188, 4, v187\n"
        "s_mul_i32 s36, s66, 12\n"
        "s_sub_u32 s36, s35, s36\n"
        "s_add_u32 s36, s36, 12\n"
        "v_add_u32 v188, s36, v185\n"
        "v_xor_b32 v188, v188, v186\n"
        "v_lshl_add_u32 v248, v188, 4, v187\n"
        "s_lshl_b32 s35, s31, 2\n"
        "s_add_u32 s35, s35, 128\n"
        "v_mov_b32 v237, s35\n"
        "s_mov_b32 s60, 0x0c0c0400\n"
        "s_mov_b32 s61, 0x0c0c0500\n"
        "s_mov_b32 s62, 0x0c0c0600\n"
        "s_mov_b32 s63, 0x0c0c0700\n"
        "global_load_dwordx4 v[238:241], v108, s[26:27]\n"
        "global_load_dwordx2 v[242:243], v110, s[26:27] offset:128\n"
        "global_load_dwordx2 v[244:245], v109, s[26:27] offset:192\n"
        "s_cmp_ge_u32 s30, 2\n"
        "s_cbranch_scc1 .Ln_none\n"
        "s_waitcnt vmcnt(3)\n"
        "v_lshl_add_u64 v[140:141], v[140:141], 4, v[136:137]\n"
        "v_lshl_add_u64 v[142:143], v[142:143], 4, v[138:139]\n"
        "v_lshl_add_u64 v[140:141], v[140:141], 4, v[132:133]\n"
        "v_lshl_add_u64 v[142:143], v[142:143], 4, v[134:135]\n"
        "v_lshl_add_u64 v[140:141], v[140:141], 4, v[128:129]\n"
        "v_lshl_add_u64 v[142:143], v[142:143], 4, v[130:131]\n"
        "v_lshl_add_u64 v[140:141], v[140:141], 4, v[124:125]\n"
        "v_lshl_add_u64 v[142:143], v[142:143], 4, v[126:127]\n"
        "v_lshl_add_u64 v[140:141], v[140:141], 4, v[120:121]\n"
        "v_lshl_add_u64 v[142:143], v[142:143], 4, v[122:123]\n"
        "v_lshl_add_u64 v[140:141], v[140:141], 4, v[116:117]\n"
        "v_lshl_add_u64 v[142:143], v[142:143], 4, v[118:119]\n"
        "v_lshl_add_u64 v[176:177], v[140:141], 4, v[112:113]\n"
        "v_lshl_add_u64 v[180:181], v[142:143], 4, v[114:115]\n"
        "s_nop 0\n"
        "v_permlane32_swap_b32 v176, v177\n"
        "v_permlane32_swap_b32 v180, v181\n"
        "buffer_store_dwordx4 v[176:179], v250, s[16:19], s75 offen sc1\n"
        "buffer_store_dwordx4 v[180:183], v251, s[16:19], s75 offen offset:1024 sc1\n"
        ".Ln_none:\n"
        "s_waitcnt lgkmcnt(0)\n"
        "s_barrier\n"
        "s_sleep 12\n"
        "buffer_load_dwordx4 v[64:67], v232, s[16:19], s64 offen sc1\n"
        "buffer_load_dwordx4 v[68:71], v233, s[16:19], s64 offen sc1\n"
        "buffer_load_dwordx4 v[72:75], v234, s[16:19], s64 offen sc1\n"
        "buffer_load_dwordx4 v[76:79], v235, s[16:19], s64 offen sc1\n"
        "buffer_load_dwordx4 v[208:211], v232, s[16:19], s65 offen sc1\n"
        "buffer_load_dwordx4 v[212:215], v233, s[16:19], s65 offen sc1\n"
        "buffer_load_dwordx4 v[216:219], v234, s[16:19], s65 offen sc1\n"
        "buffer_load_dwordx4 v[220:223], v235, s[16:19], s65 offen sc1\n"
        "ds_read_b128 v[176:179], v228 offset:16384\n"
        "ds_read_b128 v[180:183], v229 offset:16384\n"
        "ds_read_b128 v[184:187], v228 offset:18432\n"
        "ds_read_b128 v[188:191], v229 offset:18432\n"
        "ds_read_b128 v[192:195], v228 offset:20480\n"
        "ds_read_b128 v[196:199], v229 offset:20480\n"
        "ds_read_b128 v[200:203], v228 offset:22528\n"
        "ds_read_b128 v[204:207], v229 offset:22528\n"
        "ds_read_b128 v[80:83], v230 offset:16384\n"
        "ds_read_b128 v[84:87], v231 offset:16384\n"
        "ds_read_b128 v[88:91], v230 offset:18432\n"
        "ds_read_b128 v[92:95], v231 offset:18432\n"
        "ds_read_b128 v[100:103], v230 offset:20480\n"
        "ds_read_b128 v[104:107], v231 offset:20480\n"
        "s_branch .Ln_go\n"
        ".Ln_helper:\n"
        "s_lshl_b32 s35, s32, 2\n"
        "s_add_u32 s35, s35, s31\n"
        "s_lshl_b32 s35, s35, 16\n"
        "v_lshrrev_b32 v80, 4, v252\n"
        "v_xor_b32 v81, v80, v2\n"
        "v_lshl_add_u32 v82, v1, 11, s35\n"
        "v_lshl_add_u32 v8, v81, 4, v82\n"
        "v_xor_b32 v9, 64, v8\n"
        "v_add_u32 v9, 0x3c00, v9\n"
        "v_add_u32 v10, 0x7800, v8\n"
        "v_add_u32 v11, 0x7800, v9\n"
        "s_lshl_b32 s58, s31, 12\n"
        "s_add_u32 s58, s58, 65536\n"
        "s_waitcnt lgkmcnt(0)\n"
        "s_add_u32 m0, s58, 16384\n"
        "s_movk_i32 s59, 0\n"
        "buffer_load_dwordx4 v8, s[12:15], s59 offen offset:0 lds\n"
        "buffer_load_dwordx4 v9, s[12:15], s59 offen offset:1024 lds\n"
        "buffer_load_dwordx4 v10, s[12:15], s59 offen offset:2048 lds\n"
        "buffer_load_dwordx4 v11, s[12:15], s59 offen offset:3072 lds\n"
        "s_mul_i32 s34, s4, 0x7f4a7c15\n"
        "s_mul_hi_u32 s39, s4, 0x7f4a7c15\n"
        "s_mul_i32 s35, s4, 0x9e3779b9\n"
        "s_mul_i32 s36, s5, 0x7f4a7c15\n"
        "s_add_u32 s39, s39, s35\n"
        "s_add_u32 s39, s39, s36\n"
        "s_xor_b32 s34, s34, s0\n"
        "s_xor_b32 s39, s39, s1\n"
        "s_xor_b32 s34, s34, 0x5bd1e995\n"
        "s_cmp_lt_u32 s31, 3\n"
        "s_cselect_b32 s35, 0, 0x7f000000\n"
        "s_lshl_b32 s64, s33, 18\n"
        "s_add_u32 s36, s58, 49152\n"
        "v_lshl_add_u32 v12, v252, 4, s36\n"
        "v_or_b32 v13, s35, v12\n"
        "v_and_b32 v83, 3, v1\n"
        "v_lshlrev_b32 v83, 8, v83\n"
        "v_lshl_add_u32 v83, v2, 4, v83\n"
        "v_lshrrev_b32 v250, 5, v252\n"
        "v_lshl_add_u32 v250, v250, 7, v83\n"
        "v_xor_b32 v251, 0xc0, v250\n"
        "v_mov_b32 v74, s34\n"
        "v_mov_b32 v75, s39\n"
        "v_mov_b32 v78, s34\n"
        "v_mov_b32 v79, s39\n"
        "v_mov_b32 v83, 0\n"
        "s_lshl_b32 s35, s31, 2\n"
        "s_add_u32 s35, s35, 128\n"
        "v_lshl_add_u32 v84, v252, 8, s35\n"
        "ds_write_b32 v84, v83\n"
        "v_and_b32 v81, 15, v252\n"
        "s_lshr_b32 s66, s31, 1\n"
        "s_and_b32 s67, s31, 1\n"
        "v_xor_b32 v82, v80, v81\n"
        "v_lshlrev_b32 v82, 4, v82\n"
        "v_lshl_add_u32 v82, v80, 8, v82\n"
        "s_lshl_b32 s37, s31, 11\n"
        "s_add_u32 s37, s37, 98304\n"
        "v_add_u32 v88, s37, v82\n"
        "v_xor_b32 v89, 64, v88\n"
        "s_lshl_b32 s35, s32, 4\n"
        "s_add_u32 s35, s35, s67\n"
        "s_mul_i32 s35, s35, 57344\n"
        "s_mul_i32 s36, s33, 448\n"
        "s_add_u32 s35, s35, s36\n"
        "s_mul_i32 s36, s66, 192\n"
        "s_add_u32 s35, s35, s36\n"
        "v_mul_u32_u24 v82, 0x1c00, v80\n"
        "v_lshl_add_u32 v82, v81, 4, v82\n"
        "v_add_u32 v90, s35, v82\n"
        "s_lshl_b32 s36, s66, 2\n"
        "v_add_u32 v100, s36, v2\n"
        "v_add_u32 v101, 8, v100\n"
        "v_and_b32 v101, 15, v101\n"
        "v_mul_u32_u24 v82, 0x1c00, v1\n"
        "v_add_u32 v82, s35, v82\n"
        "v_lshl_add_u32 v102, v100, 4, v82\n"
        "v_lshl_add_u32 v103, v101, 4, v82\n"
        "v_lshl_add_u32 v82, v1, 8, s37\n"
        "v_xor_b32 v100, v1, v100\n"
        "v_xor_b32 v101, v1, v101\n"
        "v_lshl_add_u32 v100, v100, 4, v82\n"
        "v_lshl_add_u32 v101, v101, 4, v82\n"
        "v_subrev_u32 v81, s36, v81\n"
        "v_cmp_gt_u32 vcc, 12, v81\n"
        "v_mov_b32 v82, 0x7f000000\n"
        "s_lshl_b32 s35, s66, 3\n"
        "v_cndmask_b32 v91, v82, v90, vcc\n"
        "s_add_u32 s88, s35, 128\n"
        "s_add_u32 s89, s88, 4\n"
        "v_mov_b32 v92, s88\n"
        "s_mul_i32 s84, s66, 458752\n"
        "s_sub_u32 s85, 458752, s84\n"
        "s_cmp_eq_u32 s31, 1\n"
        "s_cbranch_scc1 .Ln_h1\n"
        "s_cmp_eq_u32 s31, 2\n"
        "s_cbranch_scc1 .Ln_h2\n"
        "s_cmp_eq_u32 s31, 3\n"
        "s_cbranch_scc1 .Ln_h3\n"
        ".Ln_h0:\n"
        "s_add_u32 s72, s32, 32\n"
        "s_mul_i32 s35, s72, 9363\n"
        "s_lshr_b32 s35, s35, 16\n"
        "s_mul_i32 s36, s35, 7\n"
        "s_sub_u32 s36, s72, s36\n"
        "s_mul_i32 s40, s35, 917504\n"
        "s_mul_i32 s37, s33, 896\n"
        "s_lshl_b32 s38, s36, 7\n"
        "s_add_u32 s40, s40, s37\n"
        "s_add_u32 s40, s40, s38\n"
        "s_add_u32 s41, s40, 14336\n"
        "s_add_u32 s42, s41, 14336\n"
        "s_add_u32 s43, s42, 14336\n"
        "s_add_u32 s44, s43, 14336\n"
        "s_add_u32 s45, s44, 14336\n"
        "s_add_u32 s46, s45, 14336\n"
        "s_add_u32 s47, s46, 14336\n"
        "s_lshl_b32 s75, s33, 4\n"
        "s_add_u32 s75, s75, s35\n"
        "s_lshl_b32 s75, s75, 3\n"
        "s_add_u32 s75, s75, s36\n"
        "s_lshl_b32 s75, s75, 11\n"
        "s_add_u32 s75, s75, 114688\n"
        "buffer_load_dwordx4 v[40:43], v249, s[8:11], s40 offen\n"
        "buffer_load_dwordx4 v[44:47], v249, s[8:11], s41 offen\n"
        "buffer_load_dwordx4 v[48:51], v249, s[8:11], s42 offen\n"
        "buffer_load_dwordx4 v[52:55], v249, s[8:11], s43 offen\n"
        "buffer_load_dwordx4 v[56:59], v249, s[8:11], s44 offen\n"
        "buffer_load_dwordx4 v[60:63], v249, s[8:11], s45 offen\n"
        "buffer_load_dwordx4 v[64:67], v249, s[8:11], s46 offen\n"
        "buffer_load_dwordx4 v[68:71], v249, s[8:11], s47 offen\n"
        "s_waitcnt vmcnt(8)\n"
        "s_waitcnt lgkmcnt(0)\n"
        "s_barrier\n"
        "s_sleep 16\n"
        "s_add_u32 m0, s58, 32768\n"
        "s_movk_i32 s59, 128\n"
        "buffer_load_dwordx4 v8, s[12:15], s59 offen offset:0 lds\n"
        "buffer_load_dwordx4 v9, s[12:15], s59 offen offset:1024 lds\n"
        "buffer_load_dwordx4 v10, s[12:15], s59 offen offset:2048 lds\n"
        "buffer_load_dwordx4 v11, s[12:15], s59 offen offset:3072 lds\n"
        "s_add_u32 m0, s58, 65536\n"
        "s_add_u32 s76, s64, 32768\n"
        "buffer_load_dwordx4 v12, s[16:19], s76 offen offset:0 sc1 lds\n"
        "buffer_load_dwordx4 v12, s[16:19], s76 offen offset:1024 sc1 lds\n"
        "buffer_load_dwordx4 v12, s[16:19], s76 offen offset:2048 sc1 lds\n"
        "buffer_load_dwordx4 v12, s[16:19], s76 offen offset:3072 sc1 lds\n"
        "s_waitcnt vmcnt(8)\n"
        "v_lshl_add_u64 v[68:69], v[68:69], 4, v[64:65]\n"
        "v_lshl_add_u64 v[70:71], v[70:71], 4, v[66:67]\n"
        "v_lshl_add_u64 v[68:69], v[68:69], 4, v[60:61]\n"
        "v_lshl_add_u64 v[70:71], v[70:71], 4, v[62:63]\n"
        "v_lshl_add_u64 v[68:69], v[68:69], 4, v[56:57]\n"
        "v_lshl_add_u64 v[70:71], v[70:71], 4, v[58:59]\n"
        "v_lshl_add_u64 v[68:69], v[68:69], 4, v[52:53]\n"
        "v_lshl_add_u64 v[70:71], v[70:71], 4, v[54:55]\n"
        "v_lshl_add_u64 v[68:69], v[68:69], 4, v[48:49]\n"
        "v_lshl_add_u64 v[70:71], v[70:71], 4, v[50:51]\n"
        "v_lshl_add_u64 v[68:69], v[68:69], 4, v[44:45]\n"
        "v_lshl_add_u64 v[70:71], v[70:71], 4, v[46:47]\n"
        "v_lshl_add_u64 v[72:73], v[68:69], 4, v[40:41]\n"
        "v_lshl_add_u64 v[76:77], v[70:71], 4, v[42:43]\n"
        "s_nop 0\n"
        "v_permlane32_swap_b32 v72, v73\n"
        "v_permlane32_swap_b32 v76, v77\n"
        "buffer_store_dwordx4 v[72:75], v250, s[16:19], s75 offen sc1\n"
        "buffer_store_dwordx4 v[76:79], v251, s[16:19], s75 offen offset:1024 sc1\n"
        "s_add_u32 s72, s32, 96\n"
        "s_mul_i32 s35, s72, 9363\n"
        "s_lshr_b32 s35, s35, 16\n"
        "s_mul_i32 s36, s35, 7\n"
        "s_sub_u32 s36, s72, s36\n"
        "s_mul_i32 s40, s35, 917504\n"
        "s_mul_i32 s37, s33, 896\n"
        "s_lshl_b32 s38, s36, 7\n"
        "s_add_u32 s40, s40, s37\n"
        "s_add_u32 s40, s40, s38\n"
        "s_add_u32 s41, s40, 14336\n"
        "s_add_u32 s42, s41, 14336\n"
        "s_add_u32 s43, s42, 14336\n"
        "s_add_u32 s44, s43, 14336\n"
        "s_add_u32 s45, s44, 14336\n"
        "s_add_u32 s46, s45, 14336\n"
        "s_add_u32 s47, s46, 14336\n"
        "s_lshl_b32 s75, s33, 4\n"
        "s_add_u32 s75, s75, s35\n"
        "s_lshl_b32 s75, s75, 3\n"
        "s_add_u32 s75, s75, s36\n"
        "s_lshl_b32 s75, s75, 11\n"
        "s_add_u32 s75, s75, 114688\n"
        "v_mov_b32 v84, 128\n"
        "s_movk_i32 s73, 64\n"
        "1:\n"
        "s_sub_u32 s73, s73, 1\n"
        "s_cbranch_scc1 2f\n"
        "s_sleep 1\n"
        "ds_read_b128 v[96:99], v84 offset:4096\n"
        "s_waitcnt lgkmcnt(0)\n"
        "v_min3_u32 v96, v96, v97, v98\n"
        "v_min_u32 v96, v96, v99\n"
        "v_cmp_eq_u32 vcc, 0, v96\n"
        "s_cbranch_vccnz 1b\n"
        "2:\n"
        "s_add_u32 m0, s58, 0\n"
        "s_movk_i32 s59, 256\n"
        "buffer_load_dwordx4 v8, s[12:15], s59 offen offset:0 lds\n"
        "buffer_load_dwordx4 v9, s[12:15], s59 offen offset:1024 lds\n"
        "buffer_load_dwordx4 v10, s[12:15], s59 offen offset:2048 lds\n"
        "buffer_load_dwordx4 v11, s[12:15], s59 offen offset:3072 lds\n"
        "s_add_u32 m0, s58, 81920\n"
        "s_add_u32 s76, s64, 49152\n"
        "buffer_load_dwordx4 v12, s[16:19], s76 offen offset:0 sc1 lds\n"
        "buffer_load_dwordx4 v12, s[16:19], s76 offen offset:1024 sc1 lds\n"
        "buffer_load_dwordx4 v12, s[16:19], s76 offen offset:2048 sc1 lds\n"
        "buffer_load_dwordx4 v12, s[16:19], s76 offen offset:3072 sc1 lds\n"
        "s_mov_b64 exec, 0\n"
        "s_add_u32 s94, s86, .Ln_tp0-.Ln_here\n"
        "s_addc_u32 s95, s87, 0\n"
        "s_swappc_b64 s[70:71], s[94:95]\n"
        "s_mov_b64 exec, -1\n"
        "s_waitcnt vmcnt(10)\n"
        "s_barrier\n"
        "s_add_u32 m0, s58, 16384\n"
        "s_movk_i32 s59, 384\n"
        "buffer_load_dwordx4 v8, s[12:15], s59 offen offset:0 lds\n"
        "buffer_load_dwordx4 v9, s[12:15], s59 offen offset:1024 lds\n"
        "buffer_load_dwordx4 v10, s[12:15], s59 offen offset:2048 lds\n"
        "buffer_load_dwordx4 v11, s[12:15], s59 offen offset:3072 lds\n"
        "s_add_u32 m0, s58, 49152\n"
        "s_add_u32 s76, s64, 65536\n"
        "buffer_load_dwordx4 v12, s[16:19], s76 offen offset:0 sc1 lds\n"
        "buffer_load_dwordx4 v12, s[16:19], s76 offen offset:1024 sc1 lds\n"
        "buffer_load_dwordx4 v12, s[16:19], s76 offen offset:2048 sc1 lds\n"
        "buffer_load_dwordx4 v12, s[16:19], s76 offen offset:3072 sc1 lds\n"
        "s_waitcnt vmcnt(8)\n"
        "s_barrier\n"
        "s_add_u32 m0, s58, 65536\n"
        "s_add_u32 s76, s64, 81920\n"
        "buffer_load_dwordx4 v12, s[16:19], s76 offen offset:0 sc1 lds\n"
        "buffer_load_dwordx4 v12, s[16:19], s76 offen offset:1024 sc1 lds\n"
        "buffer_load_dwordx4 v12, s[16:19], s76 offen offset:2048 sc1 lds\n"
        "buffer_load_dwordx4 v12, s[16:19], s76 offen offset:3072 sc1 lds\n"
        "s_add_u32 m0, s58, 32768\n"
        "s_movk_i32 s59, 512\n"
        "buffer_load_dwordx4 v8, s[12:15], s59 offen offset:0 lds\n"
        "buffer_load_dwordx4 v9, s[12:15], s59 offen offset:1024 lds\n"
        "buffer_load_dwordx4 v10, s[12:15], s59 offen offset:2048 lds\n"
        "buffer_load_dwordx4 v11, s[12:15], s59 offen offset:3072 lds\n"
        "s_waitcnt vmcnt(8)\n"
        "s_barrier\n"
        "s_add_u32 m0, s58, 81920\n"
        "s_add_u32 s76, s64, 98304\n"
        "buffer_load_dwordx4 v12, s[16:19], s76 offen offset:0 sc1 lds\n"
        "buffer_load_dwordx4 v12, s[16:19], s76 offen offset:1024 sc1 lds\n"
        "buffer_load_dwordx4 v12, s[16:19], s76 offen offset:2048 sc1 lds\n"
        "buffer_load_dwordx4 v12, s[16:19], s76 offen offset:3072 sc1 lds\n"
        "s_add_u32 m0, s58, 0\n"
        "s_movk_i32 s59, 640\n"
        "buffer_load_dwordx4 v8, s[12:15], s59 offen offset:0 lds\n"
        "buffer_load_dwordx4 v9, s[12:15], s59 offen offset:1024 lds\n"
        "buffer_load_dwordx4 v10, s[12:15], s59 offen offset:2048 lds\n"
        "buffer_load_dwordx4 v11, s[12:15], s59 offen offset:3072 lds\n"
        "buffer_load_dwordx4 v[40:43], v249, s[8:11], s40 offen\n"
        "buffer_load_dwordx4 v[44:47], v249, s[8:11], s41 offen\n"
        "buffer_load_dwordx4 v[48:51], v249, s[8:11], s42 offen\n"
        "buffer_load_dwordx4 v[52:55], v249, s[8:11], s43 offen\n"
        "buffer_load_dwordx4 v[56:59], v249, s[8:11], s44 offen\n"
        "buffer_load_dwordx4 v[60:63], v249, s[8:11], s45 offen\n"
        "buffer_load_dwordx4 v[64:67], v249, s[8:11], s46 offen\n"
        "buffer_load_dwordx4 v[68:71], v249, s[8:11], s47 offen\n"
        "s_waitcnt vmcnt(16)\n"
        "s_barrier\n"
        "s_add_u32 m0, s58, 49152\n"
        "s_add_u32 s76, s64, 114688\n"
        "buffer_load_dwordx4 v12, s[16:19], s76 offen offset:0 sc1 lds\n"
        "buffer_load_dwordx4 v12, s[16:19], s76 offen offset:1024 sc1 lds\n"
        "buffer_load_dwordx4 v12, s[16:19], s76 offen offset:2048 sc1 lds\n"
        "buffer_load_dwordx4 v12, s[16:19], s76 offen offset:3072 sc1 lds\n"
        "s_add_u32 m0, s58, 16384\n"
        "s_movk_i32 s59, 768\n"
        "buffer_load_dwordx4 v8, s[12:15], s59 offen offset:0 lds\n"
        "buffer_load_dwordx4 v9, s[12:15], s59 offen offset:1024 lds\n"
        "buffer_load_dwordx4 v10, s[12:15], s59 offen offset:2048 lds\n"
        "buffer_load_dwordx4 v11, s[12:15], s59 offen offset:3072 lds\n"
        "s_waitcnt vmcnt(16)\n"
        "s_barrier\n"
        "s_add_u32 m0, s58, 65536\n"
        "s_add_u32 s76, s64, 131072\n"
        "buffer_load_dwordx4 v12, s[16:19], s76 offen offset:0 sc1 lds\n"
        "buffer_load_dwordx4 v12, s[16:19], s76 offen offset:1024 sc1 lds\n"
        "buffer_load_dwordx4 v12, s[16:19], s76 offen offset:2048 sc1 lds\n"
        "buffer_load_dwordx4 v12, s[16:19], s76 offen offset:3072 sc1 lds\n"
        "s_add_u32 m0, s58, 32768\n"
        "s_movk_i32 s59, 896\n"
        "buffer_load_dwordx4 v8, s[12:15], s59 offen offset:0 lds\n"
        "buffer_load_dwordx4 v9, s[12:15], s59 offen offset:1024 lds\n"
        "buffer_load_dwordx4 v10, s[12:15], s59 offen offset:2048 lds\n"
        "buffer_load_dwordx4 v11, s[12:15], s59 offen offset:3072 lds\n"
        "s_waitcnt vmcnt(16)\n"
        "v_lshl_add_u64 v[68:69], v[68:69], 4, v[64:65]\n"
        "v_lshl_add_u64 v[70:71], v[70:71], 4, v[66:67]\n"
        "v_lshl_add_u64 v[68:69], v[68:69], 4, v[60:61]\n"
        "v_lshl_add_u64 v[70:71], v[70:71], 4, v[62:63]\n"
        "v_lshl_add_u64 v[68:69], v[68:69], 4, v[56:57]\n"
        "v_lshl_add_u64 v[70:71], v[70:71], 4, v[58:59]\n"
        "v_lshl_add_u64 v[68:69], v[68:69], 4, v[52:53]\n"
        "v_lshl_add_u64 v[70:71], v[70:71], 4, v[54:55]\n"
        "v_lshl_add_u64 v[68:69], v[68:69], 4, v[48:49]\n"
        "v_lshl_add_u64 v[70:71], v[70:71], 4, v[50:51]\n"
        "v_lshl_add_u64 v[68:69], v[68:69], 4, v[44:45]\n"
        "v_lshl_add_u64 v[70:71], v[70:71], 4, v[46:47]\n"
        "v_lshl_add_u64 v[72:73], v[68:69], 4, v[40:41]\n"
        "v_lshl_add_u64 v[76:77], v[70:71], 4, v[42:43]\n"
        "s_nop 0\n"
        "v_permlane32_swap_b32 v72, v73\n"
        "v_permlane32_swap_b32 v76, v77\n"
        "buffer_store_dwordx4 v[72:75], v250, s[16:19], s75 offen sc1\n"
        "buffer_store_dwordx4 v[76:79], v251, s[16:19], s75 offen offset:1024 sc1\n"
        "s_waitcnt vmcnt(10)\n"
        "s_barrier\n"
        "s_add_u32 m0, s58, 81920\n"
        "s_add_u32 s76, s64, 147456\n"
        "buffer_load_dwordx4 v12, s[16:19], s76 offen offset:0 sc1 lds\n"
        "buffer_load_dwordx4 v12, s[16:19], s76 offen offset:1024 sc1 lds\n"
        "buffer_load_dwordx4 v12, s[16:19], s76 offen offset:2048 sc1 lds\n"
        "buffer_load_dwordx4 v12, s[16:19], s76 offen offset:3072 sc1 lds\n"
        "s_add_u32 m0, s58, 0\n"
        "s_movk_i32 s59, 1024\n"
        "buffer_load_dwordx4 v8, s[12:15], s59 offen offset:0 lds\n"
        "buffer_load_dwordx4 v9, s[12:15], s59 offen offset:1024 lds\n"
        "buffer_load_dwordx4 v10, s[12:15], s59 offen offset:2048 lds\n"
        "buffer_load_dwordx4 v11, s[12:15], s59 offen offset:3072 lds\n"
        "s_waitcnt vmcnt(10)\n"
        "s_barrier\n"
        "s_add_u32 m0, s58, 49152\n"
        "s_add_u32 s76, s64, 163840\n"
        "buffer_load_dwordx4 v12, s[16:19], s76 offen offset:0 sc1 lds\n"
        "buffer_load_dwordx4 v12, s[16:19], s76 offen offset:1024 sc1 lds\n"
        "buffer_load_dwordx4 v12, s[16:19], s76 offen offset:2048 sc1 lds\n"
        "buffer_load_dwordx4 v12, s[16:19], s76 offen offset:3072 sc1 lds\n"
        "s_add_u32 m0, s58, 16384\n"
        "s_movk_i32 s59, 1152\n"
        "buffer_load_dwordx4 v8, s[12:15], s59 offen offset:0 lds\n"
        "buffer_load_dwordx4 v9, s[12:15], s59 offen offset:1024 lds\n"
        "buffer_load_dwordx4 v10, s[12:15], s59 offen offset:2048 lds\n"
        "buffer_load_dwordx4 v11, s[12:15], s59 offen offset:3072 lds\n"
        "s_waitcnt vmcnt(8)\n"
        "s_barrier\n"
        "s_add_u32 m0, s58, 65536\n"
        "s_add_u32 s76, s64, 180224\n"
        "buffer_load_dwordx4 v12, s[16:19], s76 offen offset:0 sc1 lds\n"
        "buffer_load_dwordx4 v12, s[16:19], s76 offen offset:1024 sc1 lds\n"
        "buffer_load_dwordx4 v12, s[16:19], s76 offen offset:2048 sc1 lds\n"
        "buffer_load_dwordx4 v12, s[16:19], s76 offen offset:3072 sc1 lds\n"
        "s_add_u32 m0, s58, 32768\n"
        "s_movk_i32 s59, 1280\n"
        "buffer_load_dwordx4 v8, s[12:15], s59 offen offset:0 lds\n"
        "buffer_load_dwordx4 v9, s[12:15], s59 offen offset:1024 lds\n"
        "buffer_load_dwordx4 v10, s[12:15], s59 offen offset:2048 lds\n"
        "buffer_load_dwordx4 v11, s[12:15], s59 offen offset:3072 lds\n"
        "s_waitcnt vmcnt(8)\n"
        "s_barrier\n"
        "s_add_u32 m0, s58, 81920\n"
        "s_add_u32 s76, s64, 196608\n"
        "buffer_load_dwordx4 v12, s[16:19], s76 offen offset:0 sc1 lds\n"
        "buffer_load_dwordx4 v12, s[16:19], s76 offen offset:1024 sc1 lds\n"
        "buffer_load_dwordx4 v12, s[16:19], s76 offen offset:2048 sc1 lds\n"
        "buffer_load_dwordx4 v12, s[16:19], s76 offen offset:3072 sc1 lds\n"
        "s_add_u32 m0, s58, 0\n"
        "s_movk_i32 s59, 1408\n"
        "buffer_load_dwordx4 v8, s[12:15], s59 offen offset:0 lds\n"
        "buffer_load_dwordx4 v9, s[12:15], s59 offen offset:1024 lds\n"
        "buffer_load_dwordx4 v10, s[12:15], s59 offen offset:2048 lds\n"
        "buffer_load_dwordx4 v11, s[12:15], s59 offen offset:3072 lds\n"
        "s_waitcnt vmcnt(8)\n"
        "s_barrier\n"
        "s_add_u32 m0, s58, 49152\n"
        "s_add_u32 s76, s64, 212992\n"
        "buffer_load_dwordx4 v12, s[16:19], s76 offen offset:0 sc1 lds\n"
        "buffer_load_dwordx4 v12, s[16:19], s76 offen offset:1024 sc1 lds\n"
        "buffer_load_dwordx4 v12, s[16:19], s76 offen offset:2048 sc1 lds\n"
        "buffer_load_dwordx4 v12, s[16:19], s76 offen offset:3072 sc1 lds\n"
        "s_add_u32 m0, s58, 16384\n"
        "s_movk_i32 s59, 1536\n"
        "buffer_load_dwordx4 v8, s[12:15], s59 offen offset:0 lds\n"
        "buffer_load_dwordx4 v9, s[12:15], s59 offen offset:1024 lds\n"
        "buffer_load_dwordx4 v10, s[12:15], s59 offen offset:2048 lds\n"
        "buffer_load_dwordx4 v11, s[12:15], s59 offen offset:3072 lds\n"
        "s_waitcnt vmcnt(8)\n"
        "s_barrier\n"
        "s_add_u32 m0, s58, 65536\n"
        "s_add_u32 s76, s64, 229376\n"
        "buffer_load_dwordx4 v12, s[16:19], s76 offen offset:0 sc1 lds\n"
        "buffer_load_dwordx4 v12, s[16:19], s76 offen offset:1024 sc1 lds\n"
        "buffer_load_dwordx4 v12, s[16:19], s76 offen offset:2048 sc1 lds\n"
        "buffer_load_dwordx4 v12, s[16:19], s76 offen offset:3072 sc1 lds\n"
        "s_add_u32 m0, s58, 32768\n"
        "s_movk_i32 s59, 1664\n"
        "buffer_load_dwordx4 v8, s[12:15], s59 offen offset:0 lds\n"
        "buffer_load_dwordx4 v9, s[12:15], s59 offen offset:1024 lds\n"
        "buffer_load_dwordx4 v10, s[12:15], s59 offen offset:2048 lds\n"
        "buffer_load_dwordx4 v11, s[12:15], s59 offen offset:3072 lds\n"
        "s_waitcnt vmcnt(8)\n"
        "s_barrier\n"
        "s_add_u32 m0, s58, 81920\n"
        "s_add_u32 s76, s64, 245760\n"
        "buffer_load_dwordx4 v12, s[16:19], s76 offen offset:0 sc1 lds\n"
        "buffer_load_dwordx4 v12, s[16:19], s76 offen offset:1024 sc1 lds\n"
        "buffer_load_dwordx4 v12, s[16:19], s76 offen offset:2048 sc1 lds\n"
        "buffer_load_dwordx4 v12, s[16:19], s76 offen offset:3072 sc1 lds\n"
        "s_add_u32 m0, s58, 0\n"
        "s_movk_i32 s59, 1792\n"
        "buffer_load_dwordx4 v8, s[12:15], s59 offen offset:0 lds\n"
        "buffer_load_dwordx4 v9, s[12:15], s59 offen offset:1024 lds\n"
        "buffer_load_dwordx4 v10, s[12:15], s59 offen offset:2048 lds\n"
        "buffer_load_dwordx4 v11, s[12:15], s59 offen offset:3072 lds\n"
        "s_waitcnt vmcnt(8)\n"
        "s_barrier\n"
        "s_add_u32 m0, s58, 16384\n"
        "s_movk_i32 s59, 1920\n"
        "buffer_load_dwordx4 v8, s[12:15], s59 offen offset:0 lds\n"
        "buffer_load_dwordx4 v9, s[12:15], s59 offen offset:1024 lds\n"
        "buffer_load_dwordx4 v10, s[12:15], s59 offen offset:2048 lds\n"
        "buffer_load_dwordx4 v11, s[12:15], s59 offen offset:3072 lds\n"
        "s_waitcnt vmcnt(0)\n"
        "s_barrier\n"
        "s_branch .Ln_ht\n"
        ".Ln_h1:\n"
        "s_add_u32 s72, s32, 48\n"
        "s_mul_i32 s35, s72, 9363\n"
        "s_lshr_b32 s35, s35, 16\n"
        "s_mul_i32 s36, s35, 7\n"
        "s_sub_u32 s36, s72, s36\n"
        "s_mul_i32 s40, s35, 917504\n"
        "s_mul_i32 s37, s33, 896\n"
        "s_lshl_b32 s38, s36, 7\n"
        "s_add_u32 s40, s40, s37\n"
        "s_add_u32 s40, s40, s38\n"
        "s_add_u32 s41, s40, 14336\n"
        "s_add_u32 s42, s41, 14336\n"
        "s_add_u32 s43, s42, 14336\n"
        "s_add_u32 s44, s43, 14336\n"
        "s_add_u32 s45, s44, 14336\n"
        "s_add_u32 s46, s45, 14336\n"
        "s_add_u32 s47, s46, 14336\n"
        "s_lshl_b32 s75, s33, 4\n"
        "s_add_u32 s75, s75, s35\n"
        "s_lshl_b32 s75, s75, 3\n"
        "s_add_u32 s75, s75, s36\n"
        "s_lshl_b32 s75, s75, 11\n"
        "s_add_u32 s75, s75, 114688\n"
        "s_waitcnt vmcnt(0)\n"
        "s_waitcnt lgkmcnt(0)\n"
        "s_barrier\n"
        "s_sleep 16\n"
        "s_add_u32 m0, s58, 32768\n"
        "s_movk_i32 s59, 128\n"
        "buffer_load_dwordx4 v8, s[12:15], s59 offen offset:0 lds\n"
        "buffer_load_dwordx4 v9, s[12:15], s59 offen offset:1024 lds\n"
        "buffer_load_dwordx4 v10, s[12:15], s59 offen offset:2048 lds\n"
        "buffer_load_dwordx4 v11, s[12:15], s59 offen offset:3072 lds\n"
        "s_add_u32 m0, s58, 65536\n"
        "s_add_u32 s76, s64, 32768\n"
        "buffer_load_dwordx4 v12, s[16:19], s76 offen offset:0 sc1 lds\n"
        "buffer_load_dwordx4 v12, s[16:19], s76 offen offset:1024 sc1 lds\n"
        "buffer_load_dwordx4 v12, s[16:19], s76 offen offset:2048 sc1 lds\n"
        "buffer_load_dwordx4 v12, s[16:19], s76 offen offset:3072 sc1 lds\n"
        "buffer_load_dwordx4 v[40:43], v249, s[8:11], s40 offen\n"
        "buffer_load_dwordx4 v[44:47], v249, s[8:11], s41 offen\n"
        "buffer_load_dwordx4 v[48:51], v249, s[8:11], s42 offen\n"
        "buffer_load_dwordx4 v[52:55], v249, s[8:11], s43 offen\n"
        "buffer_load_dwordx4 v[56:59], v249, s[8:11], s44 offen\n"
        "buffer_load_dwordx4 v[60:63], v249, s[8:11], s45 offen\n"
        "buffer_load_dwordx4 v[64:67], v249, s[8:11], s46 offen\n"
        "buffer_load_dwordx4 v[68:71], v249, s[8:11], s47 offen\n"
        "v_mov_b32 v84, 128\n"
        "s_movk_i32 s73, 64\n"
        "1:\n"
        "s_sub_u32 s73, s73, 1\n"
        "s_cbranch_scc1 2f\n"
        "s_sleep 1\n"
        "ds_read_b128 v[96:99], v84 offset:4096\n"
        "s_waitcnt lgkmcnt(0)\n"
        "v_min3_u32 v96, v96, v97, v98\n"
        "v_min_u32 v96, v96, v99\n"
        "v_cmp_eq_u32 vcc, 0, v96\n"
        "s_cbranch_vccnz 1b\n"
        "2:\n"
        "s_add_u32 m0, s58, 0\n"
        "s_movk_i32 s59, 256\n"
        "buffer_load_dwordx4 v8, s[12:15], s59 offen offset:0 lds\n"
        "buffer_load_dwordx4 v9, s[12:15], s59 offen offset:1024 lds\n"
        "buffer_load_dwordx4 v10, s[12:15], s59 offen offset:2048 lds\n"
        "buffer_load_dwordx4 v11, s[12:15], s59 offen offset:3072 lds\n"
        "s_add_u32 m0, s58, 81920\n"
        "s_add_u32 s76, s64, 49152\n"
        "buffer_load_dwordx4 v12, s[16:19], s76 offen offset:0 sc1 lds\n"
        "buffer_load_dwordx4 v12, s[16:19], s76 offen offset:1024 sc1 lds\n"
        "buffer_load_dwordx4 v12, s[16:19], s76 offen offset:2048 sc1 lds\n"
        "buffer_load_dwordx4 v12, s[16:19], s76 offen offset:3072 sc1 lds\n"
        "s_mov_b64 exec, 0\n"
        "s_add_u32 s94, s86, .Ln_tp1-.Ln_here\n"
        "s_addc_u32 s95, s87, 0\n"
        "s_swappc_b64 s[70:71], s[94:95]\n"
        "s_mov_b64 exec, -1\n"
        "s_waitcnt vmcnt(16)\n"
        "s_barrier\n"
        "s_add_u32 m0, s58, 16384\n"
        "s_movk_i32 s59, 384\n"
        "buffer_load_dwordx4 v8, s[12:15], s59 offen offset:0 lds\n"
        "buffer_load_dwordx4 v9, s[12:15], s59 offen offset:1024 lds\n"
        "buffer_load_dwordx4 v10, s[12:15], s59 offen offset:2048 lds\n"
        "buffer_load_dwordx4 v11, s[12:15], s59 offen offset:3072 lds\n"
        "s_add_u32 m0, s58, 49152\n"
        "s_add_u32 s76, s64, 65536\n"
        "buffer_load_dwordx4 v12, s[16:19], s76 offen offset:0 sc1 lds\n"
        "buffer_load_dwordx4 v12, s[16:19], s76 offen offset:1024 sc1 lds\n"
        "buffer_load_dwordx4 v12, s[16:19], s76 offen offset:2048 sc1 lds\n"
        "buffer_load_dwordx4 v12, s[16:19], s76 offen offset:3072 sc1 lds\n"
        "s_waitcnt vmcnt(16)\n"
        "v_lshl_add_u64 v[68:69], v[68:69], 4, v[64:65]\n"
        "v_lshl_add_u64 v[70:71], v[70:71], 4, v[66:67]\n"
        "v_lshl_add_u64 v[68:69], v[68:69], 4, v[60:61]\n"
        "v_lshl_add_u64 v[70:71], v[70:71], 4, v[62:63]\n"
        "v_lshl_add_u64 v[68:69], v[68:69], 4, v[56:57]\n"
        "v_lshl_add_u64 v[70:71], v[70:71], 4, v[58:59]\n"
        "v_lshl_add_u64 v[68:69], v[68:69], 4, v[52:53]\n"
        "v_lshl_add_u64 v[70:71], v[70:71], 4, v[54:55]\n"
        "v_lshl_add_u64 v[68:69], v[68:69], 4, v[48:49]\n"
        "v_lshl_add_u64 v[70:71], v[70:71], 4, v[50:51]\n"
        "v_lshl_add_u64 v[68:69], v[68:69], 4, v[44:45]\n"
        "v_lshl_add_u64 v[70:71], v[70:71], 4, v[46:47]\n"
        "v_lshl_add_u64 v[72:73], v[68:69], 4, v[40:41]\n"
        "v_lshl_add_u64 v[76:77], v[70:71], 4, v[42:43]\n"
        "s_nop 0\n"
        "v_permlane32_swap_b32 v72, v73\n"
        "v_permlane32_swap_b32 v76, v77\n"
        "buffer_store_dwordx4 v[72:75], v250, s[16:19], s75 offen sc1\n"
        "buffer_store_dwordx4 v[76:79], v251, s[16:19], s75 offen offset:1024 sc1\n"
        "s_waitcnt vmcnt(10)\n"
        "s_barrier\n"
        "s_add_u32 m0, s58, 65536\n"
        "s_add_u32 s76, s64, 81920\n"
        "buffer_load_dwordx4 v12, s[16:19], s76 offen offset:0 sc1 lds\n"
        "buffer_load_dwordx4 v12, s[16:19], s76 offen offset:1024 sc1 lds\n"
        "buffer_load_dwordx4 v12, s[16:19], s76 offen offset:2048 sc1 lds\n"
        "buffer_load_dwordx4 v12, s[16:19], s76 offen offset:3072 sc1 lds\n"
        "s_add_u32 m0, s58, 32768\n"
        "s_movk_i32 s59, 512\n"
        "buffer_load_dwordx4 v8, s[12:15], s59 offen offset:0 lds\n"
        "buffer_load_dwordx4 v9, s[12:15], s59 offen offset:1024 lds\n"
        "buffer_load_dwordx4 v10, s[12:15], s59 offen offset:2048 lds\n"
        "buffer_load_dwordx4 v11, s[12:15], s59 offen offset:3072 lds\n"
        "s_waitcnt vmcnt(10)\n"
        "s_barrier\n"
        "s_add_u32 m0, s58, 81920\n"
        "s_add_u32 s76, s64, 98304\n"
        "buffer_load_dwordx4 v12, s[16:19], s76 offen offset:0 sc1 lds\n"
        "buffer_load_dwordx4 v12, s[16:19], s76 offen offset:1024 sc1 lds\n"
        "buffer_load_dwordx4 v12, s[16:19], s76 offen offset:2048 sc1 lds\n"
        "buffer_load_dwordx4 v12, s[16:19], s76 offen offset:3072 sc1 lds\n"
        "s_add_u32 m0, s58, 0\n"
        "s_movk_i32 s59, 640\n"
        "buffer_load_dwordx4 v8, s[12:15], s59 offen offset:0 lds\n"
        "buffer_load_dwordx4 v9, s[12:15], s59 offen offset:1024 lds\n"
        "buffer_load_dwordx4 v10, s[12:15], s59 offen offset:2048 lds\n"
        "buffer_load_dwordx4 v11, s[12:15], s59 offen offset:3072 lds\n"
        "s_waitcnt vmcnt(8)\n"
        "s_barrier\n"
        "s_add_u32 m0, s58, 49152\n"
        "s_add_u32 s76, s64, 114688\n"
        "buffer_load_dwordx4 v12, s[16:19], s76 offen offset:0 sc1 lds\n"
        "buffer_load_dwordx4 v12, s[16:19], s76 offen offset:1024 sc1 lds\n"
        "buffer_load_dwordx4 v12, s[16:19], s76 offen offset:2048 sc1 lds\n"
        "buffer_load_dwordx4 v12, s[16:19], s76 offen offset:3072 sc1 lds\n"
        "s_add_u32 m0, s58, 16384\n"
        "s_movk_i32 s59, 768\n"
        "buffer_load_dwordx4 v8, s[12:15], s59 offen offset:0 lds\n"
        "buffer_load_dwordx4 v9, s[12:15], s59 offen offset:1024 lds\n"
        "buffer_load_dwordx4 v10, s[12:15], s59 offen offset:2048 lds\n"
        "buffer_load_dwordx4 v11, s[12:15], s59 offen offset:3072 lds\n"
        "s_waitcnt vmcnt(8)\n"
        "s_barrier\n"
        "s_add_u32 m0, s58, 65536\n"
        "s_add_u32 s76, s64, 131072\n"
        "buffer_load_dwordx4 v12, s[16:19], s76 offen offset:0 sc1 lds\n"
        "buffer_load_dwordx4 v12, s[16:19], s76 offen offset:1024 sc1 lds\n"
        "buffer_load_dwordx4 v12, s[16:19], s76 offen offset:2048 sc1 lds\n"
        "buffer_load_dwordx4 v12, s[16:19], s76 offen offset:3072 sc1 lds\n"
        "s_add_u32 m0, s58, 32768\n"
        "s_movk_i32 s59, 896\n"
        "buffer_load_dwordx4 v8, s[12:15], s59 offen offset:0 lds\n"
        "buffer_load_dwordx4 v9, s[12:15], s59 offen offset:1024 lds\n"
        "buffer_load_dwordx4 v10, s[12:15], s59 offen offset:2048 lds\n"
        "buffer_load_dwordx4 v11, s[12:15], s59 offen offset:3072 lds\n"
        "s_waitcnt vmcnt(8)\n"
        "s_barrier\n"
        "s_add_u32 m0, s58, 81920\n"
        "s_add_u32 s76, s64, 147456\n"
        "buffer_load_dwordx4 v12, s[16:19], s76 offen offset:0 sc1 lds\n"
        "buffer_load_dwordx4 v12, s[16:19], s76 offen offset:1024 sc1 lds\n"
        "buffer_load_dwordx4 v12, s[16:19], s76 offen offset:2048 sc1 lds\n"
        "buffer_load_dwordx4 v12, s[16:19], s76 offen offset:3072 sc1 lds\n"
        "s_add_u32 m0, s58, 0\n"
        "s_movk_i32 s59, 1024\n"
        "buffer_load_dwordx4 v8, s[12:15], s59 offen offset:0 lds\n"
        "buffer_load_dwordx4 v9, s[12:15], s59 offen offset:1024 lds\n"
        "buffer_load_dwordx4 v10, s[12:15], s59 offen offset:2048 lds\n"
        "buffer_load_dwordx4 v11, s[12:15], s59 offen offset:3072 lds\n"
        "s_waitcnt vmcnt(8)\n"
        "s_barrier\n"
        "s_add_u32 m0, s58, 49152\n"
        "s_add_u32 s76, s64, 163840\n"
        "buffer_load_dwordx4 v12, s[16:19], s76 offen offset:0 sc1 lds\n"
        "buffer_load_dwordx4 v12, s[16:19], s76 offen offset:1024 sc1 lds\n"
        "buffer_load_dwordx4 v12, s[16:19], s76 offen offset:2048 sc1 lds\n"
        "buffer_load_dwordx4 v12, s[16:19], s76 offen offset:3072 sc1 lds\n"
        "s_add_u32 m0, s58, 16384\n"
        "s_movk_i32 s59, 1152\n"
        "buffer_load_dwordx4 v8, s[12:15], s59 offen offset:0 lds\n"
        "buffer_load_dwordx4 v9, s[12:15], s59 offen offset:1024 lds\n"
        "buffer_load_dwordx4 v10, s[12:15], s59 offen offset:2048 lds\n"
        "buffer_load_dwordx4 v11, s[12:15], s59 offen offset:3072 lds\n"
        "s_waitcnt vmcnt(8)\n"
        "s_barrier\n"
        "s_add_u32 m0, s58, 65536\n"
        "s_add_u32 s76, s64, 180224\n"
        "buffer_load_dwordx4 v12, s[16:19], s76 offen offset:0 sc1 lds\n"
        "buffer_load_dwordx4 v12, s[16:19], s76 offen offset:1024 sc1 lds\n"
        "buffer_load_dwordx4 v12, s[16:19], s76 offen offset:2048 sc1 lds\n"
        "buffer_load_dwordx4 v12, s[16:19], s76 offen offset:3072 sc1 lds\n"
        "s_add_u32 m0, s58, 32768\n"
        "s_movk_i32 s59, 1280\n"
        "buffer_load_dwordx4 v8, s[12:15], s59 offen offset:0 lds\n"
        "buffer_load_dwordx4 v9, s[12:15], s59 offen offset:1024 lds\n"
        "buffer_load_dwordx4 v10, s[12:15], s59 offen offset:2048 lds\n"
        "buffer_load_dwordx4 v11, s[12:15], s59 offen offset:3072 lds\n"
        "s_waitcnt vmcnt(8)\n"
        "s_barrier\n"
        "s_add_u32 m0, s58, 81920\n"
        "s_add_u32 s76, s64, 196608\n"
        "buffer_load_dwordx4 v12, s[16:19], s76 offen offset:0 sc1 lds\n"
        "buffer_load_dwordx4 v12, s[16:19], s76 offen offset:1024 sc1 lds\n"
        "buffer_load_dwordx4 v12, s[16:19], s76 offen offset:2048 sc1 lds\n"
        "buffer_load_dwordx4 v12, s[16:19], s76 offen offset:3072 sc1 lds\n"
        "s_add_u32 m0, s58, 0\n"
        "s_movk_i32 s59, 1408\n"
        "buffer_load_dwordx4 v8, s[12:15], s59 offen offset:0 lds\n"
        "buffer_load_dwordx4 v9, s[12:15], s59 offen offset:1024 lds\n"
        "buffer_load_dwordx4 v10, s[12:15], s59 offen offset:2048 lds\n"
        "buffer_load_dwordx4 v11, s[12:15], s59 offen offset:3072 lds\n"
        "s_waitcnt vmcnt(8)\n"
        "s_barrier\n"
        "s_add_u32 m0, s58, 49152\n"
        "s_add_u32 s76, s64, 212992\n"
        "buffer_load_dwordx4 v12, s[16:19], s76 offen offset:0 sc1 lds\n"
        "buffer_load_dwordx4 v12, s[16:19], s76 offen offset:1024 sc1 lds\n"
        "buffer_load_dwordx4 v12, s[16:19], s76 offen offset:2048 sc1 lds\n"
        "buffer_load_dwordx4 v12, s[16:19], s76 offen offset:3072 sc1 lds\n"
        "s_add_u32 m0, s58, 16384\n"
        "s_movk_i32 s59, 1536\n"
        "buffer_load_dwordx4 v8, s[12:15], s59 offen offset:0 lds\n"
        "buffer_load_dwordx4 v9, s[12:15], s59 offen offset:1024 lds\n"
        "buffer_load_dwordx4 v10, s[12:15], s59 offen offset:2048 lds\n"
        "buffer_load_dwordx4 v11, s[12:15], s59 offen offset:3072 lds\n"
        "s_waitcnt vmcnt(8)\n"
        "s_barrier\n"
        "s_add_u32 m0, s58, 65536\n"
        "s_add_u32 s76, s64, 229376\n"
        "buffer_load_dwordx4 v12, s[16:19], s76 offen offset:0 sc1 lds\n"
        "buffer_load_dwordx4 v12, s[16:19], s76 offen offset:1024 sc1 lds\n"
        "buffer_load_dwordx4 v12, s[16:19], s76 offen offset:2048 sc1 lds\n"
        "buffer_load_dwordx4 v12, s[16:19], s76 offen offset:3072 sc1 lds\n"
        "s_add_u32 m0, s58, 32768\n"
        "s_movk_i32 s59, 1664\n"
        "buffer_load_dwordx4 v8, s[12:15], s59 offen offset:0 lds\n"
        "buffer_load_dwordx4 v9, s[12:15], s59 offen offset:1024 lds\n"
        "buffer_load_dwordx4 v10, s[12:15], s59 offen offset:2048 lds\n"
        "buffer_load_dwordx4 v11, s[12:15], s59 offen offset:3072 lds\n"
        "s_waitcnt vmcnt(8)\n"
        "s_barrier\n"
        "s_add_u32 m0, s58, 81920\n"
        "s_add_u32 s76, s64, 245760\n"
        "buffer_load_dwordx4 v12, s[16:19], s76 offen offset:0 sc1 lds\n"
        "buffer_load_dwordx4 v12, s[16:19], s76 offen offset:1024 sc1 lds\n"
        "buffer_load_dwordx4 v12, s[16:19], s76 offen offset:2048 sc1 lds\n"
        "buffer_load_dwordx4 v12, s[16:19], s76 offen offset:3072 sc1 lds\n"
        "s_add_u32 m0, s58, 0\n"
        "s_movk_i32 s59, 1792\n"
        "buffer_load_dwordx4 v8, s[12:15], s59 offen offset:0 lds\n"
        "buffer_load_dwordx4 v9, s[12:15], s59 offen offset:1024 lds\n"
        "buffer_load_dwordx4 v10, s[12:15], s59 offen offset:2048 lds\n"
        "buffer_load_dwordx4 v11, s[12:15], s59 offen offset:3072 lds\n"
        "s_waitcnt vmcnt(8)\n"
        "s_barrier\n"
        "s_add_u32 m0, s58, 16384\n"
        "s_movk_i32 s59, 1920\n"
        "buffer_load_dwordx4 v8, s[12:15], s59 offen offset:0 lds\n"
        "buffer_load_dwordx4 v9, s[12:15], s59 offen offset:1024 lds\n"
        "buffer_load_dwordx4 v10, s[12:15], s59 offen offset:2048 lds\n"
        "buffer_load_dwordx4 v11, s[12:15], s59 offen offset:3072 lds\n"
        "s_waitcnt vmcnt(0)\n"
        "s_barrier\n"
        "s_branch .Ln_ht\n"
        ".Ln_h2:\n"
        "s_add_u32 s72, s32, 64\n"
        "s_mul_i32 s35, s72, 9363\n"
        "s_lshr_b32 s35, s35, 16\n"
        "s_mul_i32 s36, s35, 7\n"
        "s_sub_u32 s36, s72, s36\n"
        "s_mul_i32 s40, s35, 917504\n"
        "s_mul_i32 s37, s33, 896\n"
        "s_lshl_b32 s38, s36, 7\n"
        "s_add_u32 s40, s40, s37\n"
        "s_add_u32 s40, s40, s38\n"
        "s_add_u32 s41, s40, 14336\n"
        "s_add_u32 s42, s41, 14336\n"
        "s_add_u32 s43, s42, 14336\n"
        "s_add_u32 s44, s43, 14336\n"
        "s_add_u32 s45, s44, 14336\n"
        "s_add_u32 s46, s45, 14336\n"
        "s_add_u32 s47, s46, 14336\n"
        "s_lshl_b32 s75, s33, 4\n"
        "s_add_u32 s75, s75, s35\n"
        "s_lshl_b32 s75, s75, 3\n"
        "s_add_u32 s75, s75, s36\n"
        "s_lshl_b32 s75, s75, 11\n"
        "s_add_u32 s75, s75, 114688\n"
        "s_waitcnt vmcnt(0)\n"
        "s_waitcnt lgkmcnt(0)\n"
        "s_barrier\n"
        "s_sleep 16\n"
        "s_add_u32 m0, s58, 32768\n"
        "s_movk_i32 s59, 128\n"
        "buffer_load_dwordx4 v8, s[12:15], s59 offen offset:0 lds\n"
        "buffer_load_dwordx4 v9, s[12:15], s59 offen offset:1024 lds\n"
        "buffer_load_dwordx4 v10, s[12:15], s59 offen offset:2048 lds\n"
        "buffer_load_dwordx4 v11, s[12:15], s59 offen offset:3072 lds\n"
        "s_add_u32 m0, s58, 65536\n"
        "s_add_u32 s76, s64, 32768\n"
        "buffer_load_dwordx4 v12, s[16:19], s76 offen offset:0 sc1 lds\n"
        "buffer_load_dwordx4 v12, s[16:19], s76 offen offset:1024 sc1 lds\n"
        "buffer_load_dwordx4 v12, s[16:19], s76 offen offset:2048 sc1 lds\n"
        "buffer_load_dwordx4 v12, s[16:19], s76 offen offset:3072 sc1 lds\n"
        "v_mov_b32 v84, 128\n"
        "s_movk_i32 s73, 64\n"
        "1:\n"
        "s_sub_u32 s73, s73, 1\n"
        "s_cbranch_scc1 2f\n"
        "s_sleep 1\n"
        "ds_read_b128 v[96:99], v84 offset:4096\n"
        "s_waitcnt lgkmcnt(0)\n"
        "v_min3_u32 v96, v96, v97, v98\n"
        "v_min_u32 v96, v96, v99\n"
        "v_cmp_eq_u32 vcc, 0, v96\n"
        "s_cbranch_vccnz 1b\n"
        "2:\n"
        "s_add_u32 m0, s58, 0\n"
        "s_movk_i32 s59, 256\n"
        "buffer_load_dwordx4 v8, s[12:15], s59 offen offset:0 lds\n"
        "buffer_load_dwordx4 v9, s[12:15], s59 offen offset:1024 lds\n"
        "buffer_load_dwordx4 v10, s[12:15], s59 offen offset:2048 lds\n"
        "buffer_load_dwordx4 v11, s[12:15], s59 offen offset:3072 lds\n"
        "s_add_u32 m0, s58, 81920\n"
        "s_add_u32 s76, s64, 49152\n"
        "buffer_load_dwordx4 v12, s[16:19], s76 offen offset:0 sc1 lds\n"
        "buffer_load_dwordx4 v12, s[16:19], s76 offen offset:1024 sc1 lds\n"
        "buffer_load_dwordx4 v12, s[16:19], s76 offen offset:2048 sc1 lds\n"
        "buffer_load_dwordx4 v12, s[16:19], s76 offen offset:3072 sc1 lds\n"
        "s_mov_b64 exec, 0\n"
        "s_add_u32 s94, s86, .Ln_tp2-.Ln_here\n"
        "s_addc_u32 s95, s87, 0\n"
        "s_swappc_b64 s[70:71], s[94:95]\n"
        "s_mov_b64 exec, -1\n"
        "s_waitcnt vmcnt(8)\n"
        "s_barrier\n"
        "s_add_u32 m0, s58, 16384\n"
        "s_movk_i32 s59, 384\n"
        "buffer_load_dwordx4 v8, s[12:15], s59 offen offset:0 lds\n"
        "buffer_load_dwordx4 v9, s[12:15], s59 offen offset:1024 lds\n"
        "buffer_load_dwordx4 v10, s[12:15], s59 offen offset:2048 lds\n"
        "buffer_load_dwordx4 v11, s[12:15], s59 offen offset:3072 lds\n"
        "s_add_u32 m0, s58, 49152\n"
        "s_add_u32 s76, s64, 65536\n"
        "buffer_load_dwordx4 v12, s[16:19], s76 offen offset:0 sc1 lds\n"
        "buffer_load_dwordx4 v12, s[16:19], s76 offen offset:1024 sc1 lds\n"
        "buffer_load_dwordx4 v12, s[16:19], s76 offen offset:2048 sc1 lds\n"
        "buffer_load_dwordx4 v12, s[16:19], s76 offen offset:3072 sc1 lds\n"
        "buffer_load_dwordx4 v[40:43], v249, s[8:11], s40 offen\n"
        "buffer_load_dwordx4 v[44:47], v249, s[8:11], s41 offen\n"
        "buffer_load_dwordx4 v[48:51], v249, s[8:11], s42 offen\n"
        "buffer_load_dwordx4 v[52:55], v249, s[8:11], s43 offen\n"
        "buffer_load_dwordx4 v[56:59], v249, s[8:11], s44 offen\n"
        "buffer_load_dwordx4 v[60:63], v249, s[8:11], s45 offen\n"
        "buffer_load_dwordx4 v[64:67], v249, s[8:11], s46 offen\n"
        "buffer_load_dwordx4 v[68:71], v249, s[8:11], s47 offen\n"
        "s_waitcnt vmcnt(16)\n"
        "s_barrier\n"
        "s_add_u32 m0, s58, 65536\n"
        "s_add_u32 s76, s64, 81920\n"
        "buffer_load_dwordx4 v12, s[16:19], s76 offen offset:0 sc1 lds\n"
        "buffer_load_dwordx4 v12, s[16:19], s76 offen offset:1024 sc1 lds\n"
        "buffer_load_dwordx4 v12, s[16:19], s76 offen offset:2048 sc1 lds\n"
        "buffer_load_dwordx4 v12, s[16:19], s76 offen offset:3072 sc1 lds\n"
        "s_add_u32 m0, s58, 32768\n"
        "s_movk_i32 s59, 512\n"
        "buffer_load_dwordx4 v8, s[12:15], s59 offen offset:0 lds\n"
        "buffer_load_dwordx4 v9, s[12:15], s59 offen offset:1024 lds\n"
        "buffer_load_dwordx4 v10, s[12:15], s59 offen offset:2048 lds\n"
        "buffer_load_dwordx4 v11, s[12:15], s59 offen offset:3072 lds\n"
        "s_waitcnt vmcnt(16)\n"
        "s_barrier\n"
        "s_add_u32 m0, s58, 81920\n"
        "s_add_u32 s76, s64, 98304\n"
        "buffer_load_dwordx4 v12, s[16:19], s76 offen offset:0 sc1 lds\n"
        "buffer_load_dwordx4 v12, s[16:19], s76 offen offset:1024 sc1 lds\n"
        "buffer_load_dwordx4 v12, s[16:19], s76 offen offset:2048 sc1 lds\n"
        "buffer_load_dwordx4 v12, s[16:19], s76 offen offset:3072 sc1 lds\n"
        "s_add_u32 m0, s58, 0\n"
        "s_movk_i32 s59, 640\n"
        "buffer_load_dwordx4 v8, s[12:15], s59 offen offset:0 lds\n"
        "buffer_load_dwordx4 v9, s[12:15], s59 offen offset:1024 lds\n"
        "buffer_load_dwordx4 v10, s[12:15], s59 offen offset:2048 lds\n"
        "buffer_load_dwordx4 v11, s[12:15], s59 offen offset:3072 lds\n"
        "s_waitcnt vmcnt(16)\n"
        "v_lshl_add_u64 v[68:69], v[68:69], 4, v[64:65]\n"
        "v_lshl_add_u64 v[70:71], v[70:71], 4, v[66:67]\n"
        "v_lshl_add_u64 v[68:69], v[68:69], 4, v[60:61]\n"
        "v_lshl_add_u64 v[70:71], v[70:71], 4, v[62:63]\n"
        "v_lshl_add_u64 v[68:69], v[68:69], 4, v[56:57]\n"
        "v_lshl_add_u64 v[70:71], v[70:71], 4, v[58:59]\n"
        "v_lshl_add_u64 v[68:69], v[68:69], 4, v[52:53]\n"
        "v_lshl_add_u64 v[70:71], v[70:71], 4, v[54:55]\n"
        "v_lshl_add_u64 v[68:69], v[68:69], 4, v[48:49]\n"
        "v_lshl_add_u64 v[70:71], v[70:71], 4, v[50:51]\n"
        "v_lshl_add_u64 v[68:69], v[68:69], 4, v[44:45]\n"
        "v_lshl_add_u64 v[70:71], v[70:71], 4, v[46:47]\n"
        "v_lshl_add_u64 v[72:73], v[68:69], 4, v[40:41]\n"
        "v_lshl_add_u64 v[76:77], v[70:71], 4, v[42:43]\n"
        "s_nop 0\n"
        "v_permlane32_swap_b32 v72, v73\n"
        "v_permlane32_swap_b32 v76, v77\n"
        "buffer_store_dwordx4 v[72:75], v250, s[16:19], s75 offen sc1\n"
        "buffer_store_dwordx4 v[76:79], v251, s[16:19], s75 offen offset:1024 sc1\n"
        "s_waitcnt vmcnt(10)\n"
        "s_barrier\n"
        "s_add_u32 m0, s58, 49152\n"
        "s_add_u32 s76, s64, 114688\n"
        "buffer_load_dwordx4 v12, s[16:19], s76 offen offset:0 sc1 lds\n"
        "buffer_load_dwordx4 v12, s[16:19], s76 offen offset:1024 sc1 lds\n"
        "buffer_load_dwordx4 v12, s[16:19], s76 offen offset:2048 sc1 lds\n"
        "buffer_load_dwordx4 v12, s[16:19], s76 offen offset:3072 sc1 lds\n"
        "s_add_u32 m0, s58, 16384\n"
        "s_movk_i32 s59, 768\n"
        "buffer_load_dwordx4 v8, s[12:15], s59 offen offset:0 lds\n"
        "buffer_load_dwordx4 v9, s[12:15], s59 offen offset:1024 lds\n"
        "buffer_load_dwordx4 v10, s[12:15], s59 offen offset:2048 lds\n"
        "buffer_load_dwordx4 v11, s[12:15], s59 offen offset:3072 lds\n"
        "s_waitcnt vmcnt(10)\n"
        "s_barrier\n"
        "s_add_u32 m0, s58, 65536\n"
        "s_add_u32 s76, s64, 131072\n"
        "buffer_load_dwordx4 v12, s[16:19], s76 offen offset:0 sc1 lds\n"
        "buffer_load_dwordx4 v12, s[16:19], s76 offen offset:1024 sc1 lds\n"
        "buffer_load_dwordx4 v12, s[16:19], s76 offen offset:2048 sc1 lds\n"
        "buffer_load_dwordx4 v12, s[16:19], s76 offen offset:3072 sc1 lds\n"
        "s_add_u32 m0, s58, 32768\n"
        "s_movk_i32 s59, 896\n"
        "buffer_load_dwordx4 v8, s[12:15], s59 offen offset:0 lds\n"
        "buffer_load_dwordx4 v9, s[12:15], s59 offen offset:1024 lds\n"
        "buffer_load_dwordx4 v10, s[12:15], s59 offen offset:2048 lds\n"
        "buffer_load_dwordx4 v11, s[12:15], s59 offen offset:3072 lds\n"
        "s_waitcnt vmcnt(8)\n"
        "s_barrier\n"
        "s_add_u32 m0, s58, 81920\n"
        "s_add_u32 s76, s64, 147456\n"
        "buffer_load_dwordx4 v12, s[16:19], s76 offen offset:0 sc1 lds\n"
        "buffer_load_dwordx4 v12, s[16:19], s76 offen offset:1024 sc1 lds\n"
        "buffer_load_dwordx4 v12, s[16:19], s76 offen offset:2048 sc1 lds\n"
        "buffer_load_dwordx4 v12, s[16:19], s76 offen offset:3072 sc1 lds\n"
        "s_add_u32 m0, s58, 0\n"
        "s_movk_i32 s59, 1024\n"
        "buffer_load_dwordx4 v8, s[12:15], s59 offen offset:0 lds\n"
        "buffer_load_dwordx4 v9, s[12:15], s59 offen offset:1024 lds\n"
        "buffer_load_dwordx4 v10, s[12:15], s59 offen offset:2048 lds\n"
        "buffer_load_dwordx4 v11, s[12:15], s59 offen offset:3072 lds\n"
        "s_waitcnt vmcnt(8)\n"
        "s_barrier\n"
        "s_add_u32 m0, s58, 49152\n"
        "s_add_u32 s76, s64, 163840\n"
        "buffer_load_dwordx4 v12, s[16:19], s76 offen offset:0 sc1 lds\n"
        "buffer_load_dwordx4 v12, s[16:19], s76 offen offset:1024 sc1 lds\n"
        "buffer_load_dwordx4 v12, s[16:19], s76 offen offset:2048 sc1 lds\n"
        "buffer_load_dwordx4 v12, s[16:19], s76 offen offset:3072 sc1 lds\n"
        "s_add_u32 m0, s58, 16384\n"
        "s_movk_i32 s59, 1152\n"
        "buffer_load_dwordx4 v8, s[12:15], s59 offen offset:0 lds\n"
        "buffer_load_dwordx4 v9, s[12:15], s59 offen offset:1024 lds\n"
        "buffer_load_dwordx4 v10, s[12:15], s59 offen offset:2048 lds\n"
        "buffer_load_dwordx4 v11, s[12:15], s59 offen offset:3072 lds\n"
        "s_waitcnt vmcnt(8)\n"
        "s_barrier\n"
        "s_add_u32 m0, s58, 65536\n"
        "s_add_u32 s76, s64, 180224\n"
        "buffer_load_dwordx4 v12, s[16:19], s76 offen offset:0 sc1 lds\n"
        "buffer_load_dwordx4 v12, s[16:19], s76 offen offset:1024 sc1 lds\n"
        "buffer_load_dwordx4 v12, s[16:19], s76 offen offset:2048 sc1 lds\n"
        "buffer_load_dwordx4 v12, s[16:19], s76 offen offset:3072 sc1 lds\n"
        "s_add_u32 m0, s58, 32768\n"
        "s_movk_i32 s59, 1280\n"
        "buffer_load_dwordx4 v8, s[12:15], s59 offen offset:0 lds\n"
        "buffer_load_dwordx4 v9, s[12:15], s59 offen offset:1024 lds\n"
        "buffer_load_dwordx4 v10, s[12:15], s59 offen offset:2048 lds\n"
        "buffer_load_dwordx4 v11, s[12:15], s59 offen offset:3072 lds\n"
        "s_waitcnt vmcnt(8)\n"
        "s_barrier\n"
        "s_add_u32 m0, s58, 81920\n"
        "s_add_u32 s76, s64, 196608\n"
        "buffer_load_dwordx4 v12, s[16:19], s76 offen offset:0 sc1 lds\n"
        "buffer_load_dwordx4 v12, s[16:19], s76 offen offset:1024 sc1 lds\n"
        "buffer_load_dwordx4 v12, s[16:19], s76 offen offset:2048 sc1 lds\n"
        "buffer_load_dwordx4 v12, s[16:19], s76 offen offset:3072 sc1 lds\n"
        "s_add_u32 m0, s58, 0\n"
        "s_movk_i32 s59, 1408\n"
        "buffer_load_dwordx4 v8, s[12:15], s59 offen offset:0 lds\n"
        "buffer_load_dwordx4 v9, s[12:15], s59 offen offset:1024 lds\n"
        "buffer_load_dwordx4 v10, s[12:15], s59 offen offset:2048 lds\n"
        "buffer_load_dwordx4 v11, s[12:15], s59 offen offset:3072 lds\n"
        "s_waitcnt vmcnt(8)\n"
        "s_barrier\n"
        "s_add_u32 m0, s58, 49152\n"
        "s_add_u32 s76, s64, 212992\n"
        "buffer_load_dwordx4 v12, s[16:19], s76 offen offset:0 sc1 lds\n"
        "buffer_load_dwordx4 v12, s[16:19], s76 offen offset:1024 sc1 lds\n"
        "buffer_load_dwordx4 v12, s[16:19], s76 offen offset:2048 sc1 lds\n"
        "buffer_load_dwordx4 v12, s[16:19], s76 offen offset:3072 sc1 lds\n"
        "s_add_u32 m0, s58, 16384\n"
        "s_movk_i32 s59, 1536\n"
        "buffer_load_dwordx4 v8, s[12:15], s59 offen offset:0 lds\n"
        "buffer_load_dwordx4 v9, s[12:15], s59 offen offset:1024 lds\n"
        "buffer_load_dwordx4 v10, s[12:15], s59 offen offset:2048 lds\n"
        "buffer_load_dwordx4 v11, s[12:15], s59 offen offset:3072 lds\n"
        "s_waitcnt vmcnt(8)\n"
        "s_barrier\n"
        "s_add_u32 m0, s58, 65536\n"
        "s_add_u32 s76, s64, 229376\n"
        "buffer_load_dwordx4 v12, s[16:19], s76 offen offset:0 sc1 lds\n"
        "buffer_load_dwordx4 v12, s[16:19], s76 offen offset:1024 sc1 lds\n"
        "buffer_load_dwordx4 v12, s[16:19], s76 offen offset:2048 sc1 lds\n"
        "buffer_load_dwordx4 v12, s[16:19], s76 offen offset:3072 sc1 lds\n"
        "s_add_u32 m0, s58, 32768\n"
        "s_movk_i32 s59, 1664\n"
        "buffer_load_dwordx4 v8, s[12:15], s59 offen offset:0 lds\n"
        "buffer_load_dwordx4 v9, s[12:15], s59 offen offset:1024 lds\n"
        "buffer_load_dwordx4 v10, s[12:15], s59 offen offset:2048 lds\n"
        "buffer_load_dwordx4 v11, s[12:15], s59 offen offset:3072 lds\n"
        "s_waitcnt vmcnt(8)\n"
        "s_barrier\n"
        "s_add_u32 m0, s58, 81920\n"
        "s_add_u32 s76, s64, 245760\n"
        "buffer_load_dwordx4 v12, s[16:19], s76 offen offset:0 sc1 lds\n"
        "buffer_load_dwordx4 v12, s[16:19], s76 offen offset:1024 sc1 lds\n"
        "buffer_load_dwordx4 v12, s[16:19], s76 offen offset:2048 sc1 lds\n"
        "buffer_load_dwordx4 v12, s[16:19], s76 offen offset:3072 sc1 lds\n"
        "s_add_u32 m0, s58, 0\n"
        "s_movk_i32 s59, 1792\n"
        "buffer_load_dwordx4 v8, s[12:15], s59 offen offset:0 lds\n"
        "buffer_load_dwordx4 v9, s[12:15], s59 offen offset:1024 lds\n"
        "buffer_load_dwordx4 v10, s[12:15], s59 offen offset:2048 lds\n"
        "buffer_load_dwordx4 v11, s[12:15], s59 offen offset:3072 lds\n"
        "s_waitcnt vmcnt(8)\n"
        "s_barrier\n"
        "s_add_u32 m0, s58, 16384\n"
        "s_movk_i32 s59, 1920\n"
        "buffer_load_dwordx4 v8, s[12:15], s59 offen offset:0 lds\n"
        "buffer_load_dwordx4 v9, s[12:15], s59 offen offset:1024 lds\n"
        "buffer_load_dwordx4 v10, s[12:15], s59 offen offset:2048 lds\n"
        "buffer_load_dwordx4 v11, s[12:15], s59 offen offset:3072 lds\n"
        "s_waitcnt vmcnt(0)\n"
        "s_barrier\n"
        "s_branch .Ln_ht\n"
        ".Ln_h3:\n"
        "s_add_u32 s72, s32, 80\n"
        "s_mul_i32 s35, s72, 9363\n"
        "s_lshr_b32 s35, s35, 16\n"
        "s_mul_i32 s36, s35, 7\n"
        "s_sub_u32 s36, s72, s36\n"
        "s_mul_i32 s40, s35, 917504\n"
        "s_mul_i32 s37, s33, 896\n"
        "s_lshl_b32 s38, s36, 7\n"
        "s_add_u32 s40, s40, s37\n"
        "s_add_u32 s40, s40, s38\n"
        "s_add_u32 s41, s40, 14336\n"
        "s_add_u32 s42, s41, 14336\n"
        "s_add_u32 s43, s42, 14336\n"
        "s_add_u32 s44, s43, 14336\n"
        "s_add_u32 s45, s44, 14336\n"
        "s_add_u32 s46, s45, 14336\n"
        "s_add_u32 s47, s46, 14336\n"
        "s_lshl_b32 s75, s33, 4\n"
        "s_add_u32 s75, s75, s35\n"
        "s_lshl_b32 s75, s75, 3\n"
        "s_add_u32 s75, s75, s36\n"
        "s_lshl_b32 s75, s75, 11\n"
        "s_add_u32 s75, s75, 114688\n"
        "s_waitcnt vmcnt(0)\n"
        "s_waitcnt lgkmcnt(0)\n"
        "s_barrier\n"
        "s_sleep 16\n"
        "s_add_u32 m0, s58, 32768\n"
        "s_movk_i32 s59, 128\n"
        "buffer_load_dwordx4 v8, s[12:15], s59 offen offset:0 lds\n"
        "buffer_load_dwordx4 v9, s[12:15], s59 offen offset:1024 lds\n"
        "buffer_load_dwordx4 v10, s[12:15], s59 offen offset:2048 lds\n"
        "buffer_load_dwordx4 v11, s[12:15], s59 offen offset:3072 lds\n"
        "s_add_u32 m0, s58, 65536\n"
        "s_add_u32 s76, s64, 32768\n"
        "buffer_load_dwordx4 v12, s[16:19], s76 offen offset:0 sc1 lds\n"
        "buffer_load_dwordx4 v12, s[16:19], s76 offen offset:1024 sc1 lds\n"
        "v_mov_b32 v84, 128\n"
        "s_movk_i32 s73, 64\n"
        "1:\n"
        "s_sub_u32 s73, s73, 1\n"
        "s_cbranch_scc1 2f\n"
        "s_sleep 1\n"
        "ds_read_b128 v[96:99], v84 offset:4096\n"
        "s_waitcnt lgkmcnt(0)\n"
        "v_min3_u32 v96, v96, v97, v98\n"
        "v_min_u32 v96, v96, v99\n"
        "v_cmp_eq_u32 vcc, 0, v96\n"
        "s_cbranch_vccnz 1b\n"
        "2:\n"
        "s_add_u32 m0, s58, 0\n"
        "s_movk_i32 s59, 256\n"
        "buffer_load_dwordx4 v8, s[12:15], s59 offen offset:0 lds\n"
        "buffer_load_dwordx4 v9, s[12:15], s59 offen offset:1024 lds\n"
        "buffer_load_dwordx4 v10, s[12:15], s59 offen offset:2048 lds\n"
        "buffer_load_dwordx4 v11, s[12:15], s59 offen offset:3072 lds\n"
        "s_add_u32 m0, s58, 81920\n"
        "s_add_u32 s76, s64, 49152\n"
        "buffer_load_dwordx4 v12, s[16:19], s76 offen offset:0 sc1 lds\n"
        "buffer_load_dwordx4 v12, s[16:19], s76 offen offset:1024 sc1 lds\n"
        "s_mov_b64 exec, 0\n"
        "s_add_u32 s94, s86, .Ln_tp3-.Ln_here\n"
        "s_addc_u32 s95, s87, 0\n"
        "s_swappc_b64 s[70:71], s[94:95]\n"
        "s_mov_b64 exec, -1\n"
        "s_waitcnt vmcnt(6)\n"
        "s_barrier\n"
        "s_add_u32 m0, s58, 16384\n"
        "s_movk_i32 s59, 384\n"
        "buffer_load_dwordx4 v8, s[12:15], s59 offen offset:0 lds\n"
        "buffer_load_dwordx4 v9, s[12:15], s59 offen offset:1024 lds\n"
        "buffer_load_dwordx4 v10, s[12:15], s59 offen offset:2048 lds\n"
        "buffer_load_dwordx4 v11, s[12:15], s59 offen offset:3072 lds\n"
        "s_add_u32 m0, s58, 49152\n"
        "s_add_u32 s76, s64, 65536\n"
        "buffer_load_dwordx4 v12, s[16:19], s76 offen offset:0 sc1 lds\n"
        "buffer_load_dwordx4 v12, s[16:19], s76 offen offset:1024 sc1 lds\n"
        "s_waitcnt vmcnt(6)\n"
        "s_barrier\n"
        "s_add_u32 m0, s58, 65536\n"
        "s_add_u32 s76, s64, 81920\n"
        "buffer_load_dwordx4 v12, s[16:19], s76 offen offset:0 sc1 lds\n"
        "buffer_load_dwordx4 v12, s[16:19], s76 offen offset:1024 sc1 lds\n"
        "s_add_u32 m0, s58, 32768\n"
        "s_movk_i32 s59, 512\n"
        "buffer_load_dwordx4 v8, s[12:15], s59 offen offset:0 lds\n"
        "buffer_load_dwordx4 v9, s[12:15], s59 offen offset:1024 lds\n"
        "buffer_load_dwordx4 v10, s[12:15], s59 offen offset:2048 lds\n"
        "buffer_load_dwordx4 v11, s[12:15], s59 offen offset:3072 lds\n"
        "buffer_load_dwordx4 v[40:43], v249, s[8:11], s40 offen\n"
        "buffer_load_dwordx4 v[44:47], v249, s[8:11], s41 offen\n"
        "buffer_load_dwordx4 v[48:51], v249, s[8:11], s42 offen\n"
        "buffer_load_dwordx4 v[52:55], v249, s[8:11], s43 offen\n"
        "buffer_load_dwordx4 v[56:59], v249, s[8:11], s44 offen\n"
        "buffer_load_dwordx4 v[60:63], v249, s[8:11], s45 offen\n"
        "buffer_load_dwordx4 v[64:67], v249, s[8:11], s46 offen\n"
        "buffer_load_dwordx4 v[68:71], v249, s[8:11], s47 offen\n"
        "s_waitcnt vmcnt(14)\n"
        "s_barrier\n"
        "s_add_u32 m0, s58, 81920\n"
        "s_add_u32 s76, s64, 98304\n"
        "buffer_load_dwordx4 v12, s[16:19], s76 offen offset:0 sc1 lds\n"
        "buffer_load_dwordx4 v12, s[16:19], s76 offen offset:1024 sc1 lds\n"
        "s_add_u32 m0, s58, 0\n"
        "s_movk_i32 s59, 640\n"
        "buffer_load_dwordx4 v8, s[12:15], s59 offen offset:0 lds\n"
        "buffer_load_dwordx4 v9, s[12:15], s59 offen offset:1024 lds\n"
        "buffer_load_dwordx4 v10, s[12:15], s59 offen offset:2048 lds\n"
        "buffer_load_dwordx4 v11, s[12:15], s59 offen offset:3072 lds\n"
        "s_waitcnt vmcnt(14)\n"
        "s_barrier\n"
        "s_add_u32 m0, s58, 49152\n"
        "s_add_u32 s76, s64, 114688\n"
        "buffer_load_dwordx4 v12, s[16:19], s76 offen offset:0 sc1 lds\n"
        "buffer_load_dwordx4 v12, s[16:19], s76 offen offset:1024 sc1 lds\n"
        "s_add_u32 m0, s58, 16384\n"
        "s_movk_i32 s59, 768\n"
        "buffer_load_dwordx4 v8, s[12:15], s59 offen offset:0 lds\n"
        "buffer_load_dwordx4 v9, s[12:15], s59 offen offset:1024 lds\n"
        "buffer_load_dwordx4 v10, s[12:15], s59 offen offset:2048 lds\n"
        "buffer_load_dwordx4 v11, s[12:15], s59 offen offset:3072 lds\n"
        "s_waitcnt vmcnt(12)\n"
        "v_lshl_add_u64 v[68:69], v[68:69], 4, v[64:65]\n"
        "v_lshl_add_u64 v[70:71], v[70:71], 4, v[66:67]\n"
        "v_lshl_add_u64 v[68:69], v[68:69], 4, v[60:61]\n"
        "v_lshl_add_u64 v[70:71], v[70:71], 4, v[62:63]\n"
        "v_lshl_add_u64 v[68:69], v[68:69], 4, v[56:57]\n"
        "v_lshl_add_u64 v[70:71], v[70:71], 4, v[58:59]\n"
        "v_lshl_add_u64 v[68:69], v[68:69], 4, v[52:53]\n"
        "v_lshl_add_u64 v[70:71], v[70:71], 4, v[54:55]\n"
        "v_lshl_add_u64 v[68:69], v[68:69], 4, v[48:49]\n"
        "v_lshl_add_u64 v[70:71], v[70:71], 4, v[50:51]\n"
        "v_lshl_add_u64 v[68:69], v[68:69], 4, v[44:45]\n"
        "v_lshl_add_u64 v[70:71], v[70:71], 4, v[46:47]\n"
        "v_lshl_add_u64 v[72:73], v[68:69], 4, v[40:41]\n"
        "v_lshl_add_u64 v[76:77], v[70:71], 4, v[42:43]\n"
        "s_nop 0\n"
        "v_permlane32_swap_b32 v72, v73\n"
        "v_permlane32_swap_b32 v76, v77\n"
        "buffer_store_dwordx4 v[72:75], v250, s[16:19], s75 offen sc1\n"
        "buffer_store_dwordx4 v[76:79], v251, s[16:19], s75 offen offset:1024 sc1\n"
        "s_waitcnt vmcnt(8)\n"
        "s_barrier\n"
        "s_add_u32 m0, s58, 65536\n"
        "s_add_u32 s76, s64, 131072\n"
        "buffer_load_dwordx4 v12, s[16:19], s76 offen offset:0 sc1 lds\n"
        "buffer_load_dwordx4 v12, s[16:19], s76 offen offset:1024 sc1 lds\n"
        "s_add_u32 m0, s58, 32768\n"
        "s_movk_i32 s59, 896\n"
        "buffer_load_dwordx4 v8, s[12:15], s59 offen offset:0 lds\n"
        "buffer_load_dwordx4 v9, s[12:15], s59 offen offset:1024 lds\n"
        "buffer_load_dwordx4 v10, s[12:15], s59 offen offset:2048 lds\n"
        "buffer_load_dwordx4 v11, s[12:15], s59 offen offset:3072 lds\n"
        "s_waitcnt vmcnt(8)\n"
        "s_barrier\n"
        "s_add_u32 m0, s58, 81920\n"
        "s_add_u32 s76, s64, 147456\n"
        "buffer_load_dwordx4 v12, s[16:19], s76 offen offset:0 sc1 lds\n"
        "buffer_load_dwordx4 v12, s[16:19], s76 offen offset:1024 sc1 lds\n"
        "s_add_u32 m0, s58, 0\n"
        "s_movk_i32 s59, 1024\n"
        "buffer_load_dwordx4 v8, s[12:15], s59 offen offset:0 lds\n"
        "buffer_load_dwordx4 v9, s[12:15], s59 offen offset:1024 lds\n"
        "buffer_load_dwordx4 v10, s[12:15], s59 offen offset:2048 lds\n"
        "buffer_load_dwordx4 v11, s[12:15], s59 offen offset:3072 lds\n"
        "s_waitcnt vmcnt(6)\n"
        "s_barrier\n"
        "s_add_u32 m0, s58, 49152\n"
        "s_add_u32 s76, s64, 163840\n"
        "buffer_load_dwordx4 v12, s[16:19], s76 offen offset:0 sc1 lds\n"
        "buffer_load_dwordx4 v12, s[16:19], s76 offen offset:1024 sc1 lds\n"
        "s_add_u32 m0, s58, 16384\n"
        "s_movk_i32 s59, 1152\n"
        "buffer_load_dwordx4 v8, s[12:15], s59 offen offset:0 lds\n"
        "buffer_load_dwordx4 v9, s[12:15], s59 offen offset:1024 lds\n"
        "buffer_load_dwordx4 v10, s[12:15], s59 offen offset:2048 lds\n"
        "buffer_load_dwordx4 v11, s[12:15], s59 offen offset:3072 lds\n"
        "s_waitcnt vmcnt(6)\n"
        "s_barrier\n"
        "s_add_u32 m0, s58, 65536\n"
        "s_add_u32 s76, s64, 180224\n"
        "buffer_load_dwordx4 v12, s[16:19], s76 offen offset:0 sc1 lds\n"
        "buffer_load_dwordx4 v12, s[16:19], s76 offen offset:1024 sc1 lds\n"
        "s_add_u32 m0, s58, 32768\n"
        "s_movk_i32 s59, 1280\n"
        "buffer_load_dwordx4 v8, s[12:15], s59 offen offset:0 lds\n"
        "buffer_load_dwordx4 v9, s[12:15], s59 offen offset:1024 lds\n"
        "buffer_load_dwordx4 v10, s[12:15], s59 offen offset:2048 lds\n"
        "buffer_load_dwordx4 v11, s[12:15], s59 offen offset:3072 lds\n"
        "s_waitcnt vmcnt(6)\n"
        "s_barrier\n"
        "s_add_u32 m0, s58, 81920\n"
        "s_add_u32 s76, s64, 196608\n"
        "buffer_load_dwordx4 v12, s[16:19], s76 offen offset:0 sc1 lds\n"
        "buffer_load_dwordx4 v12, s[16:19], s76 offen offset:1024 sc1 lds\n"
        "s_add_u32 m0, s58, 0\n"
        "s_movk_i32 s59, 1408\n"
        "buffer_load_dwordx4 v8, s[12:15], s59 offen offset:0 lds\n"
        "buffer_load_dwordx4 v9, s[12:15], s59 offen offset:1024 lds\n"
        "buffer_load_dwordx4 v10, s[12:15], s59 offen offset:2048 lds\n"
        "buffer_load_dwordx4 v11, s[12:15], s59 offen offset:3072 lds\n"
        "s_waitcnt vmcnt(6)\n"
        "s_barrier\n"
        "s_add_u32 m0, s58, 49152\n"
        "s_add_u32 s76, s64, 212992\n"
        "buffer_load_dwordx4 v12, s[16:19], s76 offen offset:0 sc1 lds\n"
        "buffer_load_dwordx4 v12, s[16:19], s76 offen offset:1024 sc1 lds\n"
        "s_add_u32 m0, s58, 16384\n"
        "s_movk_i32 s59, 1536\n"
        "buffer_load_dwordx4 v8, s[12:15], s59 offen offset:0 lds\n"
        "buffer_load_dwordx4 v9, s[12:15], s59 offen offset:1024 lds\n"
        "buffer_load_dwordx4 v10, s[12:15], s59 offen offset:2048 lds\n"
        "buffer_load_dwordx4 v11, s[12:15], s59 offen offset:3072 lds\n"
        "s_waitcnt vmcnt(6)\n"
        "s_barrier\n"
        "s_add_u32 m0, s58, 65536\n"
        "s_add_u32 s76, s64, 229376\n"
        "buffer_load_dwordx4 v12, s[16:19], s76 offen offset:0 sc1 lds\n"
        "buffer_load_dwordx4 v12, s[16:19], s76 offen offset:1024 sc1 lds\n"
        "s_add_u32 m0, s58, 32768\n"
        "s_movk_i32 s59, 1664\n"
        "buffer_load_dwordx4 v8, s[12:15], s59 offen offset:0 lds\n"
        "buffer_load_dwordx4 v9, s[12:15], s59 offen offset:1024 lds\n"
        "buffer_load_dwordx4 v10, s[12:15], s59 offen offset:2048 lds\n"
        "buffer_load_dwordx4 v11, s[12:15], s59 offen offset:3072 lds\n"
        "s_waitcnt vmcnt(6)\n"
        "s_barrier\n"
        "s_add_u32 m0, s58, 81920\n"
        "s_add_u32 s76, s64, 245760\n"
        "buffer_load_dwordx4 v12, s[16:19], s76 offen offset:0 sc1 lds\n"
        "buffer_load_dwordx4 v12, s[16:19], s76 offen offset:1024 sc1 lds\n"
        "s_add_u32 m0, s58, 0\n"
        "s_movk_i32 s59, 1792\n"
        "buffer_load_dwordx4 v8, s[12:15], s59 offen offset:0 lds\n"
        "buffer_load_dwordx4 v9, s[12:15], s59 offen offset:1024 lds\n"
        "buffer_load_dwordx4 v10, s[12:15], s59 offen offset:2048 lds\n"
        "buffer_load_dwordx4 v11, s[12:15], s59 offen offset:3072 lds\n"
        "s_waitcnt vmcnt(6)\n"
        "s_barrier\n"
        "s_add_u32 m0, s58, 16384\n"
        "s_movk_i32 s59, 1920\n"
        "buffer_load_dwordx4 v8, s[12:15], s59 offen offset:0 lds\n"
        "buffer_load_dwordx4 v9, s[12:15], s59 offen offset:1024 lds\n"
        "buffer_load_dwordx4 v10, s[12:15], s59 offen offset:2048 lds\n"
        "buffer_load_dwordx4 v11, s[12:15], s59 offen offset:3072 lds\n"
        "s_waitcnt vmcnt(0)\n"
        "s_barrier\n"
        ".Ln_ht:\n"
        "s_barrier\n"
        "1:\n"
        "s_sleep 1\n"
        "ds_read_b64 v[80:81], v92 offset:0\n"
        "s_waitcnt lgkmcnt(0)\n"
        "v_cmp_ne_u64 vcc, s[88:89], v[80:81]\n"
        "s_cbranch_vccnz 1b\n"
        "ds_read_b128 v[168:171], v100 offset:0\n"
        "s_add_u32 s35, s84, 0\n"
        "s_waitcnt lgkmcnt(0)\n"
        "buffer_store_dwordx4 v[168:171], v102, s[20:23], s35 offen nt\n"
        "1:\n"
        "s_sleep 1\n"
        "ds_read_b64 v[80:81], v92 offset:256\n"
        "s_waitcnt lgkmcnt(0)\n"
        "v_cmp_ne_u64 vcc, s[88:89], v[80:81]\n"
        "s_cbranch_vccnz 1b\n"
        "ds_read_b128 v[176:179], v100 offset:8192\n"
        "s_add_u32 s35, s84, 114688\n"
        "s_waitcnt lgkmcnt(0)\n"
        "buffer_store_dwordx4 v[176:179], v102, s[20:23], s35 offen nt\n"
        "1:\n"
        "s_sleep 1\n"
        "ds_read_b64 v[80:81], v92 offset:512\n"
        "s_waitcnt lgkmcnt(0)\n"
        "v_cmp_ne_u64 vcc, s[88:89], v[80:81]\n"
        "s_cbranch_vccnz 1b\n"
        "ds_read_b128 v[184:187], v101 offset:0\n"
        "s_add_u32 s35, s84, 0\n"
        "s_waitcnt lgkmcnt(0)\n"
        "buffer_store_dwordx4 v[184:187], v103, s[20:23], s35 offen nt\n"
        "1:\n"
        "s_sleep 1\n"
        "ds_read_b64 v[80:81], v92 offset:768\n"
        "s_waitcnt lgkmcnt(0)\n"
        "v_cmp_ne_u64 vcc, s[88:89], v[80:81]\n"
        "s_cbranch_vccnz 1b\n"
        "ds_read_b128 v[192:195], v101 offset:8192\n"
        "s_add_u32 s35, s84, 114688\n"
        "s_waitcnt lgkmcnt(0)\n"
        "buffer_store_dwordx4 v[192:195], v103, s[20:23], s35 offen nt\n"
        "1:\n"
        "s_sleep 1\n"
        "ds_read_b64 v[80:81], v92 offset:1024\n"
        "s_waitcnt lgkmcnt(0)\n"
        "v_cmp_ne_u64 vcc, s[88:89], v[80:81]\n"
        "s_cbranch_vccnz 1b\n"
        "ds_read_b128 v[200:203], v88 offset:16384\n"
        "ds_read_b128 v[204:207], v89 offset:17408\n"
        "s_add_u32 s35, s84, 229376\n"
        "s_add_u32 s36, s35, 28672\n"
        "s_waitcnt lgkmcnt(1)\n"
        "buffer_store_dwordx4 v[200:203], v90, s[20:23], s35 offen nt\n"
        "s_waitcnt lgkmcnt(0)\n"
        "buffer_store_dwordx4 v[204:207], v90, s[20:23], s36 offen nt\n"
        "1:\n"
        "s_sleep 1\n"
        "ds_read_b64 v[80:81], v92 offset:1280\n"
        "s_waitcnt lgkmcnt(0)\n"
        "v_cmp_ne_u64 vcc, s[88:89], v[80:81]\n"
        "s_cbranch_vccnz 1b\n"
        "ds_read_b128 v[208:211], v88 offset:24576\n"
        "ds_read_b128 v[212:215], v89 offset:25600\n"
        "s_add_u32 s35, s84, 344064\n"
        "s_add_u32 s36, s35, 28672\n"
        "s_waitcnt lgkmcnt(1)\n"
        "buffer_store_dwordx4 v[208:211], v90, s[20:23], s35 offen nt\n"
        "s_waitcnt lgkmcnt(0)\n"
        "buffer_store_dwordx4 v[212:215], v90, s[20:23], s36 offen nt\n"
        "1:\n"
        "s_sleep 1\n"
        "ds_read_b64 v[80:81], v92 offset:1536\n"
        "s_waitcnt lgkmcnt(0)\n"
        "v_cmp_ne_u64 vcc, s[88:89], v[80:81]\n"
        "s_cbranch_vccnz 1b\n"
        "ds_read_b128 v[216:219], v88 offset:32768\n"
        "ds_read_b128 v[220:223], v89 offset:33792\n"
        "s_add_u32 s35, s85, 0\n"
        "s_add_u32 s36, s35, 28672\n"
        "s_waitcnt lgkmcnt(1)\n"
        "buffer_store_dwordx4 v[216:219], v91, s[20:23], s35 offen nt\n"
        "s_waitcnt lgkmcnt(0)\n"
        "buffer_store_dwordx4 v[220:223], v91, s[20:23], s36 offen nt\n"
        "1:\n"
        "s_sleep 1\n"
        "ds_read_b64 v[80:81], v92 offset:1792\n"
        "s_waitcnt lgkmcnt(0)\n"
        "v_cmp_ne_u64 vcc, s[88:89], v[80:81]\n"
        "s_cbranch_vccnz 1b\n"
        "ds_read_b128 v[224:227], v88 offset:40960\n"
        "ds_read_b128 v[228:231], v89 offset:41984\n"
        "s_add_u32 s35, s85, 114688\n"
        "s_add_u32 s36, s35, 28672\n"
        "s_waitcnt lgkmcnt(1)\n"
        "buffer_store_dwordx4 v[224:227], v91, s[20:23], s35 offen nt\n"
        "s_waitcnt lgkmcnt(0)\n"
        "buffer_store_dwordx4 v[228:231], v91, s[20:23], s36 offen nt\n"
        "1:\n"
        "s_sleep 1\n"
        "ds_read_b64 v[80:81], v92 offset:2048\n"
        "s_waitcnt lgkmcnt(0)\n"
        "v_cmp_ne_u64 vcc, s[88:89], v[80:81]\n"
        "s_cbranch_vccnz 1b\n"
        "ds_read_b128 v[232:235], v88 offset:49152\n"
        "ds_read_b128 v[236:239], v89 offset:50176\n"
        "s_add_u32 s35, s85, 229376\n"
        "s_add_u32 s36, s35, 28672\n"
        "s_waitcnt lgkmcnt(1)\n"
        "buffer_store_dwordx4 v[232:235], v91, s[20:23], s35 offen nt\n"
        "s_waitcnt lgkmcnt(0)\n"
        "buffer_store_dwordx4 v[236:239], v91, s[20:23], s36 offen nt\n"
        "1:\n"
        "s_sleep 1\n"
        "ds_read_b64 v[80:81], v92 offset:2304\n"
        "s_waitcnt lgkmcnt(0)\n"
        "v_cmp_ne_u64 vcc, s[88:89], v[80:81]\n"
        "s_cbranch_vccnz 1b\n"
        "ds_read_b128 v[240:243], v88 offset:57344\n"
        "ds_read_b128 v[244:247], v89 offset:58368\n"
        "s_add_u32 s35, s85, 344064\n"
        "s_add_u32 s36, s35, 28672\n"
        "s_waitcnt lgkmcnt(1)\n"
        "buffer_store_dwordx4 v[240:243], v91, s[20:23], s35 offen nt\n"
        "s_waitcnt lgkmcnt(0)\n"
        "buffer_store_dwordx4 v[244:247], v91, s[20:23], s36 offen nt\n"
        "s_endpgm\n"
        ".p2align 12\n"
        ".Ln_r0:\n"
        "s_add_u32 s73, s73, 1\n"
        "s_cmp_lt_u32 s73, s74\n"
        "s_cbranch_scc0 .Ln_f0\n"
        "buffer_load_dwordx4 v[64:67], v232, s[16:19], s64 offen sc1\n"
        "buffer_load_dwordx4 v[68:71], v233, s[16:19], s64 offen sc1\n"
        "buffer_load_dwordx4 v[72:75], v234, s[16:19], s64 offen sc1\n"
        "buffer_load_dwordx4 v[76:79], v235, s[16:19], s64 offen sc1\n"
        "buffer_load_dwordx4 v[208:211], v232, s[16:19], s65 offen sc1\n"
        "buffer_load_dwordx4 v[212:215], v233, s[16:19], s65 offen sc1\n"
        "buffer_load_dwordx4 v[216:219], v234, s[16:19], s65 offen sc1\n"
        "buffer_load_dwordx4 v[220:223], v235, s[16:19], s65 offen sc1\n"
        "s_waitcnt vmcnt(0)\n"
        ".Ln_go:\n"
        "s_waitcnt vmcnt(4)\n"
        "v_bitop3_b32 v66, v66, v70, s34 bitop3:0x7e\n"
        "v_bitop3_b32 v67, v67, v71, s39 bitop3:0x7e\n"
        "v_bitop3_b32 v74, v74, v78, s34 bitop3:0x7e\n"
        "v_bitop3_b32 v75, v75, v79, s39 bitop3:0x7e\n"
        "v_or3_b32 v66, v66, v67, v74\n"
        "v_or_b32 v66, v66, v75\n"
        "v_cmp_ne_u32 vcc, 0, v66\n"
        "s_cbranch_vccnz .Ln_r0\n"
        ".Ln_b0:\n"
        "v_perm_b32 v224, v64, v236, s60\n"
        "v_perm_b32 v225, v64, v236, s61\n"
        "ds_read_b32 v112, v224\n"
        "v_perm_b32 v226, v64, v236, s62\n"
        "ds_read_b32 v113, v225\n"
        "v_perm_b32 v227, v64, v236, s63\n"
        "ds_read_b32 v114, v226\n"
        "v_perm_b32 v224, v65, v236, s60\n"
        "ds_read_b32 v115, v227\n"
        "v_perm_b32 v225, v65, v236, s61\n"
        "ds_read_b32 v116, v224\n"
        "v_perm_b32 v226, v65, v236, s62\n"
        "ds_read_b32 v117, v225\n"
        "v_perm_b32 v227, v65, v236, s63\n"
        "ds_read_b32 v118, v226\n"
        "v_perm_b32 v224, v68, v236, s60\n"
        "ds_read_b32 v119, v227\n"
        "v_perm_b32 v225, v68, v236, s61\n"
        "ds_read_b32 v120, v224\n"
        "ds_read_b32 v121, v225\n"
        "ds_write_b32 v237, v237 offset:4096\n"
        "v_perm_b32 v226, v68, v236, s62\n"
        "s_waitcnt lgkmcnt(7)\n"
        "v_mfma_f32_16x16x32_bf16 v[0:3], v[112:115], v[176:179], 0\n"
        "ds_read_b32 v122, v226\n"
        "v_perm_b32 v227, v68, v236, s63\n"
        "v_perm_b32 v224, v69, v236, s60\n"
        "s_waitcnt lgkmcnt(4)\n"
        "v_mfma_f32_16x16x32_bf16 v[0:3], v[116:119], v[180:183], v[0:3]\n"
        "ds_read_b32 v123, v227\n"
        "ds_read_b32 v124, v224\n"
        "v_perm_b32 v225, v69, v236, s61\n"
        "v_mfma_f32_16x16x32_bf16 v[16:19], v[112:115], v[184:187], 0\n"
        "ds_read_b32 v125, v225\n"
        "v_perm_b32 v226, v69, v236, s62\n"
        "v_perm_b32 v227, v69, v236, s63\n"
        "v_mfma_f32_16x16x32_bf16 v[16:19], v[116:119], v[188:191], v[16:19]\n"
        "ds_read_b32 v126, v226\n"
        "ds_read_b32 v127, v227\n"
        "v_perm_b32 v224, v72, v236, s60\n"
        "v_mfma_f32_16x16x32_bf16 v[32:35], v[112:115], v[192:195], 0\n"
        "ds_read_b32 v128, v224\n"
        "v_perm_b32 v225, v72, v236, s61\n"
        "v_perm_b32 v226, v72, v236, s62\n"
        "v_mfma_f32_16x16x32_bf16 v[32:35], v[116:119], v[196:199], v[32:35]\n"
        "ds_read_b32 v129, v225\n"
        "ds_read_b32 v130, v226\n"
        "v_perm_b32 v227, v72, v236, s63\n"
        "v_mfma_f32_16x16x32_bf16 v[48:51], v[112:115], v[200:203], 0\n"
        "ds_read_b32 v131, v227\n"
        "v_perm_b32 v224, v73, v236, s60\n"
        "v_perm_b32 v225, v73, v236, s61\n"
        "s_waitcnt vmcnt(0)\n"
        "v_mfma_f32_16x16x32_bf16 v[48:51], v[116:119], v[204:207], v[48:51]\n"
        "ds_read_b32 v132, v224\n"
        "ds_read_b32 v133, v225\n"
        "v_perm_b32 v226, v73, v236, s62\n"
        "v_bitop3_b32 v210, v210, v214, s34 bitop3:0x7e\n"
        "s_waitcnt lgkmcnt(10)\n"
        "v_mfma_f32_16x16x32_bf16 v[4:7], v[120:123], v[176:179], 0\n"
        "ds_read_b32 v134, v226\n"
        "v_perm_b32 v227, v73, v236, s63\n"
        "v_perm_b32 v224, v76, v236, s60\n"
        "v_bitop3_b32 v211, v211, v215, s39 bitop3:0x7e\n"
        "s_waitcnt lgkmcnt(7)\n"
        "v_mfma_f32_16x16x32_bf16 v[4:7], v[124:127], v[180:183], v[4:7]\n"
        "ds_read_b32 v135, v227\n"
        "ds_read_b32 v136, v224\n"
        "v_perm_b32 v225, v76, v236, s61\n"
        "v_bitop3_b32 v218, v218, v222, s34 bitop3:0x7e\n"
        "v_mfma_f32_16x16x32_bf16 v[20:23], v[120:123], v[184:187], 0\n"
        "ds_read_b32 v137, v225\n"
        "v_perm_b32 v226, v76, v236, s62\n"
        "v_perm_b32 v227, v76, v236, s63\n"
        "v_bitop3_b32 v219, v219, v223, s39 bitop3:0x7e\n"
        "v_mfma_f32_16x16x32_bf16 v[20:23], v[124:127], v[188:191], v[20:23]\n"
        "ds_read_b32 v138, v226\n"
        "ds_read_b32 v139, v227\n"
        "v_perm_b32 v224, v77, v236, s60\n"
        "v_or3_b32 v210, v210, v211, v218\n"
        "v_mfma_f32_16x16x32_bf16 v[36:39], v[120:123], v[192:195], 0\n"
        "ds_read_b32 v140, v224\n"
        "v_perm_b32 v225, v77, v236, s61\n"
        "v_perm_b32 v226, v77, v236, s62\n"
        "v_or_b32 v210, v210, v219\n"
        "v_mfma_f32_16x16x32_bf16 v[36:39], v[124:127], v[196:199], v[36:39]\n"
        "ds_read_b32 v141, v225\n"
        "ds_read_b32 v142, v226\n"
        "v_perm_b32 v227, v77, v236, s63\n"
        "v_cmp_ne_u32 vcc, 0, v210\n"
        "v_mfma_f32_16x16x32_bf16 v[52:55], v[120:123], v[200:203], 0\n"
        "ds_read_b32 v143, v227\n"
        "s_cbranch_vccnz .Ln_f1\n"
        ".Ln_b1:\n"
        "v_perm_b32 v224, v208, v236, s60\n"
        "v_perm_b32 v225, v208, v236, s61\n"
        "v_mfma_f32_16x16x32_bf16 v[52:55], v[124:127], v[204:207], v[52:55]\n"
        "ds_read_b32 v144, v224\n"
        "ds_read_b32 v145, v225\n"
        "v_perm_b32 v226, v208, v236, s62\n"
        "s_waitcnt lgkmcnt(14)\n"
        "v_mfma_f32_16x16x32_bf16 v[8:11], v[128:131], v[176:179], 0\n"
        "ds_read_b32 v146, v226\n"
        "v_perm_b32 v227, v208, v236, s63\n"
        "v_perm_b32 v224, v209, v236, s60\n"
        "s_waitcnt lgkmcnt(11)\n"
        "v_mfma_f32_16x16x32_bf16 v[8:11], v[132:135], v[180:183], v[8:11]\n"
        "ds_read_b32 v147, v227\n"
        "ds_read_b32 v148, v224\n"
        "v_perm_b32 v225, v209, v236, s61\n"
        "v_mfma_f32_16x16x32_bf16 v[24:27], v[128:131], v[184:187], 0\n"
        "ds_read_b32 v149, v225\n"
        "v_perm_b32 v226, v209, v236, s62\n"
        "v_perm_b32 v227, v209, v236, s63\n"
        "v_mfma_f32_16x16x32_bf16 v[24:27], v[132:135], v[188:191], v[24:27]\n"
        "ds_read_b32 v150, v226\n"
        "ds_read_b32 v151, v227\n"
        "v_perm_b32 v224, v212, v236, s60\n"
        "v_mfma_f32_16x16x32_bf16 v[40:43], v[128:131], v[192:195], 0\n"
        "ds_read_b32 v152, v224\n"
        "v_perm_b32 v225, v212, v236, s61\n"
        "v_perm_b32 v226, v212, v236, s62\n"
        "v_mfma_f32_16x16x32_bf16 v[40:43], v[132:135], v[196:199], v[40:43]\n"
        "ds_read_b32 v153, v225\n"
        "ds_read_b32 v154, v226\n"
        "v_perm_b32 v227, v212, v236, s63\n"
        "v_mfma_f32_16x16x32_bf16 v[56:59], v[128:131], v[200:203], 0\n"
        "ds_read_b32 v155, v227\n"
        "v_perm_b32 v224, v213, v236, s60\n"
        "v_perm_b32 v225, v213, v236, s61\n"
        "v_mfma_f32_16x16x32_bf16 v[56:59], v[132:135], v[204:207], v[56:59]\n"
        "ds_read_b32 v156, v224\n"
        "ds_read_b32 v157, v225\n"
        "v_perm_b32 v226, v213, v236, s62\n"
        "s_waitcnt lgkmcnt(15)\n"
        "v_mfma_f32_16x16x32_bf16 v[12:15], v[136:139], v[176:179], 0\n"
        "ds_read_b32 v158, v226\n"
        "v_perm_b32 v227, v213, v236, s63\n"
        "s_waitcnt lgkmcnt(15)\n"
        "v_mfma_f32_16x16x32_bf16 v[12:15], v[140:143], v[180:183], v[12:15]\n"
        "ds_read_b32 v159, v227\n"
        "v_perm_b32 v224, v216, v236, s60\n"
        "v_mfma_f32_16x16x32_bf16 v[28:31], v[136:139], v[184:187], 0\n"
        "ds_read_b32 v160, v224\n"
        "v_perm_b32 v225, v216, v236, s61\n"
        "v_mfma_f32_16x16x32_bf16 v[28:31], v[140:143], v[188:191], v[28:31]\n"
        "ds_read_b32 v161, v225\n"
        "v_perm_b32 v226, v216, v236, s62\n"
        "v_mfma_f32_16x16x32_bf16 v[44:47], v[136:139], v[192:195], 0\n"
        "ds_read_b32 v162, v226\n"
        "v_perm_b32 v227, v216, v236, s63\n"
        "v_mfma_f32_16x16x32_bf16 v[44:47], v[140:143], v[196:199], v[44:47]\n"
        "ds_read_b32 v163, v227\n"
        "v_perm_b32 v224, v217, v236, s60\n"
        "v_mfma_f32_16x16x32_bf16 v[60:63], v[136:139], v[200:203], 0\n"
        "ds_read_b32 v164, v224\n"
        "v_perm_b32 v225, v217, v236, s61\n"
        "v_mfma_f32_16x16x32_bf16 v[60:63], v[140:143], v[204:207], v[60:63]\n"
        "ds_read_b32 v165, v225\n"
        "v_mfma_f32_16x16x32_bf16 v[64:67], v[112:115], v[80:83], 0\n"
        "ds_read_b128 v[200:203], v230 offset:22528\n"
        "v_mfma_f32_16x16x32_bf16 v[64:67], v[116:119], v[84:87], v[64:67]\n"
        "ds_read_b128 v[204:207], v231 offset:22528\n"
        "v_perm_b32 v226, v217, v236, s62\n"
        "v_mfma_f32_16x16x32_bf16 v[68:71], v[120:123], v[80:83], 0\n"
        "ds_read_b32 v166, v226\n"
        "v_perm_b32 v227, v217, v236, s63\n"
        "v_mfma_f32_16x16x32_bf16 v[68:71], v[124:127], v[84:87], v[68:71]\n"
        "ds_read_b32 v167, v227\n"
        "v_perm_b32 v224, v220, v236, s60\n"
        "v_mfma_f32_16x16x32_bf16 v[72:75], v[128:131], v[80:83], 0\n"
        "ds_read_b32 v168, v224\n"
        "v_perm_b32 v225, v220, v236, s61\n"
        "v_mfma_f32_16x16x32_bf16 v[72:75], v[132:135], v[84:87], v[72:75]\n"
        "ds_read_b32 v169, v225\n"
        "v_perm_b32 v226, v220, v236, s62\n"
        "v_mfma_f32_16x16x32_bf16 v[76:79], v[112:115], v[88:91], 0\n"
        "ds_read_b32 v170, v226\n"
        "v_perm_b32 v227, v220, v236, s63\n"
        "v_mfma_f32_16x16x32_bf16 v[76:79], v[116:119], v[92:95], v[76:79]\n"
        "ds_read_b32 v171, v227\n"
        "v_perm_b32 v224, v221, v236, s60\n"
        "v_mfma_f32_16x16x32_bf16 v[80:83], v[120:123], v[88:91], 0\n"
        "ds_read_b32 v172, v224\n"
        "v_perm_b32 v225, v221, v236, s61\n"
        "v_mfma_f32_16x16x32_bf16 v[80:83], v[124:127], v[92:95], v[80:83]\n"
        "ds_read_b32 v173, v225\n"
        "v_perm_b32 v226, v221, v236, s62\n"
        "v_mfma_f32_16x16x32_bf16 v[84:87], v[128:131], v[88:91], 0\n"
        "ds_read_b32 v174, v226\n"
        "v_perm_b32 v227, v221, v236, s63\n"
        "v_mfma_f32_16x16x32_bf16 v[84:87], v[132:135], v[92:95], v[84:87]\n"
        "ds_read_b32 v175, v227\n"
        "s_waitcnt lgkmcnt(10)\n"
        "s_barrier\n"
        "v_mfma_f32_16x16x32_bf16 v[88:91], v[112:115], v[100:103], 0\n"
        "ds_read_b128 v[208:211], v232 offset:16384\n"
        "v_mfma_f32_16x16x32_bf16 v[88:91], v[116:119], v[104:107], v[88:91]\n"
        "ds_read_b128 v[212:215], v233 offset:16384\n"
        "v_mfma_f32_16x16x32_bf16 v[92:95], v[120:123], v[100:103], 0\n"
        "v_mfma_f32_16x16x32_bf16 v[92:95], v[124:127], v[104:107], v[92:95]\n"
        "ds_read_b128 v[216:219], v234 offset:16384\n"
        "v_mfma_f32_16x16x32_bf16 v[96:99], v[128:131], v[100:103], 0\n"
        "ds_read_b128 v[220:223], v235 offset:16384\n"
        "v_mfma_f32_16x16x32_bf16 v[96:99], v[132:135], v[104:107], v[96:99]\n"
        "v_mfma_f32_16x16x32_bf16 v[100:103], v[112:115], v[200:203], 0\n"
        "ds_read_b128 v[176:179], v228 offset:32768\n"
        "v_mfma_f32_16x16x32_bf16 v[100:103], v[116:119], v[204:207], v[100:103]\n"
        "ds_read_b128 v[180:183], v229 offset:32768\n"
        "v_mfma_f32_16x16x32_bf16 v[104:107], v[120:123], v[200:203], 0\n"
        "v_mfma_f32_16x16x32_bf16 v[104:107], v[124:127], v[204:207], v[104:107]\n"
        "ds_read_b128 v[184:187], v228 offset:34816\n"
        "v_mfma_f32_16x16x32_bf16 v[108:111], v[128:131], v[200:203], 0\n"
        "ds_read_b128 v[188:191], v229 offset:34816\n"
        "v_mfma_f32_16x16x32_bf16 v[108:111], v[132:135], v[204:207], v[108:111]\n"
        "s_waitcnt lgkmcnt(2)\n"
        "v_mfma_f32_16x16x32_bf16 v[0:3], v[144:147], v[176:179], v[0:3]\n"
        "ds_read_b128 v[192:195], v228 offset:36864\n"
        "v_bitop3_b32 v210, v210, v214, s34 bitop3:0x7e\n"
        "v_mfma_f32_16x16x32_bf16 v[0:3], v[148:151], v[180:183], v[0:3]\n"
        "ds_read_b128 v[196:199], v229 offset:36864\n"
        "v_bitop3_b32 v211, v211, v215, s39 bitop3:0x7e\n"
        "v_mfma_f32_16x16x32_bf16 v[4:7], v[156:159], v[180:183], v[4:7]\n"
        "ds_read_b128 v[200:203], v228 offset:38912\n"
        "v_bitop3_b32 v218, v218, v222, s34 bitop3:0x7e\n"
        "v_mfma_f32_16x16x32_bf16 v[4:7], v[152:155], v[176:179], v[4:7]\n"
        "ds_read_b128 v[204:207], v229 offset:38912\n"
        "v_perm_b32 v224, v208, v236, s60\n"
        "v_mfma_f32_16x16x32_bf16 v[8:11], v[160:163], v[176:179], v[8:11]\n"
        "ds_read_b32 v112, v224\n"
        "v_perm_b32 v225, v208, v236, s61\n"
        "v_mfma_f32_16x16x32_bf16 v[8:11], v[164:167], v[180:183], v[8:11]\n"
        "ds_read_b32 v113, v225\n"
        "v_perm_b32 v226, v208, v236, s62\n"
        "v_mfma_f32_16x16x32_bf16 v[12:15], v[172:175], v[180:183], v[12:15]\n"
        "ds_read_b32 v114, v226\n"
        "v_perm_b32 v227, v208, v236, s63\n"
        "v_mfma_f32_16x16x32_bf16 v[12:15], v[168:171], v[176:179], v[12:15]\n"
        "ds_read_b32 v115, v227\n"
        "v_bitop3_b32 v219, v219, v223, s39 bitop3:0x7e\n"
        "s_waitcnt lgkmcnt(8)\n"
        "v_mfma_f32_16x16x32_bf16 v[28:31], v[168:171], v[184:187], v[28:31]\n"
        "ds_read_b128 v[176:179], v230 offset:32768\n"
        "v_or3_b32 v210, v210, v211, v218\n"
        "v_mfma_f32_16x16x32_bf16 v[28:31], v[172:175], v[188:191], v[28:31]\n"
        "ds_read_b128 v[180:183], v231 offset:32768\n"
        "v_perm_b32 v224, v209, v236, s60\n"
        "v_mfma_f32_16x16x32_bf16 v[24:27], v[164:167], v[188:191], v[24:27]\n"
        "ds_read_b32 v116, v224\n"
        "v_perm_b32 v225, v209, v236, s61\n"
        "v_mfma_f32_16x16x32_bf16 v[24:27], v[160:163], v[184:187], v[24:27]\n"
        "ds_read_b32 v117, v225\n"
        "v_perm_b32 v226, v209, v236, s62\n"
        "v_mfma_f32_16x16x32_bf16 v[20:23], v[152:155], v[184:187], v[20:23]\n"
        "ds_read_b32 v118, v226\n"
        "v_perm_b32 v227, v209, v236, s63\n"
        "v_mfma_f32_16x16x32_bf16 v[20:23], v[156:159], v[188:191], v[20:23]\n"
        "ds_read_b32 v119, v227\n"
        "v_perm_b32 v224, v212, v236, s60\n"
        "v_mfma_f32_16x16x32_bf16 v[16:19], v[148:151], v[188:191], v[16:19]\n"
        "ds_read_b32 v120, v224\n"
        "v_perm_b32 v225, v212, v236, s61\n"
        "v_mfma_f32_16x16x32_bf16 v[16:19], v[144:147], v[184:187], v[16:19]\n"
        "ds_read_b32 v121, v225\n"
        "v_or_b32 v210, v210, v219\n"
        "s_waitcnt lgkmcnt(14)\n"
        "v_mfma_f32_16x16x32_bf16 v[32:35], v[144:147], v[192:195], v[32:35]\n"
        "ds_read_b128 v[184:187], v230 offset:34816\n"
        "v_cmp_ne_u32 vcc, 0, v210\n"
        "v_mfma_f32_16x16x32_bf16 v[32:35], v[148:151], v[196:199], v[32:35]\n"
        "ds_read_b128 v[188:191], v231 offset:34816\n"
        "v_perm_b32 v226, v212, v236, s62\n"
        "v_mfma_f32_16x16x32_bf16 v[36:39], v[156:159], v[196:199], v[36:39]\n"
        "ds_read_b32 v122, v226\n"
        "v_perm_b32 v227, v212, v236, s63\n"
        "v_mfma_f32_16x16x32_bf16 v[36:39], v[152:155], v[192:195], v[36:39]\n"
        "ds_read_b32 v123, v227\n"
        "v_perm_b32 v224, v213, v236, s60\n"
        "v_mfma_f32_16x16x32_bf16 v[40:43], v[160:163], v[192:195], v[40:43]\n"
        "ds_read_b32 v124, v224\n"
        "v_perm_b32 v225, v213, v236, s61\n"
        "v_mfma_f32_16x16x32_bf16 v[40:43], v[164:167], v[196:199], v[40:43]\n"
        "ds_read_b32 v125, v225\n"
        "v_perm_b32 v226, v213, v236, s62\n"
        "v_mfma_f32_16x16x32_bf16 v[44:47], v[172:175], v[196:199], v[44:47]\n"
        "ds_read_b32 v126, v226\n"
        "v_perm_b32 v227, v213, v236, s63\n"
        "v_mfma_f32_16x16x32_bf16 v[44:47], v[168:171], v[192:195], v[44:47]\n"
        "ds_read_b32 v127, v227\n"
        "s_waitcnt lgkmcnt(15)\n"
        "v_mfma_f32_16x16x32_bf16 v[60:63], v[168:171], v[200:203], v[60:63]\n"
        "ds_read_b128 v[192:195], v230 offset:36864\n"
        "s_cbranch_vccnz .Ln_f2\n"
        ".Ln_b2:\n"
        "v_mfma_f32_16x16x32_bf16 v[60:63], v[172:175], v[204:207], v[60:63]\n"
        "ds_read_b128 v[196:199], v231 offset:36864\n"
        "v_perm_b32 v224, v216, v236, s60\n"
        "v_mfma_f32_16x16x32_bf16 v[56:59], v[164:167], v[204:207], v[56:59]\n"
        "ds_read_b32 v128, v224\n"
        "v_perm_b32 v225, v216, v236, s61\n"
        "v_mfma_f32_16x16x32_bf16 v[56:59], v[160:163], v[200:203], v[56:59]\n"
        "ds_read_b32 v129, v225\n"
        "v_perm_b32 v226, v216, v236, s62\n"
        "v_mfma_f32_16x16x32_bf16 v[52:55], v[152:155], v[200:203], v[52:55]\n"
        "ds_read_b32 v130, v226\n"
        "v_perm_b32 v227, v216, v236, s63\n"
        "v_mfma_f32_16x16x32_bf16 v[52:55], v[156:159], v[204:207], v[52:55]\n"
        "ds_read_b32 v131, v227\n"
        "v_perm_b32 v224, v217, v236, s60\n"
        "v_mfma_f32_16x16x32_bf16 v[48:51], v[148:151], v[204:207], v[48:51]\n"
        "ds_read_b32 v132, v224\n"
        "v_perm_b32 v225, v217, v236, s61\n"
        "v_mfma_f32_16x16x32_bf16 v[48:51], v[144:147], v[200:203], v[48:51]\n"
        "ds_read_b32 v133, v225\n"
        "s_waitcnt lgkmcnt(15)\n"
        "v_mfma_f32_16x16x32_bf16 v[64:67], v[144:147], v[176:179], v[64:67]\n"
        "ds_read_b128 v[200:203], v230 offset:38912\n"
        "v_mfma_f32_16x16x32_bf16 v[64:67], v[148:151], v[180:183], v[64:67]\n"
        "ds_read_b128 v[204:207], v231 offset:38912\n"
        "v_perm_b32 v226, v217, v236, s62\n"
        "v_mfma_f32_16x16x32_bf16 v[68:71], v[156:159], v[180:183], v[68:71]\n"
        "ds_read_b32 v134, v226\n"
        "v_perm_b32 v227, v217, v236, s63\n"
        "v_mfma_f32_16x16x32_bf16 v[68:71], v[152:155], v[176:179], v[68:71]\n"
        "ds_read_b32 v135, v227\n"
        "v_perm_b32 v224, v220, v236, s60\n"
        "v_mfma_f32_16x16x32_bf16 v[72:75], v[160:163], v[176:179], v[72:75]\n"
        "ds_read_b32 v136, v224\n"
        "v_perm_b32 v225, v220, v236, s61\n"
        "v_mfma_f32_16x16x32_bf16 v[72:75], v[164:167], v[180:183], v[72:75]\n"
        "ds_read_b32 v137, v225\n"
        "v_perm_b32 v226, v220, v236, s62\n"
        "s_waitcnt lgkmcnt(15)\n"
        "v_mfma_f32_16x16x32_bf16 v[84:87], v[164:167], v[188:191], v[84:87]\n"
        "ds_read_b32 v138, v226\n"
        "v_perm_b32 v227, v220, v236, s63\n"
        "v_mfma_f32_16x16x32_bf16 v[84:87], v[160:163], v[184:187], v[84:87]\n"
        "ds_read_b32 v139, v227\n"
        "v_perm_b32 v224, v221, v236, s60\n"
        "v_mfma_f32_16x16x32_bf16 v[80:83], v[152:155], v[184:187], v[80:83]\n"
        "ds_read_b32 v140, v224\n"
        "v_perm_b32 v225, v221, v236, s61\n"
        "v_mfma_f32_16x16x32_bf16 v[80:83], v[156:159], v[188:191], v[80:83]\n"
        "ds_read_b32 v141, v225\n"
        "v_perm_b32 v226, v221, v236, s62\n"
        "v_mfma_f32_16x16x32_bf16 v[76:79], v[148:151], v[188:191], v[76:79]\n"
        "ds_read_b32 v142, v226\n"
        "v_perm_b32 v227, v221, v236, s63\n"
        "v_mfma_f32_16x16x32_bf16 v[76:79], v[144:147], v[184:187], v[76:79]\n"
        "ds_read_b32 v143, v227\n"
        "s_waitcnt lgkmcnt(10)\n"
        "s_barrier\n"
        "v_mfma_f32_16x16x32_bf16 v[88:91], v[144:147], v[192:195], v[88:91]\n"
        "ds_read_b128 v[208:211], v232 offset:32768\n"
        "v_mfma_f32_16x16x32_bf16 v[88:91], v[148:151], v[196:199], v[88:91]\n"
        "ds_read_b128 v[212:215], v233 offset:32768\n"
        "v_mfma_f32_16x16x32_bf16 v[92:95], v[156:159], v[196:199], v[92:95]\n"
        "v_mfma_f32_16x16x32_bf16 v[92:95], v[152:155], v[192:195], v[92:95]\n"
        "ds_read_b128 v[216:219], v234 offset:32768\n"
        "v_mfma_f32_16x16x32_bf16 v[96:99], v[160:163], v[192:195], v[96:99]\n"
        "ds_read_b128 v[220:223], v235 offset:32768\n"
        "v_mfma_f32_16x16x32_bf16 v[96:99], v[164:167], v[196:199], v[96:99]\n"
        "v_mfma_f32_16x16x32_bf16 v[108:111], v[164:167], v[204:207], v[108:111]\n"
        "ds_read_b128 v[176:179], v228 offset:0\n"
        "v_mfma_f32_16x16x32_bf16 v[108:111], v[160:163], v[200:203], v[108:111]\n"
        "ds_read_b128 v[180:183], v229 offset:0\n"
        "v_mfma_f32_16x16x32_bf16 v[104:107], v[152:155], v[200:203], v[104:107]\n"
        "v_mfma_f32_16x16x32_bf16 v[104:107], v[156:159], v[204:207], v[104:107]\n"
        "ds_read_b128 v[184:187], v228 offset:2048\n"
        "v_mfma_f32_16x16x32_bf16 v[100:103], v[148:151], v[204:207], v[100:103]\n"
        "ds_read_b128 v[188:191], v229 offset:2048\n"
        "v_mfma_f32_16x16x32_bf16 v[100:103], v[144:147], v[200:203], v[100:103]\n"
        "s_waitcnt lgkmcnt(2)\n"
        "v_mfma_f32_16x16x32_bf16 v[0:3], v[112:115], v[176:179], v[0:3]\n"
        "ds_read_b128 v[192:195], v228 offset:4096\n"
        "v_bitop3_b32 v210, v210, v214, s34 bitop3:0x7e\n"
        "v_mfma_f32_16x16x32_bf16 v[0:3], v[116:119], v[180:183], v[0:3]\n"
        "ds_read_b128 v[196:199], v229 offset:4096\n"
        "v_bitop3_b32 v211, v211, v215, s39 bitop3:0x7e\n"
        "v_mfma_f32_16x16x32_bf16 v[4:7], v[124:127], v[180:183], v[4:7]\n"
        "ds_read_b128 v[200:203], v228 offset:6144\n"
        "v_bitop3_b32 v218, v218, v222, s34 bitop3:0x7e\n"
        "v_mfma_f32_16x16x32_bf16 v[4:7], v[120:123], v[176:179], v[4:7]\n"
        "ds_read_b128 v[204:207], v229 offset:6144\n"
        "v_perm_b32 v224, v208, v236, s60\n"
        "v_mfma_f32_16x16x32_bf16 v[8:11], v[128:131], v[176:179], v[8:11]\n"
        "ds_read_b32 v144, v224\n"
        "v_perm_b32 v225, v208, v236, s61\n"
        "v_mfma_f32_16x16x32_bf16 v[8:11], v[132:135], v[180:183], v[8:11]\n"
        "ds_read_b32 v145, v225\n"
        "v_perm_b32 v226, v208, v236, s62\n"
        "v_mfma_f32_16x16x32_bf16 v[12:15], v[140:143], v[180:183], v[12:15]\n"
        "ds_read_b32 v146, v226\n"
        "v_perm_b32 v227, v208, v236, s63\n"
        "v_mfma_f32_16x16x32_bf16 v[12:15], v[136:139], v[176:179], v[12:15]\n"
        "ds_read_b32 v147, v227\n"
        "v_bitop3_b32 v219, v219, v223, s39 bitop3:0x7e\n"
        "s_waitcnt lgkmcnt(8)\n"
        "v_mfma_f32_16x16x32_bf16 v[28:31], v[136:139], v[184:187], v[28:31]\n"
        "ds_read_b128 v[176:179], v230 offset:0\n"
        "v_or3_b32 v210, v210, v211, v218\n"
        "v_mfma_f32_16x16x32_bf16 v[28:31], v[140:143], v[188:191], v[28:31]\n"
        "ds_read_b128 v[180:183], v231 offset:0\n"
        "v_perm_b32 v224, v209, v236, s60\n"
        "v_mfma_f32_16x16x32_bf16 v[24:27], v[132:135], v[188:191], v[24:27]\n"
        "ds_read_b32 v148, v224\n"
        "v_perm_b32 v225, v209, v236, s61\n"
        "v_mfma_f32_16x16x32_bf16 v[24:27], v[128:131], v[184:187], v[24:27]\n"
        "ds_read_b32 v149, v225\n"
        "v_perm_b32 v226, v209, v236, s62\n"
        "v_mfma_f32_16x16x32_bf16 v[20:23], v[120:123], v[184:187], v[20:23]\n"
        "ds_read_b32 v150, v226\n"
        "v_perm_b32 v227, v209, v236, s63\n"
        "v_mfma_f32_16x16x32_bf16 v[20:23], v[124:127], v[188:191], v[20:23]\n"
        "ds_read_b32 v151, v227\n"
        "v_perm_b32 v224, v212, v236, s60\n"
        "v_mfma_f32_16x16x32_bf16 v[16:19], v[116:119], v[188:191], v[16:19]\n"
        "ds_read_b32 v152, v224\n"
        "v_perm_b32 v225, v212, v236, s61\n"
        "v_mfma_f32_16x16x32_bf16 v[16:19], v[112:115], v[184:187], v[16:19]\n"
        "ds_read_b32 v153, v225\n"
        "v_or_b32 v210, v210, v219\n"
        "s_waitcnt lgkmcnt(14)\n"
        "v_mfma_f32_16x16x32_bf16 v[32:35], v[112:115], v[192:195], v[32:35]\n"
        "ds_read_b128 v[184:187], v230 offset:2048\n"
        "v_cmp_ne_u32 vcc, 0, v210\n"
        "v_mfma_f32_16x16x32_bf16 v[32:35], v[116:119], v[196:199], v[32:35]\n"
        "ds_read_b128 v[188:191], v231 offset:2048\n"
        "v_perm_b32 v226, v212, v236, s62\n"
        "v_mfma_f32_16x16x32_bf16 v[36:39], v[124:127], v[196:199], v[36:39]\n"
        "ds_read_b32 v154, v226\n"
        "v_perm_b32 v227, v212, v236, s63\n"
        "v_mfma_f32_16x16x32_bf16 v[36:39], v[120:123], v[192:195], v[36:39]\n"
        "ds_read_b32 v155, v227\n"
        "v_perm_b32 v224, v213, v236, s60\n"
        "v_mfma_f32_16x16x32_bf16 v[40:43], v[128:131], v[192:195], v[40:43]\n"
        "ds_read_b32 v156, v224\n"
        "v_perm_b32 v225, v213, v236, s61\n"
        "v_mfma_f32_16x16x32_bf16 v[40:43], v[132:135], v[196:199], v[40:43]\n"
        "ds_read_b32 v157, v225\n"
        "v_perm_b32 v226, v213, v236, s62\n"
        "v_mfma_f32_16x16x32_bf16 v[44:47], v[140:143], v[196:199], v[44:47]\n"
        "ds_read_b32 v158, v226\n"
        "v_perm_b32 v227, v213, v236, s63\n"
        "v_mfma_f32_16x16x32_bf16 v[44:47], v[136:139], v[192:195], v[44:47]\n"
        "ds_read_b32 v159, v227\n"
        "s_waitcnt lgkmcnt(15)\n"
        "v_mfma_f32_16x16x32_bf16 v[60:63], v[136:139], v[200:203], v[60:63]\n"
        "ds_read_b128 v[192:195], v230 offset:4096\n"
        "s_cbranch_vccnz .Ln_f3\n"
        ".Ln_b3:\n"
        "v_mfma_f32_16x16x32_bf16 v[60:63], v[140:143], v[204:207], v[60:63]\n"
        "ds_read_b128 v[196:199], v231 offset:4096\n"
        "v_perm_b32 v224, v216, v236, s60\n"
        "v_mfma_f32_16x16x32_bf16 v[56:59], v[132:135], v[204:207], v[56:59]\n"
        "ds_read_b32 v160, v224\n"
        "v_perm_b32 v225, v216, v236, s61\n"
        "v_mfma_f32_16x16x32_bf16 v[56:59], v[128:131], v[200:203], v[56:59]\n"
        "ds_read_b32 v161, v225\n"
        "v_perm_b32 v226, v216, v236, s62\n"
        "v_mfma_f32_16x16x32_bf16 v[52:55], v[120:123], v[200:203], v[52:55]\n"
        "ds_read_b32 v162, v226\n"
        "v_perm_b32 v227, v216, v236, s63\n"
        "v_mfma_f32_16x16x32_bf16 v[52:55], v[124:127], v[204:207], v[52:55]\n"
        "ds_read_b32 v163, v227\n"
        "v_perm_b32 v224, v217, v236, s60\n"
        "v_mfma_f32_16x16x32_bf16 v[48:51], v[116:119], v[204:207], v[48:51]\n"
        "ds_read_b32 v164, v224\n"
        "v_perm_b32 v225, v217, v236, s61\n"
        "v_mfma_f32_16x16x32_bf16 v[48:51], v[112:115], v[200:203], v[48:51]\n"
        "ds_read_b32 v165, v225\n"
        "s_waitcnt lgkmcnt(15)\n"
        "v_mfma_f32_16x16x32_bf16 v[64:67], v[112:115], v[176:179], v[64:67]\n"
        "ds_read_b128 v[200:203], v230 offset:6144\n"
        "v_mfma_f32_16x16x32_bf16 v[64:67], v[116:119], v[180:183], v[64:67]\n"
        "ds_read_b128 v[204:207], v231 offset:6144\n"
        "v_perm_b32 v226, v217, v236, s62\n"
        "v_mfma_f32_16x16x32_bf16 v[68:71], v[124:127], v[180:183], v[68:71]\n"
        "ds_read_b32 v166, v226\n"
        "v_perm_b32 v227, v217, v236, s63\n"
        "v_mfma_f32_16x16x32_bf16 v[68:71], v[120:123], v[176:179], v[68:71]\n"
        "ds_read_b32 v167, v227\n"
        "v_perm_b32 v224, v220, v236, s60\n"
        "v_mfma_f32_16x16x32_bf16 v[72:75], v[128:131], v[176:179], v[72:75]\n"
        "ds_read_b32 v168, v224\n"
        "v_perm_b32 v225, v220, v236, s61\n"
        "v_mfma_f32_16x16x32_bf16 v[72:75], v[132:135], v[180:183], v[72:75]\n"
        "ds_read_b32 v169, v225\n"
        "v_perm_b32 v226, v220, v236, s62\n"
        "s_waitcnt lgkmcnt(15)\n"
        "v_mfma_f32_16x16x32_bf16 v[84:87], v[132:135], v[188:191], v[84:87]\n"
        "ds_read_b32 v170, v226\n"
        "v_perm_b32 v227, v220, v236, s63\n"
        "v_mfma_f32_16x16x32_bf16 v[84:87], v[128:131], v[184:187], v[84:87]\n"
        "ds_read_b32 v171, v227\n"
        "v_perm_b32 v224, v221, v236, s60\n"
        "v_mfma_f32_16x16x32_bf16 v[80:83], v[120:123], v[184:187], v[80:83]\n"
        "ds_read_b32 v172, v224\n"
        "v_perm_b32 v225, v221, v236, s61\n"
        "v_mfma_f32_16x16x32_bf16 v[80:83], v[124:127], v[188:191], v[80:83]\n"
        "ds_read_b32 v173, v225\n"
        "v_perm_b32 v226, v221, v236, s62\n"
        "v_mfma_f32_16x16x32_bf16 v[76:79], v[116:119], v[188:191], v[76:79]\n"
        "ds_read_b32 v174, v226\n"
        "v_perm_b32 v227, v221, v236, s63\n"
        "v_mfma_f32_16x16x32_bf16 v[76:79], v[112:115], v[184:187], v[76:79]\n"
        "ds_read_b32 v175, v227\n"
        "s_waitcnt lgkmcnt(10)\n"
        "s_barrier\n"
        "v_mfma_f32_16x16x32_bf16 v[88:91], v[112:115], v[192:195], v[88:91]\n"
        "ds_read_b128 v[208:211], v232 offset:0\n"
        "v_mfma_f32_16x16x32_bf16 v[88:91], v[116:119], v[196:199], v[88:91]\n"
        "ds_read_b128 v[212:215], v233 offset:0\n"
        "v_mfma_f32_16x16x32_bf16 v[92:95], v[124:127], v[196:199], v[92:95]\n"
        "v_mfma_f32_16x16x32_bf16 v[92:95], v[120:123], v[192:195], v[92:95]\n"
        "ds_read_b128 v[216:219], v234 offset:0\n"
        ".Ln_tp0:\n"
        "s_cbranch_execz .Ln_ret\n"
        "v_mfma_f32_16x16x32_bf16 v[96:99], v[128:131], v[192:195], v[96:99]\n"
        "ds_read_b128 v[220:223], v235 offset:0\n"
        "v_mfma_f32_16x16x32_bf16 v[96:99], v[132:135], v[196:199], v[96:99]\n"
        "v_mfma_f32_16x16x32_bf16 v[108:111], v[132:135], v[204:207], v[108:111]\n"
        "ds_read_b128 v[176:179], v228 offset:16384\n"
        "v_mfma_f32_16x16x32_bf16 v[108:111], v[128:131], v[200:203], v[108:111]\n"
        "ds_read_b128 v[180:183], v229 offset:16384\n"
        "v_mfma_f32_16x16x32_bf16 v[104:107], v[120:123], v[200:203], v[104:107]\n"
        "v_mfma_f32_16x16x32_bf16 v[104:107], v[124:127], v[204:207], v[104:107]\n"
        "ds_read_b128 v[184:187], v228 offset:18432\n"
        "v_mfma_f32_16x16x32_bf16 v[100:103], v[116:119], v[204:207], v[100:103]\n"
        "ds_read_b128 v[188:191], v229 offset:18432\n"
        "v_mfma_f32_16x16x32_bf16 v[100:103], v[112:115], v[200:203], v[100:103]\n"
        "s_waitcnt lgkmcnt(2)\n"
        "v_mfma_f32_16x16x32_bf16 v[0:3], v[144:147], v[176:179], v[0:3]\n"
        "ds_read_b128 v[192:195], v228 offset:20480\n"
        "v_bitop3_b32 v210, v210, v214, s34 bitop3:0x7e\n"
        "v_mfma_f32_16x16x32_bf16 v[0:3], v[148:151], v[180:183], v[0:3]\n"
        "ds_read_b128 v[196:199], v229 offset:20480\n"
        "v_bitop3_b32 v211, v211, v215, s39 bitop3:0x7e\n"
        "v_mfma_f32_16x16x32_bf16 v[4:7], v[156:159], v[180:183], v[4:7]\n"
        "ds_read_b128 v[200:203], v228 offset:22528\n"
        "v_bitop3_b32 v218, v218, v222, s34 bitop3:0x7e\n"
        "v_mfma_f32_16x16x32_bf16 v[4:7], v[152:155], v[176:179], v[4:7]\n"
        "ds_read_b128 v[204:207], v229 offset:22528\n"
        "v_perm_b32 v224, v208, v236, s60\n"
        "v_mfma_f32_16x16x32_bf16 v[8:11], v[160:163], v[176:179], v[8:11]\n"
        "ds_read_b32 v112, v224\n"
        "v_perm_b32 v225, v208, v236, s61\n"
        "v_mfma_f32_16x16x32_bf16 v[8:11], v[164:167], v[180:183], v[8:11]\n"
        "ds_read_b32 v113, v225\n"
        "v_perm_b32 v226, v208, v236, s62\n"
        "v_mfma_f32_16x16x32_bf16 v[12:15], v[172:175], v[180:183], v[12:15]\n"
        "ds_read_b32 v114, v226\n"
        "v_perm_b32 v227, v208, v236, s63\n"
        "v_mfma_f32_16x16x32_bf16 v[12:15], v[168:171], v[176:179], v[12:15]\n"
        "ds_read_b32 v115, v227\n"
        "v_bitop3_b32 v219, v219, v223, s39 bitop3:0x7e\n"
        "s_waitcnt lgkmcnt(8)\n"
        "v_mfma_f32_16x16x32_bf16 v[28:31], v[168:171], v[184:187], v[28:31]\n"
        "ds_read_b128 v[176:179], v230 offset:16384\n"
        "v_or3_b32 v210, v210, v211, v218\n"
        "v_mfma_f32_16x16x32_bf16 v[28:31], v[172:175], v[188:191], v[28:31]\n"
        "ds_read_b128 v[180:183], v231 offset:16384\n"
        "v_perm_b32 v224, v209, v236, s60\n"
        "v_mfma_f32_16x16x32_bf16 v[24:27], v[164:167], v[188:191], v[24:27]\n"
        "ds_read_b32 v116, v224\n"
        "v_perm_b32 v225, v209, v236, s61\n"
        "v_mfma_f32_16x16x32_bf16 v[24:27], v[160:163], v[184:187], v[24:27]\n"
        "ds_read_b32 v117, v225\n"
        "v_perm_b32 v226, v209, v236, s62\n"
        "v_mfma_f32_16x16x32_bf16 v[20:23], v[152:155], v[184:187], v[20:23]\n"
        "ds_read_b32 v118, v226\n"
        "v_perm_b32 v227, v209, v236, s63\n"
        "v_mfma_f32_16x16x32_bf16 v[20:23], v[156:159], v[188:191], v[20:23]\n"
        "ds_read_b32 v119, v227\n"
        "v_perm_b32 v224, v212, v236, s60\n"
        "v_mfma_f32_16x16x32_bf16 v[16:19], v[148:151], v[188:191], v[16:19]\n"
        "ds_read_b32 v120, v224\n"
        "v_perm_b32 v225, v212, v236, s61\n"
        "v_mfma_f32_16x16x32_bf16 v[16:19], v[144:147], v[184:187], v[16:19]\n"
        "ds_read_b32 v121, v225\n"
        "v_or_b32 v210, v210, v219\n"
        "s_waitcnt lgkmcnt(14)\n"
        "v_mfma_f32_16x16x32_bf16 v[32:35], v[144:147], v[192:195], v[32:35]\n"
        "ds_read_b128 v[184:187], v230 offset:18432\n"
        "v_cmp_ne_u32 vcc, 0, v210\n"
        "v_mfma_f32_16x16x32_bf16 v[32:35], v[148:151], v[196:199], v[32:35]\n"
        "ds_read_b128 v[188:191], v231 offset:18432\n"
        "v_perm_b32 v226, v212, v236, s62\n"
        "v_mfma_f32_16x16x32_bf16 v[36:39], v[156:159], v[196:199], v[36:39]\n"
        "ds_read_b32 v122, v226\n"
        "v_perm_b32 v227, v212, v236, s63\n"
        "v_mfma_f32_16x16x32_bf16 v[36:39], v[152:155], v[192:195], v[36:39]\n"
        "ds_read_b32 v123, v227\n"
        "v_perm_b32 v224, v213, v236, s60\n"
        "v_mfma_f32_16x16x32_bf16 v[40:43], v[160:163], v[192:195], v[40:43]\n"
        "ds_read_b32 v124, v224\n"
        "v_perm_b32 v225, v213, v236, s61\n"
        "v_mfma_f32_16x16x32_bf16 v[40:43], v[164:167], v[196:199], v[40:43]\n"
        "ds_read_b32 v125, v225\n"
        "v_perm_b32 v226, v213, v236, s62\n"
        "v_mfma_f32_16x16x32_bf16 v[44:47], v[172:175], v[196:199], v[44:47]\n"
        "ds_read_b32 v126, v226\n"
        "v_perm_b32 v227, v213, v236, s63\n"
        "v_mfma_f32_16x16x32_bf16 v[44:47], v[168:171], v[192:195], v[44:47]\n"
        "ds_read_b32 v127, v227\n"
        "s_waitcnt lgkmcnt(15)\n"
        "v_mfma_f32_16x16x32_bf16 v[60:63], v[168:171], v[200:203], v[60:63]\n"
        "ds_read_b128 v[192:195], v230 offset:20480\n"
        "s_cbranch_vccnz .Ln_f4\n"
        ".Ln_b4:\n"
        "v_mfma_f32_16x16x32_bf16 v[60:63], v[172:175], v[204:207], v[60:63]\n"
        "ds_read_b128 v[196:199], v231 offset:20480\n"
        "v_perm_b32 v224, v216, v236, s60\n"
        "v_mfma_f32_16x16x32_bf16 v[56:59], v[164:167], v[204:207], v[56:59]\n"
        "ds_read_b32 v128, v224\n"
        "v_perm_b32 v225, v216, v236, s61\n"
        "v_mfma_f32_16x16x32_bf16 v[56:59], v[160:163], v[200:203], v[56:59]\n"
        "ds_read_b32 v129, v225\n"
        "v_perm_b32 v226, v216, v236, s62\n"
        "v_mfma_f32_16x16x32_bf16 v[52:55], v[152:155], v[200:203], v[52:55]\n"
        "ds_read_b32 v130, v226\n"
        "v_perm_b32 v227, v216, v236, s63\n"
        "v_mfma_f32_16x16x32_bf16 v[52:55], v[156:159], v[204:207], v[52:55]\n"
        "ds_read_b32 v131, v227\n"
        "v_perm_b32 v224, v217, v236, s60\n"
        "v_mfma_f32_16x16x32_bf16 v[48:51], v[148:151], v[204:207], v[48:51]\n"
        "ds_read_b32 v132, v224\n"
        "v_perm_b32 v225, v217, v236, s61\n"
        "v_mfma_f32_16x16x32_bf16 v[48:51], v[144:147], v[200:203], v[48:51]\n"
        "ds_read_b32 v133, v225\n"
        "s_waitcnt lgkmcnt(15)\n"
        "v_mfma_f32_16x16x32_bf16 v[64:67], v[144:147], v[176:179], v[64:67]\n"
        "ds_read_b128 v[200:203], v230 offset:22528\n"
        "v_mfma_f32_16x16x32_bf16 v[64:67], v[148:151], v[180:183], v[64:67]\n"
        "ds_read_b128 v[204:207], v231 offset:22528\n"
        "v_perm_b32 v226, v217, v236, s62\n"
        "v_mfma_f32_16x16x32_bf16 v[68:71], v[156:159], v[180:183], v[68:71]\n"
        "ds_read_b32 v134, v226\n"
        "v_perm_b32 v227, v217, v236, s63\n"
        "v_mfma_f32_16x16x32_bf16 v[68:71], v[152:155], v[176:179], v[68:71]\n"
        "ds_read_b32 v135, v227\n"
        "v_perm_b32 v224, v220, v236, s60\n"
        "v_mfma_f32_16x16x32_bf16 v[72:75], v[160:163], v[176:179], v[72:75]\n"
        "ds_read_b32 v136, v224\n"
        "v_perm_b32 v225, v220, v236, s61\n"
        "v_mfma_f32_16x16x32_bf16 v[72:75], v[164:167], v[180:183], v[72:75]\n"
        "ds_read_b32 v137, v225\n"
        "v_perm_b32 v226, v220, v236, s62\n"
        "s_waitcnt lgkmcnt(15)\n"
        "v_mfma_f32_16x16x32_bf16 v[84:87], v[164:167], v[188:191], v[84:87]\n"
        "ds_read_b32 v138, v226\n"
        "v_perm_b32 v227, v220, v236, s63\n"
        "v_mfma_f32_16x16x32_bf16 v[84:87], v[160:163], v[184:187], v[84:87]\n"
        "ds_read_b32 v139, v227\n"
        "v_perm_b32 v224, v221, v236, s60\n"
        "v_mfma_f32_16x16x32_bf16 v[80:83], v[152:155], v[184:187], v[80:83]\n"
        "ds_read_b32 v140, v224\n"
        "v_perm_b32 v225, v221, v236, s61\n"
        "v_mfma_f32_16x16x32_bf16 v[80:83], v[156:159], v[188:191], v[80:83]\n"
        "ds_read_b32 v141, v225\n"
        "v_perm_b32 v226, v221, v236, s62\n"
        "v_mfma_f32_16x16x32_bf16 v[76:79], v[148:151], v[188:191], v[76:79]\n"
        "ds_read_b32 v142, v226\n"
        "v_perm_b32 v227, v221, v236, s63\n"
        "v_mfma_f32_16x16x32_bf16 v[76:79], v[144:147], v[184:187], v[76:79]\n"
        "ds_read_b32 v143, v227\n"
        "s_waitcnt lgkmcnt(10)\n"
        "s_barrier\n"
        "v_mfma_f32_16x16x32_bf16 v[88:91], v[144:147], v[192:195], v[88:91]\n"
        "ds_read_b128 v[208:211], v232 offset:16384\n"
        "v_mfma_f32_16x16x32_bf16 v[88:91], v[148:151], v[196:199], v[88:91]\n"
        "ds_read_b128 v[212:215], v233 offset:16384\n"
        "v_mfma_f32_16x16x32_bf16 v[92:95], v[156:159], v[196:199], v[92:95]\n"
        "v_mfma_f32_16x16x32_bf16 v[92:95], v[152:155], v[192:195], v[92:95]\n"
        "ds_read_b128 v[216:219], v234 offset:16384\n"
        "v_mfma_f32_16x16x32_bf16 v[96:99], v[160:163], v[192:195], v[96:99]\n"
        "ds_read_b128 v[220:223], v235 offset:16384\n"
        "v_mfma_f32_16x16x32_bf16 v[96:99], v[164:167], v[196:199], v[96:99]\n"
        "v_mfma_f32_16x16x32_bf16 v[108:111], v[164:167], v[204:207], v[108:111]\n"
        "ds_read_b128 v[176:179], v228 offset:32768\n"
        "v_mfma_f32_16x16x32_bf16 v[108:111], v[160:163], v[200:203], v[108:111]\n"
        "ds_read_b128 v[180:183], v229 offset:32768\n"
        "v_mfma_f32_16x16x32_bf16 v[104:107], v[152:155], v[200:203], v[104:107]\n"
        "v_mfma_f32_16x16x32_bf16 v[104:107], v[156:159], v[204:207], v[104:107]\n"
        "ds_read_b128 v[184:187], v228 offset:34816\n"
        "v_mfma_f32_16x16x32_bf16 v[100:103], v[148:151], v[204:207], v[100:103]\n"
        "ds_read_b128 v[188:191], v229 offset:34816\n"
        "v_mfma_f32_16x16x32_bf16 v[100:103], v[144:147], v[200:203], v[100:103]\n"
        "s_waitcnt lgkmcnt(2)\n"
        "v_mfma_f32_16x16x32_bf16 v[0:3], v[112:115], v[176:179], v[0:3]\n"
        "ds_read_b128 v[192:195], v228 offset:36864\n"
        "v_bitop3_b32 v210, v210, v214, s34 bitop3:0x7e\n"
        "v_mfma_f32_16x16x32_bf16 v[0:3], v[116:119], v[180:183], v[0:3]\n"
        "ds_read_b128 v[196:199], v229 offset:36864\n"
        "v_bitop3_b32 v211, v211, v215, s39 bitop3:0x7e\n"
        "v_mfma_f32_16x16x32_bf16 v[4:7], v[124:127], v[180:183], v[4:7]\n"
        "ds_read_b128 v[200:203], v228 offset:38912\n"
        "v_bitop3_b32 v218, v218, v222, s34 bitop3:0x7e\n"
        "v_mfma_f32_16x16x32_bf16 v[4:7], v[120:123], v[176:179], v[4:7]\n"
        "ds_read_b128 v[204:207], v229 offset:38912\n"
        "v_perm_b32 v224, v208, v236, s60\n"
        "v_mfma_f32_16x16x32_bf16 v[8:11], v[128:131], v[176:179], v[8:11]\n"
        "ds_read_b32 v144, v224\n"
        "v_perm_b32 v225, v208, v236, s61\n"
        "v_mfma_f32_16x16x32_bf16 v[8:11], v[132:135], v[180:183], v[8:11]\n"
        "ds_read_b32 v145, v225\n"
        "v_perm_b32 v226, v208, v236, s62\n"
        "v_mfma_f32_16x16x32_bf16 v[12:15], v[140:143], v[180:183], v[12:15]\n"
        "ds_read_b32 v146, v226\n"
        "v_perm_b32 v227, v208, v236, s63\n"
        "v_mfma_f32_16x16x32_bf16 v[12:15], v[136:139], v[176:179], v[12:15]\n"
        "ds_read_b32 v147, v227\n"
        "v_bitop3_b32 v219, v219, v223, s39 bitop3:0x7e\n"
        "s_waitcnt lgkmcnt(8)\n"
        "v_mfma_f32_16x16x32_bf16 v[28:31], v[136:139], v[184:187], v[28:31]\n"
        "ds_read_b128 v[176:179], v230 offset:32768\n"
        "v_or3_b32 v210, v210, v211, v218\n"
        "v_mfma_f32_16x16x32_bf16 v[28:31], v[140:143], v[188:191], v[28:31]\n"
        "ds_read_b128 v[180:183], v231 offset:32768\n"
        "v_perm_b32 v224, v209, v236, s60\n"
        "v_mfma_f32_16x16x32_bf16 v[24:27], v[132:135], v[188:191], v[24:27]\n"
        "ds_read_b32 v148, v224\n"
        "v_perm_b32 v225, v209, v236, s61\n"
        "v_mfma_f32_16x16x32_bf16 v[24:27], v[128:131], v[184:187], v[24:27]\n"
        "ds_read_b32 v149, v225\n"
        "v_perm_b32 v226, v209, v236, s62\n"
        "v_mfma_f32_16x16x32_bf16 v[20:23], v[120:123], v[184:187], v[20:23]\n"
        "ds_read_b32 v150, v226\n"
        "v_perm_b32 v227, v209, v236, s63\n"
        "v_mfma_f32_16x16x32_bf16 v[20:23], v[124:127], v[188:191], v[20:23]\n"
        "ds_read_b32 v151, v227\n"
        "v_perm_b32 v224, v212, v236, s60\n"
        "v_mfma_f32_16x16x32_bf16 v[16:19], v[116:119], v[188:191], v[16:19]\n"
        "ds_read_b32 v152, v224\n"
        "v_perm_b32 v225, v212, v236, s61\n"
        "v_mfma_f32_16x16x32_bf16 v[16:19], v[112:115], v[184:187], v[16:19]\n"
        "ds_read_b32 v153, v225\n"
        "v_or_b32 v210, v210, v219\n"
        "s_waitcnt lgkmcnt(14)\n"
        "v_mfma_f32_16x16x32_bf16 v[32:35], v[112:115], v[192:195], v[32:35]\n"
        "ds_read_b128 v[184:187], v230 offset:34816\n"
        "v_cmp_ne_u32 vcc, 0, v210\n"
        "v_mfma_f32_16x16x32_bf16 v[32:35], v[116:119], v[196:199], v[32:35]\n"
        "ds_read_b128 v[188:191], v231 offset:34816\n"
        "v_perm_b32 v226, v212, v236, s62\n"
        "v_mfma_f32_16x16x32_bf16 v[36:39], v[124:127], v[196:199], v[36:39]\n"
        "ds_read_b32 v154, v226\n"
        "v_perm_b32 v227, v212, v236, s63\n"
        "v_mfma_f32_16x16x32_bf16 v[36:39], v[120:123], v[192:195], v[36:39]\n"
        "ds_read_b32 v155, v227\n"
        "v_perm_b32 v224, v213, v236, s60\n"
        "v_mfma_f32_16x16x32_bf16 v[40:43], v[128:131], v[192:195], v[40:43]\n"
        "ds_read_b32 v156, v224\n"
        "v_perm_b32 v225, v213, v236, s61\n"
        "v_mfma_f32_16x16x32_bf16 v[40:43], v[132:135], v[196:199], v[40:43]\n"
        "ds_read_b32 v157, v225\n"
        "v_perm_b32 v226, v213, v236, s62\n"
        "v_mfma_f32_16x16x32_bf16 v[44:47], v[140:143], v[196:199], v[44:47]\n"
        "ds_read_b32 v158, v226\n"
        "v_perm_b32 v227, v213, v236, s63\n"
        "v_mfma_f32_16x16x32_bf16 v[44:47], v[136:139], v[192:195], v[44:47]\n"
        "ds_read_b32 v159, v227\n"
        "s_waitcnt lgkmcnt(15)\n"
        "v_mfma_f32_16x16x32_bf16 v[60:63], v[136:139], v[200:203], v[60:63]\n"
        "ds_read_b128 v[192:195], v230 offset:36864\n"
        "s_cbranch_vccnz .Ln_f5\n"
        ".Ln_b5:\n"
        "v_mfma_f32_16x16x32_bf16 v[60:63], v[140:143], v[204:207], v[60:63]\n"
        "ds_read_b128 v[196:199], v231 offset:36864\n"
        "v_perm_b32 v224, v216, v236, s60\n"
        "v_mfma_f32_16x16x32_bf16 v[56:59], v[132:135], v[204:207], v[56:59]\n"
        "ds_read_b32 v160, v224\n"
        "v_perm_b32 v225, v216, v236, s61\n"
        "v_mfma_f32_16x16x32_bf16 v[56:59], v[128:131], v[200:203], v[56:59]\n"
        "ds_read_b32 v161, v225\n"
        "v_perm_b32 v226, v216, v236, s62\n"
        "v_mfma_f32_16x16x32_bf16 v[52:55], v[120:123], v[200:203], v[52:55]\n"
        "ds_read_b32 v162, v226\n"
        "v_perm_b32 v227, v216, v236, s63\n"
        "v_mfma_f32_16x16x32_bf16 v[52:55], v[124:127], v[204:207], v[52:55]\n"
        "ds_read_b32 v163, v227\n"
        "v_perm_b32 v224, v217, v236, s60\n"
        "v_mfma_f32_16x16x32_bf16 v[48:51], v[116:119], v[204:207], v[48:51]\n"
        "ds_read_b32 v164, v224\n"
        "v_perm_b32 v225, v217, v236, s61\n"
        "v_mfma_f32_16x16x32_bf16 v[48:51], v[112:115], v[200:203], v[48:51]\n"
        "ds_read_b32 v165, v225\n"
        "s_waitcnt lgkmcnt(15)\n"
        "v_mfma_f32_16x16x32_bf16 v[64:67], v[112:115], v[176:179], v[64:67]\n"
        "ds_read_b128 v[200:203], v230 offset:38912\n"
        "v_mfma_f32_16x16x32_bf16 v[64:67], v[116:119], v[180:183], v[64:67]\n"
        "ds_read_b128 v[204:207], v231 offset:38912\n"
        "v_perm_b32 v226, v217, v236, s62\n"
        "v_mfma_f32_16x16x32_bf16 v[68:71], v[124:127], v[180:183], v[68:71]\n"
        "ds_read_b32 v166, v226\n"
        "v_perm_b32 v227, v217, v236, s63\n"
        "v_mfma_f32_16x16x32_bf16 v[68:71], v[120:123], v[176:179], v[68:71]\n"
        "ds_read_b32 v167, v227\n"
        "v_perm_b32 v224, v220, v236, s60\n"
        "v_mfma_f32_16x16x32_bf16 v[72:75], v[128:131], v[176:179], v[72:75]\n"
        "ds_read_b32 v168, v224\n"
        "v_perm_b32 v225, v220, v236, s61\n"
        "v_mfma_f32_16x16x32_bf16 v[72:75], v[132:135], v[180:183], v[72:75]\n"
        "ds_read_b32 v169, v225\n"
        "v_perm_b32 v226, v220, v236, s62\n"
        "s_waitcnt lgkmcnt(15)\n"
        "v_mfma_f32_16x16x32_bf16 v[84:87], v[132:135], v[188:191], v[84:87]\n"
        "ds_read_b32 v170, v226\n"
        "v_perm_b32 v227, v220, v236, s63\n"
        "v_mfma_f32_16x16x32_bf16 v[84:87], v[128:131], v[184:187], v[84:87]\n"
        "ds_read_b32 v171, v227\n"
        "v_perm_b32 v224, v221, v236, s60\n"
        "v_mfma_f32_16x16x32_bf16 v[80:83], v[120:123], v[184:187], v[80:83]\n"
        "ds_read_b32 v172, v224\n"
        "v_perm_b32 v225, v221, v236, s61\n"
        "v_mfma_f32_16x16x32_bf16 v[80:83], v[124:127], v[188:191], v[80:83]\n"
        "ds_read_b32 v173, v225\n"
        "v_perm_b32 v226, v221, v236, s62\n"
        "v_mfma_f32_16x16x32_bf16 v[76:79], v[116:119], v[188:191], v[76:79]\n"
        "ds_read_b32 v174, v226\n"
        "v_perm_b32 v227, v221, v236, s63\n"
        "v_mfma_f32_16x16x32_bf16 v[76:79], v[112:115], v[184:187], v[76:79]\n"
        "ds_read_b32 v175, v227\n"
        "s_waitcnt lgkmcnt(10)\n"
        "s_barrier\n"
        "v_mfma_f32_16x16x32_bf16 v[88:91], v[112:115], v[192:195], v[88:91]\n"
        "ds_read_b128 v[208:211], v232 offset:32768\n"
        "v_mfma_f32_16x16x32_bf16 v[88:91], v[116:119], v[196:199], v[88:91]\n"
        "ds_read_b128 v[212:215], v233 offset:32768\n"
        "v_mfma_f32_16x16x32_bf16 v[92:95], v[124:127], v[196:199], v[92:95]\n"
        "v_mfma_f32_16x16x32_bf16 v[92:95], v[120:123], v[192:195], v[92:95]\n"
        "ds_read_b128 v[216:219], v234 offset:32768\n"
        "v_mfma_f32_16x16x32_bf16 v[96:99], v[128:131], v[192:195], v[96:99]\n"
        "ds_read_b128 v[220:223], v235 offset:32768\n"
        "v_mfma_f32_16x16x32_bf16 v[96:99], v[132:135], v[196:199], v[96:99]\n"
        "v_mfma_f32_16x16x32_bf16 v[108:111], v[132:135], v[204:207], v[108:111]\n"
        "ds_read_b128 v[176:179], v228 offset:0\n"
        "v_mfma_f32_16x16x32_bf16 v[108:111], v[128:131], v[200:203], v[108:111]\n"
        "ds_read_b128 v[180:183], v229 offset:0\n"
        "v_mfma_f32_16x16x32_bf16 v[104:107], v[120:123], v[200:203], v[104:107]\n"
        "v_mfma_f32_16x16x32_bf16 v[104:107], v[124:127], v[204:207], v[104:107]\n"
        "ds_read_b128 v[184:187], v228 offset:2048\n"
        "v_mfma_f32_16x16x32_bf16 v[100:103], v[116:119], v[204:207], v[100:103]\n"
        "ds_read_b128 v[188:191], v229 offset:2048\n"
        "v_mfma_f32_16x16x32_bf16 v[100:103], v[112:115], v[200:203], v[100:103]\n"
        "s_waitcnt lgkmcnt(2)\n"
        "v_mfma_f32_16x16x32_bf16 v[0:3], v[144:147], v[176:179], v[0:3]\n"
        "ds_read_b128 v[192:195], v228 offset:4096\n"
        "v_bitop3_b32 v210, v210, v214, s34 bitop3:0x7e\n"
        "v_mfma_f32_16x16x32_bf16 v[0:3], v[148:151], v[180:183], v[0:3]\n"
        "ds_read_b128 v[196:199], v229 offset:4096\n"
        "v_bitop3_b32 v211, v211, v215, s39 bitop3:0x7e\n"
        "v_mfma_f32_16x16x32_bf16 v[4:7], v[156:159], v[180:183], v[4:7]\n"
        "ds_read_b128 v[200:203], v228 offset:6144\n"
        "v_bitop3_b32 v218, v218, v222, s34 bitop3:0x7e\n"
        "v_mfma_f32_16x16x32_bf16 v[4:7], v[152:155], v[176:179], v[4:7]\n"
        "ds_read_b128 v[204:207], v229 offset:6144\n"
        "v_perm_b32 v224, v208, v236, s60\n"
        "v_mfma_f32_16x16x32_bf16 v[8:11], v[160:163], v[176:179], v[8:11]\n"
        "ds_read_b32 v112, v224\n"
        "v_perm_b32 v225, v208, v236, s61\n"
        "v_mfma_f32_16x16x32_bf16 v[8:11], v[164:167], v[180:183], v[8:11]\n"
        "ds_read_b32 v113, v225\n"
        "v_perm_b32 v226, v208, v236, s62\n"
        "v_mfma_f32_16x16x32_bf16 v[12:15], v[172:175], v[180:183], v[12:15]\n"
        "ds_read_b32 v114, v226\n"
        "v_perm_b32 v227, v208, v236, s63\n"
        "v_mfma_f32_16x16x32_bf16 v[12:15], v[168:171], v[176:179], v[12:15]\n"
        "ds_read_b32 v115, v227\n"
        "v_bitop3_b32 v219, v219, v223, s39 bitop3:0x7e\n"
        "s_waitcnt lgkmcnt(8)\n"
        "v_mfma_f32_16x16x32_bf16 v[28:31], v[168:171], v[184:187], v[28:31]\n"
        "ds_read_b128 v[176:179], v230 offset:0\n"
        "v_or3_b32 v210, v210, v211, v218\n"
        "v_mfma_f32_16x16x32_bf16 v[28:31], v[172:175], v[188:191], v[28:31]\n"
        "ds_read_b128 v[180:183], v231 offset:0\n"
        "v_perm_b32 v224, v209, v236, s60\n"
        "v_mfma_f32_16x16x32_bf16 v[24:27], v[164:167], v[188:191], v[24:27]\n"
        "ds_read_b32 v116, v224\n"
        "v_perm_b32 v225, v209, v236, s61\n"
        "v_mfma_f32_16x16x32_bf16 v[24:27], v[160:163], v[184:187], v[24:27]\n"
        "ds_read_b32 v117, v225\n"
        "v_perm_b32 v226, v209, v236, s62\n"
        "v_mfma_f32_16x16x32_bf16 v[20:23], v[152:155], v[184:187], v[20:23]\n"
        "ds_read_b32 v118, v226\n"
        "v_perm_b32 v227, v209, v236, s63\n"
        "v_mfma_f32_16x16x32_bf16 v[20:23], v[156:159], v[188:191], v[20:23]\n"
        "ds_read_b32 v119, v227\n"
        "v_perm_b32 v224, v212, v236, s60\n"
        "v_mfma_f32_16x16x32_bf16 v[16:19], v[148:151], v[188:191], v[16:19]\n"
        "ds_read_b32 v120, v224\n"
        "v_perm_b32 v225, v212, v236, s61\n"
        "v_mfma_f32_16x16x32_bf16 v[16:19], v[144:147], v[184:187], v[16:19]\n"
        "ds_read_b32 v121, v225\n"
        "v_or_b32 v210, v210, v219\n"
        "s_waitcnt lgkmcnt(14)\n"
        "v_mfma_f32_16x16x32_bf16 v[32:35], v[144:147], v[192:195], v[32:35]\n"
        "ds_read_b128 v[184:187], v230 offset:2048\n"
        "v_cmp_ne_u32 vcc, 0, v210\n"
        "v_mfma_f32_16x16x32_bf16 v[32:35], v[148:151], v[196:199], v[32:35]\n"
        "ds_read_b128 v[188:191], v231 offset:2048\n"
        "v_perm_b32 v226, v212, v236, s62\n"
        "v_mfma_f32_16x16x32_bf16 v[36:39], v[156:159], v[196:199], v[36:39]\n"
        "ds_read_b32 v122, v226\n"
        "v_perm_b32 v227, v212, v236, s63\n"
        "v_mfma_f32_16x16x32_bf16 v[36:39], v[152:155], v[192:195], v[36:39]\n"
        "ds_read_b32 v123, v227\n"
        "v_perm_b32 v224, v213, v236, s60\n"
        "v_mfma_f32_16x16x32_bf16 v[40:43], v[160:163], v[192:195], v[40:43]\n"
        "ds_read_b32 v124, v224\n"
        "v_perm_b32 v225, v213, v236, s61\n"
        "v_mfma_f32_16x16x32_bf16 v[40:43], v[164:167], v[196:199], v[40:43]\n"
        "ds_read_b32 v125, v225\n"
        "v_perm_b32 v226, v213, v236, s62\n"
        "v_mfma_f32_16x16x32_bf16 v[44:47], v[172:175], v[196:199], v[44:47]\n"
        "ds_read_b32 v126, v226\n"
        "v_perm_b32 v227, v213, v236, s63\n"
        "v_mfma_f32_16x16x32_bf16 v[44:47], v[168:171], v[192:195], v[44:47]\n"
        "ds_read_b32 v127, v227\n"
        "s_waitcnt lgkmcnt(15)\n"
        "v_mfma_f32_16x16x32_bf16 v[60:63], v[168:171], v[200:203], v[60:63]\n"
        "ds_read_b128 v[192:195], v230 offset:4096\n"
        "s_cbranch_vccnz .Ln_f6\n"
        ".Ln_b6:\n"
        "v_mfma_f32_16x16x32_bf16 v[60:63], v[172:175], v[204:207], v[60:63]\n"
        "ds_read_b128 v[196:199], v231 offset:4096\n"
        "v_perm_b32 v224, v216, v236, s60\n"
        "v_mfma_f32_16x16x32_bf16 v[56:59], v[164:167], v[204:207], v[56:59]\n"
        "ds_read_b32 v128, v224\n"
        "v_perm_b32 v225, v216, v236, s61\n"
        "v_mfma_f32_16x16x32_bf16 v[56:59], v[160:163], v[200:203], v[56:59]\n"
        "ds_read_b32 v129, v225\n"
        "v_perm_b32 v226, v216, v236, s62\n"
        "v_mfma_f32_16x16x32_bf16 v[52:55], v[152:155], v[200:203], v[52:55]\n"
        "ds_read_b32 v130, v226\n"
        "v_perm_b32 v227, v216, v236, s63\n"
        "v_mfma_f32_16x16x32_bf16 v[52:55], v[156:159], v[204:207], v[52:55]\n"
        "ds_read_b32 v131, v227\n"
        "v_perm_b32 v224, v217, v236, s60\n"
        "v_mfma_f32_16x16x32_bf16 v[48:51], v[148:151], v[204:207], v[48:51]\n"
        "ds_read_b32 v132, v224\n"
        "v_perm_b32 v225, v217, v236, s61\n"
        "v_mfma_f32_16x16x32_bf16 v[48:51], v[144:147], v[200:203], v[48:51]\n"
        "ds_read_b32 v133, v225\n"
        "s_waitcnt lgkmcnt(15)\n"
        "v_mfma_f32_16x16x32_bf16 v[64:67], v[144:147], v[176:179], v[64:67]\n"
        "ds_read_b128 v[200:203], v230 offset:6144\n"
        "v_mfma_f32_16x16x32_bf16 v[64:67], v[148:151], v[180:183], v[64:67]\n"
        "ds_read_b128 v[204:207], v231 offset:6144\n"
        "v_perm_b32 v226, v217, v236, s62\n"
        "v_mfma_f32_16x16x32_bf16 v[68:71], v[156:159], v[180:183], v[68:71]\n"
        "ds_read_b32 v134, v226\n"
        "v_perm_b32 v227, v217, v236, s63\n"
        "v_mfma_f32_16x16x32_bf16 v[68:71], v[152:155], v[176:179], v[68:71]\n"
        "ds_read_b32 v135, v227\n"
        "v_perm_b32 v224, v220, v236, s60\n"
        "v_mfma_f32_16x16x32_bf16 v[72:75], v[160:163], v[176:179], v[72:75]\n"
        "ds_read_b32 v136, v224\n"
        "v_perm_b32 v225, v220, v236, s61\n"
        "v_mfma_f32_16x16x32_bf16 v[72:75], v[164:167], v[180:183], v[72:75]\n"
        "ds_read_b32 v137, v225\n"
        "v_perm_b32 v226, v220, v236, s62\n"
        "s_waitcnt lgkmcnt(15)\n"
        "v_mfma_f32_16x16x32_bf16 v[84:87], v[164:167], v[188:191], v[84:87]\n"
        "ds_read_b32 v138, v226\n"
        "v_perm_b32 v227, v220, v236, s63\n"
        "v_mfma_f32_16x16x32_bf16 v[84:87], v[160:163], v[184:187], v[84:87]\n"
        "ds_read_b32 v139, v227\n"
        "v_perm_b32 v224, v221, v236, s60\n"
        "v_mfma_f32_16x16x32_bf16 v[80:83], v[152:155], v[184:187], v[80:83]\n"
        "ds_read_b32 v140, v224\n"
        "v_perm_b32 v225, v221, v236, s61\n"
        "v_mfma_f32_16x16x32_bf16 v[80:83], v[156:159], v[188:191], v[80:83]\n"
        "ds_read_b32 v141, v225\n"
        "v_perm_b32 v226, v221, v236, s62\n"
        "v_mfma_f32_16x16x32_bf16 v[76:79], v[148:151], v[188:191], v[76:79]\n"
        "ds_read_b32 v142, v226\n"
        "v_perm_b32 v227, v221, v236, s63\n"
        "v_mfma_f32_16x16x32_bf16 v[76:79], v[144:147], v[184:187], v[76:79]\n"
        "ds_read_b32 v143, v227\n"
        "s_waitcnt lgkmcnt(10)\n"
        "s_barrier\n"
        "v_mfma_f32_16x16x32_bf16 v[88:91], v[144:147], v[192:195], v[88:91]\n"
        "ds_read_b128 v[208:211], v232 offset:0\n"
        "v_mfma_f32_16x16x32_bf16 v[88:91], v[148:151], v[196:199], v[88:91]\n"
        "ds_read_b128 v[212:215], v233 offset:0\n"
        "v_mfma_f32_16x16x32_bf16 v[92:95], v[156:159], v[196:199], v[92:95]\n"
        "v_mfma_f32_16x16x32_bf16 v[92:95], v[152:155], v[192:195], v[92:95]\n"
        "ds_read_b128 v[216:219], v234 offset:0\n"
        "v_mfma_f32_16x16x32_bf16 v[96:99], v[160:163], v[192:195], v[96:99]\n"
        "ds_read_b128 v[220:223], v235 offset:0\n"
        "v_mfma_f32_16x16x32_bf16 v[96:99], v[164:167], v[196:199], v[96:99]\n"
        "v_mfma_f32_16x16x32_bf16 v[108:111], v[164:167], v[204:207], v[108:111]\n"
        "ds_read_b128 v[176:179], v228 offset:16384\n"
        "v_mfma_f32_16x16x32_bf16 v[108:111], v[160:163], v[200:203], v[108:111]\n"
        "ds_read_b128 v[180:183], v229 offset:16384\n"
        "v_mfma_f32_16x16x32_bf16 v[104:107], v[152:155], v[200:203], v[104:107]\n"
        "v_mfma_f32_16x16x32_bf16 v[104:107], v[156:159], v[204:207], v[104:107]\n"
        "ds_read_b128 v[184:187], v228 offset:18432\n"
        "v_mfma_f32_16x16x32_bf16 v[100:103], v[148:151], v[204:207], v[100:103]\n"
        "ds_read_b128 v[188:191], v229 offset:18432\n"
        "v_mfma_f32_16x16x32_bf16 v[100:103], v[144:147], v[200:203], v[100:103]\n"
        "s_waitcnt lgkmcnt(2)\n"
        "v_mfma_f32_16x16x32_bf16 v[0:3], v[112:115], v[176:179], v[0:3]\n"
        "ds_read_b128 v[192:195], v228 offset:20480\n"
        "v_bitop3_b32 v210, v210, v214, s34 bitop3:0x7e\n"
        "v_mfma_f32_16x16x32_bf16 v[0:3], v[116:119], v[180:183], v[0:3]\n"
        "ds_read_b128 v[196:199], v229 offset:20480\n"
        "v_bitop3_b32 v211, v211, v215, s39 bitop3:0x7e\n"
        "v_mfma_f32_16x16x32_bf16 v[4:7], v[124:127], v[180:183], v[4:7]\n"
        "ds_read_b128 v[200:203], v228 offset:22528\n"
        "v_bitop3_b32 v218, v218, v222, s34 bitop3:0x7e\n"
        "v_mfma_f32_16x16x32_bf16 v[4:7], v[120:123], v[176:179], v[4:7]\n"
        "ds_read_b128 v[204:207], v229 offset:22528\n"
        "v_perm_b32 v224, v208, v236, s60\n"
        "v_mfma_f32_16x16x32_bf16 v[8:11], v[128:131], v[176:179], v[8:11]\n"
        "ds_read_b32 v144, v224\n"
        "v_perm_b32 v225, v208, v236, s61\n"
        "v_mfma_f32_16x16x32_bf16 v[8:11], v[132:135], v[180:183], v[8:11]\n"
        "ds_read_b32 v145, v225\n"
        "v_perm_b32 v226, v208, v236, s62\n"
        "v_mfma_f32_16x16x32_bf16 v[12:15], v[140:143], v[180:183], v[12:15]\n"
        "ds_read_b32 v146, v226\n"
        "v_perm_b32 v227, v208, v236, s63\n"
        "v_mfma_f32_16x16x32_bf16 v[12:15], v[136:139], v[176:179], v[12:15]\n"
        "ds_read_b32 v147, v227\n"
        "v_bitop3_b32 v219, v219, v223, s39 bitop3:0x7e\n"
        "s_waitcnt lgkmcnt(8)\n"
        "v_mfma_f32_16x16x32_bf16 v[28:31], v[136:139], v[184:187], v[28:31]\n"
        "ds_read_b128 v[176:179], v230 offset:16384\n"
        "v_or3_b32 v210, v210, v211, v218\n"
        "v_mfma_f32_16x16x32_bf16 v[28:31], v[140:143], v[188:191], v[28:31]\n"
        "ds_read_b128 v[180:183], v231 offset:16384\n"
        "v_perm_b32 v224, v209, v236, s60\n"
        "v_mfma_f32_16x16x32_bf16 v[24:27], v[132:135], v[188:191], v[24:27]\n"
        "ds_read_b32 v148, v224\n"
        "v_perm_b32 v225, v209, v236, s61\n"
        "v_mfma_f32_16x16x32_bf16 v[24:27], v[128:131], v[184:187], v[24:27]\n"
        "ds_read_b32 v149, v225\n"
        "v_perm_b32 v226, v209, v236, s62\n"
        "v_mfma_f32_16x16x32_bf16 v[20:23], v[120:123], v[184:187], v[20:23]\n"
        "ds_read_b32 v150, v226\n"
        "v_perm_b32 v227, v209, v236, s63\n"
        "v_mfma_f32_16x16x32_bf16 v[20:23], v[124:127], v[188:191], v[20:23]\n"
        "ds_read_b32 v151, v227\n"
        "v_perm_b32 v224, v212, v236, s60\n"
        "v_mfma_f32_16x16x32_bf16 v[16:19], v[116:119], v[188:191], v[16:19]\n"
        "ds_read_b32 v152, v224\n"
        "v_perm_b32 v225, v212, v236, s61\n"
        "v_mfma_f32_16x16x32_bf16 v[16:19], v[112:115], v[184:187], v[16:19]\n"
        "ds_read_b32 v153, v225\n"
        ".Ln_tp1:\n"
        "s_cbranch_execz .Ln_ret\n"
        "v_or_b32 v210, v210, v219\n"
        "s_waitcnt lgkmcnt(14)\n"
        "v_mfma_f32_16x16x32_bf16 v[32:35], v[112:115], v[192:195], v[32:35]\n"
        "ds_read_b128 v[184:187], v230 offset:18432\n"
        "v_cmp_ne_u32 vcc, 0, v210\n"
        "v_mfma_f32_16x16x32_bf16 v[32:35], v[116:119], v[196:199], v[32:35]\n"
        "ds_read_b128 v[188:191], v231 offset:18432\n"
        "v_perm_b32 v226, v212, v236, s62\n"
        "v_mfma_f32_16x16x32_bf16 v[36:39], v[124:127], v[196:199], v[36:39]\n"
        "ds_read_b32 v154, v226\n"
        "v_perm_b32 v227, v212, v236, s63\n"
        "v_mfma_f32_16x16x32_bf16 v[36:39], v[120:123], v[192:195], v[36:39]\n"
        "ds_read_b32 v155, v227\n"
        "v_perm_b32 v224, v213, v236, s60\n"
        "v_mfma_f32_16x16x32_bf16 v[40:43], v[128:131], v[192:195], v[40:43]\n"
        "ds_read_b32 v156, v224\n"
        "v_perm_b32 v225, v213, v236, s61\n"
        "v_mfma_f32_16x16x32_bf16 v[40:43], v[132:135], v[196:199], v[40:43]\n"
        "ds_read_b32 v157, v225\n"
        "v_perm_b32 v226, v213, v236, s62\n"
        "v_mfma_f32_16x16x32_bf16 v[44:47], v[140:143], v[196:199], v[44:47]\n"
        "ds_read_b32 v158, v226\n"
        "v_perm_b32 v227, v213, v236, s63\n"
        "v_mfma_f32_16x16x32_bf16 v[44:47], v[136:139], v[192:195], v[44:47]\n"
        "ds_read_b32 v159, v227\n"
        "s_waitcnt lgkmcnt(15)\n"
        "v_mfma_f32_16x16x32_bf16 v[60:63], v[136:139], v[200:203], v[60:63]\n"
        "ds_read_b128 v[192:195], v230 offset:20480\n"
        "s_cbranch_vccnz .Ln_f7\n"
        ".Ln_b7:\n"
        "v_mfma_f32_16x16x32_bf16 v[60:63], v[140:143], v[204:207], v[60:63]\n"
        "ds_read_b128 v[196:199], v231 offset:20480\n"
        "v_perm_b32 v224, v216, v236, s60\n"
        "v_mfma_f32_16x16x32_bf16 v[56:59], v[132:135], v[204:207], v[56:59]\n"
        "ds_read_b32 v160, v224\n"
        "v_perm_b32 v225, v216, v236, s61\n"
        "v_mfma_f32_16x16x32_bf16 v[56:59], v[128:131], v[200:203], v[56:59]\n"
        "ds_read_b32 v161, v225\n"
        "v_perm_b32 v226, v216, v236, s62\n"
        "v_mfma_f32_16x16x32_bf16 v[52:55], v[120:123], v[200:203], v[52:55]\n"
        "ds_read_b32 v162, v226\n"
        "v_perm_b32 v227, v216, v236, s63\n"
        "v_mfma_f32_16x16x32_bf16 v[52:55], v[124:127], v[204:207], v[52:55]\n"
        "ds_read_b32 v163, v227\n"
        "v_perm_b32 v224, v217, v236, s60\n"
        "v_mfma_f32_16x16x32_bf16 v[48:51], v[116:119], v[204:207], v[48:51]\n"
        "ds_read_b32 v164, v224\n"
        "v_perm_b32 v225, v217, v236, s61\n"
        "v_mfma_f32_16x16x32_bf16 v[48:51], v[112:115], v[200:203], v[48:51]\n"
        "ds_read_b32 v165, v225\n"
        "s_waitcnt lgkmcnt(15)\n"
        "v_mfma_f32_16x16x32_bf16 v[64:67], v[112:115], v[176:179], v[64:67]\n"
        "ds_read_b128 v[200:203], v230 offset:22528\n"
        "v_mfma_f32_16x16x32_bf16 v[64:67], v[116:119], v[180:183], v[64:67]\n"
        "ds_read_b128 v[204:207], v231 offset:22528\n"
        "v_perm_b32 v226, v217, v236, s62\n"
        "v_mfma_f32_16x16x32_bf16 v[68:71], v[124:127], v[180:183], v[68:71]\n"
        "ds_read_b32 v166, v226\n"
        "v_perm_b32 v227, v217, v236, s63\n"
        "v_mfma_f32_16x16x32_bf16 v[68:71], v[120:123], v[176:179], v[68:71]\n"
        "ds_read_b32 v167, v227\n"
        "v_perm_b32 v224, v220, v236, s60\n"
        "v_mfma_f32_16x16x32_bf16 v[72:75], v[128:131], v[176:179], v[72:75]\n"
        "ds_read_b32 v168, v224\n"
        "v_perm_b32 v225, v220, v236, s61\n"
        "v_mfma_f32_16x16x32_bf16 v[72:75], v[132:135], v[180:183], v[72:75]\n"
        "ds_read_b32 v169, v225\n"
        "v_perm_b32 v226, v220, v236, s62\n"
        "s_waitcnt lgkmcnt(15)\n"
        "v_mfma_f32_16x16x32_bf16 v[84:87], v[132:135], v[188:191], v[84:87]\n"
        "ds_read_b32 v170, v226\n"
        "v_perm_b32 v227, v220, v236, s63\n"
        "v_mfma_f32_16x16x32_bf16 v[84:87], v[128:131], v[184:187], v[84:87]\n"
        "ds_read_b32 v171, v227\n"
        "v_perm_b32 v224, v221, v236, s60\n"
        "v_mfma_f32_16x16x32_bf16 v[80:83], v[120:123], v[184:187], v[80:83]\n"
        "ds_read_b32 v172, v224\n"
        "v_perm_b32 v225, v221, v236, s61\n"
        "v_mfma_f32_16x16x32_bf16 v[80:83], v[124:127], v[188:191], v[80:83]\n"
        "ds_read_b32 v173, v225\n"
        "v_perm_b32 v226, v221, v236, s62\n"
        "v_mfma_f32_16x16x32_bf16 v[76:79], v[116:119], v[188:191], v[76:79]\n"
        "ds_read_b32 v174, v226\n"
        "v_perm_b32 v227, v221, v236, s63\n"
        "v_mfma_f32_16x16x32_bf16 v[76:79], v[112:115], v[184:187], v[76:79]\n"
        "ds_read_b32 v175, v227\n"
        "s_waitcnt lgkmcnt(10)\n"
        "s_barrier\n"
        "v_mfma_f32_16x16x32_bf16 v[88:91], v[112:115], v[192:195], v[88:91]\n"
        "ds_read_b128 v[208:211], v232 offset:16384\n"
        "v_mfma_f32_16x16x32_bf16 v[88:91], v[116:119], v[196:199], v[88:91]\n"
        "ds_read_b128 v[212:215], v233 offset:16384\n"
        "v_mfma_f32_16x16x32_bf16 v[92:95], v[124:127], v[196:199], v[92:95]\n"
        "v_mfma_f32_16x16x32_bf16 v[92:95], v[120:123], v[192:195], v[92:95]\n"
        "ds_read_b128 v[216:219], v234 offset:16384\n"
        "v_mfma_f32_16x16x32_bf16 v[96:99], v[128:131], v[192:195], v[96:99]\n"
        "ds_read_b128 v[220:223], v235 offset:16384\n"
        "v_mfma_f32_16x16x32_bf16 v[96:99], v[132:135], v[196:199], v[96:99]\n"
        "v_mfma_f32_16x16x32_bf16 v[108:111], v[132:135], v[204:207], v[108:111]\n"
        "ds_read_b128 v[176:179], v228 offset:32768\n"
        "v_mfma_f32_16x16x32_bf16 v[108:111], v[128:131], v[200:203], v[108:111]\n"
        "ds_read_b128 v[180:183], v229 offset:32768\n"
        "v_mfma_f32_16x16x32_bf16 v[104:107], v[120:123], v[200:203], v[104:107]\n"
        "v_mfma_f32_16x16x32_bf16 v[104:107], v[124:127], v[204:207], v[104:107]\n"
        "ds_read_b128 v[184:187], v228 offset:34816\n"
        "v_mfma_f32_16x16x32_bf16 v[100:103], v[116:119], v[204:207], v[100:103]\n"
        "ds_read_b128 v[188:191], v229 offset:34816\n"
        "v_mfma_f32_16x16x32_bf16 v[100:103], v[112:115], v[200:203], v[100:103]\n"
        "s_waitcnt lgkmcnt(2)\n"
        "v_mfma_f32_16x16x32_bf16 v[0:3], v[144:147], v[176:179], v[0:3]\n"
        "ds_read_b128 v[192:195], v228 offset:36864\n"
        "v_bitop3_b32 v210, v210, v214, s34 bitop3:0x7e\n"
        "v_mfma_f32_16x16x32_bf16 v[0:3], v[148:151], v[180:183], v[0:3]\n"
        "ds_read_b128 v[196:199], v229 offset:36864\n"
        "v_bitop3_b32 v211, v211, v215, s39 bitop3:0x7e\n"
        "v_mfma_f32_16x16x32_bf16 v[4:7], v[156:159], v[180:183], v[4:7]\n"
        "ds_read_b128 v[200:203], v228 offset:38912\n"
        "v_bitop3_b32 v218, v218, v222, s34 bitop3:0x7e\n"
        "v_mfma_f32_16x16x32_bf16 v[4:7], v[152:155], v[176:179], v[4:7]\n"
        "ds_read_b128 v[204:207], v229 offset:38912\n"
        "v_perm_b32 v224, v208, v236, s60\n"
        "v_mfma_f32_16x16x32_bf16 v[8:11], v[160:163], v[176:179], v[8:11]\n"
        "ds_read_b32 v112, v224\n"
        "v_perm_b32 v225, v208, v236, s61\n"
        "v_mfma_f32_16x16x32_bf16 v[8:11], v[164:167], v[180:183], v[8:11]\n"
        "ds_read_b32 v113, v225\n"
        "v_perm_b32 v226, v208, v236, s62\n"
        "v_mfma_f32_16x16x32_bf16 v[12:15], v[172:175], v[180:183], v[12:15]\n"
        "ds_read_b32 v114, v226\n"
        "v_perm_b32 v227, v208, v236, s63\n"
        "v_mfma_f32_16x16x32_bf16 v[12:15], v[168:171], v[176:179], v[12:15]\n"
        "ds_read_b32 v115, v227\n"
        "v_bitop3_b32 v219, v219, v223, s39 bitop3:0x7e\n"
        "s_waitcnt lgkmcnt(8)\n"
        "v_mfma_f32_16x16x32_bf16 v[28:31], v[168:171], v[184:187], v[28:31]\n"
        "ds_read_b128 v[176:179], v230 offset:32768\n"
        "v_or3_b32 v210, v210, v211, v218\n"
        "v_mfma_f32_16x16x32_bf16 v[28:31], v[172:175], v[188:191], v[28:31]\n"
        "ds_read_b128 v[180:183], v231 offset:32768\n"
        "v_perm_b32 v224, v209, v236, s60\n"
        "v_mfma_f32_16x16x32_bf16 v[24:27], v[164:167], v[188:191], v[24:27]\n"
        "ds_read_b32 v116, v224\n"
        "v_perm_b32 v225, v209, v236, s61\n"
        "v_mfma_f32_16x16x32_bf16 v[24:27], v[160:163], v[184:187], v[24:27]\n"
        "ds_read_b32 v117, v225\n"
        "v_perm_b32 v226, v209, v236, s62\n"
        "v_mfma_f32_16x16x32_bf16 v[20:23], v[152:155], v[184:187], v[20:23]\n"
        "ds_read_b32 v118, v226\n"
        "v_perm_b32 v227, v209, v236, s63\n"
        "v_mfma_f32_16x16x32_bf16 v[20:23], v[156:159], v[188:191], v[20:23]\n"
        "ds_read_b32 v119, v227\n"
        "v_perm_b32 v224, v212, v236, s60\n"
        "v_mfma_f32_16x16x32_bf16 v[16:19], v[148:151], v[188:191], v[16:19]\n"
        "ds_read_b32 v120, v224\n"
        "v_perm_b32 v225, v212, v236, s61\n"
        "v_mfma_f32_16x16x32_bf16 v[16:19], v[144:147], v[184:187], v[16:19]\n"
        "ds_read_b32 v121, v225\n"
        "v_or_b32 v210, v210, v219\n"
        "s_waitcnt lgkmcnt(14)\n"
        "v_mfma_f32_16x16x32_bf16 v[32:35], v[144:147], v[192:195], v[32:35]\n"
        "ds_read_b128 v[184:187], v230 offset:34816\n"
        "v_cmp_ne_u32 vcc, 0, v210\n"
        "v_mfma_f32_16x16x32_bf16 v[32:35], v[148:151], v[196:199], v[32:35]\n"
        "ds_read_b128 v[188:191], v231 offset:34816\n"
        "v_perm_b32 v226, v212, v236, s62\n"
        "v_mfma_f32_16x16x32_bf16 v[36:39], v[156:159], v[196:199], v[36:39]\n"
        "ds_read_b32 v122, v226\n"
        "v_perm_b32 v227, v212, v236, s63\n"
        "v_mfma_f32_16x16x32_bf16 v[36:39], v[152:155], v[192:195], v[36:39]\n"
        "ds_read_b32 v123, v227\n"
        "v_perm_b32 v224, v213, v236, s60\n"
        "v_mfma_f32_16x16x32_bf16 v[40:43], v[160:163], v[192:195], v[40:43]\n"
        "ds_read_b32 v124, v224\n"
        "v_perm_b32 v225, v213, v236, s61\n"
        "v_mfma_f32_16x16x32_bf16 v[40:43], v[164:167], v[196:199], v[40:43]\n"
        "ds_read_b32 v125, v225\n"
        "v_perm_b32 v226, v213, v236, s62\n"
        "v_mfma_f32_16x16x32_bf16 v[44:47], v[172:175], v[196:199], v[44:47]\n"
        "ds_read_b32 v126, v226\n"
        "v_perm_b32 v227, v213, v236, s63\n"
        "v_mfma_f32_16x16x32_bf16 v[44:47], v[168:171], v[192:195], v[44:47]\n"
        "ds_read_b32 v127, v227\n"
        "s_waitcnt lgkmcnt(15)\n"
        "v_mfma_f32_16x16x32_bf16 v[60:63], v[168:171], v[200:203], v[60:63]\n"
        "ds_read_b128 v[192:195], v230 offset:36864\n"
        "s_cbranch_vccnz .Ln_f8\n"
        ".Ln_b8:\n"
        "v_mfma_f32_16x16x32_bf16 v[60:63], v[172:175], v[204:207], v[60:63]\n"
        "ds_read_b128 v[196:199], v231 offset:36864\n"
        "v_perm_b32 v224, v216, v236, s60\n"
        "v_mfma_f32_16x16x32_bf16 v[56:59], v[164:167], v[204:207], v[56:59]\n"
        "ds_read_b32 v128, v224\n"
        "v_perm_b32 v225, v216, v236, s61\n"
        "v_mfma_f32_16x16x32_bf16 v[56:59], v[160:163], v[200:203], v[56:59]\n"
        "ds_read_b32 v129, v225\n"
        "v_perm_b32 v226, v216, v236, s62\n"
        "v_mfma_f32_16x16x32_bf16 v[52:55], v[152:155], v[200:203], v[52:55]\n"
        "ds_read_b32 v130, v226\n"
        "v_perm_b32 v227, v216, v236, s63\n"
        "v_mfma_f32_16x16x32_bf16 v[52:55], v[156:159], v[204:207], v[52:55]\n"
        "ds_read_b32 v131, v227\n"
        "v_perm_b32 v224, v217, v236, s60\n"
        "v_mfma_f32_16x16x32_bf16 v[48:51], v[148:151], v[204:207], v[48:51]\n"
        "ds_read_b32 v132, v224\n"
        "v_perm_b32 v225, v217, v236, s61\n"
        "v_mfma_f32_16x16x32_bf16 v[48:51], v[144:147], v[200:203], v[48:51]\n"
        "ds_read_b32 v133, v225\n"
        "s_waitcnt lgkmcnt(15)\n"
        "v_mfma_f32_16x16x32_bf16 v[64:67], v[144:147], v[176:179], v[64:67]\n"
        "ds_read_b128 v[200:203], v230 offset:38912\n"
        "v_mfma_f32_16x16x32_bf16 v[64:67], v[148:151], v[180:183], v[64:67]\n"
        "ds_read_b128 v[204:207], v231 offset:38912\n"
        "v_perm_b32 v226, v217, v236, s62\n"
        "v_mfma_f32_16x16x32_bf16 v[68:71], v[156:159], v[180:183], v[68:71]\n"
        "ds_read_b32 v134, v226\n"
        "v_perm_b32 v227, v217, v236, s63\n"
        "v_mfma_f32_16x16x32_bf16 v[68:71], v[152:155], v[176:179], v[68:71]\n"
        "ds_read_b32 v135, v227\n"
        "v_perm_b32 v224, v220, v236, s60\n"
        "v_mfma_f32_16x16x32_bf16 v[72:75], v[160:163], v[176:179], v[72:75]\n"
        "ds_read_b32 v136, v224\n"
        "v_perm_b32 v225, v220, v236, s61\n"
        "v_mfma_f32_16x16x32_bf16 v[72:75], v[164:167], v[180:183], v[72:75]\n"
        "ds_read_b32 v137, v225\n"
        "v_perm_b32 v226, v220, v236, s62\n"
        "s_waitcnt lgkmcnt(15)\n"
        "v_mfma_f32_16x16x32_bf16 v[84:87], v[164:167], v[188:191], v[84:87]\n"
        "ds_read_b32 v138, v226\n"
        "v_perm_b32 v227, v220, v236, s63\n"
        "v_mfma_f32_16x16x32_bf16 v[84:87], v[160:163], v[184:187], v[84:87]\n"
        "ds_read_b32 v139, v227\n"
        "v_perm_b32 v224, v221, v236, s60\n"
        "v_mfma_f32_16x16x32_bf16 v[80:83], v[152:155], v[184:187], v[80:83]\n"
        "ds_read_b32 v140, v224\n"
        "v_perm_b32 v225, v221, v236, s61\n"
        "v_mfma_f32_16x16x32_bf16 v[80:83], v[156:159], v[188:191], v[80:83]\n"
        "ds_read_b32 v141, v225\n"
        "v_perm_b32 v226, v221, v236, s62\n"
        "v_mfma_f32_16x16x32_bf16 v[76:79], v[148:151], v[188:191], v[76:79]\n"
        "ds_read_b32 v142, v226\n"
        "v_perm_b32 v227, v221, v236, s63\n"
        "v_mfma_f32_16x16x32_bf16 v[76:79], v[144:147], v[184:187], v[76:79]\n"
        "ds_read_b32 v143, v227\n"
        "s_waitcnt lgkmcnt(10)\n"
        "s_barrier\n"
        "v_mfma_f32_16x16x32_bf16 v[88:91], v[144:147], v[192:195], v[88:91]\n"
        "ds_read_b128 v[208:211], v232 offset:32768\n"
        "v_mfma_f32_16x16x32_bf16 v[88:91], v[148:151], v[196:199], v[88:91]\n"
        "ds_read_b128 v[212:215], v233 offset:32768\n"
        "v_mfma_f32_16x16x32_bf16 v[92:95], v[156:159], v[196:199], v[92:95]\n"
        "v_mfma_f32_16x16x32_bf16 v[92:95], v[152:155], v[192:195], v[92:95]\n"
        "ds_read_b128 v[216:219], v234 offset:32768\n"
        "v_mfma_f32_16x16x32_bf16 v[96:99], v[160:163], v[192:195], v[96:99]\n"
        "ds_read_b128 v[220:223], v235 offset:32768\n"
        "v_mfma_f32_16x16x32_bf16 v[96:99], v[164:167], v[196:199], v[96:99]\n"
        "v_mfma_f32_16x16x32_bf16 v[108:111], v[164:167], v[204:207], v[108:111]\n"
        "ds_read_b128 v[176:179], v228 offset:0\n"
        "v_mfma_f32_16x16x32_bf16 v[108:111], v[160:163], v[200:203], v[108:111]\n"
        "ds_read_b128 v[180:183], v229 offset:0\n"
        "v_mfma_f32_16x16x32_bf16 v[104:107], v[152:155], v[200:203], v[104:107]\n"
        "v_mfma_f32_16x16x32_bf16 v[104:107], v[156:159], v[204:207], v[104:107]\n"
        "ds_read_b128 v[184:187], v228 offset:2048\n"
        "v_mfma_f32_16x16x32_bf16 v[100:103], v[148:151], v[204:207], v[100:103]\n"
        "ds_read_b128 v[188:191], v229 offset:2048\n"
        "v_mfma_f32_16x16x32_bf16 v[100:103], v[144:147], v[200:203], v[100:103]\n"
        "s_waitcnt lgkmcnt(2)\n"
        "v_mfma_f32_16x16x32_bf16 v[0:3], v[112:115], v[176:179], v[0:3]\n"
        "ds_read_b128 v[192:195], v228 offset:4096\n"
        "v_bitop3_b32 v210, v210, v214, s34 bitop3:0x7e\n"
        "v_mfma_f32_16x16x32_bf16 v[0:3], v[116:119], v[180:183], v[0:3]\n"
        "ds_read_b128 v[196:199], v229 offset:4096\n"
        "v_bitop3_b32 v211, v211, v215, s39 bitop3:0x7e\n"
        "v_mfma_f32_16x16x32_bf16 v[4:7], v[124:127], v[180:183], v[4:7]\n"
        "ds_read_b128 v[200:203], v228 offset:6144\n"
        "v_bitop3_b32 v218, v218, v222, s34 bitop3:0x7e\n"
        "v_mfma_f32_16x16x32_bf16 v[4:7], v[120:123], v[176:179], v[4:7]\n"
        "ds_read_b128 v[204:207], v229 offset:6144\n"
        "v_perm_b32 v224, v208, v236, s60\n"
        "v_mfma_f32_16x16x32_bf16 v[8:11], v[128:131], v[176:179], v[8:11]\n"
        "ds_read_b32 v144, v224\n"
        "v_perm_b32 v225, v208, v236, s61\n"
        "v_mfma_f32_16x16x32_bf16 v[8:11], v[132:135], v[180:183], v[8:11]\n"
        "ds_read_b32 v145, v225\n"
        "v_perm_b32 v226, v208, v236, s62\n"
        "v_mfma_f32_16x16x32_bf16 v[12:15], v[140:143], v[180:183], v[12:15]\n"
        "ds_read_b32 v146, v226\n"
        "v_perm_b32 v227, v208, v236, s63\n"
        "v_mfma_f32_16x16x32_bf16 v[12:15], v[136:139], v[176:179], v[12:15]\n"
        "ds_read_b32 v147, v227\n"
        "v_bitop3_b32 v219, v219, v223, s39 bitop3:0x7e\n"
        "s_waitcnt lgkmcnt(8)\n"
        "v_mfma_f32_16x16x32_bf16 v[28:31], v[136:139], v[184:187], v[28:31]\n"
        "ds_read_b128 v[176:179], v230 offset:0\n"
        "v_or3_b32 v210, v210, v211, v218\n"
        "v_mfma_f32_16x16x32_bf16 v[28:31], v[140:143], v[188:191], v[28:31]\n"
        "ds_read_b128 v[180:183], v231 offset:0\n"
        "v_perm_b32 v224, v209, v236, s60\n"
        "v_mfma_f32_16x16x32_bf16 v[24:27], v[132:135], v[188:191], v[24:27]\n"
        "ds_read_b32 v148, v224\n"
        "v_perm_b32 v225, v209, v236, s61\n"
        "v_mfma_f32_16x16x32_bf16 v[24:27], v[128:131], v[184:187], v[24:27]\n"
        "ds_read_b32 v149, v225\n"
        "v_perm_b32 v226, v209, v236, s62\n"
        "v_mfma_f32_16x16x32_bf16 v[20:23], v[120:123], v[184:187], v[20:23]\n"
        "ds_read_b32 v150, v226\n"
        "v_perm_b32 v227, v209, v236, s63\n"
        "v_mfma_f32_16x16x32_bf16 v[20:23], v[124:127], v[188:191], v[20:23]\n"
        "ds_read_b32 v151, v227\n"
        "v_perm_b32 v224, v212, v236, s60\n"
        "v_mfma_f32_16x16x32_bf16 v[16:19], v[116:119], v[188:191], v[16:19]\n"
        "ds_read_b32 v152, v224\n"
        "v_perm_b32 v225, v212, v236, s61\n"
        "v_mfma_f32_16x16x32_bf16 v[16:19], v[112:115], v[184:187], v[16:19]\n"
        "ds_read_b32 v153, v225\n"
        "v_or_b32 v210, v210, v219\n"
        "s_waitcnt lgkmcnt(14)\n"
        "v_mfma_f32_16x16x32_bf16 v[32:35], v[112:115], v[192:195], v[32:35]\n"
        "ds_read_b128 v[184:187], v230 offset:2048\n"
        "v_cmp_ne_u32 vcc, 0, v210\n"
        "v_mfma_f32_16x16x32_bf16 v[32:35], v[116:119], v[196:199], v[32:35]\n"
        "ds_read_b128 v[188:191], v231 offset:2048\n"
        "v_perm_b32 v226, v212, v236, s62\n"
        "v_mfma_f32_16x16x32_bf16 v[36:39], v[124:127], v[196:199], v[36:39]\n"
        "ds_read_b32 v154, v226\n"
        "v_perm_b32 v227, v212, v236, s63\n"
        "v_mfma_f32_16x16x32_bf16 v[36:39], v[120:123], v[192:195], v[36:39]\n"
        "ds_read_b32 v155, v227\n"
        "v_perm_b32 v224, v213, v236, s60\n"
        "v_mfma_f32_16x16x32_bf16 v[40:43], v[128:131], v[192:195], v[40:43]\n"
        "ds_read_b32 v156, v224\n"
        "v_perm_b32 v225, v213, v236, s61\n"
        "v_mfma_f32_16x16x32_bf16 v[40:43], v[132:135], v[196:199], v[40:43]\n"
        "ds_read_b32 v157, v225\n"
        "v_perm_b32 v226, v213, v236, s62\n"
        "v_mfma_f32_16x16x32_bf16 v[44:47], v[140:143], v[196:199], v[44:47]\n"
        "ds_read_b32 v158, v226\n"
        "v_perm_b32 v227, v213, v236, s63\n"
        "v_mfma_f32_16x16x32_bf16 v[44:47], v[136:139], v[192:195], v[44:47]\n"
        "ds_read_b32 v159, v227\n"
        "s_waitcnt lgkmcnt(15)\n"
        "v_mfma_f32_16x16x32_bf16 v[60:63], v[136:139], v[200:203], v[60:63]\n"
        "ds_read_b128 v[192:195], v230 offset:4096\n"
        "s_cbranch_vccnz .Ln_f9\n"
        ".Ln_b9:\n"
        "v_mfma_f32_16x16x32_bf16 v[60:63], v[140:143], v[204:207], v[60:63]\n"
        "ds_read_b128 v[196:199], v231 offset:4096\n"
        "v_perm_b32 v224, v216, v236, s60\n"
        "v_mfma_f32_16x16x32_bf16 v[56:59], v[132:135], v[204:207], v[56:59]\n"
        "ds_read_b32 v160, v224\n"
        "v_perm_b32 v225, v216, v236, s61\n"
        "v_mfma_f32_16x16x32_bf16 v[56:59], v[128:131], v[200:203], v[56:59]\n"
        "ds_read_b32 v161, v225\n"
        "v_perm_b32 v226, v216, v236, s62\n"
        "v_mfma_f32_16x16x32_bf16 v[52:55], v[120:123], v[200:203], v[52:55]\n"
        "ds_read_b32 v162, v226\n"
        "v_perm_b32 v227, v216, v236, s63\n"
        "v_mfma_f32_16x16x32_bf16 v[52:55], v[124:127], v[204:207], v[52:55]\n"
        "ds_read_b32 v163, v227\n"
        "v_perm_b32 v224, v217, v236, s60\n"
        "v_mfma_f32_16x16x32_bf16 v[48:51], v[116:119], v[204:207], v[48:51]\n"
        "ds_read_b32 v164, v224\n"
        "v_perm_b32 v225, v217, v236, s61\n"
        "v_mfma_f32_16x16x32_bf16 v[48:51], v[112:115], v[200:203], v[48:51]\n"
        "ds_read_b32 v165, v225\n"
        "s_waitcnt lgkmcnt(15)\n"
        "v_mfma_f32_16x16x32_bf16 v[64:67], v[112:115], v[176:179], v[64:67]\n"
        "ds_read_b128 v[200:203], v230 offset:6144\n"
        "v_mfma_f32_16x16x32_bf16 v[64:67], v[116:119], v[180:183], v[64:67]\n"
        "ds_read_b128 v[204:207], v231 offset:6144\n"
        "v_perm_b32 v226, v217, v236, s62\n"
        "v_mfma_f32_16x16x32_bf16 v[68:71], v[124:127], v[180:183], v[68:71]\n"
        "ds_read_b32 v166, v226\n"
        "v_perm_b32 v227, v217, v236, s63\n"
        "v_mfma_f32_16x16x32_bf16 v[68:71], v[120:123], v[176:179], v[68:71]\n"
        "ds_read_b32 v167, v227\n"
        "v_perm_b32 v224, v220, v236, s60\n"
        "v_mfma_f32_16x16x32_bf16 v[72:75], v[128:131], v[176:179], v[72:75]\n"
        "ds_read_b32 v168, v224\n"
        "v_perm_b32 v225, v220, v236, s61\n"
        "v_mfma_f32_16x16x32_bf16 v[72:75], v[132:135], v[180:183], v[72:75]\n"
        "ds_read_b32 v169, v225\n"
        "v_perm_b32 v226, v220, v236, s62\n"
        "s_waitcnt lgkmcnt(15)\n"
        "v_mfma_f32_16x16x32_bf16 v[84:87], v[132:135], v[188:191], v[84:87]\n"
        "ds_read_b32 v170, v226\n"
        "v_perm_b32 v227, v220, v236, s63\n"
        "v_mfma_f32_16x16x32_bf16 v[84:87], v[128:131], v[184:187], v[84:87]\n"
        "ds_read_b32 v171, v227\n"
        "v_perm_b32 v224, v221, v236, s60\n"
        "v_mfma_f32_16x16x32_bf16 v[80:83], v[120:123], v[184:187], v[80:83]\n"
        "ds_read_b32 v172, v224\n"
        "v_perm_b32 v225, v221, v236, s61\n"
        "v_mfma_f32_16x16x32_bf16 v[80:83], v[124:127], v[188:191], v[80:83]\n"
        "ds_read_b32 v173, v225\n"
        "v_perm_b32 v226, v221, v236, s62\n"
        "v_mfma_f32_16x16x32_bf16 v[76:79], v[116:119], v[188:191], v[76:79]\n"
        "ds_read_b32 v174, v226\n"
        "v_perm_b32 v227, v221, v236, s63\n"
        "v_mfma_f32_16x16x32_bf16 v[76:79], v[112:115], v[184:187], v[76:79]\n"
        "ds_read_b32 v175, v227\n"
        "s_waitcnt lgkmcnt(10)\n"
        "s_barrier\n"
        "v_mfma_f32_16x16x32_bf16 v[88:91], v[112:115], v[192:195], v[88:91]\n"
        "ds_read_b128 v[208:211], v232 offset:0\n"
        "v_mfma_f32_16x16x32_bf16 v[88:91], v[116:119], v[196:199], v[88:91]\n"
        "ds_read_b128 v[212:215], v233 offset:0\n"
        "v_mfma_f32_16x16x32_bf16 v[92:95], v[124:127], v[196:199], v[92:95]\n"
        "v_mfma_f32_16x16x32_bf16 v[92:95], v[120:123], v[192:195], v[92:95]\n"
        "ds_read_b128 v[216:219], v234 offset:0\n"
        "v_mfma_f32_16x16x32_bf16 v[96:99], v[128:131], v[192:195], v[96:99]\n"
        "ds_read_b128 v[220:223], v235 offset:0\n"
        "v_mfma_f32_16x16x32_bf16 v[96:99], v[132:135], v[196:199], v[96:99]\n"
        "v_mfma_f32_16x16x32_bf16 v[108:111], v[132:135], v[204:207], v[108:111]\n"
        "ds_read_b128 v[176:179], v228 offset:16384\n"
        "v_mfma_f32_16x16x32_bf16 v[108:111], v[128:131], v[200:203], v[108:111]\n"
        "ds_read_b128 v[180:183], v229 offset:16384\n"
        "v_mfma_f32_16x16x32_bf16 v[104:107], v[120:123], v[200:203], v[104:107]\n"
        "v_mfma_f32_16x16x32_bf16 v[104:107], v[124:127], v[204:207], v[104:107]\n"
        "ds_read_b128 v[184:187], v228 offset:18432\n"
        "v_mfma_f32_16x16x32_bf16 v[100:103], v[116:119], v[204:207], v[100:103]\n"
        "ds_read_b128 v[188:191], v229 offset:18432\n"
        "v_mfma_f32_16x16x32_bf16 v[100:103], v[112:115], v[200:203], v[100:103]\n"
        "s_waitcnt lgkmcnt(2)\n"
        "v_mfma_f32_16x16x32_bf16 v[0:3], v[144:147], v[176:179], v[0:3]\n"
        "ds_read_b128 v[192:195], v228 offset:20480\n"
        "v_bitop3_b32 v210, v210, v214, s34 bitop3:0x7e\n"
        "v_mfma_f32_16x16x32_bf16 v[0:3], v[148:151], v[180:183], v[0:3]\n"
        "ds_read_b128 v[196:199], v229 offset:20480\n"
        "v_bitop3_b32 v211, v211, v215, s39 bitop3:0x7e\n"
        "v_mfma_f32_16x16x32_bf16 v[4:7], v[156:159], v[180:183], v[4:7]\n"
        "ds_read_b128 v[200:203], v228 offset:22528\n"
        "v_bitop3_b32 v218, v218, v222, s34 bitop3:0x7e\n"
        "v_mfma_f32_16x16x32_bf16 v[4:7], v[152:155], v[176:179], v[4:7]\n"
        "ds_read_b128 v[204:207], v229 offset:22528\n"
        "v_perm_b32 v224, v208, v236, s60\n"
        "v_mfma_f32_16x16x32_bf16 v[8:11], v[160:163], v[176:179], v[8:11]\n"
        "ds_read_b32 v112, v224\n"
        "v_perm_b32 v225, v208, v236, s61\n"
        "v_mfma_f32_16x16x32_bf16 v[8:11], v[164:167], v[180:183], v[8:11]\n"
        "ds_read_b32 v113, v225\n"
        "v_perm_b32 v226, v208, v236, s62\n"
        "v_mfma_f32_16x16x32_bf16 v[12:15], v[172:175], v[180:183], v[12:15]\n"
        "ds_read_b32 v114, v226\n"
        "v_perm_b32 v227, v208, v236, s63\n"
        "v_mfma_f32_16x16x32_bf16 v[12:15], v[168:171], v[176:179], v[12:15]\n"
        "ds_read_b32 v115, v227\n"
        "v_bitop3_b32 v219, v219, v223, s39 bitop3:0x7e\n"
        "s_waitcnt lgkmcnt(8)\n"
        "v_mfma_f32_16x16x32_bf16 v[28:31], v[168:171], v[184:187], v[28:31]\n"
        "ds_read_b128 v[176:179], v230 offset:16384\n"
        "v_or3_b32 v210, v210, v211, v218\n"
        "v_mfma_f32_16x16x32_bf16 v[28:31], v[172:175], v[188:191], v[28:31]\n"
        "ds_read_b128 v[180:183], v231 offset:16384\n"
        "v_perm_b32 v224, v209, v236, s60\n"
        "v_mfma_f32_16x16x32_bf16 v[24:27], v[164:167], v[188:191], v[24:27]\n"
        "ds_read_b32 v116, v224\n"
        "v_perm_b32 v225, v209, v236, s61\n"
        "v_mfma_f32_16x16x32_bf16 v[24:27], v[160:163], v[184:187], v[24:27]\n"
        "ds_read_b32 v117, v225\n"
        "v_perm_b32 v226, v209, v236, s62\n"
        "v_mfma_f32_16x16x32_bf16 v[20:23], v[152:155], v[184:187], v[20:23]\n"
        "ds_read_b32 v118, v226\n"
        "v_perm_b32 v227, v209, v236, s63\n"
        "v_mfma_f32_16x16x32_bf16 v[20:23], v[156:159], v[188:191], v[20:23]\n"
        "ds_read_b32 v119, v227\n"
        "v_perm_b32 v224, v212, v236, s60\n"
        "v_mfma_f32_16x16x32_bf16 v[16:19], v[148:151], v[188:191], v[16:19]\n"
        "ds_read_b32 v120, v224\n"
        "v_perm_b32 v225, v212, v236, s61\n"
        "v_mfma_f32_16x16x32_bf16 v[16:19], v[144:147], v[184:187], v[16:19]\n"
        "ds_read_b32 v121, v225\n"
        "v_or_b32 v210, v210, v219\n"
        "s_waitcnt lgkmcnt(14)\n"
        "v_mfma_f32_16x16x32_bf16 v[32:35], v[144:147], v[192:195], v[32:35]\n"
        "ds_read_b128 v[184:187], v230 offset:18432\n"
        "v_cmp_ne_u32 vcc, 0, v210\n"
        "v_mfma_f32_16x16x32_bf16 v[32:35], v[148:151], v[196:199], v[32:35]\n"
        "ds_read_b128 v[188:191], v231 offset:18432\n"
        "v_perm_b32 v226, v212, v236, s62\n"
        "v_mfma_f32_16x16x32_bf16 v[36:39], v[156:159], v[196:199], v[36:39]\n"
        "ds_read_b32 v122, v226\n"
        "v_perm_b32 v227, v212, v236, s63\n"
        "v_mfma_f32_16x16x32_bf16 v[36:39], v[152:155], v[192:195], v[36:39]\n"
        "ds_read_b32 v123, v227\n"
        "v_perm_b32 v224, v213, v236, s60\n"
        "v_mfma_f32_16x16x32_bf16 v[40:43], v[160:163], v[192:195], v[40:43]\n"
        "ds_read_b32 v124, v224\n"
        "v_perm_b32 v225, v213, v236, s61\n"
        "v_mfma_f32_16x16x32_bf16 v[40:43], v[164:167], v[196:199], v[40:43]\n"
        "ds_read_b32 v125, v225\n"
        "v_perm_b32 v226, v213, v236, s62\n"
        "v_mfma_f32_16x16x32_bf16 v[44:47], v[172:175], v[196:199], v[44:47]\n"
        "ds_read_b32 v126, v226\n"
        "v_perm_b32 v227, v213, v236, s63\n"
        "v_mfma_f32_16x16x32_bf16 v[44:47], v[168:171], v[192:195], v[44:47]\n"
        "ds_read_b32 v127, v227\n"
        "s_waitcnt lgkmcnt(15)\n"
        "v_mfma_f32_16x16x32_bf16 v[60:63], v[168:171], v[200:203], v[60:63]\n"
        "ds_read_b128 v[192:195], v230 offset:20480\n"
        "s_cbranch_vccnz .Ln_f10\n"
        ".Ln_b10:\n"
        "v_mfma_f32_16x16x32_bf16 v[60:63], v[172:175], v[204:207], v[60:63]\n"
        "ds_read_b128 v[196:199], v231 offset:20480\n"
        "v_perm_b32 v224, v216, v236, s60\n"
        "v_mfma_f32_16x16x32_bf16 v[56:59], v[164:167], v[204:207], v[56:59]\n"
        "ds_read_b32 v128, v224\n"
        "v_perm_b32 v225, v216, v236, s61\n"
        "v_mfma_f32_16x16x32_bf16 v[56:59], v[160:163], v[200:203], v[56:59]\n"
        "ds_read_b32 v129, v225\n"
        "v_perm_b32 v226, v216, v236, s62\n"
        "v_mfma_f32_16x16x32_bf16 v[52:55], v[152:155], v[200:203], v[52:55]\n"
        "ds_read_b32 v130, v226\n"
        "v_perm_b32 v227, v216, v236, s63\n"
        "v_mfma_f32_16x16x32_bf16 v[52:55], v[156:159], v[204:207], v[52:55]\n"
        "ds_read_b32 v131, v227\n"
        "v_perm_b32 v224, v217, v236, s60\n"
        "v_mfma_f32_16x16x32_bf16 v[48:51], v[148:151], v[204:207], v[48:51]\n"
        "ds_read_b32 v132, v224\n"
        "v_perm_b32 v225, v217, v236, s61\n"
        "v_mfma_f32_16x16x32_bf16 v[48:51], v[144:147], v[200:203], v[48:51]\n"
        "ds_read_b32 v133, v225\n"
        "s_waitcnt lgkmcnt(15)\n"
        "v_mfma_f32_16x16x32_bf16 v[64:67], v[144:147], v[176:179], v[64:67]\n"
        "ds_read_b128 v[200:203], v230 offset:22528\n"
        "v_mfma_f32_16x16x32_bf16 v[64:67], v[148:151], v[180:183], v[64:67]\n"
        "ds_read_b128 v[204:207], v231 offset:22528\n"
        "v_perm_b32 v226, v217, v236, s62\n"
        "v_mfma_f32_16x16x32_bf16 v[68:71], v[156:159], v[180:183], v[68:71]\n"
        "ds_read_b32 v134, v226\n"
        "v_perm_b32 v227, v217, v236, s63\n"
        "v_mfma_f32_16x16x32_bf16 v[68:71], v[152:155], v[176:179], v[68:71]\n"
        "ds_read_b32 v135, v227\n"
        "v_perm_b32 v224, v220, v236, s60\n"
        "v_mfma_f32_16x16x32_bf16 v[72:75], v[160:163], v[176:179], v[72:75]\n"
        "ds_read_b32 v136, v224\n"
        ".Ln_tp2:\n"
        "s_cbranch_execz .Ln_ret\n"
        "v_perm_b32 v225, v220, v236, s61\n"
        "v_mfma_f32_16x16x32_bf16 v[72:75], v[164:167], v[180:183], v[72:75]\n"
        "ds_read_b32 v137, v225\n"
        "v_perm_b32 v226, v220, v236, s62\n"
        "s_waitcnt lgkmcnt(15)\n"
        "v_mfma_f32_16x16x32_bf16 v[84:87], v[164:167], v[188:191], v[84:87]\n"
        "ds_read_b32 v138, v226\n"
        "v_perm_b32 v227, v220, v236, s63\n"
        "v_mfma_f32_16x16x32_bf16 v[84:87], v[160:163], v[184:187], v[84:87]\n"
        "ds_read_b32 v139, v227\n"
        "v_perm_b32 v224, v221, v236, s60\n"
        "v_mfma_f32_16x16x32_bf16 v[80:83], v[152:155], v[184:187], v[80:83]\n"
        "ds_read_b32 v140, v224\n"
        "v_perm_b32 v225, v221, v236, s61\n"
        "v_mfma_f32_16x16x32_bf16 v[80:83], v[156:159], v[188:191], v[80:83]\n"
        "ds_read_b32 v141, v225\n"
        "v_perm_b32 v226, v221, v236, s62\n"
        "v_mfma_f32_16x16x32_bf16 v[76:79], v[148:151], v[188:191], v[76:79]\n"
        "ds_read_b32 v142, v226\n"
        "v_perm_b32 v227, v221, v236, s63\n"
        "v_mfma_f32_16x16x32_bf16 v[76:79], v[144:147], v[184:187], v[76:79]\n"
        "ds_read_b32 v143, v227\n"
        "s_waitcnt lgkmcnt(10)\n"
        "s_barrier\n"
        "v_mfma_f32_16x16x32_bf16 v[88:91], v[144:147], v[192:195], v[88:91]\n"
        "ds_read_b128 v[208:211], v232 offset:16384\n"
        "v_mfma_f32_16x16x32_bf16 v[88:91], v[148:151], v[196:199], v[88:91]\n"
        "ds_read_b128 v[212:215], v233 offset:16384\n"
        "v_mfma_f32_16x16x32_bf16 v[92:95], v[156:159], v[196:199], v[92:95]\n"
        "v_mfma_f32_16x16x32_bf16 v[92:95], v[152:155], v[192:195], v[92:95]\n"
        "ds_read_b128 v[216:219], v234 offset:16384\n"
        "v_mfma_f32_16x16x32_bf16 v[96:99], v[160:163], v[192:195], v[96:99]\n"
        "ds_read_b128 v[220:223], v235 offset:16384\n"
        "v_mfma_f32_16x16x32_bf16 v[96:99], v[164:167], v[196:199], v[96:99]\n"
        "v_mfma_f32_16x16x32_bf16 v[108:111], v[164:167], v[204:207], v[108:111]\n"
        "ds_read_b128 v[176:179], v228 offset:32768\n"
        "v_mfma_f32_16x16x32_bf16 v[108:111], v[160:163], v[200:203], v[108:111]\n"
        "ds_read_b128 v[180:183], v229 offset:32768\n"
        "v_mfma_f32_16x16x32_bf16 v[104:107], v[152:155], v[200:203], v[104:107]\n"
        "v_mfma_f32_16x16x32_bf16 v[104:107], v[156:159], v[204:207], v[104:107]\n"
        "ds_read_b128 v[184:187], v228 offset:34816\n"
        "v_mfma_f32_16x16x32_bf16 v[100:103], v[148:151], v[204:207], v[100:103]\n"
        "ds_read_b128 v[188:191], v229 offset:34816\n"
        "v_mfma_f32_16x16x32_bf16 v[100:103], v[144:147], v[200:203], v[100:103]\n"
        "s_waitcnt lgkmcnt(2)\n"
        "v_mfma_f32_16x16x32_bf16 v[0:3], v[112:115], v[176:179], v[0:3]\n"
        "ds_read_b128 v[192:195], v228 offset:36864\n"
        "v_bitop3_b32 v210, v210, v214, s34 bitop3:0x7e\n"
        "v_mfma_f32_16x16x32_bf16 v[0:3], v[116:119], v[180:183], v[0:3]\n"
        "ds_read_b128 v[196:199], v229 offset:36864\n"
        "v_bitop3_b32 v211, v211, v215, s39 bitop3:0x7e\n"
        "v_mfma_f32_16x16x32_bf16 v[4:7], v[124:127], v[180:183], v[4:7]\n"
        "ds_read_b128 v[200:203], v228 offset:38912\n"
        "v_bitop3_b32 v218, v218, v222, s34 bitop3:0x7e\n"
        "v_mfma_f32_16x16x32_bf16 v[4:7], v[120:123], v[176:179], v[4:7]\n"
        "ds_read_b128 v[204:207], v229 offset:38912\n"
        "v_perm_b32 v224, v208, v236, s60\n"
        "v_mfma_f32_16x16x32_bf16 v[8:11], v[128:131], v[176:179], v[8:11]\n"
        "ds_read_b32 v144, v224\n"
        "v_perm_b32 v225, v208, v236, s61\n"
        "v_mfma_f32_16x16x32_bf16 v[8:11], v[132:135], v[180:183], v[8:11]\n"
        "ds_read_b32 v145, v225\n"
        "v_perm_b32 v226, v208, v236, s62\n"
        "v_mfma_f32_16x16x32_bf16 v[12:15], v[140:143], v[180:183], v[12:15]\n"
        "ds_read_b32 v146, v226\n"
        "v_perm_b32 v227, v208, v236, s63\n"
        "v_mfma_f32_16x16x32_bf16 v[12:15], v[136:139], v[176:179], v[12:15]\n"
        "ds_read_b32 v147, v227\n"
        "v_bitop3_b32 v219, v219, v223, s39 bitop3:0x7e\n"
        "s_waitcnt lgkmcnt(8)\n"
        "v_mfma_f32_16x16x32_bf16 v[28:31], v[136:139], v[184:187], v[28:31]\n"
        "ds_read_b128 v[176:179], v230 offset:32768\n"
        "v_or3_b32 v210, v210, v211, v218\n"
        "v_mfma_f32_16x16x32_bf16 v[28:31], v[140:143], v[188:191], v[28:31]\n"
        "ds_read_b128 v[180:183], v231 offset:32768\n"
        "v_perm_b32 v224, v209, v236, s60\n"
        "v_mfma_f32_16x16x32_bf16 v[24:27], v[132:135], v[188:191], v[24:27]\n"
        "ds_read_b32 v148, v224\n"
        "v_perm_b32 v225, v209, v236, s61\n"
        "v_mfma_f32_16x16x32_bf16 v[24:27], v[128:131], v[184:187], v[24:27]\n"
        "ds_read_b32 v149, v225\n"
        "v_perm_b32 v226, v209, v236, s62\n"
        "v_mfma_f32_16x16x32_bf16 v[20:23], v[120:123], v[184:187], v[20:23]\n"
        "ds_read_b32 v150, v226\n"
        "v_perm_b32 v227, v209, v236, s63\n"
        "v_mfma_f32_16x16x32_bf16 v[20:23], v[124:127], v[188:191], v[20:23]\n"
        "ds_read_b32 v151, v227\n"
        "v_perm_b32 v224, v212, v236, s60\n"
        "v_mfma_f32_16x16x32_bf16 v[16:19], v[116:119], v[188:191], v[16:19]\n"
        "ds_read_b32 v152, v224\n"
        "v_perm_b32 v225, v212, v236, s61\n"
        "v_mfma_f32_16x16x32_bf16 v[16:19], v[112:115], v[184:187], v[16:19]\n"
        "ds_read_b32 v153, v225\n"
        "v_or_b32 v210, v210, v219\n"
        "s_waitcnt lgkmcnt(14)\n"
        "v_mfma_f32_16x16x32_bf16 v[32:35], v[112:115], v[192:195], v[32:35]\n"
        "ds_read_b128 v[184:187], v230 offset:34816\n"
        "v_cmp_ne_u32 vcc, 0, v210\n"
        "v_mfma_f32_16x16x32_bf16 v[32:35], v[116:119], v[196:199], v[32:35]\n"
        "ds_read_b128 v[188:191], v231 offset:34816\n"
        "v_perm_b32 v226, v212, v236, s62\n"
        "v_mfma_f32_16x16x32_bf16 v[36:39], v[124:127], v[196:199], v[36:39]\n"
        "ds_read_b32 v154, v226\n"
        "v_perm_b32 v227, v212, v236, s63\n"
        "v_mfma_f32_16x16x32_bf16 v[36:39], v[120:123], v[192:195], v[36:39]\n"
        "ds_read_b32 v155, v227\n"
        "v_perm_b32 v224, v213, v236, s60\n"
        "v_mfma_f32_16x16x32_bf16 v[40:43], v[128:131], v[192:195], v[40:43]\n"
        "ds_read_b32 v156, v224\n"
        "v_perm_b32 v225, v213, v236, s61\n"
        "v_mfma_f32_16x16x32_bf16 v[40:43], v[132:135], v[196:199], v[40:43]\n"
        "ds_read_b32 v157, v225\n"
        "v_perm_b32 v226, v213, v236, s62\n"
        "v_mfma_f32_16x16x32_bf16 v[44:47], v[140:143], v[196:199], v[44:47]\n"
        "ds_read_b32 v158, v226\n"
        "v_perm_b32 v227, v213, v236, s63\n"
        "v_mfma_f32_16x16x32_bf16 v[44:47], v[136:139], v[192:195], v[44:47]\n"
        "ds_read_b32 v159, v227\n"
        "s_waitcnt lgkmcnt(15)\n"
        "v_mfma_f32_16x16x32_bf16 v[60:63], v[136:139], v[200:203], v[60:63]\n"
        "ds_read_b128 v[192:195], v230 offset:36864\n"
        "s_cbranch_vccnz .Ln_f11\n"
        ".Ln_b11:\n"
        "v_mfma_f32_16x16x32_bf16 v[60:63], v[140:143], v[204:207], v[60:63]\n"
        "ds_read_b128 v[196:199], v231 offset:36864\n"
        "v_perm_b32 v224, v216, v236, s60\n"
        "v_mfma_f32_16x16x32_bf16 v[56:59], v[132:135], v[204:207], v[56:59]\n"
        "ds_read_b32 v160, v224\n"
        "v_perm_b32 v225, v216, v236, s61\n"
        "v_mfma_f32_16x16x32_bf16 v[56:59], v[128:131], v[200:203], v[56:59]\n"
        "ds_read_b32 v161, v225\n"
        "v_perm_b32 v226, v216, v236, s62\n"
        "v_mfma_f32_16x16x32_bf16 v[52:55], v[120:123], v[200:203], v[52:55]\n"
        "ds_read_b32 v162, v226\n"
        "v_perm_b32 v227, v216, v236, s63\n"
        "v_mfma_f32_16x16x32_bf16 v[52:55], v[124:127], v[204:207], v[52:55]\n"
        "ds_read_b32 v163, v227\n"
        "v_perm_b32 v224, v217, v236, s60\n"
        "v_mfma_f32_16x16x32_bf16 v[48:51], v[116:119], v[204:207], v[48:51]\n"
        "ds_read_b32 v164, v224\n"
        "v_perm_b32 v225, v217, v236, s61\n"
        "v_mfma_f32_16x16x32_bf16 v[48:51], v[112:115], v[200:203], v[48:51]\n"
        "ds_read_b32 v165, v225\n"
        "s_waitcnt lgkmcnt(15)\n"
        "v_mfma_f32_16x16x32_bf16 v[64:67], v[112:115], v[176:179], v[64:67]\n"
        "ds_read_b128 v[200:203], v230 offset:38912\n"
        "v_mfma_f32_16x16x32_bf16 v[64:67], v[116:119], v[180:183], v[64:67]\n"
        "ds_read_b128 v[204:207], v231 offset:38912\n"
        "v_perm_b32 v226, v217, v236, s62\n"
        "v_mfma_f32_16x16x32_bf16 v[68:71], v[124:127], v[180:183], v[68:71]\n"
        "ds_read_b32 v166, v226\n"
        "v_perm_b32 v227, v217, v236, s63\n"
        "v_mfma_f32_16x16x32_bf16 v[68:71], v[120:123], v[176:179], v[68:71]\n"
        "ds_read_b32 v167, v227\n"
        "v_perm_b32 v224, v220, v236, s60\n"
        "v_mfma_f32_16x16x32_bf16 v[72:75], v[128:131], v[176:179], v[72:75]\n"
        "ds_read_b32 v168, v224\n"
        "v_perm_b32 v225, v220, v236, s61\n"
        "v_mfma_f32_16x16x32_bf16 v[72:75], v[132:135], v[180:183], v[72:75]\n"
        "ds_read_b32 v169, v225\n"
        "v_perm_b32 v226, v220, v236, s62\n"
        "s_waitcnt lgkmcnt(15)\n"
        "v_mfma_f32_16x16x32_bf16 v[84:87], v[132:135], v[188:191], v[84:87]\n"
        "ds_read_b32 v170, v226\n"
        "v_perm_b32 v227, v220, v236, s63\n"
        "v_mfma_f32_16x16x32_bf16 v[84:87], v[128:131], v[184:187], v[84:87]\n"
        "ds_read_b32 v171, v227\n"
        "v_perm_b32 v224, v221, v236, s60\n"
        "v_mfma_f32_16x16x32_bf16 v[80:83], v[120:123], v[184:187], v[80:83]\n"
        "ds_read_b32 v172, v224\n"
        "v_perm_b32 v225, v221, v236, s61\n"
        "v_mfma_f32_16x16x32_bf16 v[80:83], v[124:127], v[188:191], v[80:83]\n"
        "ds_read_b32 v173, v225\n"
        "v_perm_b32 v226, v221, v236, s62\n"
        "v_mfma_f32_16x16x32_bf16 v[76:79], v[116:119], v[188:191], v[76:79]\n"
        "ds_read_b32 v174, v226\n"
        "v_perm_b32 v227, v221, v236, s63\n"
        "v_mfma_f32_16x16x32_bf16 v[76:79], v[112:115], v[184:187], v[76:79]\n"
        "ds_read_b32 v175, v227\n"
        "s_waitcnt lgkmcnt(10)\n"
        "s_barrier\n"
        "v_mfma_f32_16x16x32_bf16 v[88:91], v[112:115], v[192:195], v[88:91]\n"
        "ds_read_b128 v[208:211], v232 offset:32768\n"
        "v_mfma_f32_16x16x32_bf16 v[88:91], v[116:119], v[196:199], v[88:91]\n"
        "ds_read_b128 v[212:215], v233 offset:32768\n"
        "v_mfma_f32_16x16x32_bf16 v[92:95], v[124:127], v[196:199], v[92:95]\n"
        "v_mfma_f32_16x16x32_bf16 v[92:95], v[120:123], v[192:195], v[92:95]\n"
        "ds_read_b128 v[216:219], v234 offset:32768\n"
        "v_mfma_f32_16x16x32_bf16 v[96:99], v[128:131], v[192:195], v[96:99]\n"
        "ds_read_b128 v[220:223], v235 offset:32768\n"
        "v_mfma_f32_16x16x32_bf16 v[96:99], v[132:135], v[196:199], v[96:99]\n"
        "v_mfma_f32_16x16x32_bf16 v[108:111], v[132:135], v[204:207], v[108:111]\n"
        "ds_read_b128 v[176:179], v228 offset:0\n"
        "v_mfma_f32_16x16x32_bf16 v[108:111], v[128:131], v[200:203], v[108:111]\n"
        "ds_read_b128 v[180:183], v229 offset:0\n"
        "v_mfma_f32_16x16x32_bf16 v[104:107], v[120:123], v[200:203], v[104:107]\n"
        "v_mfma_f32_16x16x32_bf16 v[104:107], v[124:127], v[204:207], v[104:107]\n"
        "ds_read_b128 v[184:187], v228 offset:2048\n"
        "v_mfma_f32_16x16x32_bf16 v[100:103], v[116:119], v[204:207], v[100:103]\n"
        "ds_read_b128 v[188:191], v229 offset:2048\n"
        "v_mfma_f32_16x16x32_bf16 v[100:103], v[112:115], v[200:203], v[100:103]\n"
        "s_waitcnt lgkmcnt(2)\n"
        "v_mfma_f32_16x16x32_bf16 v[0:3], v[144:147], v[176:179], v[0:3]\n"
        "ds_read_b128 v[192:195], v228 offset:4096\n"
        "v_bitop3_b32 v210, v210, v214, s34 bitop3:0x7e\n"
        "v_mfma_f32_16x16x32_bf16 v[0:3], v[148:151], v[180:183], v[0:3]\n"
        "ds_read_b128 v[196:199], v229 offset:4096\n"
        "v_bitop3_b32 v211, v211, v215, s39 bitop3:0x7e\n"
        "v_mfma_f32_16x16x32_bf16 v[4:7], v[156:159], v[180:183], v[4:7]\n"
        "ds_read_b128 v[200:203], v228 offset:6144\n"
        "v_bitop3_b32 v218, v218, v222, s34 bitop3:0x7e\n"
        "v_mfma_f32_16x16x32_bf16 v[4:7], v[152:155], v[176:179], v[4:7]\n"
        "ds_read_b128 v[204:207], v229 offset:6144\n"
        "v_perm_b32 v224, v208, v236, s60\n"
        "v_mfma_f32_16x16x32_bf16 v[8:11], v[160:163], v[176:179], v[8:11]\n"
        "ds_read_b32 v112, v224\n"
        "v_perm_b32 v225, v208, v236, s61\n"
        "v_mfma_f32_16x16x32_bf16 v[8:11], v[164:167], v[180:183], v[8:11]\n"
        "ds_read_b32 v113, v225\n"
        "v_perm_b32 v226, v208, v236, s62\n"
        "v_mfma_f32_16x16x32_bf16 v[12:15], v[172:175], v[180:183], v[12:15]\n"
        "ds_read_b32 v114, v226\n"
        "v_perm_b32 v227, v208, v236, s63\n"
        "v_mfma_f32_16x16x32_bf16 v[12:15], v[168:171], v[176:179], v[12:15]\n"
        "ds_read_b32 v115, v227\n"
        "v_bitop3_b32 v219, v219, v223, s39 bitop3:0x7e\n"
        "s_waitcnt lgkmcnt(8)\n"
        "v_mfma_f32_16x16x32_bf16 v[28:31], v[168:171], v[184:187], v[28:31]\n"
        "ds_read_b128 v[176:179], v230 offset:0\n"
        "v_or3_b32 v210, v210, v211, v218\n"
        "v_mfma_f32_16x16x32_bf16 v[28:31], v[172:175], v[188:191], v[28:31]\n"
        "ds_read_b128 v[180:183], v231 offset:0\n"
        "v_perm_b32 v224, v209, v236, s60\n"
        "v_mfma_f32_16x16x32_bf16 v[24:27], v[164:167], v[188:191], v[24:27]\n"
        "ds_read_b32 v116, v224\n"
        "v_perm_b32 v225, v209, v236, s61\n"
        "v_mfma_f32_16x16x32_bf16 v[24:27], v[160:163], v[184:187], v[24:27]\n"
        "ds_read_b32 v117, v225\n"
        "v_perm_b32 v226, v209, v236, s62\n"
        "v_mfma_f32_16x16x32_bf16 v[20:23], v[152:155], v[184:187], v[20:23]\n"
        "ds_read_b32 v118, v226\n"
        "v_perm_b32 v227, v209, v236, s63\n"
        "v_mfma_f32_16x16x32_bf16 v[20:23], v[156:159], v[188:191], v[20:23]\n"
        "ds_read_b32 v119, v227\n"
        "v_perm_b32 v224, v212, v236, s60\n"
        "v_mfma_f32_16x16x32_bf16 v[16:19], v[148:151], v[188:191], v[16:19]\n"
        "ds_read_b32 v120, v224\n"
        "v_perm_b32 v225, v212, v236, s61\n"
        "v_mfma_f32_16x16x32_bf16 v[16:19], v[144:147], v[184:187], v[16:19]\n"
        "ds_read_b32 v121, v225\n"
        "v_or_b32 v210, v210, v219\n"
        "s_waitcnt lgkmcnt(14)\n"
        "v_mfma_f32_16x16x32_bf16 v[32:35], v[144:147], v[192:195], v[32:35]\n"
        "ds_read_b128 v[184:187], v230 offset:2048\n"
        "v_cmp_ne_u32 vcc, 0, v210\n"
        "v_mfma_f32_16x16x32_bf16 v[32:35], v[148:151], v[196:199], v[32:35]\n"
        "ds_read_b128 v[188:191], v231 offset:2048\n"
        "v_perm_b32 v226, v212, v236, s62\n"
        "v_mfma_f32_16x16x32_bf16 v[36:39], v[156:159], v[196:199], v[36:39]\n"
        "ds_read_b32 v122, v226\n"
        "v_perm_b32 v227, v212, v236, s63\n"
        "v_mfma_f32_16x16x32_bf16 v[36:39], v[152:155], v[192:195], v[36:39]\n"
        "ds_read_b32 v123, v227\n"
        "v_perm_b32 v224, v213, v236, s60\n"
        "v_mfma_f32_16x16x32_bf16 v[40:43], v[160:163], v[192:195], v[40:43]\n"
        "ds_read_b32 v124, v224\n"
        "v_perm_b32 v225, v213, v236, s61\n"
        "v_mfma_f32_16x16x32_bf16 v[40:43], v[164:167], v[196:199], v[40:43]\n"
        "ds_read_b32 v125, v225\n"
        "v_perm_b32 v226, v213, v236, s62\n"
        "v_mfma_f32_16x16x32_bf16 v[44:47], v[172:175], v[196:199], v[44:47]\n"
        "ds_read_b32 v126, v226\n"
        "v_perm_b32 v227, v213, v236, s63\n"
        "v_mfma_f32_16x16x32_bf16 v[44:47], v[168:171], v[192:195], v[44:47]\n"
        "ds_read_b32 v127, v227\n"
        "s_waitcnt lgkmcnt(15)\n"
        "v_mfma_f32_16x16x32_bf16 v[60:63], v[168:171], v[200:203], v[60:63]\n"
        "ds_read_b128 v[192:195], v230 offset:4096\n"
        "s_cbranch_vccnz .Ln_f12\n"
        ".Ln_b12:\n"
        "v_mfma_f32_16x16x32_bf16 v[60:63], v[172:175], v[204:207], v[60:63]\n"
        "ds_read_b128 v[196:199], v231 offset:4096\n"
        "v_perm_b32 v224, v216, v236, s60\n"
        "v_mfma_f32_16x16x32_bf16 v[56:59], v[164:167], v[204:207], v[56:59]\n"
        "ds_read_b32 v128, v224\n"
        "v_perm_b32 v225, v216, v236, s61\n"
        "v_mfma_f32_16x16x32_bf16 v[56:59], v[160:163], v[200:203], v[56:59]\n"
        "ds_read_b32 v129, v225\n"
        "v_perm_b32 v226, v216, v236, s62\n"
        "v_mfma_f32_16x16x32_bf16 v[52:55], v[152:155], v[200:203], v[52:55]\n"
        "ds_read_b32 v130, v226\n"
        "v_perm_b32 v227, v216, v236, s63\n"
        "v_mfma_f32_16x16x32_bf16 v[52:55], v[156:159], v[204:207], v[52:55]\n"
        "ds_read_b32 v131, v227\n"
        "v_perm_b32 v224, v217, v236, s60\n"
        "v_mfma_f32_16x16x32_bf16 v[48:51], v[148:151], v[204:207], v[48:51]\n"
        "ds_read_b32 v132, v224\n"
        "v_perm_b32 v225, v217, v236, s61\n"
        "v_mfma_f32_16x16x32_bf16 v[48:51], v[144:147], v[200:203], v[48:51]\n"
        "ds_read_b32 v133, v225\n"
        "s_waitcnt lgkmcnt(15)\n"
        "v_mfma_f32_16x16x32_bf16 v[64:67], v[144:147], v[176:179], v[64:67]\n"
        "ds_read_b128 v[200:203], v230 offset:6144\n"
        "v_mfma_f32_16x16x32_bf16 v[64:67], v[148:151], v[180:183], v[64:67]\n"
        "ds_read_b128 v[204:207], v231 offset:6144\n"
        "v_perm_b32 v226, v217, v236, s62\n"
        "v_mfma_f32_16x16x32_bf16 v[68:71], v[156:159], v[180:183], v[68:71]\n"
        "ds_read_b32 v134, v226\n"
        "v_perm_b32 v227, v217, v236, s63\n"
        "v_mfma_f32_16x16x32_bf16 v[68:71], v[152:155], v[176:179], v[68:71]\n"
        "ds_read_b32 v135, v227\n"
        "v_perm_b32 v224, v220, v236, s60\n"
        "v_mfma_f32_16x16x32_bf16 v[72:75], v[160:163], v[176:179], v[72:75]\n"
        "ds_read_b32 v136, v224\n"
        "v_perm_b32 v225, v220, v236, s61\n"
        "v_mfma_f32_16x16x32_bf16 v[72:75], v[164:167], v[180:183], v[72:75]\n"
        "ds_read_b32 v137, v225\n"
        "v_perm_b32 v226, v220, v236, s62\n"
        "s_waitcnt lgkmcnt(15)\n"
        "v_mfma_f32_16x16x32_bf16 v[84:87], v[164:167], v[188:191], v[84:87]\n"
        "ds_read_b32 v138, v226\n"
        "v_perm_b32 v227, v220, v236, s63\n"
        "v_mfma_f32_16x16x32_bf16 v[84:87], v[160:163], v[184:187], v[84:87]\n"
        "ds_read_b32 v139, v227\n"
        "v_perm_b32 v224, v221, v236, s60\n"
        "v_mfma_f32_16x16x32_bf16 v[80:83], v[152:155], v[184:187], v[80:83]\n"
        "ds_read_b32 v140, v224\n"
        "v_perm_b32 v225, v221, v236, s61\n"
        "v_mfma_f32_16x16x32_bf16 v[80:83], v[156:159], v[188:191], v[80:83]\n"
        "ds_read_b32 v141, v225\n"
        "v_perm_b32 v226, v221, v236, s62\n"
        "v_mfma_f32_16x16x32_bf16 v[76:79], v[148:151], v[188:191], v[76:79]\n"
        "ds_read_b32 v142, v226\n"
        "v_perm_b32 v227, v221, v236, s63\n"
        "v_mfma_f32_16x16x32_bf16 v[76:79], v[144:147], v[184:187], v[76:79]\n"
        "ds_read_b32 v143, v227\n"
        "s_waitcnt lgkmcnt(10)\n"
        "s_barrier\n"
        "v_mfma_f32_16x16x32_bf16 v[88:91], v[144:147], v[192:195], v[88:91]\n"
        "ds_read_b128 v[208:211], v232 offset:0\n"
        "v_mfma_f32_16x16x32_bf16 v[88:91], v[148:151], v[196:199], v[88:91]\n"
        "ds_read_b128 v[212:215], v233 offset:0\n"
        "v_mfma_f32_16x16x32_bf16 v[92:95], v[156:159], v[196:199], v[92:95]\n"
        "v_mfma_f32_16x16x32_bf16 v[92:95], v[152:155], v[192:195], v[92:95]\n"
        "ds_read_b128 v[216:219], v234 offset:0\n"
        "v_mfma_f32_16x16x32_bf16 v[96:99], v[160:163], v[192:195], v[96:99]\n"
        "ds_read_b128 v[220:223], v235 offset:0\n"
        "v_mfma_f32_16x16x32_bf16 v[96:99], v[164:167], v[196:199], v[96:99]\n"
        "v_mfma_f32_16x16x32_bf16 v[108:111], v[164:167], v[204:207], v[108:111]\n"
        "ds_read_b128 v[176:179], v228 offset:16384\n"
        "v_mfma_f32_16x16x32_bf16 v[108:111], v[160:163], v[200:203], v[108:111]\n"
        "ds_read_b128 v[180:183], v229 offset:16384\n"
        "v_mfma_f32_16x16x32_bf16 v[104:107], v[152:155], v[200:203], v[104:107]\n"
        "v_mfma_f32_16x16x32_bf16 v[104:107], v[156:159], v[204:207], v[104:107]\n"
        "ds_read_b128 v[184:187], v228 offset:18432\n"
        "v_mfma_f32_16x16x32_bf16 v[100:103], v[148:151], v[204:207], v[100:103]\n"
        "ds_read_b128 v[188:191], v229 offset:18432\n"
        "v_mfma_f32_16x16x32_bf16 v[100:103], v[144:147], v[200:203], v[100:103]\n"
        "s_waitcnt lgkmcnt(2)\n"
        "v_mfma_f32_16x16x32_bf16 v[0:3], v[112:115], v[176:179], v[0:3]\n"
        "ds_read_b128 v[192:195], v228 offset:20480\n"
        "v_bitop3_b32 v210, v210, v214, s34 bitop3:0x7e\n"
        "v_mfma_f32_16x16x32_bf16 v[0:3], v[116:119], v[180:183], v[0:3]\n"
        "ds_read_b128 v[196:199], v229 offset:20480\n"
        "v_bitop3_b32 v211, v211, v215, s39 bitop3:0x7e\n"
        "v_mfma_f32_16x16x32_bf16 v[4:7], v[124:127], v[180:183], v[4:7]\n"
        "ds_read_b128 v[200:203], v228 offset:22528\n"
        "v_bitop3_b32 v218, v218, v222, s34 bitop3:0x7e\n"
        "v_mfma_f32_16x16x32_bf16 v[4:7], v[120:123], v[176:179], v[4:7]\n"
        "ds_read_b128 v[204:207], v229 offset:22528\n"
        "v_perm_b32 v224, v208, v236, s60\n"
        "v_mfma_f32_16x16x32_bf16 v[8:11], v[128:131], v[176:179], v[8:11]\n"
        "ds_read_b32 v144, v224\n"
        "v_perm_b32 v225, v208, v236, s61\n"
        "v_mfma_f32_16x16x32_bf16 v[8:11], v[132:135], v[180:183], v[8:11]\n"
        "ds_read_b32 v145, v225\n"
        "v_perm_b32 v226, v208, v236, s62\n"
        "v_mfma_f32_16x16x32_bf16 v[12:15], v[140:143], v[180:183], v[12:15]\n"
        "ds_read_b32 v146, v226\n"
        "v_perm_b32 v227, v208, v236, s63\n"
        "v_mfma_f32_16x16x32_bf16 v[12:15], v[136:139], v[176:179], v[12:15]\n"
        "ds_read_b32 v147, v227\n"
        "v_bitop3_b32 v219, v219, v223, s39 bitop3:0x7e\n"
        "s_waitcnt lgkmcnt(8)\n"
        "v_mfma_f32_16x16x32_bf16 v[28:31], v[136:139], v[184:187], v[28:31]\n"
        "ds_read_b128 v[176:179], v230 offset:16384\n"
        "v_or3_b32 v210, v210, v211, v218\n"
        "v_mfma_f32_16x16x32_bf16 v[28:31], v[140:143], v[188:191], v[28:31]\n"
        "ds_read_b128 v[180:183], v231 offset:16384\n"
        "v_perm_b32 v224, v209, v236, s60\n"
        "v_mfma_f32_16x16x32_bf16 v[24:27], v[132:135], v[188:191], v[24:27]\n"
        "ds_read_b32 v148, v224\n"
        "v_perm_b32 v225, v209, v236, s61\n"
        "v_mfma_f32_16x16x32_bf16 v[24:27], v[128:131], v[184:187], v[24:27]\n"
        "ds_read_b32 v149, v225\n"
        "v_perm_b32 v226, v209, v236, s62\n"
        "v_mfma_f32_16x16x32_bf16 v[20:23], v[120:123], v[184:187], v[20:23]\n"
        "ds_read_b32 v150, v226\n"
        "v_perm_b32 v227, v209, v236, s63\n"
        "v_mfma_f32_16x16x32_bf16 v[20:23], v[124:127], v[188:191], v[20:23]\n"
        "ds_read_b32 v151, v227\n"
        "v_perm_b32 v224, v212, v236, s60\n"
        "v_mfma_f32_16x16x32_bf16 v[16:19], v[116:119], v[188:191], v[16:19]\n"
        "ds_read_b32 v152, v224\n"
        "v_perm_b32 v225, v212, v236, s61\n"
        "v_mfma_f32_16x16x32_bf16 v[16:19], v[112:115], v[184:187], v[16:19]\n"
        "ds_read_b32 v153, v225\n"
        "v_or_b32 v210, v210, v219\n"
        "s_waitcnt lgkmcnt(14)\n"
        "v_mfma_f32_16x16x32_bf16 v[32:35], v[112:115], v[192:195], v[32:35]\n"
        "ds_read_b128 v[184:187], v230 offset:18432\n"
        "v_cmp_ne_u32 vcc, 0, v210\n"
        "v_mfma_f32_16x16x32_bf16 v[32:35], v[116:119], v[196:199], v[32:35]\n"
        "ds_read_b128 v[188:191], v231 offset:18432\n"
        "v_perm_b32 v226, v212, v236, s62\n"
        "v_mfma_f32_16x16x32_bf16 v[36:39], v[124:127], v[196:199], v[36:39]\n"
        "ds_read_b32 v154, v226\n"
        "v_perm_b32 v227, v212, v236, s63\n"
        "v_mfma_f32_16x16x32_bf16 v[36:39], v[120:123], v[192:195], v[36:39]\n"
        "ds_read_b32 v155, v227\n"
        "v_perm_b32 v224, v213, v236, s60\n"
        "v_mfma_f32_16x16x32_bf16 v[40:43], v[128:131], v[192:195], v[40:43]\n"
        "ds_read_b32 v156, v224\n"
        "v_perm_b32 v225, v213, v236, s61\n"
        "v_mfma_f32_16x16x32_bf16 v[40:43], v[132:135], v[196:199], v[40:43]\n"
        "ds_read_b32 v157, v225\n"
        "v_perm_b32 v226, v213, v236, s62\n"
        "v_mfma_f32_16x16x32_bf16 v[44:47], v[140:143], v[196:199], v[44:47]\n"
        "ds_read_b32 v158, v226\n"
        "v_perm_b32 v227, v213, v236, s63\n"
        "v_mfma_f32_16x16x32_bf16 v[44:47], v[136:139], v[192:195], v[44:47]\n"
        "ds_read_b32 v159, v227\n"
        "s_waitcnt lgkmcnt(15)\n"
        "v_mfma_f32_16x16x32_bf16 v[60:63], v[136:139], v[200:203], v[60:63]\n"
        "ds_read_b128 v[192:195], v230 offset:20480\n"
        "s_cbranch_vccnz .Ln_f13\n"
        ".Ln_b13:\n"
        "v_mfma_f32_16x16x32_bf16 v[60:63], v[140:143], v[204:207], v[60:63]\n"
        "ds_read_b128 v[196:199], v231 offset:20480\n"
        "v_perm_b32 v224, v216, v236, s60\n"
        "v_mfma_f32_16x16x32_bf16 v[56:59], v[132:135], v[204:207], v[56:59]\n"
        "ds_read_b32 v160, v224\n"
        "v_perm_b32 v225, v216, v236, s61\n"
        "v_mfma_f32_16x16x32_bf16 v[56:59], v[128:131], v[200:203], v[56:59]\n"
        "ds_read_b32 v161, v225\n"
        "v_perm_b32 v226, v216, v236, s62\n"
        "v_mfma_f32_16x16x32_bf16 v[52:55], v[120:123], v[200:203], v[52:55]\n"
        "ds_read_b32 v162, v226\n"
        "v_perm_b32 v227, v216, v236, s63\n"
        "v_mfma_f32_16x16x32_bf16 v[52:55], v[124:127], v[204:207], v[52:55]\n"
        "ds_read_b32 v163, v227\n"
        "v_perm_b32 v224, v217, v236, s60\n"
        "v_mfma_f32_16x16x32_bf16 v[48:51], v[116:119], v[204:207], v[48:51]\n"
        "ds_read_b32 v164, v224\n"
        "v_perm_b32 v225, v217, v236, s61\n"
        "v_mfma_f32_16x16x32_bf16 v[48:51], v[112:115], v[200:203], v[48:51]\n"
        "ds_read_b32 v165, v225\n"
        "s_waitcnt lgkmcnt(15)\n"
        "v_mfma_f32_16x16x32_bf16 v[64:67], v[112:115], v[176:179], v[64:67]\n"
        "ds_read_b128 v[200:203], v230 offset:22528\n"
        "v_mfma_f32_16x16x32_bf16 v[64:67], v[116:119], v[180:183], v[64:67]\n"
        "ds_read_b128 v[204:207], v231 offset:22528\n"
        "v_perm_b32 v226, v217, v236, s62\n"
        "v_mfma_f32_16x16x32_bf16 v[68:71], v[124:127], v[180:183], v[68:71]\n"
        "ds_read_b32 v166, v226\n"
        "v_perm_b32 v227, v217, v236, s63\n"
        "v_mfma_f32_16x16x32_bf16 v[68:71], v[120:123], v[176:179], v[68:71]\n"
        "ds_read_b32 v167, v227\n"
        "v_perm_b32 v224, v220, v236, s60\n"
        "v_mfma_f32_16x16x32_bf16 v[72:75], v[128:131], v[176:179], v[72:75]\n"
        "ds_read_b32 v168, v224\n"
        "v_perm_b32 v225, v220, v236, s61\n"
        "v_mfma_f32_16x16x32_bf16 v[72:75], v[132:135], v[180:183], v[72:75]\n"
        "ds_read_b32 v169, v225\n"
        "v_perm_b32 v226, v220, v236, s62\n"
        "s_waitcnt lgkmcnt(15)\n"
        "v_mfma_f32_16x16x32_bf16 v[84:87], v[132:135], v[188:191], v[84:87]\n"
        "ds_read_b32 v170, v226\n"
        "v_perm_b32 v227, v220, v236, s63\n"
        "v_mfma_f32_16x16x32_bf16 v[84:87], v[128:131], v[184:187], v[84:87]\n"
        "ds_read_b32 v171, v227\n"
        "v_perm_b32 v224, v221, v236, s60\n"
        "v_mfma_f32_16x16x32_bf16 v[80:83], v[120:123], v[184:187], v[80:83]\n"
        "ds_read_b32 v172, v224\n"
        "v_perm_b32 v225, v221, v236, s61\n"
        "v_mfma_f32_16x16x32_bf16 v[80:83], v[124:127], v[188:191], v[80:83]\n"
        "ds_read_b32 v173, v225\n"
        "v_perm_b32 v226, v221, v236, s62\n"
        "v_mfma_f32_16x16x32_bf16 v[76:79], v[116:119], v[188:191], v[76:79]\n"
        "ds_read_b32 v174, v226\n"
        "v_perm_b32 v227, v221, v236, s63\n"
        "v_mfma_f32_16x16x32_bf16 v[76:79], v[112:115], v[184:187], v[76:79]\n"
        "ds_read_b32 v175, v227\n"
        "s_waitcnt lgkmcnt(10)\n"
        "s_barrier\n"
        "v_mfma_f32_16x16x32_bf16 v[88:91], v[112:115], v[192:195], v[88:91]\n"
        "ds_read_b128 v[208:211], v232 offset:16384\n"
        "v_mfma_f32_16x16x32_bf16 v[88:91], v[116:119], v[196:199], v[88:91]\n"
        "ds_read_b128 v[212:215], v233 offset:16384\n"
        "v_mfma_f32_16x16x32_bf16 v[92:95], v[124:127], v[196:199], v[92:95]\n"
        "v_mfma_f32_16x16x32_bf16 v[92:95], v[120:123], v[192:195], v[92:95]\n"
        "ds_read_b128 v[216:219], v234 offset:16384\n"
        "v_mfma_f32_16x16x32_bf16 v[96:99], v[128:131], v[192:195], v[96:99]\n"
        "ds_read_b128 v[220:223], v235 offset:16384\n"
        "v_mfma_f32_16x16x32_bf16 v[96:99], v[132:135], v[196:199], v[96:99]\n"
        "v_mfma_f32_16x16x32_bf16 v[108:111], v[132:135], v[204:207], v[108:111]\n"
        "ds_read_b128 v[176:179], v228 offset:32768\n"
        "v_mfma_f32_16x16x32_bf16 v[108:111], v[128:131], v[200:203], v[108:111]\n"
        "ds_read_b128 v[180:183], v229 offset:32768\n"
        "v_mfma_f32_16x16x32_bf16 v[104:107], v[120:123], v[200:203], v[104:107]\n"
        "v_mfma_f32_16x16x32_bf16 v[104:107], v[124:127], v[204:207], v[104:107]\n"
        "ds_read_b128 v[184:187], v228 offset:34816\n"
        "v_mfma_f32_16x16x32_bf16 v[100:103], v[116:119], v[204:207], v[100:103]\n"
        "ds_read_b128 v[188:191], v229 offset:34816\n"
        "v_mfma_f32_16x16x32_bf16 v[100:103], v[112:115], v[200:203], v[100:103]\n"
        "s_waitcnt lgkmcnt(2)\n"
        "v_mfma_f32_16x16x32_bf16 v[0:3], v[144:147], v[176:179], v[0:3]\n"
        "ds_read_b128 v[192:195], v228 offset:36864\n"
        "v_bitop3_b32 v210, v210, v214, s34 bitop3:0x7e\n"
        "v_mfma_f32_16x16x32_bf16 v[0:3], v[148:151], v[180:183], v[0:3]\n"
        "ds_read_b128 v[196:199], v229 offset:36864\n"
        "v_bitop3_b32 v211, v211, v215, s39 bitop3:0x7e\n"
        "v_mfma_f32_16x16x32_bf16 v[4:7], v[156:159], v[180:183], v[4:7]\n"
        "ds_read_b128 v[200:203], v228 offset:38912\n"
        "v_bitop3_b32 v218, v218, v222, s34 bitop3:0x7e\n"
        "v_mfma_f32_16x16x32_bf16 v[4:7], v[152:155], v[176:179], v[4:7]\n"
        "ds_read_b128 v[204:207], v229 offset:38912\n"
        "v_perm_b32 v224, v208, v236, s60\n"
        "v_mfma_f32_16x16x32_bf16 v[8:11], v[160:163], v[176:179], v[8:11]\n"
        "ds_read_b32 v112, v224\n"
        "v_perm_b32 v225, v208, v236, s61\n"
        "v_mfma_f32_16x16x32_bf16 v[8:11], v[164:167], v[180:183], v[8:11]\n"
        "ds_read_b32 v113, v225\n"
        ".Ln_tp3:\n"
        "s_cbranch_execz .Ln_ret\n"
        "v_perm_b32 v226, v208, v236, s62\n"
        "v_mfma_f32_16x16x32_bf16 v[12:15], v[172:175], v[180:183], v[12:15]\n"
        "ds_read_b32 v114, v226\n"
        "v_perm_b32 v227, v208, v236, s63\n"
        "v_mfma_f32_16x16x32_bf16 v[12:15], v[168:171], v[176:179], v[12:15]\n"
        "ds_read_b32 v115, v227\n"
        "v_bitop3_b32 v219, v219, v223, s39 bitop3:0x7e\n"
        "s_waitcnt lgkmcnt(8)\n"
        "v_mfma_f32_16x16x32_bf16 v[28:31], v[168:171], v[184:187], v[28:31]\n"
        "ds_read_b128 v[176:179], v230 offset:32768\n"
        "v_or3_b32 v210, v210, v211, v218\n"
        "v_mfma_f32_16x16x32_bf16 v[28:31], v[172:175], v[188:191], v[28:31]\n"
        "ds_read_b128 v[180:183], v231 offset:32768\n"
        "v_perm_b32 v224, v209, v236, s60\n"
        "v_mfma_f32_16x16x32_bf16 v[24:27], v[164:167], v[188:191], v[24:27]\n"
        "ds_read_b32 v116, v224\n"
        "v_perm_b32 v225, v209, v236, s61\n"
        "v_mfma_f32_16x16x32_bf16 v[24:27], v[160:163], v[184:187], v[24:27]\n"
        "ds_read_b32 v117, v225\n"
        "v_perm_b32 v226, v209, v236, s62\n"
        "v_mfma_f32_16x16x32_bf16 v[20:23], v[152:155], v[184:187], v[20:23]\n"
        "ds_read_b32 v118, v226\n"
        "v_perm_b32 v227, v209, v236, s63\n"
        "v_mfma_f32_16x16x32_bf16 v[20:23], v[156:159], v[188:191], v[20:23]\n"
        "ds_read_b32 v119, v227\n"
        "v_perm_b32 v224, v212, v236, s60\n"
        "v_mfma_f32_16x16x32_bf16 v[16:19], v[148:151], v[188:191], v[16:19]\n"
        "ds_read_b32 v120, v224\n"
        "v_perm_b32 v225, v212, v236, s61\n"
        "v_mfma_f32_16x16x32_bf16 v[16:19], v[144:147], v[184:187], v[16:19]\n"
        "ds_read_b32 v121, v225\n"
        "v_or_b32 v210, v210, v219\n"
        "s_waitcnt lgkmcnt(14)\n"
        "v_mfma_f32_16x16x32_bf16 v[32:35], v[144:147], v[192:195], v[32:35]\n"
        "ds_read_b128 v[184:187], v230 offset:34816\n"
        "v_cmp_ne_u32 vcc, 0, v210\n"
        "v_mfma_f32_16x16x32_bf16 v[32:35], v[148:151], v[196:199], v[32:35]\n"
        "ds_read_b128 v[188:191], v231 offset:34816\n"
        "v_perm_b32 v226, v212, v236, s62\n"
        "v_mfma_f32_16x16x32_bf16 v[36:39], v[156:159], v[196:199], v[36:39]\n"
        "ds_read_b32 v122, v226\n"
        "v_perm_b32 v227, v212, v236, s63\n"
        "v_mfma_f32_16x16x32_bf16 v[36:39], v[152:155], v[192:195], v[36:39]\n"
        "ds_read_b32 v123, v227\n"
        "v_perm_b32 v224, v213, v236, s60\n"
        "v_mfma_f32_16x16x32_bf16 v[40:43], v[160:163], v[192:195], v[40:43]\n"
        "ds_read_b32 v124, v224\n"
        "v_perm_b32 v225, v213, v236, s61\n"
        "v_mfma_f32_16x16x32_bf16 v[40:43], v[164:167], v[196:199], v[40:43]\n"
        "ds_read_b32 v125, v225\n"
        "v_perm_b32 v226, v213, v236, s62\n"
        "v_mfma_f32_16x16x32_bf16 v[44:47], v[172:175], v[196:199], v[44:47]\n"
        "ds_read_b32 v126, v226\n"
        "v_perm_b32 v227, v213, v236, s63\n"
        "v_mfma_f32_16x16x32_bf16 v[44:47], v[168:171], v[192:195], v[44:47]\n"
        "ds_read_b32 v127, v227\n"
        "s_waitcnt lgkmcnt(15)\n"
        "v_mfma_f32_16x16x32_bf16 v[60:63], v[168:171], v[200:203], v[60:63]\n"
        "ds_read_b128 v[192:195], v230 offset:36864\n"
        "s_cbranch_vccnz .Ln_f14\n"
        ".Ln_b14:\n"
        "v_mfma_f32_16x16x32_bf16 v[60:63], v[172:175], v[204:207], v[60:63]\n"
        "ds_read_b128 v[196:199], v231 offset:36864\n"
        "v_perm_b32 v224, v216, v236, s60\n"
        "v_mfma_f32_16x16x32_bf16 v[56:59], v[164:167], v[204:207], v[56:59]\n"
        "ds_read_b32 v128, v224\n"
        "v_perm_b32 v225, v216, v236, s61\n"
        "v_mfma_f32_16x16x32_bf16 v[56:59], v[160:163], v[200:203], v[56:59]\n"
        "ds_read_b32 v129, v225\n"
        "v_perm_b32 v226, v216, v236, s62\n"
        "v_mfma_f32_16x16x32_bf16 v[52:55], v[152:155], v[200:203], v[52:55]\n"
        "ds_read_b32 v130, v226\n"
        "v_perm_b32 v227, v216, v236, s63\n"
        "v_mfma_f32_16x16x32_bf16 v[52:55], v[156:159], v[204:207], v[52:55]\n"
        "ds_read_b32 v131, v227\n"
        "v_perm_b32 v224, v217, v236, s60\n"
        "v_mfma_f32_16x16x32_bf16 v[48:51], v[148:151], v[204:207], v[48:51]\n"
        "ds_read_b32 v132, v224\n"
        "v_perm_b32 v225, v217, v236, s61\n"
        "v_mfma_f32_16x16x32_bf16 v[48:51], v[144:147], v[200:203], v[48:51]\n"
        "ds_read_b32 v133, v225\n"
        "s_waitcnt lgkmcnt(15)\n"
        "v_mfma_f32_16x16x32_bf16 v[64:67], v[144:147], v[176:179], v[64:67]\n"
        "ds_read_b128 v[200:203], v230 offset:38912\n"
        "v_mfma_f32_16x16x32_bf16 v[64:67], v[148:151], v[180:183], v[64:67]\n"
        "ds_read_b128 v[204:207], v231 offset:38912\n"
        "v_perm_b32 v226, v217, v236, s62\n"
        "v_mfma_f32_16x16x32_bf16 v[68:71], v[156:159], v[180:183], v[68:71]\n"
        "ds_read_b32 v134, v226\n"
        "v_perm_b32 v227, v217, v236, s63\n"
        "v_mfma_f32_16x16x32_bf16 v[68:71], v[152:155], v[176:179], v[68:71]\n"
        "ds_read_b32 v135, v227\n"
        "v_perm_b32 v224, v220, v236, s60\n"
        "v_mfma_f32_16x16x32_bf16 v[72:75], v[160:163], v[176:179], v[72:75]\n"
        "ds_read_b32 v136, v224\n"
        "v_perm_b32 v225, v220, v236, s61\n"
        "v_mfma_f32_16x16x32_bf16 v[72:75], v[164:167], v[180:183], v[72:75]\n"
        "ds_read_b32 v137, v225\n"
        "v_perm_b32 v226, v220, v236, s62\n"
        "s_waitcnt lgkmcnt(15)\n"
        "v_mfma_f32_16x16x32_bf16 v[84:87], v[164:167], v[188:191], v[84:87]\n"
        "ds_read_b32 v138, v226\n"
        "v_perm_b32 v227, v220, v236, s63\n"
        "v_mfma_f32_16x16x32_bf16 v[84:87], v[160:163], v[184:187], v[84:87]\n"
        "ds_read_b32 v139, v227\n"
        "v_perm_b32 v224, v221, v236, s60\n"
        "v_mfma_f32_16x16x32_bf16 v[80:83], v[152:155], v[184:187], v[80:83]\n"
        "ds_read_b32 v140, v224\n"
        "v_perm_b32 v225, v221, v236, s61\n"
        "v_mfma_f32_16x16x32_bf16 v[80:83], v[156:159], v[188:191], v[80:83]\n"
        "ds_read_b32 v141, v225\n"
        "v_perm_b32 v226, v221, v236, s62\n"
        "v_mfma_f32_16x16x32_bf16 v[76:79], v[148:151], v[188:191], v[76:79]\n"
        "ds_read_b32 v142, v226\n"
        "v_perm_b32 v227, v221, v236, s63\n"
        "v_mfma_f32_16x16x32_bf16 v[76:79], v[144:147], v[184:187], v[76:79]\n"
        "ds_read_b32 v143, v227\n"
        "s_waitcnt lgkmcnt(10)\n"
        "s_barrier\n"
        "v_mfma_f32_16x16x32_bf16 v[88:91], v[144:147], v[192:195], v[88:91]\n"
        "ds_read_b128 v[208:211], v232 offset:32768\n"
        "v_mfma_f32_16x16x32_bf16 v[88:91], v[148:151], v[196:199], v[88:91]\n"
        "ds_read_b128 v[212:215], v233 offset:32768\n"
        "v_mfma_f32_16x16x32_bf16 v[92:95], v[156:159], v[196:199], v[92:95]\n"
        "v_mfma_f32_16x16x32_bf16 v[92:95], v[152:155], v[192:195], v[92:95]\n"
        "ds_read_b128 v[216:219], v234 offset:32768\n"
        "v_mfma_f32_16x16x32_bf16 v[96:99], v[160:163], v[192:195], v[96:99]\n"
        "ds_read_b128 v[220:223], v235 offset:32768\n"
        "v_mfma_f32_16x16x32_bf16 v[96:99], v[164:167], v[196:199], v[96:99]\n"
        "v_mfma_f32_16x16x32_bf16 v[108:111], v[164:167], v[204:207], v[108:111]\n"
        "ds_read_b128 v[176:179], v228 offset:0\n"
        "v_mfma_f32_16x16x32_bf16 v[108:111], v[160:163], v[200:203], v[108:111]\n"
        "ds_read_b128 v[180:183], v229 offset:0\n"
        "v_mfma_f32_16x16x32_bf16 v[104:107], v[152:155], v[200:203], v[104:107]\n"
        "v_mfma_f32_16x16x32_bf16 v[104:107], v[156:159], v[204:207], v[104:107]\n"
        "ds_read_b128 v[184:187], v228 offset:2048\n"
        "v_mfma_f32_16x16x32_bf16 v[100:103], v[148:151], v[204:207], v[100:103]\n"
        "ds_read_b128 v[188:191], v229 offset:2048\n"
        "v_mfma_f32_16x16x32_bf16 v[100:103], v[144:147], v[200:203], v[100:103]\n"
        "s_waitcnt lgkmcnt(2)\n"
        "v_perm_b32 v224, v208, v236, s60\n"
        "v_mfma_f32_16x16x32_bf16 v[0:3], v[112:115], v[176:179], v[0:3]\n"
        "ds_read_b32 v144, v224\n"
        "ds_read_b128 v[192:195], v228 offset:16384\n"
        "v_perm_b32 v225, v208, v236, s61\n"
        "v_mfma_f32_16x16x32_bf16 v[0:3], v[116:119], v[180:183], v[0:3]\n"
        "ds_read_b32 v145, v225\n"
        "ds_read_b128 v[196:199], v229 offset:16384\n"
        "v_perm_b32 v226, v208, v236, s62\n"
        "v_mfma_f32_16x16x32_bf16 v[4:7], v[120:123], v[176:179], v[4:7]\n"
        "ds_read_b32 v146, v226\n"
        "ds_read_b128 v[200:203], v228 offset:18432\n"
        "v_perm_b32 v227, v208, v236, s63\n"
        "v_mfma_f32_16x16x32_bf16 v[4:7], v[124:127], v[180:183], v[4:7]\n"
        "ds_read_b32 v147, v227\n"
        "ds_read_b128 v[204:207], v229 offset:18432\n"
        "v_perm_b32 v224, v209, v236, s60\n"
        "v_mfma_f32_16x16x32_bf16 v[8:11], v[128:131], v[176:179], v[8:11]\n"
        "ds_read_b32 v148, v224\n"
        "v_perm_b32 v225, v209, v236, s61\n"
        "v_bitop3_b32 v210, v210, v214, s34 bitop3:0x7e\n"
        "v_mfma_f32_16x16x32_bf16 v[8:11], v[132:135], v[180:183], v[8:11]\n"
        "ds_read_b32 v149, v225\n"
        "v_perm_b32 v226, v209, v236, s62\n"
        "v_bitop3_b32 v211, v211, v215, s39 bitop3:0x7e\n"
        "v_mfma_f32_16x16x32_bf16 v[12:15], v[136:139], v[176:179], v[12:15]\n"
        "ds_read_b32 v150, v226\n"
        "v_perm_b32 v227, v209, v236, s63\n"
        "v_bitop3_b32 v218, v218, v222, s34 bitop3:0x7e\n"
        "v_mfma_f32_16x16x32_bf16 v[12:15], v[140:143], v[180:183], v[12:15]\n"
        "ds_read_b32 v151, v227\n"
        "v_perm_b32 v224, v212, v236, s60\n"
        "v_bitop3_b32 v219, v219, v223, s39 bitop3:0x7e\n"
        "s_waitcnt lgkmcnt(12)\n"
        "s_barrier\n"
        "v_mfma_f32_16x16x32_bf16 v[16:19], v[112:115], v[184:187], v[16:19]\n"
        "ds_read_b32 v152, v224\n"
        "ds_read_b128 v[176:179], v228 offset:4096\n"
        "v_perm_b32 v225, v212, v236, s61\n"
        "v_mfma_f32_16x16x32_bf16 v[16:19], v[116:119], v[188:191], v[16:19]\n"
        "ds_read_b32 v153, v225\n"
        "ds_read_b128 v[180:183], v229 offset:4096\n"
        "v_perm_b32 v226, v212, v236, s62\n"
        "v_mfma_f32_16x16x32_bf16 v[20:23], v[120:123], v[184:187], v[20:23]\n"
        "ds_read_b32 v154, v226\n"
        "v_perm_b32 v227, v212, v236, s63\n"
        "v_or3_b32 v210, v210, v211, v218\n"
        "v_mfma_f32_16x16x32_bf16 v[20:23], v[124:127], v[188:191], v[20:23]\n"
        "ds_read_b32 v155, v227\n"
        "v_perm_b32 v224, v213, v236, s60\n"
        "v_or_b32 v210, v210, v219\n"
        "v_mfma_f32_16x16x32_bf16 v[24:27], v[128:131], v[184:187], v[24:27]\n"
        "ds_read_b32 v156, v224\n"
        "v_perm_b32 v225, v213, v236, s61\n"
        "v_cmp_ne_u32 vcc, 0, v210\n"
        "v_mfma_f32_16x16x32_bf16 v[24:27], v[132:135], v[188:191], v[24:27]\n"
        "ds_read_b32 v157, v225\n"
        "v_perm_b32 v226, v213, v236, s62\n"
        "v_mfma_f32_16x16x32_bf16 v[28:31], v[136:139], v[184:187], v[28:31]\n"
        "ds_read_b32 v158, v226\n"
        "s_cbranch_vccnz .Ln_f15\n"
        ".Ln_b15:\n"
        "v_perm_b32 v227, v213, v236, s63\n"
        "v_mfma_f32_16x16x32_bf16 v[28:31], v[140:143], v[188:191], v[28:31]\n"
        "ds_read_b32 v159, v227\n"
        "v_perm_b32 v224, v216, v236, s60\n"
        "v_lshlrev_b32 v208, 16, v238\n"
        "s_waitcnt lgkmcnt(15)\n"
        "v_mfma_f32_16x16x32_bf16 v[0:3], v[144:147], v[192:195], v[0:3]\n"
        "ds_read_b32 v160, v224\n"
        "ds_read_b128 v[184:187], v228 offset:20480\n"
        "v_perm_b32 v225, v216, v236, s61\n"
        "s_waitcnt lgkmcnt(12)\n"
        "v_mfma_f32_16x16x32_bf16 v[0:3], v[148:151], v[196:199], v[0:3]\n"
        "ds_read_b32 v161, v225\n"
        "ds_read_b128 v[188:191], v229 offset:20480\n"
        "v_perm_b32 v226, v216, v236, s62\n"
        "v_mfma_f32_16x16x32_bf16 v[16:19], v[144:147], v[200:203], v[16:19]\n"
        "ds_read_b32 v162, v226\n"
        "v_perm_b32 v227, v216, v236, s63\n"
        "v_and_b32 v209, 0xffff0000, v238\n"
        "v_mfma_f32_16x16x32_bf16 v[16:19], v[148:151], v[204:207], v[16:19]\n"
        "ds_read_b32 v163, v227\n"
        "v_perm_b32 v224, v217, v236, s60\n"
        "v_lshlrev_b32 v210, 16, v239\n"
        "s_waitcnt lgkmcnt(10)\n"
        "v_mfma_f32_16x16x32_bf16 v[4:7], v[152:155], v[192:195], v[4:7]\n"
        "ds_read_b32 v164, v224\n"
        "v_perm_b32 v225, v217, v236, s61\n"
        "v_and_b32 v211, 0xffff0000, v239\n"
        "s_waitcnt lgkmcnt(7)\n"
        "v_mfma_f32_16x16x32_bf16 v[4:7], v[156:159], v[196:199], v[4:7]\n"
        "ds_read_b32 v165, v225\n"
        "v_perm_b32 v226, v217, v236, s62\n"
        "v_lshlrev_b32 v212, 16, v240\n"
        "v_mfma_f32_16x16x32_bf16 v[20:23], v[152:155], v[200:203], v[20:23]\n"
        "ds_read_b32 v166, v226\n"
        "v_perm_b32 v227, v217, v236, s63\n"
        "v_and_b32 v213, 0xffff0000, v240\n"
        "v_mfma_f32_16x16x32_bf16 v[20:23], v[156:159], v[204:207], v[20:23]\n"
        "ds_read_b32 v167, v227\n"
        "v_perm_b32 v224, v220, v236, s60\n"
        "v_lshlrev_b32 v214, 16, v241\n"
        "v_mfma_f32_16x16x32_bf16 v[32:35], v[112:115], v[176:179], v[32:35]\n"
        "ds_read_b32 v168, v224\n"
        "v_perm_b32 v225, v220, v236, s61\n"
        "v_and_b32 v215, 0xffff0000, v241\n"
        "v_mfma_f32_16x16x32_bf16 v[32:35], v[116:119], v[180:183], v[32:35]\n"
        "ds_read_b32 v169, v225\n"
        "v_perm_b32 v226, v220, v236, s62\n"
        "v_mul_f32 v0, v0, v208\n"
        "v_mfma_f32_16x16x32_bf16 v[36:39], v[120:123], v[176:179], v[36:39]\n"
        "ds_read_b32 v170, v226\n"
        "v_perm_b32 v227, v220, v236, s63\n"
        "v_mul_f32 v1, v1, v210\n"
        "v_mfma_f32_16x16x32_bf16 v[36:39], v[124:127], v[180:183], v[36:39]\n"
        "ds_read_b32 v171, v227\n"
        "v_perm_b32 v224, v221, v236, s60\n"
        "v_mul_f32 v2, v2, v212\n"
        "v_mfma_f32_16x16x32_bf16 v[40:43], v[128:131], v[176:179], v[40:43]\n"
        "ds_read_b32 v172, v224\n"
        "v_perm_b32 v225, v221, v236, s61\n"
        "v_mul_f32 v3, v3, v214\n"
        "v_mfma_f32_16x16x32_bf16 v[40:43], v[132:135], v[180:183], v[40:43]\n"
        "ds_read_b32 v173, v225\n"
        "v_perm_b32 v226, v221, v236, s62\n"
        "v_mul_f32 v4, v4, v209\n"
        "v_mfma_f32_16x16x32_bf16 v[44:47], v[136:139], v[176:179], v[44:47]\n"
        "ds_read_b32 v174, v226\n"
        "v_perm_b32 v227, v221, v236, s63\n"
        "v_cvt_pk_bf16_f32 v0, v0, v4\n"
        "v_mfma_f32_16x16x32_bf16 v[44:47], v[140:143], v[180:183], v[44:47]\n"
        "ds_read_b32 v175, v227\n"
        "v_mul_f32 v5, v5, v211\n"
        "v_cvt_pk_bf16_f32 v1, v1, v5\n"
        "s_waitcnt lgkmcnt(12)\n"
        "v_mfma_f32_16x16x32_bf16 v[8:11], v[160:163], v[192:195], v[8:11]\n"
        "ds_read_b128 v[176:179], v228 offset:6144\n"
        "v_mul_f32 v6, v6, v213\n"
        "v_cvt_pk_bf16_f32 v2, v2, v6\n"
        "s_waitcnt lgkmcnt(9)\n"
        "v_mfma_f32_16x16x32_bf16 v[8:11], v[164:167], v[196:199], v[8:11]\n"
        "ds_read_b128 v[180:183], v229 offset:6144\n"
        "v_mul_f32 v7, v7, v215\n"
        "v_cvt_pk_bf16_f32 v3, v3, v7\n"
        "v_mfma_f32_16x16x32_bf16 v[24:27], v[160:163], v[200:203], v[24:27]\n"
        "ds_write_b128 v246, v[0:3] offset:0\n"
        "ds_write_b32 v237, v237 offset:0\n"
        "v_mul_f32 v16, v16, v208\n"
        "v_mfma_f32_16x16x32_bf16 v[24:27], v[164:167], v[204:207], v[24:27]\n"
        "v_mul_f32 v17, v17, v210\n"
        "v_mul_f32 v18, v18, v212\n"
        "v_mul_f32 v19, v19, v214\n"
        "s_waitcnt lgkmcnt(8)\n"
        "v_mfma_f32_16x16x32_bf16 v[12:15], v[168:171], v[192:195], v[12:15]\n"
        "v_mul_f32 v20, v20, v209\n"
        "v_cvt_pk_bf16_f32 v16, v16, v20\n"
        "v_mul_f32 v21, v21, v211\n"
        "s_waitcnt lgkmcnt(4)\n"
        "v_mfma_f32_16x16x32_bf16 v[12:15], v[172:175], v[196:199], v[12:15]\n"
        "v_cvt_pk_bf16_f32 v17, v17, v21\n"
        "v_mul_f32 v22, v22, v213\n"
        "v_cvt_pk_bf16_f32 v18, v18, v22\n"
        "v_mfma_f32_16x16x32_bf16 v[28:31], v[168:171], v[200:203], v[28:31]\n"
        "ds_read_b128 v[192:195], v228 offset:22528\n"
        "v_mul_f32 v23, v23, v215\n"
        "v_cvt_pk_bf16_f32 v19, v19, v23\n"
        "v_mfma_f32_16x16x32_bf16 v[28:31], v[172:175], v[204:207], v[28:31]\n"
        "ds_read_b128 v[196:199], v229 offset:22528\n"
        "ds_write_b128 v246, v[16:19] offset:8192\n"
        "ds_write_b32 v237, v237 offset:256\n"
        "v_mfma_f32_16x16x32_bf16 v[32:35], v[144:147], v[184:187], v[32:35]\n"
        "ds_read_b128 v[200:203], v230 offset:0\n"
        "v_lshlrev_b32 v216, 16, v242\n"
        "v_and_b32 v217, 0xffff0000, v242\n"
        "v_mfma_f32_16x16x32_bf16 v[32:35], v[148:151], v[188:191], v[32:35]\n"
        "ds_read_b128 v[204:207], v231 offset:0\n"
        "v_lshlrev_b32 v218, 16, v243\n"
        "v_and_b32 v219, 0xffff0000, v243\n"
        "v_mfma_f32_16x16x32_bf16 v[36:39], v[152:155], v[184:187], v[36:39]\n"
        "v_lshlrev_b32 v220, 16, v244\n"
        "v_and_b32 v221, 0xffff0000, v244\n"
        "v_lshlrev_b32 v222, 16, v245\n"
        "v_mfma_f32_16x16x32_bf16 v[36:39], v[156:159], v[188:191], v[36:39]\n"
        "v_and_b32 v223, 0xffff0000, v245\n"
        "v_mul_f32 v8, v8, v216\n"
        "v_mul_f32 v9, v9, v217\n"
        "v_mfma_f32_16x16x32_bf16 v[40:43], v[160:163], v[184:187], v[40:43]\n"
        "v_mul_f32 v10, v10, v218\n"
        "v_mul_f32 v11, v11, v219\n"
        "v_cvt_pk_bf16_f32 v8, v8, v9\n"
        "v_mfma_f32_16x16x32_bf16 v[40:43], v[164:167], v[188:191], v[40:43]\n"
        "v_cvt_pk_bf16_f32 v9, v10, v11\n"
        "v_mfma_f32_16x16x32_bf16 v[44:47], v[168:171], v[184:187], v[44:47]\n"
        "ds_write_b64 v247, v[8:9] offset:0\n"
        "v_mul_f32 v12, v12, v220\n"
        "v_mul_f32 v13, v13, v221\n"
        "v_mfma_f32_16x16x32_bf16 v[44:47], v[172:175], v[188:191], v[44:47]\n"
        "v_mul_f32 v14, v14, v222\n"
        "v_mul_f32 v15, v15, v223\n"
        "v_cvt_pk_bf16_f32 v12, v12, v13\n"
        "s_waitcnt lgkmcnt(9)\n"
        "v_mfma_f32_16x16x32_bf16 v[48:51], v[112:115], v[176:179], v[48:51]\n"
        "ds_read_b128 v[184:187], v230 offset:16384\n"
        "v_cvt_pk_bf16_f32 v13, v14, v15\n"
        "v_mfma_f32_16x16x32_bf16 v[48:51], v[116:119], v[180:183], v[48:51]\n"
        "ds_read_b128 v[188:191], v231 offset:16384\n"
        "ds_write_b64 v248, v[12:13] offset:0\n"
        "ds_write_b32 v237, v237 offset:512\n"
        "s_waitcnt lgkmcnt(9)\n"
        "v_mfma_f32_16x16x32_bf16 v[48:51], v[144:147], v[192:195], v[48:51]\n"
        "v_mul_f32 v24, v24, v216\n"
        "v_mul_f32 v25, v25, v217\n"
        "v_mul_f32 v26, v26, v218\n"
        "v_mfma_f32_16x16x32_bf16 v[48:51], v[148:151], v[196:199], v[48:51]\n"
        "v_mul_f32 v27, v27, v219\n"
        "v_cvt_pk_bf16_f32 v24, v24, v25\n"
        "v_cvt_pk_bf16_f32 v25, v26, v27\n"
        "v_mfma_f32_16x16x32_bf16 v[52:55], v[120:123], v[176:179], v[52:55]\n"
        "ds_write_b64 v247, v[24:25] offset:8192\n"
        "v_mul_f32 v28, v28, v220\n"
        "v_mul_f32 v29, v29, v221\n"
        "v_mfma_f32_16x16x32_bf16 v[52:55], v[124:127], v[180:183], v[52:55]\n"
        "v_mul_f32 v30, v30, v222\n"
        "v_mul_f32 v31, v31, v223\n"
        "v_cvt_pk_bf16_f32 v28, v28, v29\n"
        "v_mfma_f32_16x16x32_bf16 v[52:55], v[152:155], v[192:195], v[52:55]\n"
        "v_cvt_pk_bf16_f32 v29, v30, v31\n"
        "v_mfma_f32_16x16x32_bf16 v[52:55], v[156:159], v[196:199], v[52:55]\n"
        "ds_write_b64 v248, v[28:29] offset:8192\n"
        "ds_write_b32 v237, v237 offset:768\n"
        "v_mul_f32 v32, v32, v208\n"
        "v_mfma_f32_16x16x32_bf16 v[56:59], v[128:131], v[176:179], v[56:59]\n"
        "v_mul_f32 v33, v33, v210\n"
        "v_mul_f32 v34, v34, v212\n"
        "v_mul_f32 v35, v35, v214\n"
        "v_mfma_f32_16x16x32_bf16 v[56:59], v[132:135], v[180:183], v[56:59]\n"
        "v_mul_f32 v36, v36, v209\n"
        "v_cvt_pk_bf16_f32 v32, v32, v36\n"
        "v_mul_f32 v37, v37, v211\n"
        "v_mfma_f32_16x16x32_bf16 v[56:59], v[160:163], v[192:195], v[56:59]\n"
        "v_cvt_pk_bf16_f32 v33, v33, v37\n"
        "v_mul_f32 v38, v38, v213\n"
        "v_cvt_pk_bf16_f32 v34, v34, v38\n"
        "v_mfma_f32_16x16x32_bf16 v[56:59], v[164:167], v[196:199], v[56:59]\n"
        "v_mul_f32 v39, v39, v215\n"
        "v_cvt_pk_bf16_f32 v35, v35, v39\n"
        "v_mfma_f32_16x16x32_bf16 v[60:63], v[136:139], v[176:179], v[60:63]\n"
        "ds_write_b128 v246, v[32:35] offset:16384\n"
        "v_mul_f32 v40, v40, v216\n"
        "v_mul_f32 v41, v41, v217\n"
        "v_mfma_f32_16x16x32_bf16 v[60:63], v[140:143], v[180:183], v[60:63]\n"
        "v_mul_f32 v42, v42, v218\n"
        "v_mul_f32 v43, v43, v219\n"
        "v_cvt_pk_bf16_f32 v40, v40, v41\n"
        "v_mfma_f32_16x16x32_bf16 v[60:63], v[168:171], v[192:195], v[60:63]\n"
        "ds_read_b128 v[176:179], v230 offset:2048\n"
        "v_cvt_pk_bf16_f32 v41, v42, v43\n"
        "v_mfma_f32_16x16x32_bf16 v[60:63], v[172:175], v[196:199], v[60:63]\n"
        "ds_read_b128 v[180:183], v231 offset:2048\n"
        "ds_write_b64 v247, v[40:41] offset:16384\n"
        "v_mul_f32 v44, v44, v220\n"
        "s_waitcnt lgkmcnt(12)\n"
        "v_mfma_f32_16x16x32_bf16 v[64:67], v[112:115], v[200:203], v[64:67]\n"
        "ds_read_b128 v[192:195], v230 offset:18432\n"
        "v_mul_f32 v45, v45, v221\n"
        "v_mul_f32 v46, v46, v222\n"
        "v_mfma_f32_16x16x32_bf16 v[64:67], v[116:119], v[204:207], v[64:67]\n"
        "ds_read_b128 v[196:199], v231 offset:18432\n"
        "v_mul_f32 v47, v47, v223\n"
        "v_cvt_pk_bf16_f32 v44, v44, v45\n"
        "s_waitcnt lgkmcnt(11)\n"
        "v_mfma_f32_16x16x32_bf16 v[64:67], v[144:147], v[184:187], v[64:67]\n"
        "v_cvt_pk_bf16_f32 v45, v46, v47\n"
        "v_mfma_f32_16x16x32_bf16 v[64:67], v[148:151], v[188:191], v[64:67]\n"
        "ds_write_b64 v248, v[44:45] offset:16384\n"
        "ds_write_b32 v237, v237 offset:1024\n"
        "v_mul_f32 v48, v48, v208\n"
        "v_mfma_f32_16x16x32_bf16 v[68:71], v[120:123], v[200:203], v[68:71]\n"
        "v_mul_f32 v49, v49, v210\n"
        "v_mul_f32 v50, v50, v212\n"
        "v_mul_f32 v51, v51, v214\n"
        "v_mfma_f32_16x16x32_bf16 v[68:71], v[124:127], v[204:207], v[68:71]\n"
        "v_mul_f32 v52, v52, v209\n"
        "v_cvt_pk_bf16_f32 v48, v48, v52\n"
        "v_mul_f32 v53, v53, v211\n"
        "v_mfma_f32_16x16x32_bf16 v[68:71], v[152:155], v[184:187], v[68:71]\n"
        "v_cvt_pk_bf16_f32 v49, v49, v53\n"
        "v_mul_f32 v54, v54, v213\n"
        "v_cvt_pk_bf16_f32 v50, v50, v54\n"
        "v_mfma_f32_16x16x32_bf16 v[68:71], v[156:159], v[188:191], v[68:71]\n"
        "v_mul_f32 v55, v55, v215\n"
        "v_cvt_pk_bf16_f32 v51, v51, v55\n"
        "v_mfma_f32_16x16x32_bf16 v[72:75], v[128:131], v[200:203], v[72:75]\n"
        "ds_write_b128 v246, v[48:51] offset:24576\n"
        "v_mul_f32 v56, v56, v216\n"
        "v_mul_f32 v57, v57, v217\n"
        "v_mfma_f32_16x16x32_bf16 v[72:75], v[132:135], v[204:207], v[72:75]\n"
        "v_mul_f32 v58, v58, v218\n"
        "v_mul_f32 v59, v59, v219\n"
        "v_cvt_pk_bf16_f32 v56, v56, v57\n"
        "v_mfma_f32_16x16x32_bf16 v[72:75], v[160:163], v[184:187], v[72:75]\n"
        "ds_read_b128 v[200:203], v230 offset:4096\n"
        "v_cvt_pk_bf16_f32 v57, v58, v59\n"
        "v_mfma_f32_16x16x32_bf16 v[72:75], v[164:167], v[188:191], v[72:75]\n"
        "ds_read_b128 v[204:207], v231 offset:4096\n"
        "ds_write_b64 v247, v[56:57] offset:24576\n"
        "v_mul_f32 v60, v60, v220\n"
        "s_waitcnt lgkmcnt(9)\n"
        "v_mfma_f32_16x16x32_bf16 v[76:79], v[112:115], v[176:179], v[76:79]\n"
        "ds_read_b128 v[184:187], v230 offset:20480\n"
        "v_mul_f32 v61, v61, v221\n"
        "v_mul_f32 v62, v62, v222\n"
        "v_mfma_f32_16x16x32_bf16 v[76:79], v[116:119], v[180:183], v[76:79]\n"
        "ds_read_b128 v[188:191], v231 offset:20480\n"
        "v_mul_f32 v63, v63, v223\n"
        "v_cvt_pk_bf16_f32 v60, v60, v61\n"
        "s_waitcnt lgkmcnt(8)\n"
        "v_mfma_f32_16x16x32_bf16 v[76:79], v[144:147], v[192:195], v[76:79]\n"
        "v_cvt_pk_bf16_f32 v61, v62, v63\n"
        "v_mfma_f32_16x16x32_bf16 v[76:79], v[148:151], v[196:199], v[76:79]\n"
        "ds_write_b64 v248, v[60:61] offset:24576\n"
        "ds_write_b32 v237, v237 offset:1280\n"
        "v_mul_f32 v64, v64, v208\n"
        "v_mfma_f32_16x16x32_bf16 v[80:83], v[120:123], v[176:179], v[80:83]\n"
        "v_mul_f32 v65, v65, v210\n"
        "v_mul_f32 v66, v66, v212\n"
        "v_mul_f32 v67, v67, v214\n"
        "v_mfma_f32_16x16x32_bf16 v[80:83], v[124:127], v[180:183], v[80:83]\n"
        "v_mul_f32 v68, v68, v209\n"
        "v_cvt_pk_bf16_f32 v64, v64, v68\n"
        "v_mul_f32 v69, v69, v211\n"
        "v_mfma_f32_16x16x32_bf16 v[80:83], v[152:155], v[192:195], v[80:83]\n"
        "v_cvt_pk_bf16_f32 v65, v65, v69\n"
        "v_mul_f32 v70, v70, v213\n"
        "v_cvt_pk_bf16_f32 v66, v66, v70\n"
        "v_mfma_f32_16x16x32_bf16 v[80:83], v[156:159], v[196:199], v[80:83]\n"
        "v_mul_f32 v71, v71, v215\n"
        "v_cvt_pk_bf16_f32 v67, v67, v71\n"
        "v_mfma_f32_16x16x32_bf16 v[84:87], v[128:131], v[176:179], v[84:87]\n"
        "ds_write_b128 v246, v[64:67] offset:32768\n"
        "v_mul_f32 v72, v72, v216\n"
        "v_mul_f32 v73, v73, v217\n"
        "v_mfma_f32_16x16x32_bf16 v[84:87], v[132:135], v[180:183], v[84:87]\n"
        "v_mul_f32 v74, v74, v218\n"
        "v_mul_f32 v75, v75, v219\n"
        "v_cvt_pk_bf16_f32 v72, v72, v73\n"
        "v_mfma_f32_16x16x32_bf16 v[84:87], v[160:163], v[192:195], v[84:87]\n"
        "ds_read_b128 v[176:179], v230 offset:6144\n"
        "v_cvt_pk_bf16_f32 v73, v74, v75\n"
        "v_mfma_f32_16x16x32_bf16 v[84:87], v[164:167], v[196:199], v[84:87]\n"
        "ds_read_b128 v[180:183], v231 offset:6144\n"
        "ds_write_b64 v247, v[72:73] offset:32768\n"
        "ds_write_b32 v237, v237 offset:1536\n"
        "s_waitcnt lgkmcnt(10)\n"
        "v_mfma_f32_16x16x32_bf16 v[88:91], v[112:115], v[200:203], v[88:91]\n"
        "ds_read_b128 v[192:195], v230 offset:22528\n"
        "v_mul_f32 v76, v76, v208\n"
        "v_mul_f32 v77, v77, v210\n"
        "v_mfma_f32_16x16x32_bf16 v[88:91], v[116:119], v[204:207], v[88:91]\n"
        "ds_read_b128 v[196:199], v231 offset:22528\n"
        "v_mul_f32 v78, v78, v212\n"
        "v_mul_f32 v79, v79, v214\n"
        "s_waitcnt lgkmcnt(9)\n"
        "v_mfma_f32_16x16x32_bf16 v[88:91], v[144:147], v[184:187], v[88:91]\n"
        "v_mul_f32 v80, v80, v209\n"
        "v_cvt_pk_bf16_f32 v76, v76, v80\n"
        "v_mul_f32 v81, v81, v211\n"
        "v_mfma_f32_16x16x32_bf16 v[88:91], v[148:151], v[188:191], v[88:91]\n"
        "v_cvt_pk_bf16_f32 v77, v77, v81\n"
        "v_mul_f32 v82, v82, v213\n"
        "v_cvt_pk_bf16_f32 v78, v78, v82\n"
        "v_mfma_f32_16x16x32_bf16 v[92:95], v[120:123], v[200:203], v[92:95]\n"
        "v_mul_f32 v83, v83, v215\n"
        "v_cvt_pk_bf16_f32 v79, v79, v83\n"
        "v_mfma_f32_16x16x32_bf16 v[92:95], v[124:127], v[204:207], v[92:95]\n"
        "ds_write_b128 v246, v[76:79] offset:40960\n"
        "v_mul_f32 v84, v84, v216\n"
        "v_mul_f32 v85, v85, v217\n"
        "v_mfma_f32_16x16x32_bf16 v[92:95], v[152:155], v[184:187], v[92:95]\n"
        "v_mul_f32 v86, v86, v218\n"
        "v_mul_f32 v87, v87, v219\n"
        "v_cvt_pk_bf16_f32 v84, v84, v85\n"
        "v_mfma_f32_16x16x32_bf16 v[92:95], v[156:159], v[188:191], v[92:95]\n"
        "v_cvt_pk_bf16_f32 v85, v86, v87\n"
        "v_mfma_f32_16x16x32_bf16 v[96:99], v[128:131], v[200:203], v[96:99]\n"
        "ds_write_b64 v247, v[84:85] offset:40960\n"
        "ds_write_b32 v237, v237 offset:1792\n"
        "v_mul_f32 v88, v88, v208\n"
        "v_mfma_f32_16x16x32_bf16 v[96:99], v[132:135], v[204:207], v[96:99]\n"
        "v_mul_f32 v89, v89, v210\n"
        "v_mul_f32 v90, v90, v212\n"
        "v_mul_f32 v91, v91, v214\n"
        "v_mfma_f32_16x16x32_bf16 v[96:99], v[160:163], v[184:187], v[96:99]\n"
        "v_mul_f32 v92, v92, v209\n"
        "v_cvt_pk_bf16_f32 v88, v88, v92\n"
        "v_mul_f32 v93, v93, v211\n"
        "v_mfma_f32_16x16x32_bf16 v[96:99], v[164:167], v[188:191], v[96:99]\n"
        "v_cvt_pk_bf16_f32 v89, v89, v93\n"
        "v_mul_f32 v94, v94, v213\n"
        "v_cvt_pk_bf16_f32 v90, v90, v94\n"
        "s_waitcnt lgkmcnt(7)\n"
        "v_mfma_f32_16x16x32_bf16 v[100:103], v[112:115], v[176:179], v[100:103]\n"
        "v_mul_f32 v95, v95, v215\n"
        "v_cvt_pk_bf16_f32 v91, v91, v95\n"
        "v_mfma_f32_16x16x32_bf16 v[100:103], v[116:119], v[180:183], v[100:103]\n"
        "ds_write_b128 v246, v[88:91] offset:49152\n"
        "v_mul_f32 v96, v96, v216\n"
        "v_mul_f32 v97, v97, v217\n"
        "s_waitcnt lgkmcnt(4)\n"
        "v_mfma_f32_16x16x32_bf16 v[100:103], v[144:147], v[192:195], v[100:103]\n"
        "v_mul_f32 v98, v98, v218\n"
        "v_mul_f32 v99, v99, v219\n"
        "v_cvt_pk_bf16_f32 v96, v96, v97\n"
        "v_mfma_f32_16x16x32_bf16 v[100:103], v[148:151], v[196:199], v[100:103]\n"
        "v_cvt_pk_bf16_f32 v97, v98, v99\n"
        "v_mfma_f32_16x16x32_bf16 v[104:107], v[120:123], v[176:179], v[104:107]\n"
        "ds_write_b64 v247, v[96:97] offset:49152\n"
        "ds_write_b32 v237, v237 offset:2048\n"
        "v_mfma_f32_16x16x32_bf16 v[104:107], v[124:127], v[180:183], v[104:107]\n"
        "v_mfma_f32_16x16x32_bf16 v[104:107], v[152:155], v[192:195], v[104:107]\n"
        "v_mfma_f32_16x16x32_bf16 v[104:107], v[156:159], v[196:199], v[104:107]\n"
        "v_mfma_f32_16x16x32_bf16 v[108:111], v[128:131], v[176:179], v[108:111]\n"
        "v_mul_f32 v100, v100, v208\n"
        "v_mul_f32 v101, v101, v210\n"
        "v_mul_f32 v102, v102, v212\n"
        "v_mfma_f32_16x16x32_bf16 v[108:111], v[132:135], v[180:183], v[108:111]\n"
        "v_mul_f32 v103, v103, v214\n"
        "v_mfma_f32_16x16x32_bf16 v[108:111], v[160:163], v[192:195], v[108:111]\n"
        "v_mfma_f32_16x16x32_bf16 v[108:111], v[164:167], v[196:199], v[108:111]\n"
        "v_mul_f32 v104, v104, v209\n"
        "v_cvt_pk_bf16_f32 v100, v100, v104\n"
        "v_mul_f32 v105, v105, v211\n"
        "s_nop 7\n"
        "v_cvt_pk_bf16_f32 v101, v101, v105\n"
        "v_mul_f32 v106, v106, v213\n"
        "v_cvt_pk_bf16_f32 v102, v102, v106\n"
        "v_mul_f32 v107, v107, v215\n"
        "v_cvt_pk_bf16_f32 v103, v103, v107\n"
        "ds_write_b128 v246, v[100:103] offset:57344\n"
        "v_mul_f32 v108, v108, v216\n"
        "v_mul_f32 v109, v109, v217\n"
        "v_mul_f32 v110, v110, v218\n"
        "v_mul_f32 v111, v111, v219\n"
        "v_cvt_pk_bf16_f32 v108, v108, v109\n"
        "v_cvt_pk_bf16_f32 v109, v110, v111\n"
        "ds_write_b64 v247, v[108:109] offset:57344\n"
        "ds_write_b32 v237, v237 offset:2304\n"
        "s_endpgm\n"
        ".Ln_f1:\n"
        "s_movk_i32 s77, 1\n"
        "s_getpc_b64 s[70:71]\n"
        "s_branch .Ln_rec1\n"
        "s_branch .Ln_b1\n"
        ".Ln_f2:\n"
        "s_movk_i32 s77, 2\n"
        "s_getpc_b64 s[70:71]\n"
        "s_branch .Ln_rec0\n"
        "s_branch .Ln_b2\n"
        ".Ln_f3:\n"
        "s_movk_i32 s77, 3\n"
        "s_getpc_b64 s[70:71]\n"
        "s_branch .Ln_rec1\n"
        "s_branch .Ln_b3\n"
        ".Ln_f4:\n"
        "s_movk_i32 s77, 4\n"
        "s_getpc_b64 s[70:71]\n"
        "s_branch .Ln_rec0\n"
        "s_branch .Ln_b4\n"
        ".Ln_f5:\n"
        "s_movk_i32 s77, 5\n"
        "s_getpc_b64 s[70:71]\n"
        "s_branch .Ln_rec1\n"
        "s_branch .Ln_b5\n"
        ".Ln_f6:\n"
        "s_movk_i32 s77, 6\n"
        "s_getpc_b64 s[70:71]\n"
        "s_branch .Ln_rec0\n"
        "s_branch .Ln_b6\n"
        ".Ln_f7:\n"
        "s_movk_i32 s77, 7\n"
        "s_getpc_b64 s[70:71]\n"
        "s_branch .Ln_rec1\n"
        "s_branch .Ln_b7\n"
        ".Ln_f8:\n"
        "s_movk_i32 s77, 8\n"
        "s_getpc_b64 s[70:71]\n"
        "s_branch .Ln_rec0\n"
        "s_branch .Ln_b8\n"
        ".Ln_f9:\n"
        "s_movk_i32 s77, 9\n"
        "s_getpc_b64 s[70:71]\n"
        "s_branch .Ln_rec1\n"
        "s_branch .Ln_b9\n"
        ".Ln_f10:\n"
        "s_movk_i32 s77, 10\n"
        "s_getpc_b64 s[70:71]\n"
        "s_branch .Ln_rec0\n"
        "s_branch .Ln_b10\n"
        ".Ln_f11:\n"
        "s_movk_i32 s77, 11\n"
        "s_getpc_b64 s[70:71]\n"
        "s_branch .Ln_rec1\n"
        "s_branch .Ln_b11\n"
        ".Ln_f12:\n"
        "s_movk_i32 s77, 12\n"
        "s_getpc_b64 s[70:71]\n"
        "s_branch .Ln_rec0\n"
        "s_branch .Ln_b12\n"
        ".Ln_f13:\n"
        "s_movk_i32 s77, 13\n"
        "s_getpc_b64 s[70:71]\n"
        "s_branch .Ln_rec1\n"
        "s_branch .Ln_b13\n"
        ".Ln_f14:\n"
        "s_movk_i32 s77, 14\n"
        "s_getpc_b64 s[70:71]\n"
        "s_branch .Ln_rec0\n"
        "s_branch .Ln_b14\n"
        ".Ln_f15:\n"
        "s_movk_i32 s77, 15\n"
        "s_getpc_b64 s[70:71]\n"
        "s_branch .Ln_rec1\n"
        "s_branch .Ln_b15\n"
        ".Ln_f0:\n"
        "s_movk_i32 s77, 0\n"
        "s_waitcnt vmcnt(0)\n"
        "v_mov_b32 v144, v208\n"
        "v_mov_b32 v145, v209\n"
        "v_mov_b32 v146, v210\n"
        "v_mov_b32 v147, v211\n"
        "v_mov_b32 v148, v212\n"
        "v_mov_b32 v149, v213\n"
        "v_mov_b32 v150, v214\n"
        "v_mov_b32 v151, v215\n"
        "v_mov_b32 v152, v216\n"
        "v_mov_b32 v153, v217\n"
        "v_mov_b32 v154, v218\n"
        "v_mov_b32 v155, v219\n"
        "v_mov_b32 v156, v220\n"
        "v_mov_b32 v157, v221\n"
        "v_mov_b32 v158, v222\n"
        "v_mov_b32 v159, v223\n"
        "s_getpc_b64 s[70:71]\n"
        "s_branch .Ln_rec0\n"
        "v_mov_b32 v64, v208\n"
        "v_mov_b32 v208, v144\n"
        "v_mov_b32 v65, v209\n"
        "v_mov_b32 v209, v145\n"
        "v_mov_b32 v66, v210\n"
        "v_mov_b32 v210, v146\n"
        "v_mov_b32 v67, v211\n"
        "v_mov_b32 v211, v147\n"
        "v_mov_b32 v68, v212\n"
        "v_mov_b32 v212, v148\n"
        "v_mov_b32 v69, v213\n"
        "v_mov_b32 v213, v149\n"
        "v_mov_b32 v70, v214\n"
        "v_mov_b32 v214, v150\n"
        "v_mov_b32 v71, v215\n"
        "v_mov_b32 v215, v151\n"
        "v_mov_b32 v72, v216\n"
        "v_mov_b32 v216, v152\n"
        "v_mov_b32 v73, v217\n"
        "v_mov_b32 v217, v153\n"
        "v_mov_b32 v74, v218\n"
        "v_mov_b32 v218, v154\n"
        "v_mov_b32 v75, v219\n"
        "v_mov_b32 v219, v155\n"
        "v_mov_b32 v76, v220\n"
        "v_mov_b32 v220, v156\n"
        "v_mov_b32 v77, v221\n"
        "v_mov_b32 v221, v157\n"
        "v_mov_b32 v78, v222\n"
        "v_mov_b32 v222, v158\n"
        "v_mov_b32 v79, v223\n"
        "v_mov_b32 v223, v159\n"
        "s_branch .Ln_b0\n"
        ".Ln_rec0:\n"
        "s_waitcnt lgkmcnt(0)\n"
        "s_lshl_b32 s76, s77, 14\n"
        "s_add_u32 s76, s76, s64\n"
        "3:\n"
        "s_add_u32 s73, s73, 1\n"
        "s_cmp_lt_u32 s73, s74\n"
        "s_cbranch_scc1 2f\n"
        "s_mov_b32 s74, 0\n"
        "s_mov_b32 s78, 0\n"
        "1:\n"
        "s_lshl_b32 s72, s66, 2\n"
        "s_add_u32 s72, s72, 2\n"
        "s_cmp_eq_u32 s78, 0\n"
        "s_cselect_b32 s72, s68, s72\n"
        "s_cmp_eq_u32 s78, 2\n"
        "s_cselect_b32 s72, 3, s72\n"
        "s_mul_i32 s35, s77, 7\n"
        "s_add_u32 s72, s72, s35\n"
        "s_mul_i32 s35, s72, 9363\n"
        "s_lshr_b32 s35, s35, 16\n"
        "s_mul_i32 s36, s35, 7\n"
        "s_sub_u32 s36, s72, s36\n"
        "s_mul_i32 s40, s35, 917504\n"
        "s_mul_i32 s37, s33, 896\n"
        "s_lshl_b32 s38, s36, 7\n"
        "s_add_u32 s40, s40, s37\n"
        "s_add_u32 s40, s40, s38\n"
        "s_add_u32 s41, s40, 14336\n"
        "s_add_u32 s42, s41, 14336\n"
        "s_add_u32 s43, s42, 14336\n"
        "s_add_u32 s44, s43, 14336\n"
        "s_add_u32 s45, s44, 14336\n"
        "s_add_u32 s46, s45, 14336\n"
        "s_add_u32 s47, s46, 14336\n"
        "s_lshl_b32 s75, s33, 4\n"
        "s_add_u32 s75, s75, s35\n"
        "s_lshl_b32 s75, s75, 3\n"
        "s_add_u32 s75, s75, s36\n"
        "s_lshl_b32 s75, s75, 11\n"
        "s_add_u32 s75, s75, 114688\n"
        "buffer_load_dwordx4 v[112:115], v249, s[8:11], s40 offen\n"
        "buffer_load_dwordx4 v[116:119], v249, s[8:11], s41 offen\n"
        "buffer_load_dwordx4 v[120:123], v249, s[8:11], s42 offen\n"
        "buffer_load_dwordx4 v[124:127], v249, s[8:11], s43 offen\n"
        "buffer_load_dwordx4 v[128:131], v249, s[8:11], s44 offen\n"
        "buffer_load_dwordx4 v[132:135], v249, s[8:11], s45 offen\n"
        "buffer_load_dwordx4 v[136:139], v249, s[8:11], s46 offen\n"
        "buffer_load_dwordx4 v[140:143], v249, s[8:11], s47 offen\n"
        "v_mov_b32 v210, s34\n"
        "v_mov_b32 v211, s39\n"
        "v_mov_b32 v214, s34\n"
        "v_mov_b32 v215, s39\n"
        "s_waitcnt vmcnt(0)\n"
        "v_lshl_add_u64 v[140:141], v[140:141], 4, v[136:137]\n"
        "v_lshl_add_u64 v[142:143], v[142:143], 4, v[138:139]\n"
        "v_lshl_add_u64 v[140:141], v[140:141], 4, v[132:133]\n"
        "v_lshl_add_u64 v[142:143], v[142:143], 4, v[134:135]\n"
        "v_lshl_add_u64 v[140:141], v[140:141], 4, v[128:129]\n"
        "v_lshl_add_u64 v[142:143], v[142:143], 4, v[130:131]\n"
        "v_lshl_add_u64 v[140:141], v[140:141], 4, v[124:125]\n"
        "v_lshl_add_u64 v[142:143], v[142:143], 4, v[126:127]\n"
        "v_lshl_add_u64 v[140:141], v[140:141], 4, v[120:121]\n"
        "v_lshl_add_u64 v[142:143], v[142:143], 4, v[122:123]\n"
        "v_lshl_add_u64 v[140:141], v[140:141], 4, v[116:117]\n"
        "v_lshl_add_u64 v[142:143], v[142:143], 4, v[118:119]\n"
        "v_lshl_add_u64 v[208:209], v[140:141], 4, v[112:113]\n"
        "v_lshl_add_u64 v[212:213], v[142:143], 4, v[114:115]\n"
        "s_nop 0\n"
        "v_permlane32_swap_b32 v208, v209\n"
        "v_permlane32_swap_b32 v212, v213\n"
        "buffer_store_dwordx4 v[208:211], v250, s[16:19], s75 offen sc1\n"
        "buffer_store_dwordx4 v[212:215], v251, s[16:19], s75 offen offset:1024 sc1\n"
        "s_waitcnt vmcnt(0)\n"
        "s_add_u32 s78, s78, 1\n"
        "s_cmp_lt_u32 s78, 3\n"
        "s_cbranch_scc1 1b\n"
        "2:\n"
        "s_sleep 1\n"
        "buffer_load_dwordx4 v[208:211], v232, s[16:19], s76 offen sc1\n"
        "buffer_load_dwordx4 v[212:215], v233, s[16:19], s76 offen sc1\n"
        "buffer_load_dwordx4 v[216:219], v234, s[16:19], s76 offen sc1\n"
        "buffer_load_dwordx4 v[220:223], v235, s[16:19], s76 offen sc1\n"
        "s_waitcnt vmcnt(0)\n"
        "v_bitop3_b32 v210, v210, v214, s34 bitop3:0x7e\n"
        "v_bitop3_b32 v211, v211, v215, s39 bitop3:0x7e\n"
        "v_bitop3_b32 v218, v218, v222, s34 bitop3:0x7e\n"
        "v_bitop3_b32 v219, v219, v223, s39 bitop3:0x7e\n"
        "v_or3_b32 v210, v210, v211, v218\n"
        "v_or_b32 v210, v210, v219\n"
        "v_cmp_ne_u32 vcc, 0, v210\n"
        "s_cbranch_vccnz 3b\n"
        "4:\n"
        "v_perm_b32 v224, v208, v236, s60\n"
        "v_perm_b32 v225, v208, v236, s61\n"
        "v_perm_b32 v226, v208, v236, s62\n"
        "v_perm_b32 v227, v208, v236, s63\n"
        "ds_read_b32 v112, v224\n"
        "v_perm_b32 v224, v209, v236, s60\n"
        "ds_read_b32 v113, v225\n"
        "v_perm_b32 v225, v209, v236, s61\n"
        "ds_read_b32 v114, v226\n"
        "v_perm_b32 v226, v209, v236, s62\n"
        "ds_read_b32 v115, v227\n"
        "v_perm_b32 v227, v209, v236, s63\n"
        "ds_read_b32 v116, v224\n"
        "v_perm_b32 v224, v212, v236, s60\n"
        "ds_read_b32 v117, v225\n"
        "v_perm_b32 v225, v212, v236, s61\n"
        "ds_read_b32 v118, v226\n"
        "v_perm_b32 v226, v212, v236, s62\n"
        "ds_read_b32 v119, v227\n"
        "v_perm_b32 v227, v212, v236, s63\n"
        "ds_read_b32 v120, v224\n"
        "v_perm_b32 v224, v213, v236, s60\n"
        "ds_read_b32 v121, v225\n"
        "v_perm_b32 v225, v213, v236, s61\n"
        "ds_read_b32 v122, v226\n"
        "v_perm_b32 v226, v213, v236, s62\n"
        "ds_read_b32 v123, v227\n"
        "v_perm_b32 v227, v213, v236, s63\n"
        "ds_read_b32 v124, v224\n"
        "v_perm_b32 v224, v216, v236, s60\n"
        "ds_read_b32 v125, v225\n"
        "v_perm_b32 v225, v216, v236, s61\n"
        "ds_read_b32 v126, v226\n"
        "v_perm_b32 v226, v216, v236, s62\n"
        "ds_read_b32 v127, v227\n"
        "v_perm_b32 v227, v216, v236, s63\n"
        "ds_read_b32 v128, v224\n"
        "v_perm_b32 v224, v217, v236, s60\n"
        "ds_read_b32 v129, v225\n"
        "v_perm_b32 v225, v217, v236, s61\n"
        "ds_read_b32 v130, v226\n"
        "v_perm_b32 v226, v217, v236, s62\n"
        "ds_read_b32 v131, v227\n"
        "v_perm_b32 v227, v217, v236, s63\n"
        "ds_read_b32 v132, v224\n"
        "v_perm_b32 v224, v220, v236, s60\n"
        "ds_read_b32 v133, v225\n"
        "v_perm_b32 v225, v220, v236, s61\n"
        "ds_read_b32 v134, v226\n"
        "v_perm_b32 v226, v220, v236, s62\n"
        "ds_read_b32 v135, v227\n"
        "v_perm_b32 v227, v220, v236, s63\n"
        "ds_read_b32 v136, v224\n"
        "v_perm_b32 v224, v221, v236, s60\n"
        "ds_read_b32 v137, v225\n"
        "v_perm_b32 v225, v221, v236, s61\n"
        "ds_read_b32 v138, v226\n"
        "v_perm_b32 v226, v221, v236, s62\n"
        "ds_read_b32 v139, v227\n"
        "v_perm_b32 v227, v221, v236, s63\n"
        "ds_read_b32 v140, v224\n"
        "ds_read_b32 v141, v225\n"
        "ds_read_b32 v142, v226\n"
        "ds_read_b32 v143, v227\n"
        "s_waitcnt lgkmcnt(0)\n"
        "s_add_u32 s70, s70, 4\n"
        "s_addc_u32 s71, s71, 0\n"
        "s_setpc_b64 s[70:71]\n"
        ".Ln_rec1:\n"
        "s_waitcnt lgkmcnt(0)\n"
        "s_lshl_b32 s76, s77, 14\n"
        "s_add_u32 s76, s76, s64\n"
        "3:\n"
        "s_add_u32 s73, s73, 1\n"
        "s_cmp_lt_u32 s73, s74\n"
        "s_cbranch_scc1 2f\n"
        "s_mov_b32 s74, 0\n"
        "s_mov_b32 s78, 0\n"
        "1:\n"
        "s_lshl_b32 s72, s66, 2\n"
        "s_add_u32 s72, s72, 2\n"
        "s_cmp_eq_u32 s78, 0\n"
        "s_cselect_b32 s72, s68, s72\n"
        "s_cmp_eq_u32 s78, 2\n"
        "s_cselect_b32 s72, 3, s72\n"
        "s_mul_i32 s35, s77, 7\n"
        "s_add_u32 s72, s72, s35\n"
        "s_mul_i32 s35, s72, 9363\n"
        "s_lshr_b32 s35, s35, 16\n"
        "s_mul_i32 s36, s35, 7\n"
        "s_sub_u32 s36, s72, s36\n"
        "s_mul_i32 s40, s35, 917504\n"
        "s_mul_i32 s37, s33, 896\n"
        "s_lshl_b32 s38, s36, 7\n"
        "s_add_u32 s40, s40, s37\n"
        "s_add_u32 s40, s40, s38\n"
        "s_add_u32 s41, s40, 14336\n"
        "s_add_u32 s42, s41, 14336\n"
        "s_add_u32 s43, s42, 14336\n"
        "s_add_u32 s44, s43, 14336\n"
        "s_add_u32 s45, s44, 14336\n"
        "s_add_u32 s46, s45, 14336\n"
        "s_add_u32 s47, s46, 14336\n"
        "s_lshl_b32 s75, s33, 4\n"
        "s_add_u32 s75, s75, s35\n"
        "s_lshl_b32 s75, s75, 3\n"
        "s_add_u32 s75, s75, s36\n"
        "s_lshl_b32 s75, s75, 11\n"
        "s_add_u32 s75, s75, 114688\n"
        "buffer_load_dwordx4 v[144:147], v249, s[8:11], s40 offen\n"
        "buffer_load_dwordx4 v[148:151], v249, s[8:11], s41 offen\n"
        "buffer_load_dwordx4 v[152:155], v249, s[8:11], s42 offen\n"
        "buffer_load_dwordx4 v[156:159], v249, s[8:11], s43 offen\n"
        "buffer_load_dwordx4 v[160:163], v249, s[8:11], s44 offen\n"
        "buffer_load_dwordx4 v[164:167], v249, s[8:11], s45 offen\n"
        "buffer_load_dwordx4 v[168:171], v249, s[8:11], s46 offen\n"
        "buffer_load_dwordx4 v[172:175], v249, s[8:11], s47 offen\n"
        "v_mov_b32 v210, s34\n"
        "v_mov_b32 v211, s39\n"
        "v_mov_b32 v214, s34\n"
        "v_mov_b32 v215, s39\n"
        "s_waitcnt vmcnt(0)\n"
        "v_lshl_add_u64 v[172:173], v[172:173], 4, v[168:169]\n"
        "v_lshl_add_u64 v[174:175], v[174:175], 4, v[170:171]\n"
        "v_lshl_add_u64 v[172:173], v[172:173], 4, v[164:165]\n"
        "v_lshl_add_u64 v[174:175], v[174:175], 4, v[166:167]\n"
        "v_lshl_add_u64 v[172:173], v[172:173], 4, v[160:161]\n"
        "v_lshl_add_u64 v[174:175], v[174:175], 4, v[162:163]\n"
        "v_lshl_add_u64 v[172:173], v[172:173], 4, v[156:157]\n"
        "v_lshl_add_u64 v[174:175], v[174:175], 4, v[158:159]\n"
        "v_lshl_add_u64 v[172:173], v[172:173], 4, v[152:153]\n"
        "v_lshl_add_u64 v[174:175], v[174:175], 4, v[154:155]\n"
        "v_lshl_add_u64 v[172:173], v[172:173], 4, v[148:149]\n"
        "v_lshl_add_u64 v[174:175], v[174:175], 4, v[150:151]\n"
        "v_lshl_add_u64 v[208:209], v[172:173], 4, v[144:145]\n"
        "v_lshl_add_u64 v[212:213], v[174:175], 4, v[146:147]\n"
        "s_nop 0\n"
        "v_permlane32_swap_b32 v208, v209\n"
        "v_permlane32_swap_b32 v212, v213\n"
        "buffer_store_dwordx4 v[208:211], v250, s[16:19], s75 offen sc1\n"
        "buffer_store_dwordx4 v[212:215], v251, s[16:19], s75 offen offset:1024 sc1\n"
        "s_waitcnt vmcnt(0)\n"
        "s_add_u32 s78, s78, 1\n"
        "s_cmp_lt_u32 s78, 3\n"
        "s_cbranch_scc1 1b\n"
        "2:\n"
        "s_sleep 1\n"
        "buffer_load_dwordx4 v[208:211], v232, s[16:19], s76 offen sc1\n"
        "buffer_load_dwordx4 v[212:215], v233, s[16:19], s76 offen sc1\n"
        "buffer_load_dwordx4 v[216:219], v234, s[16:19], s76 offen sc1\n"
        "buffer_load_dwordx4 v[220:223], v235, s[16:19], s76 offen sc1\n"
        "s_waitcnt vmcnt(0)\n"
        "v_bitop3_b32 v210, v210, v214, s34 bitop3:0x7e\n"
        "v_bitop3_b32 v211, v211, v215, s39 bitop3:0x7e\n"
        "v_bitop3_b32 v218, v218, v222, s34 bitop3:0x7e\n"
        "v_bitop3_b32 v219, v219, v223, s39 bitop3:0x7e\n"
        "v_or3_b32 v210, v210, v211, v218\n"
        "v_or_b32 v210, v210, v219\n"
        "v_cmp_ne_u32 vcc, 0, v210\n"
        "s_cbranch_vccnz 3b\n"
        "4:\n"
        "v_perm_b32 v224, v208, v236, s60\n"
        "v_perm_b32 v225, v208, v236, s61\n"
        "v_perm_b32 v226, v208, v236, s62\n"
        "v_perm_b32 v227, v208, v236, s63\n"
        "ds_read_b32 v144, v224\n"
        "v_perm_b32 v224, v209, v236, s60\n"
        "ds_read_b32 v145, v225\n"
        "v_perm_b32 v225, v209, v236, s61\n"
        "ds_read_b32 v146, v226\n"
        "v_perm_b32 v226, v209, v236, s62\n"
        "ds_read_b32 v147, v227\n"
        "v_perm_b32 v227, v209, v236, s63\n"
        "ds_read_b32 v148, v224\n"
        "v_perm_b32 v224, v212, v236, s60\n"
        "ds_read_b32 v149, v225\n"
        "v_perm_b32 v225, v212, v236, s61\n"
        "ds_read_b32 v150, v226\n"
        "v_perm_b32 v226, v212, v236, s62\n"
        "ds_read_b32 v151, v227\n"
        "v_perm_b32 v227, v212, v236, s63\n"
        "ds_read_b32 v152, v224\n"
        "v_perm_b32 v224, v213, v236, s60\n"
        "ds_read_b32 v153, v225\n"
        "v_perm_b32 v225, v213, v236, s61\n"
        "ds_read_b32 v154, v226\n"
        "v_perm_b32 v226, v213, v236, s62\n"
        "ds_read_b32 v155, v227\n"
        "v_perm_b32 v227, v213, v236, s63\n"
        "ds_read_b32 v156, v224\n"
        "v_perm_b32 v224, v216, v236, s60\n"
        "ds_read_b32 v157, v225\n"
        "v_perm_b32 v225, v216, v236, s61\n"
        "ds_read_b32 v158, v226\n"
        "v_perm_b32 v226, v216, v236, s62\n"
        "ds_read_b32 v159, v227\n"
        "v_perm_b32 v227, v216, v236, s63\n"
        "ds_read_b32 v160, v224\n"
        "v_perm_b32 v224, v217, v236, s60\n"
        "ds_read_b32 v161, v225\n"
        "v_perm_b32 v225, v217, v236, s61\n"
        "ds_read_b32 v162, v226\n"
        "v_perm_b32 v226, v217, v236, s62\n"
        "ds_read_b32 v163, v227\n"
        "v_perm_b32 v227, v217, v236, s63\n"
        "ds_read_b32 v164, v224\n"
        "v_perm_b32 v224, v220, v236, s60\n"
        "ds_read_b32 v165, v225\n"
        "v_perm_b32 v225, v220, v236, s61\n"
        "ds_read_b32 v166, v226\n"
        "v_perm_b32 v226, v220, v236, s62\n"
        "ds_read_b32 v167, v227\n"
        "v_perm_b32 v227, v220, v236, s63\n"
        "ds_read_b32 v168, v224\n"
        "v_perm_b32 v224, v221, v236, s60\n"
        "ds_read_b32 v169, v225\n"
        "v_perm_b32 v225, v221, v236, s61\n"
        "ds_read_b32 v170, v226\n"
        "v_perm_b32 v226, v221, v236, s62\n"
        "ds_read_b32 v171, v227\n"
        "v_perm_b32 v227, v221, v236, s63\n"
        "ds_read_b32 v172, v224\n"
        "ds_read_b32 v173, v225\n"
        "ds_read_b32 v174, v226\n"
        "ds_read_b32 v175, v227\n"
        "s_waitcnt lgkmcnt(0)\n"
        "s_add_u32 s70, s70, 4\n"
        "s_addc_u32 s71, s71, 0\n"
        "s_setpc_b64 s[70:71]\n"
        ".Ln_ret:\n"
        "s_setpc_b64 s[70:71]\n"
        ".Ln_end:\n"
        :: "{s[0:1]}"((u64)__builtin_amdgcn_dispatch_ptr()), "{s[2:3]}"((u64)__builtin_amdgcn_kernarg_segment_ptr()), "{s[4:5]}"(llvm_dispatch_id()),
           "{s6}"(blockIdx.x), "{v0}"(threadIdx.x), "s"((unsigned)(size_t)(__attribute__((address_space(3))) unsigned char*)smem)
        : "v1", "v2", "v3", "v4", "v5", "v6", "v7", "v8", "v9", "v10", "v11", "v12", "v13", "v14", "v15", "v16", "v17", "v18", "v19", "v20", "v21", "v22", "v23", "v24", "v25", "v26", "v27", "v28", "v29", "v30", "v31", "v32", "v33", "v34", "v35", "v36", "v37", "v38", "v39", "v40", "v41", "v42", "v43", "v44", "v45", "v46", "v47", "v48", "v49", "v50", "v51", "v52", "v53", "v54", "v55", "v56", "v57", "v58", "v59", "v60", "v61", "v62", "v63", "v64", "v65", "v66", "v67", "v68", "v69", "v70", "v71", "v72", "v73", "v74", "v75", "v76", "v77", "v78", "v79", "v80", "v81", "v82", "v83", "v84", "v85", "v86", "v87", "v88", "v89", "v90", "v91", "v92", "v93", "v94", "v95", "v96", "v97", "v98", "v99", "v100", "v101", "v102", "v103", "v104", "v105", "v106", "v107", "v108", "v109", "v110", "v111", "v112", "v113", "v114", "v115", "v116", "v117", "v118", "v119", "v120", "v121", "v122", "v123", "v124", "v125", "v126", "v127", "v128", "v129", "v130", "v131", "v132", "v133", "v134", "v135", "v136", "v137", "v138", "v139", "v140", "v141", "v142", "v143", "v144", "v145", "v146", "v147", "v148", "v149", "v150", "v151", "v152", "v153", "v154", "v155", "v156", "v157", "v158", "v159", "v160", "v161", "v162", "v163", "v164", "v165", "v166", "v167", "v168", "v169", "v170", "v171", "v172", "v173", "v174", "v175", "v176", "v177", "v178", "v179", "v180", "v181", "v182", "v183", "v184", "v185", "v186", "v187", "v188", "v189", "v190", "v191", "v192", "v193", "v194", "v195", "v196", "v197", "v198", "v199", "v200", "v201", "v202", "v203", "v204", "v205", "v206", "v207", "v208", "v209", "v210", "v211", "v212", "v213", "v214", "v215", "v216", "v217", "v218", "v219", "v220", "v221", "v222", "v223", "v224", "v225", "v226", "v227", "v228", "v229", "v230", "v231", "v232", "v233", "v234", "v235", "v236", "v237", "v238", "v239", "v240", "v241", "v242", "v243", "v244", "v245", "v246", "v247", "v248", "v249", "v250", "v251", "v252", "v253", "v254", "v255", "s7", "s8", "s9", "s10", "s11", "s12", "s13", "s14", "s15", "s16", "s17", "s18", "s19", "s20", "s21", "s22", "s23", "s24", "s25", "s26", "s27", "s28", "s29", "s30", "s31", "s32", "s33", "s34", "s35", "s36", "s37", "s38", "s39", "s40", "s41", "s42", "s43", "s44", "s45", "s46", "s47", "s48", "s49", "s50", "s51", "s52", "s53", "s54", "s55", "s56", "s57", "s58", "s59", "s60", "s61", "s62", "s63", "s64", "s65", "s66", "s67", "s68", "s69", "s70", "s71", "s72", "s73", "s74", "s75", "s76", "s77", "s78", "s79", "s80", "s81", "s82", "s83", "s84", "s85", "s86", "s87", "s88", "s89", "s90", "s91", "s92", "s93", "s94", "s95", "vcc", "scc", "memory");
}
}

extern "C" void kernel_launch(void* const* d_in, const int* in_sizes, int n_in, void* d_out, int out_size, void* d_ws, size_t ws_size, hipStream_t stream) {
    const u64 fl = (u64)0x00020000 << 32;
    nf4_gemm_kernel<<<dim3(256), dim3(512), 0, stream>>>(d_in[1], fl | (1024u * 3584u * 4u), d_in[0], fl | (2048u * 1024u * 2u), (char*)d_ws + (32u << 20), fl | ((4u << 20) + 114688u),
                                                         d_out, fl | (2048u * 3584u * 2u), d_in[2], d_in[3], d_ws);
}
```

```cpp
#include <hip/hip_runtime.h>
#include <stdint.h>

extern "C" __device__ unsigned long long llvm_dispatch_id() __asm("llvm.amdgcn.dispatch.id");

namespace {
typedef unsigned long long u64;
__global__ __launch_bounds__(512, 2)
void nf4_gemm_kernel(const void* q, u64 qd, const void* x, u64 xd, void* w, u64 wd, void* o, u64 od, const void* lut, const void* scales, void* dbg) {
    __shared__ __attribute__((aligned(16))) unsigned char smem[163840];
    asm volatile(
        ".Ln_start:\n"
        "s_load_dwordx16 s[8:23], s[2:3], 0x0\n"
        "s_load_dwordx4 s[24:27], s[2:3], 0x40\n"
        "v_readfirstlane_b32 s30, v0\n"
        "v_and_b32 v252, 63, v0\n"
        "s_getpc_b64 s[86:87]\n"
        ".Ln_here:\n"
        "s_cmp_lt_u32 s30, 448\n"
        "s_cbranch_scc1 1f\n"
        "s_lshr_b32 s35, s6, 3\n"
        "s_lshl_b32 s35, s35, 7\n"
        "v_lshl_add_u32 v3, v252, 12, s35\n"
        "v_min_u32 v3, .Ln_end-.Ln_here, v3\n"
        "global_load_dword v110, v3, s[86:87]\n"
        "1:\n"
        "s_and_b32 s32, s6, 3\n"
        "s_lshl_b32 s32, s32, 2\n"
        "s_bfe_u32 s35, s6, 0x20003\n"
        "s_or_b32 s32, s32, s35\n"
        "s_and_b32 s33, s6, 4\n"
        "s_lshl_b32 s33, s33, 1\n"
        "s_lshr_b32 s35, s6, 5\n"
        "s_or_b32 s33, s33, s35\n"
        "s_lshr_b32 s30, s30, 6\n"
        "s_and_b32 s31, s30, 3\n"
        "s_and_b32 s35, s6, 7\n"
        "s_lshl_b32 s35, s35, 2\n"
        "s_lshr_b32 s69, 0x54763210, s35\n"
        "s_and_b32 s69, s69, 7\n"
        "s_lshl_b32 s69, s69, 2\n"
        "s_and_b32 s35, s32, 3\n"
        "s_or_b32 s69, s69, s35\n"
        "v_lshrrev_b32 v1, 3, v252\n"
        "v_and_b32 v2, 7, v252\n"
        "v_mul_u32_u24 v4, 0x1c000, v1\n"
        "v_lshl_add_u32 v249, v2, 4, v4\n"
        "s_cmp_ge_u32 s30, 4\n"
        "s_cbranch_scc1 .Ln_helper\n"
        "s_lshl_b32 s72, s69, 1\n"
        "s_add_u32 s72, s72, s30\n"
        "s_mul_i32 s35, s72, 9363\n"
        "s_lshr_b32 s35, s35, 16\n"
        "s_mul_i32 s36, s35, 7\n"
        "s_sub_u32 s36, s72, s36\n"
        "s_and_b32 s79, s35, 1\n"
        "s_lshl_b32 s79, s79, 3\n"
        "s_and_b32 s37, s33, 7\n"
        "s_or_b32 s79, s79, s37\n"
        "s_lshr_b32 s35, s35, 1\n"
        "s_mul_i32 s40, s35, 917504\n"
        "s_mul_i32 s37, s79, 896\n"
        "s_lshl_b32 s38, s36, 7\n"
        "s_add_u32 s40, s40, s37\n"
        "s_add_u32 s40, s40, s38\n"
        "s_add_u32 s41, s40, 14336\n"
        "s_add_u32 s42, s41, 14336\n"
        "s_add_u32 s43, s42, 14336\n"
        "s_add_u32 s44, s43, 14336\n"
        "s_add_u32 s45, s44, 14336\n"
        "s_add_u32 s46, s45, 14336\n"
        "s_add_u32 s47, s46, 14336\n"
        "s_cmp_ge_u32 s30, 2\n"
        "s_cbranch_scc1 2f\n"
        "s_waitcnt lgkmcnt(0)\n"
        "buffer_load_dwordx4 v[112:115], v249, s[8:11], s40 offen\n"
        "buffer_load_dwordx4 v[116:119], v249, s[8:11], s41 offen\n"
        "buffer_load_dwordx4 v[120:123], v249, s[8:11], s42 offen\n"
        "buffer_load_dwordx4 v[124:127], v249, s[8:11], s43 offen\n"
        "buffer_load_dwordx4 v[128:131], v249, s[8:11], s44 offen\n"
        "buffer_load_dwordx4 v[132:135], v249, s[8:11], s45 offen\n"
        "buffer_load_dwordx4 v[136:139], v249, s[8:11], s46 offen\n"
        "buffer_load_dwordx4 v[140:143], v249, s[8:11], s47 offen\n"
        "2:\n"
        "s_waitcnt lgkmcnt(0)\n"
        "s_lshl_b32 s37, s31, 3\n"
        "s_load_dwordx8 s[48:55], s[24:25], 0x0\n"
        "s_load_dwordx2 s[56:57], s[24:25], s37\n"
        "s_lshl_b32 s75, s79, 4\n"
        "s_add_u32 s75, s75, s35\n"
        "s_lshl_b32 s75, s75, 3\n"
        "s_add_u32 s75, s75, s36\n"
        "s_lshl_b32 s75, s75, 11\n"
        "s_add_u32 s75, s75, 114688\n"
        "s_mul_i32 s34, s4, 0x7f4a7c15\n"
        "s_mul_hi_u32 s39, s4, 0x7f4a7c15\n"
        "s_mul_i32 s35, s4, 0x9e3779b9\n"
        "s_mul_i32 s36, s5, 0x7f4a7c15\n"
        "s_add_u32 s39, s39, s35\n"
        "s_add_u32 s39, s39, s36\n"
        "s_xor_b32 s34, s34, s0\n"
        "s_xor_b32 s39, s39, s1\n"
        "s_xor_b32 s34, s34, 0x5bd1e995\n"
        "s_mov_b32 s73, 0\n"
        "s_mov_b32 s74, 20000\n"
        "s_lshr_b32 s66, s31, 1\n"
        "s_and_b32 s67, s31, 1\n"
        "s_lshl_b32 s68, s66, 2\n"
        "s_add_u32 s68, s68, s67\n"
        "s_lshl_b32 s64, s33, 18\n"
        "s_add_u32 s65, s64, 0x4000\n"
        "v_and_b32 v184, 15, v252\n"
        "v_lshrrev_b32 v185, 4, v252\n"
        "v_and_b32 v186, 3, v1\n"
        "v_lshlrev_b32 v186, 8, v186\n"
        "v_lshl_add_u32 v186, v2, 4, v186\n"
        "v_lshrrev_b32 v250, 5, v252\n"
        "v_lshl_add_u32 v250, v250, 7, v186\n"
        "v_xor_b32 v251, 0xc0, v250\n"
        "v_mov_b32 v178, s34\n"
        "v_mov_b32 v179, s39\n"
        "v_mov_b32 v182, s34\n"
        "v_mov_b32 v183, s39\n"
        "v_and_b32 v192, 3, v1\n"
        "v_mul_u32_u24 v193, 0x202, v192\n"
        "v_add_u32 v193, 0x0c0c0100, v193\n"
        "v_lshlrev_b32 v192, 8, v192\n"
        "v_lshrrev_b32 v144, 5, v252\n"
        "v_lshl_add_u32 v192, v144, 12, v192\n"
        "v_mul_u32_u24 v144, 0x202, v144\n"
        "v_lshlrev_b32 v144, 16, v144\n"
        "v_add_u32 v144, 0x01000c0c, v144\n"
        "v_lshl_add_u32 v192, v2, 4, v192\n"
        "s_lshl_b32 s35, s31, 14\n"
        "v_add_u32 v192, s35, v192\n"
        "v_and_b32 v186, 1, v184\n"
        "v_lshrrev_b32 v187, 1, v184\n"
        "v_lshl_add_u32 v187, v185, 4, v187\n"
        "v_mul_u32_u24 v188, 0x4c, v186\n"
        "v_xor_b32 v187, v187, v188\n"
        "s_lshl_b32 s36, s68, 11\n"
        "s_add_u32 s36, s36, 114688\n"
        "v_lshl_add_u32 v232, v187, 4, s36\n"
        "v_xor_b32 v233, 0x80, v232\n"
        "v_lshrrev_b32 v187, 2, v184\n"
        "v_lshl_add_u32 v187, v185, 4, v187\n"
        "v_lshl_add_u32 v187, v186, 3, v187\n"
        "v_bfe_u32 v188, v184, 1, 1\n"
        "v_mul_u32_u24 v188, 0x4c, v188\n"
        "s_lshl_b32 s36, s67, 2\n"
        "v_xor_b32 v188, s36, v188\n"
        "v_xor_b32 v187, v188, v187\n"
        "s_mov_b32 s36, 120832\n"
        "v_lshl_add_u32 v235, v187, 4, s36\n"
        "s_lshl_b32 s35, s66, 13\n"
        "s_sub_u32 s35, s35, 2048\n"
        "v_add_u32 v234, s35, v235\n"
        "v_and_b32 v236, 31, v252\n"
        "v_lshlrev_b32 v236, 2, v236\n"
        "v_bfe_u32 v186, v184, 1, 3\n"
        "v_xor_b32 v186, v186, v185\n"
        "v_lshlrev_b32 v186, 4, v186\n"
        "v_lshl_add_u32 v186, v184, 7, v186\n"
        "s_lshl_b32 s35, s66, 13\n"
        "s_add_u32 s35, s35, 65536\n"
        "v_add_u32 v228, s35, v186\n"
        "v_xor_b32 v229, 64, v228\n"
        "v_xor_b32 v230, 0x2000, v228\n"
        "v_xor_b32 v231, 0x2000, v229\n"
        "s_waitcnt lgkmcnt(0)\n"
        "v_mov_b32 v194, s48\n"
        "v_mov_b32 v195, s50\n"
        "v_mov_b32 v196, s52\n"
        "v_mov_b32 v197, s54\n"
        "v_perm_b32 v194, s49, v194, v193\n"
        "v_perm_b32 v195, s51, v195, v193\n"
        "v_perm_b32 v196, s53, v196, v193\n"
        "v_perm_b32 v197, s55, v197, v193\n"
        "v_perm_b32 v198, s56, s56, v144\n"
        "v_perm_b32 v199, s57, s57, v144\n"
        "v_or_b32 v144, v194, v198\n"
        "v_or_b32 v145, v194, v198\n"
        "v_or_b32 v146, v194, v198\n"
        "v_or_b32 v147, v194, v198\n"
        "ds_write_b128 v192, v[144:147] offset:0\n"
        "v_or_b32 v148, v195, v198\n"
        "v_or_b32 v149, v195, v198\n"
        "v_or_b32 v150, v195, v198\n"
        "v_or_b32 v151, v195, v198\n"
        "ds_write_b128 v192, v[148:151] offset:1024\n"
        "v_or_b32 v152, v196, v198\n"
        "v_or_b32 v153, v196, v198\n"
        "v_or_b32 v154, v196, v198\n"
        "v_or_b32 v155, v196, v198\n"
        "ds_write_b128 v192, v[152:155] offset:2048\n"
        "v_or_b32 v156, v197, v198\n"
        "v_or_b32 v157, v197, v198\n"
        "v_or_b32 v158, v197, v198\n"
        "v_or_b32 v159, v197, v198\n"
        "ds_write_b128 v192, v[156:159] offset:3072\n"
        "v_or_b32 v160, v194, v199\n"
        "v_or_b32 v161, v194, v199\n"
        "v_or_b32 v162, v194, v199\n"
        "v_or_b32 v163, v194, v199\n"
        "ds_write_b128 v192, v[160:163] offset:8192\n"
        "v_or_b32 v164, v195, v199\n"
        "v_or_b32 v165, v195, v199\n"
        "v_or_b32 v166, v195, v199\n"
        "v_or_b32 v167, v195, v199\n"
        "ds_write_b128 v192, v[164:167] offset:9216\n"
        "v_or_b32 v168, v196, v199\n"
        "v_or_b32 v169, v196, v199\n"
        "v_or_b32 v170, v196, v199\n"
        "v_or_b32 v171, v196, v199\n"
        "ds_write_b128 v192, v[168:171] offset:10240\n"
        "v_or_b32 v172, v197, v199\n"
        "v_or_b32 v173, v197, v199\n"
        "v_or_b32 v174, v197, v199\n"
        "v_or_b32 v175, v197, v199\n"
        "ds_write_b128 v192, v[172:175] offset:11264\n"
        "s_mul_i32 s35, s33, 448\n"
        "s_lshl_b32 s36, s68, 6\n"
        "s_add_u32 s36, s36, s35\n"
        "v_lshl_add_u32 v108, v185, 4, s36\n"
        "s_lshl_b32 s36, s67, 5\n"
        "s_add_u32 s35, s35, s36\n"
        "v_lshl_add_u32 v109, v185, 3, s35\n"
        "s_lshl_b32 s36, s66, 8\n"
        "v_add_u32 v110, s36, v109\n"
        "v_and_b32 v186, 7, v184\n"
        "s_lshl_b32 s35, s66, 12\n"
        "s_add_u32 s35, s35, 98304\n"
        "v_lshl_add_u32 v187, v184, 8, s35\n"
        "s_add_u32 s36, s66, s67\n"
        "s_lshl_b32 s36, s36, 2\n"
        "v_add_u32 v188, s36, v185\n"
        "v_xor_b32 v188, v188, v186\n"
        "v_lshl_add_u32 v246, v188, 4, v187\n"
        "v_and_b32 v188, 1, v185\n"
        "v_lshl_add_u32 v187, v188, 3, v187\n"
        "v_lshrrev_b32 v185, 1, v185\n"
        "s_lshl_b32 s35, s67, 1\n"
        "s_lshl_b32 s36, s66, 2\n"
        "s_add_u32 s36, s36, s35\n"
        "s_add_u32 s36, s36, 8\n"
        "v_add_u32 v188, s36, v185\n"
        "v_xor_b32 v188, v188, v186\n"
        "v_lshl_add_u32 v247, v188, 4, v187\n"
        "s_mul_i32 s36, s66, 12\n"
        "s_sub_u32 s36, s35, s36\n"
        "s_add_u32 s36, s36, 12\n"
        "v_add_u32 v188, s36, v185\n"
        "v_xor_b32 v188, v188, v186\n"
        "v_lshl_add_u32 v248, v188, 4, v187\n"
        "s_lshl_b32 s35, s31, 2\n"
        "s_add_u32 s35, s35, 128\n"
        "v_mov_b32 v237, s35\n"
        "s_mov_b32 s60, 0x0c0c0400\n"
        "s_mov_b32 s61, 0x0c0c0500\n"
        "s_mov_b32 s62, 0x0c0c0600\n"
        "s_mov_b32 s63, 0x0c0c0700\n"
        "global_load_dwordx4 v[238:241], v108, s[26:27]\n"
        "global_load_dwordx2 v[242:243], v110, s[26:27] offset:128\n"
        "global_load_dwordx2 v[244:245], v109, s[26:27] offset:192\n"
        "s_cmp_ge_u32 s30, 2\n"
        "s_cbranch_scc1 .Ln_none\n"
        "s_waitcnt vmcnt(3)\n"
        "v_lshl_add_u64 v[140:141], v[140:141], 4, v[136:137]\n"
        "v_lshl_add_u64 v[142:143], v[142:143], 4, v[138:139]\n"
        "v_lshl_add_u64 v[140:141], v[140:141], 4, v[132:133]\n"
        "v_lshl_add_u64 v[142:143], v[142:143], 4, v[134:135]\n"
        "v_lshl_add_u64 v[140:141], v[140:141], 4, v[128:129]\n"
        "v_lshl_add_u64 v[142:143], v[142:143], 4, v[130:131]\n"
        "v_lshl_add_u64 v[140:141], v[140:141], 4, v[124:125]\n"
        "v_lshl_add_u64 v[142:143], v[142:143], 4, v[126:127]\n"
        "v_lshl_add_u64 v[140:141], v[140:141], 4, v[120:121]\n"
        "v_lshl_add_u64 v[142:143], v[142:143], 4, v[122:123]\n"
        "v_lshl_add_u64 v[140:141], v[140:141], 4, v[116:117]\n"
        "v_lshl_add_u64 v[142:143], v[142:143], 4, v[118:119]\n"
        "v_lshl_add_u64 v[176:177], v[140:141], 4, v[112:113]\n"
        "v_lshl_add_u64 v[180:181], v[142:143], 4, v[114:115]\n"
        "s_nop 0\n"
        "v_permlane32_swap_b32 v176, v177\n"
        "v_permlane32_swap_b32 v180, v181\n"
        "buffer_store_dwordx4 v[176:179], v250, s[16:19], s75 offen sc1\n"
        "buffer_store_dwordx4 v[180:183], v251, s[16:19], s75 offen offset:1024 sc1\n"
        ".Ln_none:\n"
        "s_waitcnt lgkmcnt(0)\n"
        "s_barrier\n"
        "s_sleep 12\n"
        "buffer_load_dwordx4 v[64:67], v232, s[16:19], s64 offen sc1\n"
        "buffer_load_dwordx4 v[68:71], v233, s[16:19], s64 offen sc1\n"
        "buffer_load_dwordx4 v[72:75], v234, s[16:19], s64 offen sc1\n"
        "buffer_load_dwordx4 v[76:79], v235, s[16:19], s64 offen sc1\n"
        "buffer_load_dwordx4 v[208:211], v232, s[16:19], s65 offen sc1\n"
        "buffer_load_dwordx4 v[212:215], v233, s[16:19], s65 offen sc1\n"
        "buffer_load_dwordx4 v[216:219], v234, s[16:19], s65 offen sc1\n"
        "buffer_load_dwordx4 v[220:223], v235, s[16:19], s65 offen sc1\n"
        "3:\n"
        "ds_read_b128 v[176:179], v228 offset:16384\n"
        "ds_read_b128 v[180:183], v229 offset:16384\n"
        "ds_read_b128 v[184:187], v228 offset:18432\n"
        "ds_read_b128 v[188:191], v229 offset:18432\n"
        "ds_read_b128 v[192:195], v228 offset:20480\n"
        "ds_read_b128 v[196:199], v229 offset:20480\n"
        "ds_read_b128 v[200:203], v228 offset:22528\n"
        "ds_read_b128 v[204:207], v229 offset:22528\n"
        "ds_read_b128 v[80:83], v230 offset:16384\n"
        "ds_read_b128 v[84:87], v231 offset:16384\n"
        "ds_read_b128 v[88:91], v230 offset:18432\n"
        "ds_read_b128 v[92:95], v231 offset:18432\n"
        "ds_read_b128 v[100:103], v230 offset:20480\n"
        "ds_read_b128 v[104:107], v231 offset:20480\n"
        "s_branch .Ln_go\n"
        ".Ln_helper:\n"
        "s_lshl_b32 s35, s32, 2\n"
        "s_add_u32 s35, s35, s31\n"
        "s_lshl_b32 s35, s35, 16\n"
        "v_lshrrev_b32 v80, 4, v252\n"
        "v_xor_b32 v81, v80, v2\n"
        "v_lshl_add_u32 v82, v1, 11, s35\n"
        "v_lshl_add_u32 v8, v81, 4, v82\n"
        "v_xor_b32 v9, 64, v8\n"
        "v_add_u32 v9, 0x3c00, v9\n"
        "v_add_u32 v10, 0x7800, v8\n"
        "v_add_u32 v11, 0x7800, v9\n"
        "s_lshl_b32 s58, s31, 12\n"
        "s_add_u32 s58, s58, 65536\n"
        "s_waitcnt lgkmcnt(0)\n"
        "s_add_u32 m0, s58, 16384\n"
        "s_movk_i32 s59, 0\n"
        "buffer_load_dwordx4 v8, s[12:15], s59 offen offset:0 lds\n"
        "buffer_load_dwordx4 v9, s[12:15], s59 offen offset:1024 lds\n"
        "buffer_load_dwordx4 v10, s[12:15], s59 offen offset:2048 lds\n"
        "buffer_load_dwordx4 v11, s[12:15], s59 offen offset:3072 lds\n"
        "s_mul_i32 s34, s4, 0x7f4a7c15\n"
        "s_mul_hi_u32 s39, s4, 0x7f4a7c15\n"
        "s_mul_i32 s35, s4, 0x9e3779b9\n"
        "s_mul_i32 s36, s5, 0x7f4a7c15\n"
        "s_add_u32 s39, s39, s35\n"
        "s_add_u32 s39, s39, s36\n"
        "s_xor_b32 s34, s34, s0\n"
        "s_xor_b32 s39, s39, s1\n"
        "s_xor_b32 s34, s34, 0x5bd1e995\n"
        "s_cmp_lt_u32 s31, 3\n"
        "s_cselect_b32 s35, 0, 0x7f000000\n"
        "s_lshl_b32 s64, s33, 18\n"
        "s_add_u32 s36, s58, 49152\n"
        "v_lshl_add_u32 v12, v252, 4, s36\n"
        "v_or_b32 v13, s35, v12\n"
        "v_and_b32 v83, 3, v1\n"
        "v_lshlrev_b32 v83, 8, v83\n"
        "v_lshl_add_u32 v83, v2, 4, v83\n"
        "v_lshrrev_b32 v250, 5, v252\n"
        "v_lshl_add_u32 v250, v250, 7, v83\n"
        "v_xor_b32 v251, 0xc0, v250\n"
        "v_mov_b32 v74, s34\n"
        "v_mov_b32 v75, s39\n"
        "v_mov_b32 v78, s34\n"
        "v_mov_b32 v79, s39\n"
        "v_mov_b32 v83, 0\n"
        "s_lshl_b32 s35, s31, 2\n"
        "s_add_u32 s35, s35, 128\n"
        "v_lshl_add_u32 v84, v252, 8, s35\n"
        "ds_write_b32 v84, v83\n"
        "v_and_b32 v81, 15, v252\n"
        "s_lshr_b32 s66, s31, 1\n"
        "s_and_b32 s67, s31, 1\n"
        "v_xor_b32 v82, v80, v81\n"
        "v_lshlrev_b32 v82, 4, v82\n"
        "v_lshl_add_u32 v82, v80, 8, v82\n"
        "s_lshl_b32 s37, s31, 11\n"
        "s_add_u32 s37, s37, 98304\n"
        "v_add_u32 v88, s37, v82\n"
        "v_xor_b32 v89, 64, v88\n"
        "s_lshl_b32 s35, s32, 4\n"
        "s_add_u32 s35, s35, s67\n"
        "s_mul_i32 s35, s35, 57344\n"
        "s_mul_i32 s36, s33, 448\n"
        "s_add_u32 s35, s35, s36\n"
        "s_mul_i32 s36, s66, 192\n"
        "s_add_u32 s35, s35, s36\n"
        "v_mul_u32_u24 v82, 0x1c00, v80\n"
        "v_lshl_add_u32 v82, v81, 4, v82\n"
        "v_add_u32 v90, s35, v82\n"
        "s_lshl_b32 s36, s66, 2\n"
        "v_add_u32 v100, s36, v2\n"
        "v_add_u32 v101, 8, v100\n"
        "v_and_b32 v101, 15, v101\n"
        "v_mul_u32_u24 v82, 0x1c00, v1\n"
        "v_add_u32 v82, s35, v82\n"
        "v_lshl_add_u32 v102, v100, 4, v82\n"
        "v_lshl_add_u32 v103, v101, 4, v82\n"
        "v_lshl_add_u32 v82, v1, 8, s37\n"
        "v_xor_b32 v100, v1, v100\n"
        "v_xor_b32 v101, v1, v101\n"
        "v_lshl_add_u32 v100, v100, 4, v82\n"
        "v_lshl_add_u32 v101, v101, 4, v82\n"
        "v_subrev_u32 v81, s36, v81\n"
        "v_cmp_gt_u32 vcc, 12, v81\n"
        "v_mov_b32 v82, 0x7f000000\n"
        "s_lshl_b32 s35, s66, 3\n"
        "v_cndmask_b32 v91, v82, v90, vcc\n"
        "s_add_u32 s88, s35, 128\n"
        "s_add_u32 s89, s88, 4\n"
        "v_mov_b32 v92, s88\n"
        "s_mul_i32 s84, s66, 458752\n"
        "s_sub_u32 s85, 458752, s84\n"
        "s_cmp_eq_u32 s31, 1\n"
        "s_cbranch_scc1 .Ln_h1\n"
        "s_cmp_eq_u32 s31, 2\n"
        "s_cbranch_scc1 .Ln_h2\n"
        "s_cmp_eq_u32 s31, 3\n"
        "s_cbranch_scc1 .Ln_h3\n"
        ".Ln_h0:\n"
        "s_add_u32 s72, s69, 64\n"
        "s_mul_i32 s35, s72, 9363\n"
        "s_lshr_b32 s35, s35, 16\n"
        "s_mul_i32 s36, s35, 7\n"
        "s_sub_u32 s36, s72, s36\n"
        "s_and_b32 s79, s35, 1\n"
        "s_lshl_b32 s79, s79, 3\n"
        "s_and_b32 s37, s33, 7\n"
        "s_or_b32 s79, s79, s37\n"
        "s_lshr_b32 s35, s35, 1\n"
        "s_mul_i32 s40, s35, 917504\n"
        "s_mul_i32 s37, s79, 896\n"
        "s_lshl_b32 s38, s36, 7\n"
        "s_add_u32 s40, s40, s37\n"
        "s_add_u32 s40, s40, s38\n"
        "s_add_u32 s41, s40, 14336\n"
        "s_add_u32 s42, s41, 14336\n"
        "s_add_u32 s43, s42, 14336\n"
        "s_add_u32 s44, s43, 14336\n"
        "s_add_u32 s45, s44, 14336\n"
        "s_add_u32 s46, s45, 14336\n"
        "s_add_u32 s47, s46, 14336\n"
        "s_lshl_b32 s75, s79, 4\n"
        "s_add_u32 s75, s75, s35\n"
        "s_lshl_b32 s75, s75, 3\n"
        "s_add_u32 s75, s75, s36\n"
        "s_lshl_b32 s75, s75, 11\n"
        "s_add_u32 s75, s75, 114688\n"
        "buffer_load_dwordx4 v[40:43], v249, s[8:11], s40 offen\n"
        "buffer_load_dwordx4 v[44:47], v249, s[8:11], s41 offen\n"
        "buffer_load_dwordx4 v[48:51], v249, s[8:11], s42 offen\n"
        "buffer_load_dwordx4 v[52:55], v249, s[8:11], s43 offen\n"
        "buffer_load_dwordx4 v[56:59], v249, s[8:11], s44 offen\n"
        "buffer_load_dwordx4 v[60:63], v249, s[8:11], s45 offen\n"
        "buffer_load_dwordx4 v[64:67], v249, s[8:11], s46 offen\n"
        "buffer_load_dwordx4 v[68:71], v249, s[8:11], s47 offen\n"
        "s_waitcnt vmcnt(8)\n"
        "s_waitcnt lgkmcnt(0)\n"
        "s_barrier\n"
        "s_sleep 16\n"
        "s_add_u32 m0, s58, 32768\n"
        "s_movk_i32 s59, 128\n"
        "buffer_load_dwordx4 v8, s[12:15], s59 offen offset:0 lds\n"
        "buffer_load_dwordx4 v9, s[12:15], s59 offen offset:1024 lds\n"
        "buffer_load_dwordx4 v10, s[12:15], s59 offen offset:2048 lds\n"
        "buffer_load_dwordx4 v11, s[12:15], s59 offen offset:3072 lds\n"
        "s_add_u32 m0, s58, 65536\n"
        "s_add_u32 s76, s64, 32768\n"
        "buffer_load_dwordx4 v12, s[16:19], s76 offen offset:0 sc1 lds\n"
        "buffer_load_dwordx4 v12, s[16:19], s76 offen offset:1024 sc1 lds\n"
        "buffer_load_dwordx4 v12, s[16:19], s76 offen offset:2048 sc1 lds\n"
        "buffer_load_dwordx4 v12, s[16:19], s76 offen offset:3072 sc1 lds\n"
        "s_waitcnt vmcnt(8)\n"
        "v_lshl_add_u64 v[68:69], v[68:69], 4, v[64:65]\n"
        "v_lshl_add_u64 v[70:71], v[70:71], 4, v[66:67]\n"
        "v_lshl_add_u64 v[68:69], v[68:69], 4, v[60:61]\n"
        "v_lshl_add_u64 v[70:71], v[70:71], 4, v[62:63]\n"
        "v_lshl_add_u64 v[68:69], v[68:69], 4, v[56:57]\n"
        "v_lshl_add_u64 v[70:71], v[70:71], 4, v[58:59]\n"
        "v_lshl_add_u64 v[68:69], v[68:69], 4, v[52:53]\n"
        "v_lshl_add_u64 v[70:71], v[70:71], 4, v[54:55]\n"
        "v_lshl_add_u64 v[68:69], v[68:69], 4, v[48:49]\n"
        "v_lshl_add_u64 v[70:71], v[70:71], 4, v[50:51]\n"
        "v_lshl_add_u64 v[68:69], v[68:69], 4, v[44:45]\n"
        "v_lshl_add_u64 v[70:71], v[70:71], 4, v[46:47]\n"
        "v_lshl_add_u64 v[72:73], v[68:69], 4, v[40:41]\n"
        "v_lshl_add_u64 v[76:77], v[70:71], 4, v[42:43]\n"
        "s_nop 0\n"
        "v_permlane32_swap_b32 v72, v73\n"
        "v_permlane32_swap_b32 v76, v77\n"
        "buffer_store_dwordx4 v[72:75], v250, s[16:19], s75 offen sc1\n"
        "buffer_store_dwordx4 v[76:79], v251, s[16:19], s75 offen offset:1024 sc1\n"
        "s_add_u32 s72, s69, 192\n"
        "s_mul_i32 s35, s72, 9363\n"
        "s_lshr_b32 s35, s35, 16\n"
        "s_mul_i32 s36, s35, 7\n"
        "s_sub_u32 s36, s72, s36\n"
        "s_and_b32 s79, s35, 1\n"
        "s_lshl_b32 s79, s79, 3\n"
        "s_and_b32 s37, s33, 7\n"
        "s_or_b32 s79, s79, s37\n"
        "s_lshr_b32 s35, s35, 1\n"
        "s_mul_i32 s40, s35, 917504\n"
        "s_mul_i32 s37, s79, 896\n"
        "s_lshl_b32 s38, s36, 7\n"
        "s_add_u32 s40, s40, s37\n"
        "s_add_u32 s40, s40, s38\n"
        "s_add_u32 s41, s40, 14336\n"
        "s_add_u32 s42, s41, 14336\n"
        "s_add_u32 s43, s42, 14336\n"
        "s_add_u32 s44, s43, 14336\n"
        "s_add_u32 s45, s44, 14336\n"
        "s_add_u32 s46, s45, 14336\n"
        "s_add_u32 s47, s46, 14336\n"
        "s_lshl_b32 s75, s79, 4\n"
        "s_add_u32 s75, s75, s35\n"
        "s_lshl_b32 s75, s75, 3\n"
        "s_add_u32 s75, s75, s36\n"
        "s_lshl_b32 s75, s75, 11\n"
        "s_add_u32 s75, s75, 114688\n"
        "v_mov_b32 v84, 128\n"
        "s_movk_i32 s73, 64\n"
        "1:\n"
        "s_sub_u32 s73, s73, 1\n"
        "s_cbranch_scc1 2f\n"
        "s_sleep 1\n"
        "ds_read_b128 v[96:99], v84 offset:4096\n"
        "s_waitcnt lgkmcnt(0)\n"
        "v_min3_u32 v96, v96, v97, v98\n"
        "v_min_u32 v96, v96, v99\n"
        "v_cmp_eq_u32 vcc, 0, v96\n"
        "s_cbranch_vccnz 1b\n"
        "2:\n"
        "s_add_u32 m0, s58, 0\n"
        "s_movk_i32 s59, 256\n"
        "buffer_load_dwordx4 v8, s[12:15], s59 offen offset:0 lds\n"
        "buffer_load_dwordx4 v9, s[12:15], s59 offen offset:1024 lds\n"
        "buffer_load_dwordx4 v10, s[12:15], s59 offen offset:2048 lds\n"
        "buffer_load_dwordx4 v11, s[12:15], s59 offen offset:3072 lds\n"
        "s_add_u32 m0, s58, 81920\n"
        "s_add_u32 s76, s64, 49152\n"
        "buffer_load_dwordx4 v12, s[16:19], s76 offen offset:0 sc1 lds\n"
        "buffer_load_dwordx4 v12, s[16:19], s76 offen offset:1024 sc1 lds\n"
        "buffer_load_dwordx4 v12, s[16:19], s76 offen offset:2048 sc1 lds\n"
        "buffer_load_dwordx4 v12, s[16:19], s76 offen offset:3072 sc1 lds\n"
        "s_mov_b64 exec, 0\n"
        "s_add_u32 s94, s86, .Ln_tp0-.Ln_here\n"
        "s_addc_u32 s95, s87, 0\n"
        "s_swappc_b64 s[70:71], s[94:95]\n"
        "s_mov_b64 exec, -1\n"
        "s_waitcnt vmcnt(10)\n"
        "s_barrier\n"
        "s_add_u32 m0, s58, 16384\n"
        "s_movk_i32 s59, 384\n"
        "buffer_load_dwordx4 v8, s[12:15], s59 offen offset:0 lds\n"
        "buffer_load_dwordx4 v9, s[12:15], s59 offen offset:1024 lds\n"
        "buffer_load_dwordx4 v10, s[12:15], s59 offen offset:2048 lds\n"
        "buffer_load_dwordx4 v11, s[12:15], s59 offen offset:3072 lds\n"
        "s_add_u32 m0, s58, 49152\n"
        "s_add_u32 s76, s64, 65536\n"
        "buffer_load_dwordx4 v12, s[16:19], s76 offen offset:0 sc1 lds\n"
        "buffer_load_dwordx4 v12, s[16:19], s76 offen offset:1024 sc1 lds\n"
        "buffer_load_dwordx4 v12, s[16:19], s76 offen offset:2048 sc1 lds\n"
        "buffer_load_dwordx4 v12, s[16:19], s76 offen offset:3072 sc1 lds\n"
        "s_waitcnt vmcnt(8)\n"
        "s_barrier\n"
        "s_add_u32 m0, s58, 65536\n"
        "s_add_u32 s76, s64, 81920\n"
        "buffer_load_dwordx4 v12, s[16:19], s76 offen offset:0 sc1 lds\n"
        "buffer_load_dwordx4 v12, s[16:19], s76 offen offset:1024 sc1 lds\n"
        "buffer_load_dwordx4 v12, s[16:19], s76 offen offset:2048 sc1 lds\n"
        "buffer_load_dwordx4 v12, s[16:19], s76 offen offset:3072 sc1 lds\n"
        "s_add_u32 m0, s58, 32768\n"
        "s_movk_i32 s59, 512\n"
        "buffer_load_dwordx4 v8, s[12:15], s59 offen offset:0 lds\n"
        "buffer_load_dwordx4 v9, s[12:15], s59 offen offset:1024 lds\n"
        "buffer_load_dwordx4 v10, s[12:15], s59 offen offset:2048 lds\n"
        "buffer_load_dwordx4 v11, s[12:15], s59 offen offset:3072 lds\n"
        "s_waitcnt vmcnt(8)\n"
        "s_barrier\n"
        "s_add_u32 m0, s58, 81920\n"
        "s_add_u32 s76, s64, 98304\n"
        "buffer_load_dwordx4 v12, s[16:19], s76 offen offset:0 sc1 lds\n"
        "buffer_load_dwordx4 v12, s[16:19], s76 offen offset:1024 sc1 lds\n"
        "buffer_load_dwordx4 v12, s[16:19], s76 offen offset:2048 sc1 lds\n"
        "buffer_load_dwordx4 v12, s[16:19], s76 offen offset:3072 sc1 lds\n"
        "s_add_u32 m0, s58, 0\n"
        "s_movk_i32 s59, 640\n"
        "buffer_load_dwordx4 v8, s[12:15], s59 offen offset:0 lds\n"
        "buffer_load_dwordx4 v9, s[12:15], s59 offen offset:1024 lds\n"
        "buffer_load_dwordx4 v10, s[12:15], s59 offen offset:2048 lds\n"
        "buffer_load_dwordx4 v11, s[12:15], s59 offen offset:3072 lds\n"
        "buffer_load_dwordx4 v[40:43], v249, s[8:11], s40 offen\n"
        "buffer_load_dwordx4 v[44:47], v249, s[8:11], s41 offen\n"
        "buffer_load_dwordx4 v[48:51], v249, s[8:11], s42 offen\n"
        "buffer_load_dwordx4 v[52:55], v249, s[8:11], s43 offen\n"
        "buffer_load_dwordx4 v[56:59], v249, s[8:11], s44 offen\n"
        "buffer_load_dwordx4 v[60:63], v249, s[8:11], s45 offen\n"
        "buffer_load_dwordx4 v[64:67], v249, s[8:11], s46 offen\n"
        "buffer_load_dwordx4 v[68:71], v249, s[8:11], s47 offen\n"
        "s_waitcnt vmcnt(16)\n"
        "s_barrier\n"
        "s_add_u32 m0, s58, 49152\n"
        "s_add_u32 s76, s64, 114688\n"
        "buffer_load_dwordx4 v12, s[16:19], s76 offen offset:0 sc1 lds\n"
        "buffer_load_dwordx4 v12, s[16:19], s76 offen offset:1024 sc1 lds\n"
        "buffer_load_dwordx4 v12, s[16:19], s76 offen offset:2048 sc1 lds\n"
        "buffer_load_dwordx4 v12, s[16:19], s76 offen offset:3072 sc1 lds\n"
        "s_add_u32 m0, s58, 16384\n"
        "s_movk_i32 s59, 768\n"
        "buffer_load_dwordx4 v8, s[12:15], s59 offen offset:0 lds\n"
        "buffer_load_dwordx4 v9, s[12:15], s59 offen offset:1024 lds\n"
        "buffer_load_dwordx4 v10, s[12:15], s59 offen offset:2048 lds\n"
        "buffer_load_dwordx4 v11, s[12:15], s59 offen offset:3072 lds\n"
        "s_waitcnt vmcnt(16)\n"
        "s_barrier\n"
        "s_add_u32 m0, s58, 65536\n"
        "s_add_u32 s76, s64, 131072\n"
        "buffer_load_dwordx4 v12, s[16:19], s76 offen offset:0 sc1 lds\n"
        "buffer_load_dwordx4 v12, s[16:19], s76 offen offset:1024 sc1 lds\n"
        "buffer_load_dwordx4 v12, s[16:19], s76 offen offset:2048 sc1 lds\n"
        "buffer_load_dwordx4 v12, s[16:19], s76 offen offset:3072 sc1 lds\n"
        "s_add_u32 m0, s58, 32768\n"
        "s_movk_i32 s59, 896\n"
        "buffer_load_dwordx4 v8, s[12:15], s59 offen offset:0 lds\n"
        "buffer_load_dwordx4 v9, s[12:15], s59 offen offset:1024 lds\n"
        "buffer_load_dwordx4 v10, s[12:15], s59 offen offset:2048 lds\n"
        "buffer_load_dwordx4 v11, s[12:15], s59 offen offset:3072 lds\n"
        "s_waitcnt vmcnt(16)\n"
        "v_lshl_add_u64 v[68:69], v[68:69], 4, v[64:65]\n"
        "v_lshl_add_u64 v[70:71], v[70:71], 4, v[66:67]\n"
        "v_lshl_add_u64 v[68:69], v[68:69], 4, v[60:61]\n"
        "v_lshl_add_u64 v[70:71], v[70:71], 4, v[62:63]\n"
        "v_lshl_add_u64 v[68:69], v[68:69], 4, v[56:57]\n"
        "v_lshl_add_u64 v[70:71], v[70:71], 4, v[58:59]\n"
        "v_lshl_add_u64 v[68:69], v[68:69], 4, v[52:53]\n"
        "v_lshl_add_u64 v[70:71], v[70:71], 4, v[54:55]\n"
        "v_lshl_add_u64 v[68:69], v[68:69], 4, v[48:49]\n"
        "v_lshl_add_u64 v[70:71], v[70:71], 4, v[50:51]\n"
        "v_lshl_add_u64 v[68:69], v[68:69], 4, v[44:45]\n"
        "v_lshl_add_u64 v[70:71], v[70:71], 4, v[46:47]\n"
        "v_lshl_add_u64 v[72:73], v[68:69], 4, v[40:41]\n"
        "v_lshl_add_u64 v[76:77], v[70:71], 4, v[42:43]\n"
        "s_nop 0\n"
        "v_permlane32_swap_b32 v72, v73\n"
        "v_permlane32_swap_b32 v76, v77\n"
        "buffer_store_dwordx4 v[72:75], v250, s[16:19], s75 offen sc1\n"
        "buffer_store_dwordx4 v[76:79], v251, s[16:19], s75 offen offset:1024 sc1\n"
        "s_waitcnt vmcnt(10)\n"
        "s_barrier\n"
        "s_add_u32 m0, s58, 81920\n"
        "s_add_u32 s76, s64, 147456\n"
        "buffer_load_dwordx4 v12, s[16:19], s76 offen offset:0 sc1 lds\n"
        "buffer_load_dwordx4 v12, s[16:19], s76 offen offset:1024 sc1 lds\n"
        "buffer_load_dwordx4 v12, s[16:19], s76 offen offset:2048 sc1 lds\n"
        "buffer_load_dwordx4 v12, s[16:19], s76 offen offset:3072 sc1 lds\n"
        "s_add_u32 m0, s58, 0\n"
        "s_movk_i32 s59, 1024\n"
        "buffer_load_dwordx4 v8, s[12:15], s59 offen offset:0 lds\n"
        "buffer_load_dwordx4 v9, s[12:15], s59 offen offset:1024 lds\n"
        "buffer_load_dwordx4 v10, s[12:15], s59 offen offset:2048 lds\n"
        "buffer_load_dwordx4 v11, s[12:15], s59 offen offset:3072 lds\n"
        "s_waitcnt vmcnt(10)\n"
        "s_barrier\n"
        "s_add_u32 m0, s58, 49152\n"
        "s_add_u32 s76, s64, 163840\n"
        "buffer_load_dwordx4 v12, s[16:19], s76 offen offset:0 sc1 lds\n"
        "buffer_load_dwordx4 v12, s[16:19], s76 offen offset:1024 sc1 lds\n"
        "buffer_load_dwordx4 v12, s[16:19], s76 offen offset:2048 sc1 lds\n"
        "buffer_load_dwordx4 v12, s[16:19], s76 offen offset:3072 sc1 lds\n"
        "s_add_u32 m0, s58, 16384\n"
        "s_movk_i32 s59, 1152\n"
        "buffer_load_dwordx4 v8, s[12:15], s59 offen offset:0 lds\n"
        "buffer_load_dwordx4 v9, s[12:15], s59 offen offset:1024 lds\n"
        "buffer_load_dwordx4 v10, s[12:15], s59 offen offset:2048 lds\n"
        "buffer_load_dwordx4 v11, s[12:15], s59 offen offset:3072 lds\n"
        "s_waitcnt vmcnt(8)\n"
        "s_barrier\n"
        "s_add_u32 m0, s58, 65536\n"
        "s_add_u32 s76, s64, 180224\n"
        "buffer_load_dwordx4 v12, s[16:19], s76 offen offset:0 sc1 lds\n"
        "buffer_load_dwordx4 v12, s[16:19], s76 offen offset:1024 sc1 lds\n"
        "buffer_load_dwordx4 v12, s[16:19], s76 offen offset:2048 sc1 lds\n"
        "buffer_load_dwordx4 v12, s[16:19], s76 offen offset:3072 sc1 lds\n"
        "s_add_u32 m0, s58, 32768\n"
        "s_movk_i32 s59, 1280\n"
        "buffer_load_dwordx4 v8, s[12:15], s59 offen offset:0 lds\n"
        "buffer_load_dwordx4 v9, s[12:15], s59 offen offset:1024 lds\n"
        "buffer_load_dwordx4 v10, s[12:15], s59 offen offset:2048 lds\n"
        "buffer_load_dwordx4 v11, s[12:15], s59 offen offset:3072 lds\n"
        "s_waitcnt vmcnt(8)\n"
        "s_barrier\n"
        "s_add_u32 m0, s58, 81920\n"
        "s_add_u32 s76, s64, 196608\n"
        "buffer_load_dwordx4 v12, s[16:19], s76 offen offset:0 sc1 lds\n"
        "buffer_load_dwordx4 v12, s[16:19], s76 offen offset:1024 sc1 lds\n"
        "buffer_load_dwordx4 v12, s[16:19], s76 offen offset:2048 sc1 lds\n"
        "buffer_load_dwordx4 v12, s[16:19], s76 offen offset:3072 sc1 lds\n"
        "s_add_u32 m0, s58, 0\n"
        "s_movk_i32 s59, 1408\n"
        "buffer_load_dwordx4 v8, s[12:15], s59 offen offset:0 lds\n"
        "buffer_load_dwordx4 v9, s[12:15], s59 offen offset:1024 lds\n"
        "buffer_load_dwordx4 v10, s[12:15], s59 offen offset:2048 lds\n"
        "buffer_load_dwordx4 v11, s[12:15], s59 offen offset:3072 lds\n"
        "s_waitcnt vmcnt(8)\n"
        "s_barrier\n"
        "s_add_u32 m0, s58, 49152\n"
        "s_add_u32 s76, s64, 212992\n"
        "buffer_load_dwordx4 v12, s[16:19], s76 offen offset:0 sc1 lds\n"
        "buffer_load_dwordx4 v12, s[16:19], s76 offen offset:1024 sc1 lds\n"
        "buffer_load_dwordx4 v12, s[16:19], s76 offen offset:2048 sc1 lds\n"
        "buffer_load_dwordx4 v12, s[16:19], s76 offen offset:3072 sc1 lds\n"
        "s_add_u32 m0, s58, 16384\n"
        "s_movk_i32 s59, 1536\n"
        "buffer_load_dwordx4 v8, s[12:15], s59 offen offset:0 lds\n"
        "buffer_load_dwordx4 v9, s[12:15], s59 offen offset:1024 lds\n"
        "buffer_load_dwordx4 v10, s[12:15], s59 offen offset:2048 lds\n"
        "buffer_load_dwordx4 v11, s[12:15], s59 offen offset:3072 lds\n"
        "s_waitcnt vmcnt(8)\n"
        "s_barrier\n"
        "s_add_u32 m0, s58, 65536\n"
        "s_add_u32 s76, s64, 229376\n"
        "buffer_load_dwordx4 v12, s[16:19], s76 offen offset:0 sc1 lds\n"
        "buffer_load_dwordx4 v12, s[16:19], s76 offen offset:1024 sc1 lds\n"
        "buffer_load_dwordx4 v12, s[16:19], s76 offen offset:2048 sc1 lds\n"
        "buffer_load_dwordx4 v12, s[16:19], s76 offen offset:3072 sc1 lds\n"
        "s_add_u32 m0, s58, 32768\n"
        "s_movk_i32 s59, 1664\n"
        "buffer_load_dwordx4 v8, s[12:15], s59 offen offset:0 lds\n"
        "buffer_load_dwordx4 v9, s[12:15], s59 offen offset:1024 lds\n"
        "buffer_load_dwordx4 v10, s[12:15], s59 offen offset:2048 lds\n"
        "buffer_load_dwordx4 v11, s[12:15], s59 offen offset:3072 lds\n"
        "s_waitcnt vmcnt(8)\n"
        "s_barrier\n"
        "s_add_u32 m0, s58, 81920\n"
        "s_add_u32 s76, s64, 245760\n"
        "buffer_load_dwordx4 v12, s[16:19], s76 offen offset:0 sc1 lds\n"
        "buffer_load_dwordx4 v12, s[16:19], s76 offen offset:1024 sc1 lds\n"
        "buffer_load_dwordx4 v12, s[16:19], s76 offen offset:2048 sc1 lds\n"
        "buffer_load_dwordx4 v12, s[16:19], s76 offen offset:3072 sc1 lds\n"
        "s_add_u32 m0, s58, 0\n"
        "s_movk_i32 s59, 1792\n"
        "buffer_load_dwordx4 v8, s[12:15], s59 offen offset:0 lds\n"
        "buffer_load_dwordx4 v9, s[12:15], s59 offen offset:1024 lds\n"
        "buffer_load_dwordx4 v10, s[12:15], s59 offen offset:2048 lds\n"
        "buffer_load_dwordx4 v11, s[12:15], s59 offen offset:3072 lds\n"
        "s_waitcnt vmcnt(8)\n"
        "s_barrier\n"
        "s_add_u32 m0, s58, 16384\n"
        "s_movk_i32 s59, 1920\n"
        "buffer_load_dwordx4 v8, s[12:15], s59 offen offset:0 lds\n"
        "buffer_load_dwordx4 v9, s[12:15], s59 offen offset:1024 lds\n"
        "buffer_load_dwordx4 v10, s[12:15], s59 offen offset:2048 lds\n"
        "buffer_load_dwordx4 v11, s[12:15], s59 offen offset:3072 lds\n"
        "s_waitcnt vmcnt(0)\n"
        "s_barrier\n"
        "s_branch .Ln_ht\n"
        ".Ln_h1:\n"
        "s_add_u32 s72, s69, 96\n"
        "s_mul_i32 s35, s72, 9363\n"
        "s_lshr_b32 s35, s35, 16\n"
        "s_mul_i32 s36, s35, 7\n"
        "s_sub_u32 s36, s72, s36\n"
        "s_and_b32 s79, s35, 1\n"
        "s_lshl_b32 s79, s79, 3\n"
        "s_and_b32 s37, s33, 7\n"
        "s_or_b32 s79, s79, s37\n"
        "s_lshr_b32 s35, s35, 1\n"
        "s_mul_i32 s40, s35, 917504\n"
        "s_mul_i32 s37, s79, 896\n"
        "s_lshl_b32 s38, s36, 7\n"
        "s_add_u32 s40, s40, s37\n"
        "s_add_u32 s40, s40, s38\n"
        "s_add_u32 s41, s40, 14336\n"
        "s_add_u32 s42, s41, 14336\n"
        "s_add_u32 s43, s42, 14336\n"
        "s_add_u32 s44, s43, 14336\n"
        "s_add_u32 s45, s44, 14336\n"
        "s_add_u32 s46, s45, 14336\n"
        "s_add_u32 s47, s46, 14336\n"
        "s_lshl_b32 s75, s79, 4\n"
        "s_add_u32 s75, s75, s35\n"
        "s_lshl_b32 s75, s75, 3\n"
        "s_add_u32 s75, s75, s36\n"
        "s_lshl_b32 s75, s75, 11\n"
        "s_add_u32 s75, s75, 114688\n"
        "s_waitcnt vmcnt(0)\n"
        "s_waitcnt lgkmcnt(0)\n"
        "s_barrier\n"
        "s_sleep 16\n"
        "s_add_u32 m0, s58, 32768\n"
        "s_movk_i32 s59, 128\n"
        "buffer_load_dwordx4 v8, s[12:15], s59 offen offset:0 lds\n"
        "buffer_load_dwordx4 v9, s[12:15], s59 offen offset:1024 lds\n"
        "buffer_load_dwordx4 v10, s[12:15], s59 offen offset:2048 lds\n"
        "buffer_load_dwordx4 v11, s[12:15], s59 offen offset:3072 lds\n"
        "s_add_u32 m0, s58, 65536\n"
        "s_add_u32 s76, s64, 32768\n"
        "buffer_load_dwordx4 v12, s[16:19], s76 offen offset:0 sc1 lds\n"
        "buffer_load_dwordx4 v12, s[16:19], s76 offen offset:1024 sc1 lds\n"
        "buffer_load_dwordx4 v12, s[16:19], s76 offen offset:2048 sc1 lds\n"
        "buffer_load_dwordx4 v12, s[16:19], s76 offen offset:3072 sc1 lds\n"
        "buffer_load_dwordx4 v[40:43], v249, s[8:11], s40 offen\n"
        "buffer_load_dwordx4 v[44:47], v249, s[8:11], s41 offen\n"
        "buffer_load_dwordx4 v[48:51], v249, s[8:11], s42 offen\n"
        "buffer_load_dwordx4 v[52:55], v249, s[8:11], s43 offen\n"
        "buffer_load_dwordx4 v[56:59], v249, s[8:11], s44 offen\n"
        "buffer_load_dwordx4 v[60:63], v249, s[8:11], s45 offen\n"
        "buffer_load_dwordx4 v[64:67], v249, s[8:11], s46 offen\n"
        "buffer_load_dwordx4 v[68:71], v249, s[8:11], s47 offen\n"
        "v_mov_b32 v84, 128\n"
        "s_movk_i32 s73, 64\n"
        "1:\n"
        "s_sub_u32 s73, s73, 1\n"
        "s_cbranch_scc1 2f\n"
        "s_sleep 1\n"
        "ds_read_b128 v[96:99], v84 offset:4096\n"
        "s_waitcnt lgkmcnt(0)\n"
        "v_min3_u32 v96, v96, v97, v98\n"
        "v_min_u32 v96, v96, v99\n"
        "v_cmp_eq_u32 vcc, 0, v96\n"
        "s_cbranch_vccnz 1b\n"
        "2:\n"
        "s_add_u32 m0, s58, 0\n"
        "s_movk_i32 s59, 256\n"
        "buffer_load_dwordx4 v8, s[12:15], s59 offen offset:0 lds\n"
        "buffer_load_dwordx4 v9, s[12:15], s59 offen offset:1024 lds\n"
        "buffer_load_dwordx4 v10, s[12:15], s59 offen offset:2048 lds\n"
        "buffer_load_dwordx4 v11, s[12:15], s59 offen offset:3072 lds\n"
        "s_add_u32 m0, s58, 81920\n"
        "s_add_u32 s76, s64, 49152\n"
        "buffer_load_dwordx4 v12, s[16:19], s76 offen offset:0 sc1 lds\n"
        "buffer_load_dwordx4 v12, s[16:19], s76 offen offset:1024 sc1 lds\n"
        "buffer_load_dwordx4 v12, s[16:19], s76 offen offset:2048 sc1 lds\n"
        "buffer_load_dwordx4 v12, s[16:19], s76 offen offset:3072 sc1 lds\n"
        "s_mov_b64 exec, 0\n"
        "s_add_u32 s94, s86, .Ln_tp1-.Ln_here\n"
        "s_addc_u32 s95, s87, 0\n"
        "s_swappc_b64 s[70:71], s[94:95]\n"
        "s_mov_b64 exec, -1\n"
        "s_waitcnt vmcnt(16)\n"
        "s_barrier\n"
        "s_add_u32 m0, s58, 16384\n"
        "s_movk_i32 s59, 384\n"
        "buffer_load_dwordx4 v8, s[12:15], s59 offen offset:0 lds\n"
        "buffer_load_dwordx4 v9, s[12:15], s59 offen offset:1024 lds\n"
        "buffer_load_dwordx4 v10, s[12:15], s59 offen offset:2048 lds\n"
        "buffer_load_dwordx4 v11, s[12:15], s59 offen offset:3072 lds\n"
        "s_add_u32 m0, s58, 49152\n"
        "s_add_u32 s76, s64, 65536\n"
        "buffer_load_dwordx4 v12, s[16:19], s76 offen offset:0 sc1 lds\n"
        "buffer_load_dwordx4 v12, s[16:19], s76 offen offset:1024 sc1 lds\n"
        "buffer_load_dwordx4 v12, s[16:19], s76 offen offset:2048 sc1 lds\n"
        "buffer_load_dwordx4 v12, s[16:19], s76 offen offset:3072 sc1 lds\n"
        "s_waitcnt vmcnt(16)\n"
        "v_lshl_add_u64 v[68:69], v[68:69], 4, v[64:65]\n"
        "v_lshl_add_u64 v[70:71], v[70:71], 4, v[66:67]\n"
        "v_lshl_add_u64 v[68:69], v[68:69], 4, v[60:61]\n"
        "v_lshl_add_u64 v[70:71], v[70:71], 4, v[62:63]\n"
        "v_lshl_add_u64 v[68:69], v[68:69], 4, v[56:57]\n"
        "v_lshl_add_u64 v[70:71], v[70:71], 4, v[58:59]\n"
        "v_lshl_add_u64 v[68:69], v[68:69], 4, v[52:53]\n"
        "v_lshl_add_u64 v[70:71], v[70:71], 4, v[54:55]\n"
        "v_lshl_add_u64 v[68:69], v[68:69], 4, v[48:49]\n"
        "v_lshl_add_u64 v[70:71], v[70:71], 4, v[50:51]\n"
        "v_lshl_add_u64 v[68:69], v[68:69], 4, v[44:45]\n"
        "v_lshl_add_u64 v[70:71], v[70:71], 4, v[46:47]\n"
        "v_lshl_add_u64 v[72:73], v[68:69], 4, v[40:41]\n"
        "v_lshl_add_u64 v[76:77], v[70:71], 4, v[42:43]\n"
        "s_nop 0\n"
        "v_permlane32_swap_b32 v72, v73\n"
        "v_permlane32_swap_b32 v76, v77\n"
        "buffer_store_dwordx4 v[72:75], v250, s[16:19], s75 offen sc1\n"
        "buffer_store_dwordx4 v[76:79], v251, s[16:19], s75 offen offset:1024 sc1\n"
        "s_waitcnt vmcnt(10)\n"
        "s_barrier\n"
        "s_add_u32 m0, s58, 65536\n"
        "s_add_u32 s76, s64, 81920\n"
        "buffer_load_dwordx4 v12, s[16:19], s76 offen offset:0 sc1 lds\n"
        "buffer_load_dwordx4 v12, s[16:19], s76 offen offset:1024 sc1 lds\n"
        "buffer_load_dwordx4 v12, s[16:19], s76 offen offset:2048 sc1 lds\n"
        "buffer_load_dwordx4 v12, s[16:19], s76 offen offset:3072 sc1 lds\n"
        "s_add_u32 m0, s58, 32768\n"
        "s_movk_i32 s59, 512\n"
        "buffer_load_dwordx4 v8, s[12:15], s59 offen offset:0 lds\n"
        "buffer_load_dwordx4 v9, s[12:15], s59 offen offset:1024 lds\n"
        "buffer_load_dwordx4 v10, s[12:15], s59 offen offset:2048 lds\n"
        "buffer_load_dwordx4 v11, s[12:15], s59 offen offset:3072 lds\n"
        "s_waitcnt vmcnt(10)\n"
        "s_barrier\n"
        "s_add_u32 m0, s58, 81920\n"
        "s_add_u32 s76, s64, 98304\n"
        "buffer_load_dwordx4 v12, s[16:19], s76 offen offset:0 sc1 lds\n"
        "buffer_load_dwordx4 v12, s[16:19], s76 offen offset:1024 sc1 lds\n"
        "buffer_load_dwordx4 v12, s[16:19], s76 offen offset:2048 sc1 lds\n"
        "buffer_load_dwordx4 v12, s[16:19], s76 offen offset:3072 sc1 lds\n"
        "s_add_u32 m0, s58, 0\n"
        "s_movk_i32 s59, 640\n"
        "buffer_load_dwordx4 v8, s[12:15], s59 offen offset:0 lds\n"
        "buffer_load_dwordx4 v9, s[12:15], s59 offen offset:1024 lds\n"
        "buffer_load_dwordx4 v10, s[12:15], s59 offen offset:2048 lds\n"
        "buffer_load_dwordx4 v11, s[12:15], s59 offen offset:3072 lds\n"
        "s_waitcnt vmcnt(8)\n"
        "s_barrier\n"
        "s_add_u32 m0, s58, 49152\n"
        "s_add_u32 s76, s64, 114688\n"
        "buffer_load_dwordx4 v12, s[16:19], s76 offen offset:0 sc1 lds\n"
        "buffer_load_dwordx4 v12, s[16:19], s76 offen offset:1024 sc1 lds\n"
        "buffer_load_dwordx4 v12, s[16:19], s76 offen offset:2048 sc1 lds\n"
        "buffer_load_dwordx4 v12, s[16:19], s76 offen offset:3072 sc1 lds\n"
        "s_add_u32 m0, s58, 16384\n"
        "s_movk_i32 s59, 768\n"
        "buffer_load_dwordx4 v8, s[12:15], s59 offen offset:0 lds\n"
        "buffer_load_dwordx4 v9, s[12:15], s59 offen offset:1024 lds\n"
        "buffer_load_dwordx4 v10, s[12:15], s59 offen offset:2048 lds\n"
        "buffer_load_dwordx4 v11, s[12:15], s59 offen offset:3072 lds\n"
        "s_waitcnt vmcnt(8)\n"
        "s_barrier\n"
        "s_add_u32 m0, s58, 65536\n"
        "s_add_u32 s76, s64, 131072\n"
        "buffer_load_dwordx4 v12, s[16:19], s76 offen offset:0 sc1 lds\n"
        "buffer_load_dwordx4 v12, s[16:19], s76 offen offset:1024 sc1 lds\n"
        "buffer_load_dwordx4 v12, s[16:19], s76 offen offset:2048 sc1 lds\n"
        "buffer_load_dwordx4 v12, s[16:19], s76 offen offset:3072 sc1 lds\n"
        "s_add_u32 m0, s58, 32768\n"
        "s_movk_i32 s59, 896\n"
        "buffer_load_dwordx4 v8, s[12:15], s59 offen offset:0 lds\n"
        "buffer_load_dwordx4 v9, s[12:15], s59 offen offset:1024 lds\n"
        "buffer_load_dwordx4 v10, s[12:15], s59 offen offset:2048 lds\n"
        "buffer_load_dwordx4 v11, s[12:15], s59 offen offset:3072 lds\n"
        "s_waitcnt vmcnt(8)\n"
        "s_barrier\n"
        "s_add_u32 m0, s58, 81920\n"
        "s_add_u32 s76, s64, 147456\n"
        "buffer_load_dwordx4 v12, s[16:19], s76 offen offset:0 sc1 lds\n"
        "buffer_load_dwordx4 v12, s[16:19], s76 offen offset:1024 sc1 lds\n"
        "buffer_load_dwordx4 v12, s[16:19], s76 offen offset:2048 sc1 lds\n"
        "buffer_load_dwordx4 v12, s[16:19], s76 offen offset:3072 sc1 lds\n"
        "s_add_u32 m0, s58, 0\n"
        "s_movk_i32 s59, 1024\n"
        "buffer_load_dwordx4 v8, s[12:15], s59 offen offset:0 lds\n"
        "buffer_load_dwordx4 v9, s[12:15], s59 offen offset:1024 lds\n"
        "buffer_load_dwordx4 v10, s[12:15], s59 offen offset:2048 lds\n"
        "buffer_load_dwordx4 v11, s[12:15], s59 offen offset:3072 lds\n"
        "s_waitcnt vmcnt(8)\n"
        "s_barrier\n"
        "s_add_u32 m0, s58, 49152\n"
        "s_add_u32 s76, s64, 163840\n"
        "buffer_load_dwordx4 v12, s[16:19], s76 offen offset:0 sc1 lds\n"
        "buffer_load_dwordx4 v12, s[16:19], s76 offen offset:1024 sc1 lds\n"
        "buffer_load_dwordx4 v12, s[16:19], s76 offen offset:2048 sc1 lds\n"
        "buffer_load_dwordx4 v12, s[16:19], s76 offen offset:3072 sc1 lds\n"
        "s_add_u32 m0, s58, 16384\n"
        "s_movk_i32 s59, 1152\n"
        "buffer_load_dwordx4 v8, s[12:15], s59 offen offset:0 lds\n"
        "buffer_load_dwordx4 v9, s[12:15], s59 offen offset:1024 lds\n"
        "buffer_load_dwordx4 v10, s[12:15], s59 offen offset:2048 lds\n"
        "buffer_load_dwordx4 v11, s[12:15], s59 offen offset:3072 lds\n"
        "s_waitcnt vmcnt(8)\n"
        "s_barrier\n"
        "s_add_u32 m0, s58, 65536\n"
        "s_add_u32 s76, s64, 180224\n"
        "buffer_load_dwordx4 v12, s[16:19], s76 offen offset:0 sc1 lds\n"
        "buffer_load_dwordx4 v12, s[16:19], s76 offen offset:1024 sc1 lds\n"
        "buffer_load_dwordx4 v12, s[16:19], s76 offen offset:2048 sc1 lds\n"
        "buffer_load_dwordx4 v12, s[16:19], s76 offen offset:3072 sc1 lds\n"
        "s_add_u32 m0, s58, 32768\n"
        "s_movk_i32 s59, 1280\n"
        "buffer_load_dwordx4 v8, s[12:15], s59 offen offset:0 lds\n"
        "buffer_load_dwordx4 v9, s[12:15], s59 offen offset:1024 lds\n"
        "buffer_load_dwordx4 v10, s[12:15], s59 offen offset:2048 lds\n"
        "buffer_load_dwordx4 v11, s[12:15], s59 offen offset:3072 lds\n"
        "s_waitcnt vmcnt(8)\n"
        "s_barrier\n"
        "s_add_u32 m0, s58, 81920\n"
        "s_add_u32 s76, s64, 196608\n"
        "buffer_load_dwordx4 v12, s[16:19], s76 offen offset:0 sc1 lds\n"
        "buffer_load_dwordx4 v12, s[16:19], s76 offen offset:1024 sc1 lds\n"
        "buffer_load_dwordx4 v12, s[16:19], s76 offen offset:2048 sc1 lds\n"
        "buffer_load_dwordx4 v12, s[16:19], s76 offen offset:3072 sc1 lds\n"
        "s_add_u32 m0, s58, 0\n"
        "s_movk_i32 s59, 1408\n"
        "buffer_load_dwordx4 v8, s[12:15], s59 offen offset:0 lds\n"
        "buffer_load_dwordx4 v9, s[12:15], s59 offen offset:1024 lds\n"
        "buffer_load_dwordx4 v10, s[12:15], s59 offen offset:2048 lds\n"
        "buffer_load_dwordx4 v11, s[12:15], s59 offen offset:3072 lds\n"
        "s_waitcnt vmcnt(8)\n"
        "s_barrier\n"
        "s_add_u32 m0, s58, 49152\n"
        "s_add_u32 s76, s64, 212992\n"
        "buffer_load_dwordx4 v12, s[16:19], s76 offen offset:0 sc1 lds\n"
        "buffer_load_dwordx4 v12, s[16:19], s76 offen offset:1024 sc1 lds\n"
        "buffer_load_dwordx4 v12, s[16:19], s76 offen offset:2048 sc1 lds\n"
        "buffer_load_dwordx4 v12, s[16:19], s76 offen offset:3072 sc1 lds\n"
        "s_add_u32 m0, s58, 16384\n"
        "s_movk_i32 s59, 1536\n"
        "buffer_load_dwordx4 v8, s[12:15], s59 offen offset:0 lds\n"
        "buffer_load_dwordx4 v9, s[12:15], s59 offen offset:1024 lds\n"
        "buffer_load_dwordx4 v10, s[12:15], s59 offen offset:2048 lds\n"
        "buffer_load_dwordx4 v11, s[12:15], s59 offen offset:3072 lds\n"
        "s_waitcnt vmcnt(8)\n"
        "s_barrier\n"
        "s_add_u32 m0, s58, 65536\n"
        "s_add_u32 s76, s64, 229376\n"
        "buffer_load_dwordx4 v12, s[16:19], s76 offen offset:0 sc1 lds\n"
        "buffer_load_dwordx4 v12, s[16:19], s76 offen offset:1024 sc1 lds\n"
        "buffer_load_dwordx4 v12, s[16:19], s76 offen offset:2048 sc1 lds\n"
        "buffer_load_dwordx4 v12, s[16:19], s76 offen offset:3072 sc1 lds\n"
        "s_add_u32 m0, s58, 32768\n"
        "s_movk_i32 s59, 1664\n"
        "buffer_load_dwordx4 v8, s[12:15], s59 offen offset:0 lds\n"
        "buffer_load_dwordx4 v9, s[12:15], s59 offen offset:1024 lds\n"
        "buffer_load_dwordx4 v10, s[12:15], s59 offen offset:2048 lds\n"
        "buffer_load_dwordx4 v11, s[12:15], s59 offen offset:3072 lds\n"
        "s_waitcnt vmcnt(8)\n"
        "s_barrier\n"
        "s_add_u32 m0, s58, 81920\n"
        "s_add_u32 s76, s64, 245760\n"
        "buffer_load_dwordx4 v12, s[16:19], s76 offen offset:0 sc1 lds\n"
        "buffer_load_dwordx4 v12, s[16:19], s76 offen offset:1024 sc1 lds\n"
        "buffer_load_dwordx4 v12, s[16:19], s76 offen offset:2048 sc1 lds\n"
        "buffer_load_dwordx4 v12, s[16:19], s76 offen offset:3072 sc1 lds\n"
        "s_add_u32 m0, s58, 0\n"
        "s_movk_i32 s59, 1792\n"
        "buffer_load_dwordx4 v8, s[12:15], s59 offen offset:0 lds\n"
        "buffer_load_dwordx4 v9, s[12:15], s59 offen offset:1024 lds\n"
        "buffer_load_dwordx4 v10, s[12:15], s59 offen offset:2048 lds\n"
        "buffer_load_dwordx4 v11, s[12:15], s59 offen offset:3072 lds\n"
        "s_waitcnt vmcnt(8)\n"
        "s_barrier\n"
        "s_add_u32 m0, s58, 16384\n"
        "s_movk_i32 s59, 1920\n"
        "buffer_load_dwordx4 v8, s[12:15], s59 offen offset:0 lds\n"
        "buffer_load_dwordx4 v9, s[12:15], s59 offen offset:1024 lds\n"
        "buffer_load_dwordx4 v10, s[12:15], s59 offen offset:2048 lds\n"
        "buffer_load_dwordx4 v11, s[12:15], s59 offen offset:3072 lds\n"
        "s_waitcnt vmcnt(0)\n"
        "s_barrier\n"
        "s_branch .Ln_ht\n"
        ".Ln_h2:\n"
        "s_add_u32 s72, s69, 128\n"
        "s_mul_i32 s35, s72, 9363\n"
        "s_lshr_b32 s35, s35, 16\n"
        "s_mul_i32 s36, s35, 7\n"
        "s_sub_u32 s36, s72, s36\n"
        "s_and_b32 s79, s35, 1\n"
        "s_lshl_b32 s79, s79, 3\n"
        "s_and_b32 s37, s33, 7\n"
        "s_or_b32 s79, s79, s37\n"
        "s_lshr_b32 s35, s35, 1\n"
        "s_mul_i32 s40, s35, 917504\n"
        "s_mul_i32 s37, s79, 896\n"
        "s_lshl_b32 s38, s36, 7\n"
        "s_add_u32 s40, s40, s37\n"
        "s_add_u32 s40, s40, s38\n"
        "s_add_u32 s41, s40, 14336\n"
        "s_add_u32 s42, s41, 14336\n"
        "s_add_u32 s43, s42, 14336\n"
        "s_add_u32 s44, s43, 14336\n"
        "s_add_u32 s45, s44, 14336\n"
        "s_add_u32 s46, s45, 14336\n"
        "s_add_u32 s47, s46, 14336\n"
        "s_lshl_b32 s75, s79, 4\n"
        "s_add_u32 s75, s75, s35\n"
        "s_lshl_b32 s75, s75, 3\n"
        "s_add_u32 s75, s75, s36\n"
        "s_lshl_b32 s75, s75, 11\n"
        "s_add_u32 s75, s75, 114688\n"
        "s_waitcnt vmcnt(0)\n"
        "s_waitcnt lgkmcnt(0)\n"
        "s_barrier\n"
        "s_sleep 16\n"
        "s_add_u32 m0, s58, 32768\n"
        "s_movk_i32 s59, 128\n"
        "buffer_load_dwordx4 v8, s[12:15], s59 offen offset:0 lds\n"
        "buffer_load_dwordx4 v9, s[12:15], s59 offen offset:1024 lds\n"
        "buffer_load_dwordx4 v10, s[12:15], s59 offen offset:2048 lds\n"
        "buffer_load_dwordx4 v11, s[12:15], s59 offen offset:3072 lds\n"
        "s_add_u32 m0, s58, 65536\n"
        "s_add_u32 s76, s64, 32768\n"
        "buffer_load_dwordx4 v12, s[16:19], s76 offen offset:0 sc1 lds\n"
        "buffer_load_dwordx4 v12, s[16:19], s76 offen offset:1024 sc1 lds\n"
        "buffer_load_dwordx4 v12, s[16:19], s76 offen offset:2048 sc1 lds\n"
        "buffer_load_dwordx4 v12, s[16:19], s76 offen offset:3072 sc1 lds\n"
        "v_mov_b32 v84, 128\n"
        "s_movk_i32 s73, 64\n"
        "1:\n"
        "s_sub_u32 s73, s73, 1\n"
        "s_cbranch_scc1 2f\n"
        "s_sleep 1\n"
        "ds_read_b128 v[96:99], v84 offset:4096\n"
        "s_waitcnt lgkmcnt(0)\n"
        "v_min3_u32 v96, v96, v97, v98\n"
        "v_min_u32 v96, v96, v99\n"
        "v_cmp_eq_u32 vcc, 0, v96\n"
        "s_cbranch_vccnz 1b\n"
        "2:\n"
        "s_add_u32 m0, s58, 0\n"
        "s_movk_i32 s59, 256\n"
        "buffer_load_dwordx4 v8, s[12:15], s59 offen offset:0 lds\n"
        "buffer_load_dwordx4 v9, s[12:15], s59 offen offset:1024 lds\n"
        "buffer_load_dwordx4 v10, s[12:15], s59 offen offset:2048 lds\n"
        "buffer_load_dwordx4 v11, s[12:15], s59 offen offset:3072 lds\n"
        "s_add_u32 m0, s58, 81920\n"
        "s_add_u32 s76, s64, 49152\n"
        "buffer_load_dwordx4 v12, s[16:19], s76 offen offset:0 sc1 lds\n"
        "buffer_load_dwordx4 v12, s[16:19], s76 offen offset:1024 sc1 lds\n"
        "buffer_load_dwordx4 v12, s[16:19], s76 offen offset:2048 sc1 lds\n"
        "buffer_load_dwordx4 v12, s[16:19], s76 offen offset:3072 sc1 lds\n"
        "s_mov_b64 exec, 0\n"
        "s_add_u32 s94, s86, .Ln_tp2-.Ln_here\n"
        "s_addc_u32 s95, s87, 0\n"
        "s_swappc_b64 s[70:71], s[94:95]\n"
        "s_mov_b64 exec, -1\n"
        "s_waitcnt vmcnt(8)\n"
        "s_barrier\n"
        "s_add_u32 m0, s58, 16384\n"
        "s_movk_i32 s59, 384\n"
        "buffer_load_dwordx4 v8, s[12:15], s59 offen offset:0 lds\n"
        "buffer_load_dwordx4 v9, s[12:15], s59 offen offset:1024 lds\n"
        "buffer_load_dwordx4 v10, s[12:15], s59 offen offset:2048 lds\n"
        "buffer_load_dwordx4 v11, s[12:15], s59 offen offset:3072 lds\n"
        "s_add_u32 m0, s58, 49152\n"
        "s_add_u32 s76, s64, 65536\n"
        "buffer_load_dwordx4 v12, s[16:19], s76 offen offset:0 sc1 lds\n"
        "buffer_load_dwordx4 v12, s[16:19], s76 offen offset:1024 sc1 lds\n"
        "buffer_load_dwordx4 v12, s[16:19], s76 offen offset:2048 sc1 lds\n"
        "buffer_load_dwordx4 v12, s[16:19], s76 offen offset:3072 sc1 lds\n"
        "buffer_load_dwordx4 v[40:43], v249, s[8:11], s40 offen\n"
        "buffer_load_dwordx4 v[44:47], v249, s[8:11], s41 offen\n"
        "buffer_load_dwordx4 v[48:51], v249, s[8:11], s42 offen\n"
        "buffer_load_dwordx4 v[52:55], v249, s[8:11], s43 offen\n"
        "buffer_load_dwordx4 v[56:59], v249, s[8:11], s44 offen\n"
        "buffer_load_dwordx4 v[60:63], v249, s[8:11], s45 offen\n"
        "buffer_load_dwordx4 v[64:67], v249, s[8:11], s46 offen\n"
        "buffer_load_dwordx4 v[68:71], v249, s[8:11], s47 offen\n"
        "s_waitcnt vmcnt(16)\n"
        "s_barrier\n"
        "s_add_u32 m0, s58, 65536\n"
        "s_add_u32 s76, s64, 81920\n"
        "buffer_load_dwordx4 v12, s[16:19], s76 offen offset:0 sc1 lds\n"
        "buffer_load_dwordx4 v12, s[16:19], s76 offen offset:1024 sc1 lds\n"
        "buffer_load_dwordx4 v12, s[16:19], s76 offen offset:2048 sc1 lds\n"
        "buffer_load_dwordx4 v12, s[16:19], s76 offen offset:3072 sc1 lds\n"
        "s_add_u32 m0, s58, 32768\n"
        "s_movk_i32 s59, 512\n"
        "buffer_load_dwordx4 v8, s[12:15], s59 offen offset:0 lds\n"
        "buffer_load_dwordx4 v9, s[12:15], s59 offen offset:1024 lds\n"
        "buffer_load_dwordx4 v10, s[12:15], s59 offen offset:2048 lds\n"
        "buffer_load_dwordx4 v11, s[12:15], s59 offen offset:3072 lds\n"
        "s_waitcnt vmcnt(16)\n"
        "s_barrier\n"
        "s_add_u32 m0, s58, 81920\n"
        "s_add_u32 s76, s64, 98304\n"
        "buffer_load_dwordx4 v12, s[16:19], s76 offen offset:0 sc1 lds\n"
        "buffer_load_dwordx4 v12, s[16:19], s76 offen offset:1024 sc1 lds\n"
        "buffer_load_dwordx4 v12, s[16:19], s76 offen offset:2048 sc1 lds\n"
        "buffer_load_dwordx4 v12, s[16:19], s76 offen offset:3072 sc1 lds\n"
        "s_add_u32 m0, s58, 0\n"
        "s_movk_i32 s59, 640\n"
        "buffer_load_dwordx4 v8, s[12:15], s59 offen offset:0 lds\n"
        "buffer_load_dwordx4 v9, s[12:15], s59 offen offset:1024 lds\n"
        "buffer_load_dwordx4 v10, s[12:15], s59 offen offset:2048 lds\n"
        "buffer_load_dwordx4 v11, s[12:15], s59 offen offset:3072 lds\n"
        "s_waitcnt vmcnt(16)\n"
        "v_lshl_add_u64 v[68:69], v[68:69], 4, v[64:65]\n"
        "v_lshl_add_u64 v[70:71], v[70:71], 4, v[66:67]\n"
        "v_lshl_add_u64 v[68:69], v[68:69], 4, v[60:61]\n"
        "v_lshl_add_u64 v[70:71], v[70:71], 4, v[62:63]\n"
        "v_lshl_add_u64 v[68:69], v[68:69], 4, v[56:57]\n"
        "v_lshl_add_u64 v[70:71], v[70:71], 4, v[58:59]\n"
        "v_lshl_add_u64 v[68:69], v[68:69], 4, v[52:53]\n"
        "v_lshl_add_u64 v[70:71], v[70:71], 4, v[54:55]\n"
        "v_lshl_add_u64 v[68:69], v[68:69], 4, v[48:49]\n"
        "v_lshl_add_u64 v[70:71], v[70:71], 4, v[50:51]\n"
        "v_lshl_add_u64 v[68:69], v[68:69], 4, v[44:45]\n"
        "v_lshl_add_u64 v[70:71], v[70:71], 4, v[46:47]\n"
        "v_lshl_add_u64 v[72:73], v[68:69], 4, v[40:41]\n"
        "v_lshl_add_u64 v[76:77], v[70:71], 4, v[42:43]\n"
        "s_nop 0\n"
        "v_permlane32_swap_b32 v72, v73\n"
        "v_permlane32_swap_b32 v76, v77\n"
        "buffer_store_dwordx4 v[72:75], v250, s[16:19], s75 offen sc1\n"
        "buffer_store_dwordx4 v[76:79], v251, s[16:19], s75 offen offset:1024 sc1\n"
        "s_waitcnt vmcnt(10)\n"
        "s_barrier\n"
        "s_add_u32 m0, s58, 49152\n"
        "s_add_u32 s76, s64, 114688\n"
        "buffer_load_dwordx4 v12, s[16:19], s76 offen offset:0 sc1 lds\n"
        "buffer_load_dwordx4 v12, s[16:19], s76 offen offset:1024 sc1 lds\n"
        "buffer_load_dwordx4 v12, s[16:19], s76 offen offset:2048 sc1 lds\n"
        "buffer_load_dwordx4 v12, s[16:19], s76 offen offset:3072 sc1 lds\n"
        "s_add_u32 m0, s58, 16384\n"
        "s_movk_i32 s59, 768\n"
        "buffer_load_dwordx4 v8, s[12:15], s59 offen offset:0 lds\n"
        "buffer_load_dwordx4 v9, s[12:15], s59 offen offset:1024 lds\n"
        "buffer_load_dwordx4 v10, s[12:15], s59 offen offset:2048 lds\n"
        "buffer_load_dwordx4 v11, s[12:15], s59 offen offset:3072 lds\n"
        "s_waitcnt vmcnt(10)\n"
        "s_barrier\n"
        "s_add_u32 m0, s58, 65536\n"
        "s_add_u32 s76, s64, 131072\n"
        "buffer_load_dwordx4 v12, s[16:19], s76 offen offset:0 sc1 lds\n"
        "buffer_load_dwordx4 v12, s[16:19], s76 offen offset:1024 sc1 lds\n"
        "buffer_load_dwordx4 v12, s[16:19], s76 offen offset:2048 sc1 lds\n"
        "buffer_load_dwordx4 v12, s[16:19], s76 offen offset:3072 sc1 lds\n"
        "s_add_u32 m0, s58, 32768\n"
        "s_movk_i32 s59, 896\n"
        "buffer_load_dwordx4 v8, s[12:15], s59 offen offset:0 lds\n"
        "buffer_load_dwordx4 v9, s[12:15], s59 offen offset:1024 lds\n"
        "buffer_load_dwordx4 v10, s[12:15], s59 offen offset:2048 lds\n"
        "buffer_load_dwordx4 v11, s[12:15], s59 offen offset:3072 lds\n"
        "s_waitcnt vmcnt(8)\n"
        "s_barrier\n"
        "s_add_u32 m0, s58, 81920\n"
        "s_add_u32 s76, s64, 147456\n"
        "buffer_load_dwordx4 v12, s[16:19], s76 offen offset:0 sc1 lds\n"
        "buffer_load_dwordx4 v12, s[16:19], s76 offen offset:1024 sc1 lds\n"
        "buffer_load_dwordx4 v12, s[16:19], s76 offen offset:2048 sc1 lds\n"
        "buffer_load_dwordx4 v12, s[16:19], s76 offen offset:3072 sc1 lds\n"
        "s_add_u32 m0, s58, 0\n"
        "s_movk_i32 s59, 1024\n"
        "buffer_load_dwordx4 v8, s[12:15], s59 offen offset:0 lds\n"
        "buffer_load_dwordx4 v9, s[12:15], s59 offen offset:1024 lds\n"
        "buffer_load_dwordx4 v10, s[12:15], s59 offen offset:2048 lds\n"
        "buffer_load_dwordx4 v11, s[12:15], s59 offen offset:3072 lds\n"
        "s_waitcnt vmcnt(8)\n"
        "s_barrier\n"
        "s_add_u32 m0, s58, 49152\n"
        "s_add_u32 s76, s64, 163840\n"
        "buffer_load_dwordx4 v12, s[16:19], s76 offen offset:0 sc1 lds\n"
        "buffer_load_dwordx4 v12, s[16:19], s76 offen offset:1024 sc1 lds\n"
        "buffer_load_dwordx4 v12, s[16:19], s76 offen offset:2048 sc1 lds\n"
        "buffer_load_dwordx4 v12, s[16:19], s76 offen offset:3072 sc1 lds\n"
        "s_add_u32 m0, s58, 16384\n"
        "s_movk_i32 s59, 1152\n"
        "buffer_load_dwordx4 v8, s[12:15], s59 offen offset:0 lds\n"
        "buffer_load_dwordx4 v9, s[12:15], s59 offen offset:1024 lds\n"
        "buffer_load_dwordx4 v10, s[12:15], s59 offen offset:2048 lds\n"
        "buffer_load_dwordx4 v11, s[12:15], s59 offen offset:3072 lds\n"
        "s_waitcnt vmcnt(8)\n"
        "s_barrier\n"
        "s_add_u32 m0, s58, 65536\n"
        "s_add_u32 s76, s64, 180224\n"
        "buffer_load_dwordx4 v12, s[16:19], s76 offen offset:0 sc1 lds\n"
        "buffer_load_dwordx4 v12, s[16:19], s76 offen offset:1024 sc1 lds\n"
        "buffer_load_dwordx4 v12, s[16:19], s76 offen offset:2048 sc1 lds\n"
        "buffer_load_dwordx4 v12, s[16:19], s76 offen offset:3072 sc1 lds\n"
        "s_add_u32 m0, s58, 32768\n"
        "s_movk_i32 s59, 1280\n"
        "buffer_load_dwordx4 v8, s[12:15], s59 offen offset:0 lds\n"
        "buffer_load_dwordx4 v9, s[12:15], s59 offen offset:1024 lds\n"
        "buffer_load_dwordx4 v10, s[12:15], s59 offen offset:2048 lds\n"
        "buffer_load_dwordx4 v11, s[12:15], s59 offen offset:3072 lds\n"
        "s_waitcnt vmcnt(8)\n"
        "s_barrier\n"
        "s_add_u32 m0, s58, 81920\n"
        "s_add_u32 s76, s64, 196608\n"
        "buffer_load_dwordx4 v12, s[16:19], s76 offen offset:0 sc1 lds\n"
        "buffer_load_dwordx4 v12, s[16:19], s76 offen offset:1024 sc1 lds\n"
        "buffer_load_dwordx4 v12, s[16:19], s76 offen offset:2048 sc1 lds\n"
        "buffer_load_dwordx4 v12, s[16:19], s76 offen offset:3072 sc1 lds\n"
        "s_add_u32 m0, s58, 0\n"
        "s_movk_i32 s59, 1408\n"
        "buffer_load_dwordx4 v8, s[12:15], s59 offen offset:0 lds\n"
        "buffer_load_dwordx4 v9, s[12:15], s59 offen offset:1024 lds\n"
        "buffer_load_dwordx4 v10, s[12:15], s59 offen offset:2048 lds\n"
        "buffer_load_dwordx4 v11, s[12:15], s59 offen offset:3072 lds\n"
        "s_waitcnt vmcnt(8)\n"
        "s_barrier\n"
        "s_add_u32 m0, s58, 49152\n"
        "s_add_u32 s76, s64, 212992\n"
        "buffer_load_dwordx4 v12, s[16:19], s76 offen offset:0 sc1 lds\n"
        "buffer_load_dwordx4 v12, s[16:19], s76 offen offset:1024 sc1 lds\n"
        "buffer_load_dwordx4 v12, s[16:19], s76 offen offset:2048 sc1 lds\n"
        "buffer_load_dwordx4 v12, s[16:19], s76 offen offset:3072 sc1 lds\n"
        "s_add_u32 m0, s58, 16384\n"
        "s_movk_i32 s59, 1536\n"
        "buffer_load_dwordx4 v8, s[12:15], s59 offen offset:0 lds\n"
        "buffer_load_dwordx4 v9, s[12:15], s59 offen offset:1024 lds\n"
        "buffer_load_dwordx4 v10, s[12:15], s59 offen offset:2048 lds\n"
        "buffer_load_dwordx4 v11, s[12:15], s59 offen offset:3072 lds\n"
        "s_waitcnt vmcnt(8)\n"
        "s_barrier\n"
        "s_add_u32 m0, s58, 65536\n"
        "s_add_u32 s76, s64, 229376\n"
        "buffer_load_dwordx4 v12, s[16:19], s76 offen offset:0 sc1 lds\n"
        "buffer_load_dwordx4 v12, s[16:19], s76 offen offset:1024 sc1 lds\n"
        "buffer_load_dwordx4 v12, s[16:19], s76 offen offset:2048 sc1 lds\n"
        "buffer_load_dwordx4 v12, s[16:19], s76 offen offset:3072 sc1 lds\n"
        "s_add_u32 m0, s58, 32768\n"
        "s_movk_i32 s59, 1664\n"
        "buffer_load_dwordx4 v8, s[12:15], s59 offen offset:0 lds\n"
        "buffer_load_dwordx4 v9, s[12:15], s59 offen offset:1024 lds\n"
        "buffer_load_dwordx4 v10, s[12:15], s59 offen offset:2048 lds\n"
        "buffer_load_dwordx4 v11, s[12:15], s59 offen offset:3072 lds\n"
        "s_waitcnt vmcnt(8)\n"
        "s_barrier\n"
        "s_add_u32 m0, s58, 81920\n"
        "s_add_u32 s76, s64, 245760\n"
        "buffer_load_dwordx4 v12, s[16:19], s76 offen offset:0 sc1 lds\n"
        "buffer_load_dwordx4 v12, s[16:19], s76 offen offset:1024 sc1 lds\n"
        "buffer_load_dwordx4 v12, s[16:19], s76 offen offset:2048 sc1 lds\n"
        "buffer_load_dwordx4 v12, s[16:19], s76 offen offset:3072 sc1 lds\n"
        "s_add_u32 m0, s58, 0\n"
        "s_movk_i32 s59, 1792\n"
        "buffer_load_dwordx4 v8, s[12:15], s59 offen offset:0 lds\n"
        "buffer_load_dwordx4 v9, s[12:15], s59 offen offset:1024 lds\n"
        "buffer_load_dwordx4 v10, s[12:15], s59 offen offset:2048 lds\n"
        "buffer_load_dwordx4 v11, s[12:15], s59 offen offset:3072 lds\n"
        "s_waitcnt vmcnt(8)\n"
        "s_barrier\n"
        "s_add_u32 m0, s58, 16384\n"
        "s_movk_i32 s59, 1920\n"
        "buffer_load_dwordx4 v8, s[12:15], s59 offen offset:0 lds\n"
        "buffer_load_dwordx4 v9, s[12:15], s59 offen offset:1024 lds\n"
        "buffer_load_dwordx4 v10, s[12:15], s59 offen offset:2048 lds\n"
        "buffer_load_dwordx4 v11, s[12:15], s59 offen offset:3072 lds\n"
        "s_waitcnt vmcnt(0)\n"
        "s_barrier\n"
        "s_branch .Ln_ht\n"
        ".Ln_h3:\n"
        "s_add_u32 s72, s69, 160\n"
        "s_mul_i32 s35, s72, 9363\n"
        "s_lshr_b32 s35, s35, 16\n"
        "s_mul_i32 s36, s35, 7\n"
        "s_sub_u32 s36, s72, s36\n"
        "s_and_b32 s79, s35, 1\n"
        "s_lshl_b32 s79, s79, 3\n"
        "s_and_b32 s37, s33, 7\n"
        "s_or_b32 s79, s79, s37\n"
        "s_lshr_b32 s35, s35, 1\n"
        "s_mul_i32 s40, s35, 917504\n"
        "s_mul_i32 s37, s79, 896\n"
        "s_lshl_b32 s38, s36, 7\n"
        "s_add_u32 s40, s40, s37\n"
        "s_add_u32 s40, s40, s38\n"
        "s_add_u32 s41, s40, 14336\n"
        "s_add_u32 s42, s41, 14336\n"
        "s_add_u32 s43, s42, 14336\n"
        "s_add_u32 s44, s43, 14336\n"
        "s_add_u32 s45, s44, 14336\n"
        "s_add_u32 s46, s45, 14336\n"
        "s_add_u32 s47, s46, 14336\n"
        "s_lshl_b32 s75, s79, 4\n"
        "s_add_u32 s75, s75, s35\n"
        "s_lshl_b32 s75, s75, 3\n"
        "s_add_u32 s75, s75, s36\n"
        "s_lshl_b32 s75, s75, 11\n"
        "s_add_u32 s75, s75, 114688\n"
        "s_waitcnt vmcnt(0)\n"
        "s_waitcnt lgkmcnt(0)\n"
        "s_barrier\n"
        "s_sleep 16\n"
        "s_add_u32 m0, s58, 32768\n"
        "s_movk_i32 s59, 128\n"
        "buffer_load_dwordx4 v8, s[12:15], s59 offen offset:0 lds\n"
        "buffer_load_dwordx4 v9, s[12:15], s59 offen offset:1024 lds\n"
        "buffer_load_dwordx4 v10, s[12:15], s59 offen offset:2048 lds\n"
        "buffer_load_dwordx4 v11, s[12:15], s59 offen offset:3072 lds\n"
        "s_add_u32 m0, s58, 65536\n"
        "s_add_u32 s76, s64, 32768\n"
        "buffer_load_dwordx4 v12, s[16:19], s76 offen offset:0 sc1 lds\n"
        "buffer_load_dwordx4 v12, s[16:19], s76 offen offset:1024 sc1 lds\n"
        "v_mov_b32 v84, 128\n"
        "s_movk_i32 s73, 64\n"
        "1:\n"
        "s_sub_u32 s73, s73, 1\n"
        "s_cbranch_scc1 2f\n"
        "s_sleep 1\n"
        "ds_read_b128 v[96:99], v84 offset:4096\n"
        "s_waitcnt lgkmcnt(0)\n"
        "v_min3_u32 v96, v96, v97, v98\n"
        "v_min_u32 v96, v96, v99\n"
        "v_cmp_eq_u32 vcc, 0, v96\n"
        "s_cbranch_vccnz 1b\n"
        "2:\n"
        "s_add_u32 m0, s58, 0\n"
        "s_movk_i32 s59, 256\n"
        "buffer_load_dwordx4 v8, s[12:15], s59 offen offset:0 lds\n"
        "buffer_load_dwordx4 v9, s[12:15], s59 offen offset:1024 lds\n"
        "buffer_load_dwordx4 v10, s[12:15], s59 offen offset:2048 lds\n"
        "buffer_load_dwordx4 v11, s[12:15], s59 offen offset:3072 lds\n"
        "s_add_u32 m0, s58, 81920\n"
        "s_add_u32 s76, s64, 49152\n"
        "buffer_load_dwordx4 v12, s[16:19], s76 offen offset:0 sc1 lds\n"
        "buffer_load_dwordx4 v12, s[16:19], s76 offen offset:1024 sc1 lds\n"
        "s_mov_b64 exec, 0\n"
        "s_add_u32 s94, s86, .Ln_tp3-.Ln_here\n"
        "s_addc_u32 s95, s87, 0\n"
        "s_swappc_b64 s[70:71], s[94:95]\n"
        "s_mov_b64 exec, -1\n"
        "s_waitcnt vmcnt(6)\n"
        "s_barrier\n"
        "s_add_u32 m0, s58, 16384\n"
        "s_movk_i32 s59, 384\n"
        "buffer_load_dwordx4 v8, s[12:15], s59 offen offset:0 lds\n"
        "buffer_load_dwordx4 v9, s[12:15], s59 offen offset:1024 lds\n"
        "buffer_load_dwordx4 v10, s[12:15], s59 offen offset:2048 lds\n"
        "buffer_load_dwordx4 v11, s[12:15], s59 offen offset:3072 lds\n"
        "s_add_u32 m0, s58, 49152\n"
        "s_add_u32 s76, s64, 65536\n"
        "buffer_load_dwordx4 v12, s[16:19], s76 offen offset:0 sc1 lds\n"
        "buffer_load_dwordx4 v12, s[16:19], s76 offen offset:1024 sc1 lds\n"
        "s_waitcnt vmcnt(6)\n"
        "s_barrier\n"
        "s_add_u32 m0, s58, 65536\n"
        "s_add_u32 s76, s64, 81920\n"
        "buffer_load_dwordx4 v12, s[16:19], s76 offen offset:0 sc1 lds\n"
        "buffer_load_dwordx4 v12, s[16:19], s76 offen offset:1024 sc1 lds\n"
        "s_add_u32 m0, s58, 32768\n"
        "s_movk_i32 s59, 512\n"
        "buffer_load_dwordx4 v8, s[12:15], s59 offen offset:0 lds\n"
        "buffer_load_dwordx4 v9, s[12:15], s59 offen offset:1024 lds\n"
        "buffer_load_dwordx4 v10, s[12:15], s59 offen offset:2048 lds\n"
        "buffer_load_dwordx4 v11, s[12:15], s59 offen offset:3072 lds\n"
        "buffer_load_dwordx4 v[40:43], v249, s[8:11], s40 offen\n"
        "buffer_load_dwordx4 v[44:47], v249, s[8:11], s41 offen\n"
        "buffer_load_dwordx4 v[48:51], v249, s[8:11], s42 offen\n"
        "buffer_load_dwordx4 v[52:55], v249, s[8:11], s43 offen\n"
        "buffer_load_dwordx4 v[56:59], v249, s[8:11], s44 offen\n"
        "buffer_load_dwordx4 v[60:63], v249, s[8:11], s45 offen\n"
        "buffer_load_dwordx4 v[64:67], v249, s[8:11], s46 offen\n"
        "buffer_load_dwordx4 v[68:71], v249, s[8:11], s47 offen\n"
        "s_waitcnt vmcnt(14)\n"
        "s_barrier\n"
        "s_add_u32 m0, s58, 81920\n"
        "s_add_u32 s76, s64, 98304\n"
        "buffer_load_dwordx4 v12, s[16:19], s76 offen offset:0 sc1 lds\n"
        "buffer_load_dwordx4 v12, s[16:19], s76 offen offset:1024 sc1 lds\n"
        "s_add_u32 m0, s58, 0\n"
        "s_movk_i32 s59, 640\n"
        "buffer_load_dwordx4 v8, s[12:15], s59 offen offset:0 lds\n"
        "buffer_load_dwordx4 v9, s[12:15], s59 offen offset:1024 lds\n"
        "buffer_load_dwordx4 v10, s[12:15], s59 offen offset:2048 lds\n"
        "buffer_load_dwordx4 v11, s[12:15], s59 offen offset:3072 lds\n"
        "s_waitcnt vmcnt(14)\n"
        "s_barrier\n"
        "s_add_u32 m0, s58, 49152\n"
        "s_add_u32 s76, s64, 114688\n"
        "buffer_load_dwordx4 v12, s[16:19], s76 offen offset:0 sc1 lds\n"
        "buffer_load_dwordx4 v12, s[16:19], s76 offen offset:1024 sc1 lds\n"
        "s_add_u32 m0, s58, 16384\n"
        "s_movk_i32 s59, 768\n"
        "buffer_load_dwordx4 v8, s[12:15], s59 offen offset:0 lds\n"
        "buffer_load_dwordx4 v9, s[12:15], s59 offen offset:1024 lds\n"
        "buffer_load_dwordx4 v10, s[12:15], s59 offen offset:2048 lds\n"
        "buffer_load_dwordx4 v11, s[12:15], s59 offen offset:3072 lds\n"
        "s_waitcnt vmcnt(12)\n"
        "v_lshl_add_u64 v[68:69], v[68:69], 4, v[64:65]\n"
        "v_lshl_add_u64 v[70:71], v[70:71], 4, v[66:67]\n"
        "v_lshl_add_u64 v[68:69], v[68:69], 4, v[60:61]\n"
        "v_lshl_add_u64 v[70:71], v[70:71], 4, v[62:63]\n"
        "v_lshl_add_u64 v[68:69], v[68:69], 4, v[56:57]\n"
        "v_lshl_add_u64 v[70:71], v[70:71], 4, v[58:59]\n"
        "v_lshl_add_u64 v[68:69], v[68:69], 4, v[52:53]\n"
        "v_lshl_add_u64 v[70:71], v[70:71], 4, v[54:55]\n"
        "v_lshl_add_u64 v[68:69], v[68:69], 4, v[48:49]\n"
        "v_lshl_add_u64 v[70:71], v[70:71], 4, v[50:51]\n"
        "v_lshl_add_u64 v[68:69], v[68:69], 4, v[44:45]\n"
        "v_lshl_add_u64 v[70:71], v[70:71], 4, v[46:47]\n"
        "v_lshl_add_u64 v[72:73], v[68:69], 4, v[40:41]\n"
        "v_lshl_add_u64 v[76:77], v[70:71], 4, v[42:43]\n"
        "s_nop 0\n"
        "v_permlane32_swap_b32 v72, v73\n"
        "v_permlane32_swap_b32 v76, v77\n"
        "buffer_store_dwordx4 v[72:75], v250, s[16:19], s75 offen sc1\n"
        "buffer_store_dwordx4 v[76:79], v251, s[16:19], s75 offen offset:1024 sc1\n"
        "s_waitcnt vmcnt(8)\n"
        "s_barrier\n"
        "s_add_u32 m0, s58, 65536\n"
        "s_add_u32 s76, s64, 131072\n"
        "buffer_load_dwordx4 v12, s[16:19], s76 offen offset:0 sc1 lds\n"
        "buffer_load_dwordx4 v12, s[16:19], s76 offen offset:1024 sc1 lds\n"
        "s_add_u32 m0, s58, 32768\n"
        "s_movk_i32 s59, 896\n"
        "buffer_load_dwordx4 v8, s[12:15], s59 offen offset:0 lds\n"
        "buffer_load_dwordx4 v9, s[12:15], s59 offen offset:1024 lds\n"
        "buffer_load_dwordx4 v10, s[12:15], s59 offen offset:2048 lds\n"
        "buffer_load_dwordx4 v11, s[12:15], s59 offen offset:3072 lds\n"
        "s_waitcnt vmcnt(8)\n"
        "s_barrier\n"
        "s_add_u32 m0, s58, 81920\n"
        "s_add_u32 s76, s64, 147456\n"
        "buffer_load_dwordx4 v12, s[16:19], s76 offen offset:0 sc1 lds\n"
        "buffer_load_dwordx4 v12, s[16:19], s76 offen offset:1024 sc1 lds\n"
        "s_add_u32 m0, s58, 0\n"
        "s_movk_i32 s59, 1024\n"
        "buffer_load_dwordx4 v8, s[12:15], s59 offen offset:0 lds\n"
        "buffer_load_dwordx4 v9, s[12:15], s59 offen offset:1024 lds\n"
        "buffer_load_dwordx4 v10, s[12:15], s59 offen offset:2048 lds\n"
        "buffer_load_dwordx4 v11, s[12:15], s59 offen offset:3072 lds\n"
        "s_waitcnt vmcnt(6)\n"
        "s_barrier\n"
        "s_add_u32 m0, s58, 49152\n"
        "s_add_u32 s76, s64, 163840\n"
        "buffer_load_dwordx4 v12, s[16:19], s76 offen offset:0 sc1 lds\n"
        "buffer_load_dwordx4 v12, s[16:19], s76 offen offset:1024 sc1 lds\n"
        "s_add_u32 m0, s58, 16384\n"
        "s_movk_i32 s59, 1152\n"
        "buffer_load_dwordx4 v8, s[12:15], s59 offen offset:0 lds\n"
        "buffer_load_dwordx4 v9, s[12:15], s59 offen offset:1024 lds\n"
        "buffer_load_dwordx4 v10, s[12:15], s59 offen offset:2048 lds\n"
        "buffer_load_dwordx4 v11, s[12:15], s59 offen offset:3072 lds\n"
        "s_waitcnt vmcnt(6)\n"
        "s_barrier\n"
        "s_add_u32 m0, s58, 65536\n"
        "s_add_u32 s76, s64, 180224\n"
        "buffer_load_dwordx4 v12, s[16:19], s76 offen offset:0 sc1 lds\n"
        "buffer_load_dwordx4 v12, s[16:19], s76 offen offset:1024 sc1 lds\n"
        "s_add_u32 m0, s58, 32768\n"
        "s_movk_i32 s59, 1280\n"
        "buffer_load_dwordx4 v8, s[12:15], s59 offen offset:0 lds\n"
        "buffer_load_dwordx4 v9, s[12:15], s59 offen offset:1024 lds\n"
        "buffer_load_dwordx4 v10, s[12:15], s59 offen offset:2048 lds\n"
        "buffer_load_dwordx4 v11, s[12:15], s59 offen offset:3072 lds\n"
        "s_waitcnt vmcnt(6)\n"
        "s_barrier\n"
        "s_add_u32 m0, s58, 81920\n"
        "s_add_u32 s76, s64, 196608\n"
        "buffer_load_dwordx4 v12, s[16:19], s76 offen offset:0 sc1 lds\n"
        "buffer_load_dwordx4 v12, s[16:19], s76 offen offset:1024 sc1 lds\n"
        "s_add_u32 m0, s58, 0\n"
        "s_movk_i32 s59, 1408\n"
        "buffer_load_dwordx4 v8, s[12:15], s59 offen offset:0 lds\n"
        "buffer_load_dwordx4 v9, s[12:15], s59 offen offset:1024 lds\n"
        "buffer_load_dwordx4 v10, s[12:15], s59 offen offset:2048 lds\n"
        "buffer_load_dwordx4 v11, s[12:15], s59 offen offset:3072 lds\n"
        "s_waitcnt vmcnt(6)\n"
        "s_barrier\n"
        "s_add_u32 m0, s58, 49152\n"
        "s_add_u32 s76, s64, 212992\n"
        "buffer_load_dwordx4 v12, s[16:19], s76 offen offset:0 sc1 lds\n"
        "buffer_load_dwordx4 v12, s[16:19], s76 offen offset:1024 sc1 lds\n"
        "s_add_u32 m0, s58, 16384\n"
        "s_movk_i32 s59, 1536\n"
        "buffer_load_dwordx4 v8, s[12:15], s59 offen offset:0 lds\n"
        "buffer_load_dwordx4 v9, s[12:15], s59 offen offset:1024 lds\n"
        "buffer_load_dwordx4 v10, s[12:15], s59 offen offset:2048 lds\n"
        "buffer_load_dwordx4 v11, s[12:15], s59 offen offset:3072 lds\n"
        "s_waitcnt vmcnt(6)\n"
        "s_barrier\n"
        "s_add_u32 m0, s58, 65536\n"
        "s_add_u32 s76, s64, 229376\n"
        "buffer_load_dwordx4 v12, s[16:19], s76 offen offset:0 sc1 lds\n"
        "buffer_load_dwordx4 v12, s[16:19], s76 offen offset:1024 sc1 lds\n"
        "s_add_u32 m0, s58, 32768\n"
        "s_movk_i32 s59, 1664\n"
        "buffer_load_dwordx4 v8, s[12:15], s59 offen offset:0 lds\n"
        "buffer_load_dwordx4 v9, s[12:15], s59 offen offset:1024 lds\n"
        "buffer_load_dwordx4 v10, s[12:15], s59 offen offset:2048 lds\n"
        "buffer_load_dwordx4 v11, s[12:15], s59 offen offset:3072 lds\n"
        "s_waitcnt vmcnt(6)\n"
        "s_barrier\n"
        "s_add_u32 m0, s58, 81920\n"
        "s_add_u32 s76, s64, 245760\n"
        "buffer_load_dwordx4 v12, s[16:19], s76 offen offset:0 sc1 lds\n"
        "buffer_load_dwordx4 v12, s[16:19], s76 offen offset:1024 sc1 lds\n"
        "s_add_u32 m0, s58, 0\n"
        "s_movk_i32 s59, 1792\n"
        "buffer_load_dwordx4 v8, s[12:15], s59 offen offset:0 lds\n"
        "buffer_load_dwordx4 v9, s[12:15], s59 offen offset:1024 lds\n"
        "buffer_load_dwordx4 v10, s[12:15], s59 offen offset:2048 lds\n"
        "buffer_load_dwordx4 v11, s[12:15], s59 offen offset:3072 lds\n"
        "s_waitcnt vmcnt(6)\n"
        "s_barrier\n"
        "s_add_u32 m0, s58, 16384\n"
        "s_movk_i32 s59, 1920\n"
        "buffer_load_dwordx4 v8, s[12:15], s59 offen offset:0 lds\n"
        "buffer_load_dwordx4 v9, s[12:15], s59 offen offset:1024 lds\n"
        "buffer_load_dwordx4 v10, s[12:15], s59 offen offset:2048 lds\n"
        "buffer_load_dwordx4 v11, s[12:15], s59 offen offset:3072 lds\n"
        "s_waitcnt vmcnt(0)\n"
        "s_barrier\n"
        ".Ln_ht:\n"
        "s_barrier\n"
        "1:\n"
        "s_sleep 1\n"
        "ds_read_b64 v[80:81], v92 offset:0\n"
        "s_waitcnt lgkmcnt(0)\n"
        "v_cmp_ne_u64 vcc, s[88:89], v[80:81]\n"
        "s_cbranch_vccnz 1b\n"
        "ds_read_b128 v[168:171], v100 offset:0\n"
        "s_add_u32 s35, s84, 0\n"
        "s_waitcnt lgkmcnt(0)\n"
        "buffer_store_dwordx4 v[168:171], v102, s[20:23], s35 offen nt\n"
        "1:\n"
        "s_sleep 1\n"
        "ds_read_b64 v[80:81], v92 offset:256\n"
        "s_waitcnt lgkmcnt(0)\n"
        "v_cmp_ne_u64 vcc, s[88:89], v[80:81]\n"
        "s_cbranch_vccnz 1b\n"
        "ds_read_b128 v[176:179], v100 offset:8192\n"
        "s_add_u32 s35, s84, 114688\n"
        "s_waitcnt lgkmcnt(0)\n"
        "buffer_store_dwordx4 v[176:179], v102, s[20:23], s35 offen nt\n"
        "1:\n"
        "s_sleep 1\n"
        "ds_read_b64 v[80:81], v92 offset:512\n"
        "s_waitcnt lgkmcnt(0)\n"
        "v_cmp_ne_u64 vcc, s[88:89], v[80:81]\n"
        "s_cbranch_vccnz 1b\n"
        "ds_read_b128 v[184:187], v101 offset:0\n"
        "s_add_u32 s35, s84, 0\n"
        "s_waitcnt lgkmcnt(0)\n"
        "buffer_store_dwordx4 v[184:187], v103, s[20:23], s35 offen nt\n"
        "1:\n"
        "s_sleep 1\n"
        "ds_read_b64 v[80:81], v92 offset:768\n"
        "s_waitcnt lgkmcnt(0)\n"
        "v_cmp_ne_u64 vcc, s[88:89], v[80:81]\n"
        "s_cbranch_vccnz 1b\n"
        "ds_read_b128 v[192:195], v101 offset:8192\n"
        "s_add_u32 s35, s84, 114688\n"
        "s_waitcnt lgkmcnt(0)\n"
        "buffer_store_dwordx4 v[192:195], v103, s[20:23], s35 offen nt\n"
        "1:\n"
        "s_sleep 1\n"
        "ds_read_b64 v[80:81], v92 offset:1024\n"
        "s_waitcnt lgkmcnt(0)\n"
        "v_cmp_ne_u64 vcc, s[88:89], v[80:81]\n"
        "s_cbranch_vccnz 1b\n"
        "ds_read_b128 v[200:203], v88 offset:16384\n"
        "ds_read_b128 v[204:207], v89 offset:17408\n"
        "s_add_u32 s35, s84, 229376\n"
        "s_add_u32 s36, s35, 28672\n"
        "s_waitcnt lgkmcnt(1)\n"
        "buffer_store_dwordx4 v[200:203], v90, s[20:23], s35 offen nt\n"
        "s_waitcnt lgkmcnt(0)\n"
        "buffer_store_dwordx4 v[204:207], v90, s[20:23], s36 offen nt\n"
        "1:\n"
        "s_sleep 1\n"
        "ds_read_b64 v[80:81], v92 offset:1280\n"
        "s_waitcnt lgkmcnt(0)\n"
        "v_cmp_ne_u64 vcc, s[88:89], v[80:81]\n"
        "s_cbranch_vccnz 1b\n"
        "ds_read_b128 v[208:211], v88 offset:24576\n"
        "ds_read_b128 v[212:215], v89 offset:25600\n"
        "s_add_u32 s35, s84, 344064\n"
        "s_add_u32 s36, s35, 28672\n"
        "s_waitcnt lgkmcnt(1)\n"
        "buffer_store_dwordx4 v[208:211], v90, s[20:23], s35 offen nt\n"
        "s_waitcnt lgkmcnt(0)\n"
        "buffer_store_dwordx4 v[212:215], v90, s[20:23], s36 offen nt\n"
        "1:\n"
        "s_sleep 1\n"
        "ds_read_b64 v[80:81], v92 offset:1536\n"
        "s_waitcnt lgkmcnt(0)\n"
        "v_cmp_ne_u64 vcc, s[88:89], v[80:81]\n"
        "s_cbranch_vccnz 1b\n"
        "ds_read_b128 v[216:219], v88 offset:32768\n"
        "ds_read_b128 v[220:223], v89 offset:33792\n"
        "s_add_u32 s35, s85, 0\n"
        "s_add_u32 s36, s35, 28672\n"
        "s_waitcnt lgkmcnt(1)\n"
        "buffer_store_dwordx4 v[216:219], v91, s[20:23], s35 offen nt\n"
        "s_waitcnt lgkmcnt(0)\n"
        "buffer_store_dwordx4 v[220:223], v91, s[20:23], s36 offen nt\n"
        "1:\n"
        "s_sleep 1\n"
        "ds_read_b64 v[80:81], v92 offset:1792\n"
        "s_waitcnt lgkmcnt(0)\n"
        "v_cmp_ne_u64 vcc, s[88:89], v[80:81]\n"
        "s_cbranch_vccnz 1b\n"
        "ds_read_b128 v[224:227], v88 offset:40960\n"
        "ds_read_b128 v[228:231], v89 offset:41984\n"
        "s_add_u32 s35, s85, 114688\n"
        "s_add_u32 s36, s35, 28672\n"
        "s_waitcnt lgkmcnt(1)\n"
        "buffer_store_dwordx4 v[224:227], v91, s[20:23], s35 offen nt\n"
        "s_waitcnt lgkmcnt(0)\n"
        "buffer_store_dwordx4 v[228:231], v91, s[20:23], s36 offen nt\n"
        "1:\n"
        "s_sleep 1\n"
        "ds_read_b64 v[80:81], v92 offset:2048\n"
        "s_waitcnt lgkmcnt(0)\n"
        "v_cmp_ne_u64 vcc, s[88:89], v[80:81]\n"
        "s_cbranch_vccnz 1b\n"
        "ds_read_b128 v[232:235], v88 offset:49152\n"
        "ds_read_b128 v[236:239], v89 offset:50176\n"
        "s_add_u32 s35, s85, 229376\n"
        "s_add_u32 s36, s35, 28672\n"
        "s_waitcnt lgkmcnt(1)\n"
        "buffer_store_dwordx4 v[232:235], v91, s[20:23], s35 offen nt\n"
        "s_waitcnt lgkmcnt(0)\n"
        "buffer_store_dwordx4 v[236:239], v91, s[20:23], s36 offen nt\n"
        "1:\n"
        "s_sleep 1\n"
        "ds_read_b64 v[80:81], v92 offset:2304\n"
        "s_waitcnt lgkmcnt(0)\n"
        "v_cmp_ne_u64 vcc, s[88:89], v[80:81]\n"
        "s_cbranch_vccnz 1b\n"
        "ds_read_b128 v[240:243], v88 offset:57344\n"
        "ds_read_b128 v[244:247], v89 offset:58368\n"
        "s_add_u32 s35, s85, 344064\n"
        "s_add_u32 s36, s35, 28672\n"
        "s_waitcnt lgkmcnt(1)\n"
        "buffer_store_dwordx4 v[240:243], v91, s[20:23], s35 offen nt\n"
        "s_waitcnt lgkmcnt(0)\n"
        "buffer_store_dwordx4 v[244:247], v91, s[20:23], s36 offen nt\n"
        "s_endpgm\n"
        ".p2align 12\n"
        ".Ln_r0:\n"
        "s_add_u32 s73, s73, 1\n"
        "s_cmp_lt_u32 s73, s74\n"
        "s_cbranch_scc0 .Ln_f0\n"
        "buffer_load_dwordx4 v[64:67], v232, s[16:19], s64 offen sc1\n"
        "buffer_load_dwordx4 v[68:71], v233, s[16:19], s64 offen sc1\n"
        "buffer_load_dwordx4 v[72:75], v234, s[16:19], s64 offen sc1\n"
        "buffer_load_dwordx4 v[76:79], v235, s[16:19], s64 offen sc1\n"
        "buffer_load_dwordx4 v[208:211], v232, s[16:19], s65 offen sc1\n"
        "buffer_load_dwordx4 v[212:215], v233, s[16:19], s65 offen sc1\n"
        "buffer_load_dwordx4 v[216:219], v234, s[16:19], s65 offen sc1\n"
        "buffer_load_dwordx4 v[220:223], v235, s[16:19], s65 offen sc1\n"
        "s_waitcnt vmcnt(0)\n"
        ".Ln_go:\n"
        "s_waitcnt vmcnt(4)\n"
        "v_bitop3_b32 v66, v66, v70, s34 bitop3:0x7e\n"
        "v_bitop3_b32 v67, v67, v71, s39 bitop3:0x7e\n"
        "v_bitop3_b32 v74, v74, v78, s34 bitop3:0x7e\n"
        "v_bitop3_b32 v75, v75, v79, s39 bitop3:0x7e\n"
        "v_or3_b32 v66, v66, v67, v74\n"
        "v_or_b32 v66, v66, v75\n"
        "v_cmp_ne_u32 vcc, 0, v66\n"
        "s_cbranch_vccnz .Ln_r0\n"
        ".Ln_b0:\n"
        "v_perm_b32 v224, v64, v236, s60\n"
        "v_perm_b32 v225, v64, v236, s61\n"
        "ds_read_b32 v112, v224\n"
        "v_perm_b32 v226, v64, v236, s62\n"
        "ds_read_b32 v113, v225\n"
        "v_perm_b32 v227, v64, v236, s63\n"
        "ds_read_b32 v114, v226\n"
        "v_perm_b32 v224, v65, v236, s60\n"
        "ds_read_b32 v115, v227\n"
        "v_perm_b32 v225, v65, v236, s61\n"
        "ds_read_b32 v116, v224\n"
        "v_perm_b32 v226, v65, v236, s62\n"
        "ds_read_b32 v117, v225\n"
        "v_perm_b32 v227, v65, v236, s63\n"
        "ds_read_b32 v118, v226\n"
        "v_perm_b32 v224, v68, v236, s60\n"
        "ds_read_b32 v119, v227\n"
        "v_perm_b32 v225, v68, v236, s61\n"
        "ds_read_b32 v120, v224\n"
        "ds_read_b32 v121, v225\n"
        "ds_write_b32 v237, v237 offset:4096\n"
        "v_perm_b32 v226, v68, v236, s62\n"
        "s_waitcnt lgkmcnt(7)\n"
        "v_mfma_f32_16x16x32_bf16 v[0:3], v[112:115], v[176:179], 0\n"
        "ds_read_b32 v122, v226\n"
        "v_perm_b32 v227, v68, v236, s63\n"
        "v_perm_b32 v224, v69, v236, s60\n"
        "s_waitcnt lgkmcnt(4)\n"
        "v_mfma_f32_16x16x32_bf16 v[0:3], v[116:119], v[180:183], v[0:3]\n"
        "ds_read_b32 v123, v227\n"
        "ds_read_b32 v124, v224\n"
        "v_perm_b32 v225, v69, v236, s61\n"
        "v_mfma_f32_16x16x32_bf16 v[16:19], v[112:115], v[184:187], 0\n"
        "ds_read_b32 v125, v225\n"
        "v_perm_b32 v226, v69, v236, s62\n"
        "v_perm_b32 v227, v69, v236, s63\n"
        "v_mfma_f32_16x16x32_bf16 v[16:19], v[116:119], v[188:191], v[16:19]\n"
        "ds_read_b32 v126, v226\n"
        "ds_read_b32 v127, v227\n"
        "v_perm_b32 v224, v72, v236, s60\n"
        "v_mfma_f32_16x16x32_bf16 v[32:35], v[112:115], v[192:195], 0\n"
        "ds_read_b32 v128, v224\n"
        "v_perm_b32 v225, v72, v236, s61\n"
        "v_perm_b32 v226, v72, v236, s62\n"
        "v_mfma_f32_16x16x32_bf16 v[32:35], v[116:119], v[196:199], v[32:35]\n"
        "ds_read_b32 v129, v225\n"
        "ds_read_b32 v130, v226\n"
        "v_perm_b32 v227, v72, v236, s63\n"
        "v_mfma_f32_16x16x32_bf16 v[48:51], v[112:115], v[200:203], 0\n"
        "ds_read_b32 v131, v227\n"
        "v_perm_b32 v224, v73, v236, s60\n"
        "v_perm_b32 v225, v73, v236, s61\n"
        "s_waitcnt vmcnt(0)\n"
        "v_mfma_f32_16x16x32_bf16 v[48:51], v[116:119], v[204:207], v[48:51]\n"
        "ds_read_b32 v132, v224\n"
        "ds_read_b32 v133, v225\n"
        "v_perm_b32 v226, v73, v236, s62\n"
        "v_bitop3_b32 v210, v210, v214, s34 bitop3:0x7e\n"
        "s_waitcnt lgkmcnt(10)\n"
        "v_mfma_f32_16x16x32_bf16 v[4:7], v[120:123], v[176:179], 0\n"
        "ds_read_b32 v134, v226\n"
        "v_perm_b32 v227, v73, v236, s63\n"
        "v_perm_b32 v224, v76, v236, s60\n"
        "v_bitop3_b32 v211, v211, v215, s39 bitop3:0x7e\n"
        "s_waitcnt lgkmcnt(7)\n"
        "v_mfma_f32_16x16x32_bf16 v[4:7], v[124:127], v[180:183], v[4:7]\n"
        "ds_read_b32 v135, v227\n"
        "ds_read_b32 v136, v224\n"
        "v_perm_b32 v225, v76, v236, s61\n"
        "v_bitop3_b32 v218, v218, v222, s34 bitop3:0x7e\n"
        "v_mfma_f32_16x16x32_bf16 v[20:23], v[120:123], v[184:187], 0\n"
        "ds_read_b32 v137, v225\n"
        "v_perm_b32 v226, v76, v236, s62\n"
        "v_perm_b32 v227, v76, v236, s63\n"
        "v_bitop3_b32 v219, v219, v223, s39 bitop3:0x7e\n"
        "v_mfma_f32_16x16x32_bf16 v[20:23], v[124:127], v[188:191], v[20:23]\n"
        "ds_read_b32 v138, v226\n"
        "ds_read_b32 v139, v227\n"
        "v_perm_b32 v224, v77, v236, s60\n"
        "v_or3_b32 v210, v210, v211, v218\n"
        "v_mfma_f32_16x16x32_bf16 v[36:39], v[120:123], v[192:195], 0\n"
        "ds_read_b32 v140, v224\n"
        "v_perm_b32 v225, v77, v236, s61\n"
        "v_perm_b32 v226, v77, v236, s62\n"
        "v_or_b32 v210, v210, v219\n"
        "v_mfma_f32_16x16x32_bf16 v[36:39], v[124:127], v[196:199], v[36:39]\n"
        "ds_read_b32 v141, v225\n"
        "ds_read_b32 v142, v226\n"
        "v_perm_b32 v227, v77, v236, s63\n"
        "v_cmp_ne_u32 vcc, 0, v210\n"
        "v_mfma_f32_16x16x32_bf16 v[52:55], v[120:123], v[200:203], 0\n"
        "ds_read_b32 v143, v227\n"
        "s_cbranch_vccnz .Ln_f1\n"
        ".Ln_b1:\n"
        "v_perm_b32 v224, v208, v236, s60\n"
        "v_perm_b32 v225, v208, v236, s61\n"
        "v_mfma_f32_16x16x32_bf16 v[52:55], v[124:127], v[204:207], v[52:55]\n"
        "ds_read_b32 v144, v224\n"
        "ds_read_b32 v145, v225\n"
        "v_perm_b32 v226, v208, v236, s62\n"
        "s_waitcnt lgkmcnt(14)\n"
        "v_mfma_f32_16x16x32_bf16 v[8:11], v[128:131], v[176:179], 0\n"
        "ds_read_b32 v146, v226\n"
        "v_perm_b32 v227, v208, v236, s63\n"
        "v_perm_b32 v224, v209, v236, s60\n"
        "s_waitcnt lgkmcnt(11)\n"
        "v_mfma_f32_16x16x32_bf16 v[8:11], v[132:135], v[180:183], v[8:11]\n"
        "ds_read_b32 v147, v227\n"
        "ds_read_b32 v148, v224\n"
        "v_perm_b32 v225, v209, v236, s61\n"
        "v_mfma_f32_16x16x32_bf16 v[24:27], v[128:131], v[184:187], 0\n"
        "ds_read_b32 v149, v225\n"
        "v_perm_b32 v226, v209, v236, s62\n"
        "v_perm_b32 v227, v209, v236, s63\n"
        "v_mfma_f32_16x16x32_bf16 v[24:27], v[132:135], v[188:191], v[24:27]\n"
        "ds_read_b32 v150, v226\n"
        "ds_read_b32 v151, v227\n"
        "v_perm_b32 v224, v212, v236, s60\n"
        "v_mfma_f32_16x16x32_bf16 v[40:43], v[128:131], v[192:195], 0\n"
        "ds_read_b32 v152, v224\n"
        "v_perm_b32 v225, v212, v236, s61\n"
        "v_perm_b32 v226, v212, v236, s62\n"
        "v_mfma_f32_16x16x32_bf16 v[40:43], v[132:135], v[196:199], v[40:43]\n"
        "ds_read_b32 v153, v225\n"
        "ds_read_b32 v154, v226\n"
        "v_perm_b32 v227, v212, v236, s63\n"
        "v_mfma_f32_16x16x32_bf16 v[56:59], v[128:131], v[200:203], 0\n"
        "ds_read_b32 v155, v227\n"
        "v_perm_b32 v224, v213, v236, s60\n"
        "v_perm_b32 v225, v213, v236, s61\n"
        "v_mfma_f32_16x16x32_bf16 v[56:59], v[132:135], v[204:207], v[56:59]\n"
        "ds_read_b32 v156, v224\n"
        "ds_read_b32 v157, v225\n"
        "v_perm_b32 v226, v213, v236, s62\n"
        "s_waitcnt lgkmcnt(15)\n"
        "v_mfma_f32_16x16x32_bf16 v[12:15], v[136:139], v[176:179], 0\n"
        "ds_read_b32 v158, v226\n"
        "v_perm_b32 v227, v213, v236, s63\n"
        "s_waitcnt lgkmcnt(15)\n"
        "v_mfma_f32_16x16x32_bf16 v[12:15], v[140:143], v[180:183], v[12:15]\n"
        "ds_read_b32 v159, v227\n"
        "v_perm_b32 v224, v216, v236, s60\n"
        "v_mfma_f32_16x16x32_bf16 v[28:31], v[136:139], v[184:187], 0\n"
        "ds_read_b32 v160, v224\n"
        "v_perm_b32 v225, v216, v236, s61\n"
        "v_mfma_f32_16x16x32_bf16 v[28:31], v[140:143], v[188:191], v[28:31]\n"
        "ds_read_b32 v161, v225\n"
        "v_perm_b32 v226, v216, v236, s62\n"
        "v_mfma_f32_16x16x32_bf16 v[44:47], v[136:139], v[192:195], 0\n"
        "ds_read_b32 v162, v226\n"
        "v_perm_b32 v227, v216, v236, s63\n"
        "v_mfma_f32_16x16x32_bf16 v[44:47], v[140:143], v[196:199], v[44:47]\n"
        "ds_read_b32 v163, v227\n"
        "v_perm_b32 v224, v217, v236, s60\n"
        "v_mfma_f32_16x16x32_bf16 v[60:63], v[136:139], v[200:203], 0\n"
        "ds_read_b32 v164, v224\n"
        "v_perm_b32 v225, v217, v236, s61\n"
        "v_mfma_f32_16x16x32_bf16 v[60:63], v[140:143], v[204:207], v[60:63]\n"
        "ds_read_b32 v165, v225\n"
        "v_mfma_f32_16x16x32_bf16 v[64:67], v[112:115], v[80:83], 0\n"
        "ds_read_b128 v[200:203], v230 offset:22528\n"
        "v_mfma_f32_16x16x32_bf16 v[64:67], v[116:119], v[84:87], v[64:67]\n"
        "ds_read_b128 v[204:207], v231 offset:22528\n"
        "v_perm_b32 v226, v217, v236, s62\n"
        "v_mfma_f32_16x16x32_bf16 v[68:71], v[120:123], v[80:83], 0\n"
        "ds_read_b32 v166, v226\n"
        "v_perm_b32 v227, v217, v236, s63\n"
        "v_mfma_f32_16x16x32_bf16 v[68:71], v[124:127], v[84:87], v[68:71]\n"
        "ds_read_b32 v167, v227\n"
        "v_perm_b32 v224, v220, v236, s60\n"
        "v_mfma_f32_16x16x32_bf16 v[72:75], v[128:131], v[80:83], 0\n"
        "ds_read_b32 v168, v224\n"
        "v_perm_b32 v225, v220, v236, s61\n"
        "v_mfma_f32_16x16x32_bf16 v[72:75], v[132:135], v[84:87], v[72:75]\n"
        "ds_read_b32 v169, v225\n"
        "v_perm_b32 v226, v220, v236, s62\n"
        "v_mfma_f32_16x16x32_bf16 v[76:79], v[112:115], v[88:91], 0\n"
        "ds_read_b32 v170, v226\n"
        "v_perm_b32 v227, v220, v236, s63\n"
        "v_mfma_f32_16x16x32_bf16 v[76:79], v[116:119], v[92:95], v[76:79]\n"
        "ds_read_b32 v171, v227\n"
        "v_perm_b32 v224, v221, v236, s60\n"
        "v_mfma_f32_16x16x32_bf16 v[80:83], v[120:123], v[88:91], 0\n"
        "ds_read_b32 v172, v224\n"
        "v_perm_b32 v225, v221, v236, s61\n"
        "v_mfma_f32_16x16x32_bf16 v[80:83], v[124:127], v[92:95], v[80:83]\n"
        "ds_read_b32 v173, v225\n"
        "v_perm_b32 v226, v221, v236, s62\n"
        "v_mfma_f32_16x16x32_bf16 v[84:87], v[128:131], v[88:91], 0\n"
        "ds_read_b32 v174, v226\n"
        "v_perm_b32 v227, v221, v236, s63\n"
        "v_mfma_f32_16x16x32_bf16 v[84:87], v[132:135], v[92:95], v[84:87]\n"
        "ds_read_b32 v175, v227\n"
        "s_waitcnt lgkmcnt(10)\n"
        "s_barrier\n"
        "v_mfma_f32_16x16x32_bf16 v[88:91], v[112:115], v[100:103], 0\n"
        "ds_read_b128 v[208:211], v232 offset:16384\n"
        "v_mfma_f32_16x16x32_bf16 v[88:91], v[116:119], v[104:107], v[88:91]\n"
        "ds_read_b128 v[212:215], v233 offset:16384\n"
        "v_mfma_f32_16x16x32_bf16 v[92:95], v[120:123], v[100:103], 0\n"
        "v_mfma_f32_16x16x32_bf16 v[92:95], v[124:127], v[104:107], v[92:95]\n"
        "ds_read_b128 v[216:219], v234 offset:16384\n"
        "v_mfma_f32_16x16x32_bf16 v[96:99], v[128:131], v[100:103], 0\n"
        "ds_read_b128 v[220:223], v235 offset:16384\n"
        "v_mfma_f32_16x16x32_bf16 v[96:99], v[132:135], v[104:107], v[96:99]\n"
        "v_mfma_f32_16x16x32_bf16 v[100:103], v[112:115], v[200:203], 0\n"
        "ds_read_b128 v[176:179], v228 offset:32768\n"
        "v_mfma_f32_16x16x32_bf16 v[100:103], v[116:119], v[204:207], v[100:103]\n"
        "ds_read_b128 v[180:183], v229 offset:32768\n"
        "v_mfma_f32_16x16x32_bf16 v[104:107], v[120:123], v[200:203], 0\n"
        "v_mfma_f32_16x16x32_bf16 v[104:107], v[124:127], v[204:207], v[104:107]\n"
        "ds_read_b128 v[184:187], v228 offset:34816\n"
        "v_mfma_f32_16x16x32_bf16 v[108:111], v[128:131], v[200:203], 0\n"
        "ds_read_b128 v[188:191], v229 offset:34816\n"
        "v_mfma_f32_16x16x32_bf16 v[108:111], v[132:135], v[204:207], v[108:111]\n"
        "s_waitcnt lgkmcnt(2)\n"
        "v_mfma_f32_16x16x32_bf16 v[0:3], v[144:147], v[176:179], v[0:3]\n"
        "ds_read_b128 v[192:195], v228 offset:36864\n"
        "v_bitop3_b32 v210, v210, v214, s34 bitop3:0x7e\n"
        "v_mfma_f32_16x16x32_bf16 v[0:3], v[148:151], v[180:183], v[0:3]\n"
        "ds_read_b128 v[196:199], v229 offset:36864\n"
        "v_bitop3_b32 v211, v211, v215, s39 bitop3:0x7e\n"
        "v_mfma_f32_16x16x32_bf16 v[4:7], v[156:159], v[180:183], v[4:7]\n"
        "ds_read_b128 v[200:203], v228 offset:38912\n"
        "v_bitop3_b32 v218, v218, v222, s34 bitop3:0x7e\n"
        "v_mfma_f32_16x16x32_bf16 v[4:7], v[152:155], v[176:179], v[4:7]\n"
        "ds_read_b128 v[204:207], v229 offset:38912\n"
        "v_perm_b32 v224, v208, v236, s60\n"
        "v_mfma_f32_16x16x32_bf16 v[8:11], v[160:163], v[176:179], v[8:11]\n"
        "ds_read_b32 v112, v224\n"
        "v_perm_b32 v225, v208, v236, s61\n"
        "v_mfma_f32_16x16x32_bf16 v[8:11], v[164:167], v[180:183], v[8:11]\n"
        "ds_read_b32 v113, v225\n"
        "v_perm_b32 v226, v208, v236, s62\n"
        "v_mfma_f32_16x16x32_bf16 v[12:15], v[172:175], v[180:183], v[12:15]\n"
        "ds_read_b32 v114, v226\n"
        "v_perm_b32 v227, v208, v236, s63\n"
        "v_mfma_f32_16x16x32_bf16 v[12:15], v[168:171], v[176:179], v[12:15]\n"
        "ds_read_b32 v115, v227\n"
        "v_bitop3_b32 v219, v219, v223, s39 bitop3:0x7e\n"
        "s_waitcnt lgkmcnt(8)\n"
        "v_mfma_f32_16x16x32_bf16 v[28:31], v[168:171], v[184:187], v[28:31]\n"
        "ds_read_b128 v[176:179], v230 offset:32768\n"
        "v_or3_b32 v210, v210, v211, v218\n"
        "v_mfma_f32_16x16x32_bf16 v[28:31], v[172:175], v[188:191], v[28:31]\n"
        "ds_read_b128 v[180:183], v231 offset:32768\n"
        "v_perm_b32 v224, v209, v236, s60\n"
        "v_mfma_f32_16x16x32_bf16 v[24:27], v[164:167], v[188:191], v[24:27]\n"
        "ds_read_b32 v116, v224\n"
        "v_perm_b32 v225, v209, v236, s61\n"
        "v_mfma_f32_16x16x32_bf16 v[24:27], v[160:163], v[184:187], v[24:27]\n"
        "ds_read_b32 v117, v225\n"
        "v_perm_b32 v226, v209, v236, s62\n"
        "v_mfma_f32_16x16x32_bf16 v[20:23], v[152:155], v[184:187], v[20:23]\n"
        "ds_read_b32 v118, v226\n"
        "v_perm_b32 v227, v209, v236, s63\n"
        "v_mfma_f32_16x16x32_bf16 v[20:23], v[156:159], v[188:191], v[20:23]\n"
        "ds_read_b32 v119, v227\n"
        "v_perm_b32 v224, v212, v236, s60\n"
        "v_mfma_f32_16x16x32_bf16 v[16:19], v[148:151], v[188:191], v[16:19]\n"
        "ds_read_b32 v120, v224\n"
        "v_perm_b32 v225, v212, v236, s61\n"
        "v_mfma_f32_16x16x32_bf16 v[16:19], v[144:147], v[184:187], v[16:19]\n"
        "ds_read_b32 v121, v225\n"
        "v_or_b32 v210, v210, v219\n"
        "s_waitcnt lgkmcnt(14)\n"
        "v_mfma_f32_16x16x32_bf16 v[32:35], v[144:147], v[192:195], v[32:35]\n"
        "ds_read_b128 v[184:187], v230 offset:34816\n"
        "v_cmp_ne_u32 vcc, 0, v210\n"
        "v_mfma_f32_16x16x32_bf16 v[32:35], v[148:151], v[196:199], v[32:35]\n"
        "ds_read_b128 v[188:191], v231 offset:34816\n"
        "v_perm_b32 v226, v212, v236, s62\n"
        "v_mfma_f32_16x16x32_bf16 v[36:39], v[156:159], v[196:199], v[36:39]\n"
        "ds_read_b32 v122, v226\n"
        "v_perm_b32 v227, v212, v236, s63\n"
        "v_mfma_f32_16x16x32_bf16 v[36:39], v[152:155], v[192:195], v[36:39]\n"
        "ds_read_b32 v123, v227\n"
        "v_perm_b32 v224, v213, v236, s60\n"
        "v_mfma_f32_16x16x32_bf16 v[40:43], v[160:163], v[192:195], v[40:43]\n"
        "ds_read_b32 v124, v224\n"
        "v_perm_b32 v225, v213, v236, s61\n"
        "v_mfma_f32_16x16x32_bf16 v[40:43], v[164:167], v[196:199], v[40:43]\n"
        "ds_read_b32 v125, v225\n"
        "v_perm_b32 v226, v213, v236, s62\n"
        "v_mfma_f32_16x16x32_bf16 v[44:47], v[172:175], v[196:199], v[44:47]\n"
        "ds_read_b32 v126, v226\n"
        "v_perm_b32 v227, v213, v236, s63\n"
        "v_mfma_f32_16x16x32_bf16 v[44:47], v[168:171], v[192:195], v[44:47]\n"
        "ds_read_b32 v127, v227\n"
        "s_waitcnt lgkmcnt(15)\n"
        "v_mfma_f32_16x16x32_bf16 v[60:63], v[168:171], v[200:203], v[60:63]\n"
        "ds_read_b128 v[192:195], v230 offset:36864\n"
        "s_cbranch_vccnz .Ln_f2\n"
        ".Ln_b2:\n"
        "v_mfma_f32_16x16x32_bf16 v[60:63], v[172:175], v[204:207], v[60:63]\n"
        "ds_read_b128 v[196:199], v231 offset:36864\n"
        "v_perm_b32 v224, v216, v236, s60\n"
        "v_mfma_f32_16x16x32_bf16 v[56:59], v[164:167], v[204:207], v[56:59]\n"
        "ds_read_b32 v128, v224\n"
        "v_perm_b32 v225, v216, v236, s61\n"
        "v_mfma_f32_16x16x32_bf16 v[56:59], v[160:163], v[200:203], v[56:59]\n"
        "ds_read_b32 v129, v225\n"
        "v_perm_b32 v226, v216, v236, s62\n"
        "v_mfma_f32_16x16x32_bf16 v[52:55], v[152:155], v[200:203], v[52:55]\n"
        "ds_read_b32 v130, v226\n"
        "v_perm_b32 v227, v216, v236, s63\n"
        "v_mfma_f32_16x16x32_bf16 v[52:55], v[156:159], v[204:207], v[52:55]\n"
        "ds_read_b32 v131, v227\n"
        "v_perm_b32 v224, v217, v236, s60\n"
        "v_mfma_f32_16x16x32_bf16 v[48:51], v[148:151], v[204:207], v[48:51]\n"
        "ds_read_b32 v132, v224\n"
        "v_perm_b32 v225, v217, v236, s61\n"
        "v_mfma_f32_16x16x32_bf16 v[48:51], v[144:147], v[200:203], v[48:51]\n"
        "ds_read_b32 v133, v225\n"
        "s_waitcnt lgkmcnt(15)\n"
        "v_mfma_f32_16x16x32_bf16 v[64:67], v[144:147], v[176:179], v[64:67]\n"
        "ds_read_b128 v[200:203], v230 offset:38912\n"
        "v_mfma_f32_16x16x32_bf16 v[64:67], v[148:151], v[180:183], v[64:67]\n"
        "ds_read_b128 v[204:207], v231 offset:38912\n"
        "v_perm_b32 v226, v217, v236, s62\n"
        "v_mfma_f32_16x16x32_bf16 v[68:71], v[156:159], v[180:183], v[68:71]\n"
        "ds_read_b32 v134, v226\n"
        "v_perm_b32 v227, v217, v236, s63\n"
        "v_mfma_f32_16x16x32_bf16 v[68:71], v[152:155], v[176:179], v[68:71]\n"
        "ds_read_b32 v135, v227\n"
        "v_perm_b32 v224, v220, v236, s60\n"
        "v_mfma_f32_16x16x32_bf16 v[72:75], v[160:163], v[176:179], v[72:75]\n"
        "ds_read_b32 v136, v224\n"
        "v_perm_b32 v225, v220, v236, s61\n"
        "v_mfma_f32_16x16x32_bf16 v[72:75], v[164:167], v[180:183], v[72:75]\n"
        "ds_read_b32 v137, v225\n"
        "v_perm_b32 v226, v220, v236, s62\n"
        "s_waitcnt lgkmcnt(15)\n"
        "v_mfma_f32_16x16x32_bf16 v[84:87], v[164:167], v[188:191], v[84:87]\n"
        "ds_read_b32 v138, v226\n"
        "v_perm_b32 v227, v220, v236, s63\n"
        "v_mfma_f32_16x16x32_bf16 v[84:87], v[160:163], v[184:187], v[84:87]\n"
        "ds_read_b32 v139, v227\n"
        "v_perm_b32 v224, v221, v236, s60\n"
        "v_mfma_f32_16x16x32_bf16 v[80:83], v[152:155], v[184:187], v[80:83]\n"
        "ds_read_b32 v140, v224\n"
        "v_perm_b32 v225, v221, v236, s61\n"
        "v_mfma_f32_16x16x32_bf16 v[80:83], v[156:159], v[188:191], v[80:83]\n"
        "ds_read_b32 v141, v225\n"
        "v_perm_b32 v226, v221, v236, s62\n"
        "v_mfma_f32_16x16x32_bf16 v[76:79], v[148:151], v[188:191], v[76:79]\n"
        "ds_read_b32 v142, v226\n"
        "v_perm_b32 v227, v221, v236, s63\n"
        "v_mfma_f32_16x16x32_bf16 v[76:79], v[144:147], v[184:187], v[76:79]\n"
        "ds_read_b32 v143, v227\n"
        "s_waitcnt lgkmcnt(10)\n"
        "s_barrier\n"
        "v_mfma_f32_16x16x32_bf16 v[88:91], v[144:147], v[192:195], v[88:91]\n"
        "ds_read_b128 v[208:211], v232 offset:32768\n"
        "v_mfma_f32_16x16x32_bf16 v[88:91], v[148:151], v[196:199], v[88:91]\n"
        "ds_read_b128 v[212:215], v233 offset:32768\n"
        "v_mfma_f32_16x16x32_bf16 v[92:95], v[156:159], v[196:199], v[92:95]\n"
        "v_mfma_f32_16x16x32_bf16 v[92:95], v[152:155], v[192:195], v[92:95]\n"
        "ds_read_b128 v[216:219], v234 offset:32768\n"
        "v_mfma_f32_16x16x32_bf16 v[96:99], v[160:163], v[192:195], v[96:99]\n"
        "ds_read_b128 v[220:223], v235 offset:32768\n"
        "v_mfma_f32_16x16x32_bf16 v[96:99], v[164:167], v[196:199], v[96:99]\n"
        "v_mfma_f32_16x16x32_bf16 v[108:111], v[164:167], v[204:207], v[108:111]\n"
        "ds_read_b128 v[176:179], v228 offset:0\n"
        "v_mfma_f32_16x16x32_bf16 v[108:111], v[160:163], v[200:203], v[108:111]\n"
        "ds_read_b128 v[180:183], v229 offset:0\n"
        "v_mfma_f32_16x16x32_bf16 v[104:107], v[152:155], v[200:203], v[104:107]\n"
        "v_mfma_f32_16x16x32_bf16 v[104:107], v[156:159], v[204:207], v[104:107]\n"
        "ds_read_b128 v[184:187], v228 offset:2048\n"
        "v_mfma_f32_16x16x32_bf16 v[100:103], v[148:151], v[204:207], v[100:103]\n"
        "ds_read_b128 v[188:191], v229 offset:2048\n"
        "v_mfma_f32_16x16x32_bf16 v[100:103], v[144:147], v[200:203], v[100:103]\n"
        "s_waitcnt lgkmcnt(2)\n"
        "v_mfma_f32_16x16x32_bf16 v[0:3], v[112:115], v[176:179], v[0:3]\n"
        "ds_read_b128 v[192:195], v228 offset:4096\n"
        "v_bitop3_b32 v210, v210, v214, s34 bitop3:0x7e\n"
        "v_mfma_f32_16x16x32_bf16 v[0:3], v[116:119], v[180:183], v[0:3]\n"
        "ds_read_b128 v[196:199], v229 offset:4096\n"
        "v_bitop3_b32 v211, v211, v215, s39 bitop3:0x7e\n"
        "v_mfma_f32_16x16x32_bf16 v[4:7], v[124:127], v[180:183], v[4:7]\n"
        "ds_read_b128 v[200:203], v228 offset:6144\n"
        "v_bitop3_b32 v218, v218, v222, s34 bitop3:0x7e\n"
        "v_mfma_f32_16x16x32_bf16 v[4:7], v[120:123], v[176:179], v[4:7]\n"
        "ds_read_b128 v[204:207], v229 offset:6144\n"
        "v_perm_b32 v224, v208, v236, s60\n"
        "v_mfma_f32_16x16x32_bf16 v[8:11], v[128:131], v[176:179], v[8:11]\n"
        "ds_read_b32 v144, v224\n"
        "v_perm_b32 v225, v208, v236, s61\n"
        "v_mfma_f32_16x16x32_bf16 v[8:11], v[132:135], v[180:183], v[8:11]\n"
        "ds_read_b32 v145, v225\n"
        "v_perm_b32 v226, v208, v236, s62\n"
        "v_mfma_f32_16x16x32_bf16 v[12:15], v[140:143], v[180:183], v[12:15]\n"
        "ds_read_b32 v146, v226\n"
        "v_perm_b32 v227, v208, v236, s63\n"
        "v_mfma_f32_16x16x32_bf16 v[12:15], v[136:139], v[176:179], v[12:15]\n"
        "ds_read_b32 v147, v227\n"
        "v_bitop3_b32 v219, v219, v223, s39 bitop3:0x7e\n"
        "s_waitcnt lgkmcnt(8)\n"
        "v_mfma_f32_16x16x32_bf16 v[28:31], v[136:139], v[184:187], v[28:31]\n"
        "ds_read_b128 v[176:179], v230 offset:0\n"
        "v_or3_b32 v210, v210, v211, v218\n"
        "v_mfma_f32_16x16x32_bf16 v[28:31], v[140:143], v[188:191], v[28:31]\n"
        "ds_read_b128 v[180:183], v231 offset:0\n"
        "v_perm_b32 v224, v209, v236, s60\n"
        "v_mfma_f32_16x16x32_bf16 v[24:27], v[132:135], v[188:191], v[24:27]\n"
        "ds_read_b32 v148, v224\n"
        "v_perm_b32 v225, v209, v236, s61\n"
        "v_mfma_f32_16x16x32_bf16 v[24:27], v[128:131], v[184:187], v[24:27]\n"
        "ds_read_b32 v149, v225\n"
        "v_perm_b32 v226, v209, v236, s62\n"
        "v_mfma_f32_16x16x32_bf16 v[20:23], v[120:123], v[184:187], v[20:23]\n"
        "ds_read_b32 v150, v226\n"
        "v_perm_b32 v227, v209, v236, s63\n"
        "v_mfma_f32_16x16x32_bf16 v[20:23], v[124:127], v[188:191], v[20:23]\n"
        "ds_read_b32 v151, v227\n"
        "v_perm_b32 v224, v212, v236, s60\n"
        "v_mfma_f32_16x16x32_bf16 v[16:19], v[116:119], v[188:191], v[16:19]\n"
        "ds_read_b32 v152, v224\n"
        "v_perm_b32 v225, v212, v236, s61\n"
        "v_mfma_f32_16x16x32_bf16 v[16:19], v[112:115], v[184:187], v[16:19]\n"
        "ds_read_b32 v153, v225\n"
        "v_or_b32 v210, v210, v219\n"
        "s_waitcnt lgkmcnt(14)\n"
        "v_mfma_f32_16x16x32_bf16 v[32:35], v[112:115], v[192:195], v[32:35]\n"
        "ds_read_b128 v[184:187], v230 offset:2048\n"
        "v_cmp_ne_u32 vcc, 0, v210\n"
        "v_mfma_f32_16x16x32_bf16 v[32:35], v[116:119], v[196:199], v[32:35]\n"
        "ds_read_b128 v[188:191], v231 offset:2048\n"
        "v_perm_b32 v226, v212, v236, s62\n"
        "v_mfma_f32_16x16x32_bf16 v[36:39], v[124:127], v[196:199], v[36:39]\n"
        "ds_read_b32 v154, v226\n"
        "v_perm_b32 v227, v212, v236, s63\n"
        "v_mfma_f32_16x16x32_bf16 v[36:39], v[120:123], v[192:195], v[36:39]\n"
        "ds_read_b32 v155, v227\n"
        "v_perm_b32 v224, v213, v236, s60\n"
        "v_mfma_f32_16x16x32_bf16 v[40:43], v[128:131], v[192:195], v[40:43]\n"
        "ds_read_b32 v156, v224\n"
        "v_perm_b32 v225, v213, v236, s61\n"
        "v_mfma_f32_16x16x32_bf16 v[40:43], v[132:135], v[196:199], v[40:43]\n"
        "ds_read_b32 v157, v225\n"
        "v_perm_b32 v226, v213, v236, s62\n"
        "v_mfma_f32_16x16x32_bf16 v[44:47], v[140:143], v[196:199], v[44:47]\n"
        "ds_read_b32 v158, v226\n"
        "v_perm_b32 v227, v213, v236, s63\n"
        "v_mfma_f32_16x16x32_bf16 v[44:47], v[136:139], v[192:195], v[44:47]\n"
        "ds_read_b32 v159, v227\n"
        "s_waitcnt lgkmcnt(15)\n"
        "v_mfma_f32_16x16x32_bf16 v[60:63], v[136:139], v[200:203], v[60:63]\n"
        "ds_read_b128 v[192:195], v230 offset:4096\n"
        "s_cbranch_vccnz .Ln_f3\n"
        ".Ln_b3:\n"
        "v_mfma_f32_16x16x32_bf16 v[60:63], v[140:143], v[204:207], v[60:63]\n"
        "ds_read_b128 v[196:199], v231 offset:4096\n"
        "v_perm_b32 v224, v216, v236, s60\n"
        "v_mfma_f32_16x16x32_bf16 v[56:59], v[132:135], v[204:207], v[56:59]\n"
        "ds_read_b32 v160, v224\n"
        "v_perm_b32 v225, v216, v236, s61\n"
        "v_mfma_f32_16x16x32_bf16 v[56:59], v[128:131], v[200:203], v[56:59]\n"
        "ds_read_b32 v161, v225\n"
        "v_perm_b32 v226, v216, v236, s62\n"
        "v_mfma_f32_16x16x32_bf16 v[52:55], v[120:123], v[200:203], v[52:55]\n"
        "ds_read_b32 v162, v226\n"
        "v_perm_b32 v227, v216, v236, s63\n"
        "v_mfma_f32_16x16x32_bf16 v[52:55], v[124:127], v[204:207], v[52:55]\n"
        "ds_read_b32 v163, v227\n"
        "v_perm_b32 v224, v217, v236, s60\n"
        "v_mfma_f32_16x16x32_bf16 v[48:51], v[116:119], v[204:207], v[48:51]\n"
        "ds_read_b32 v164, v224\n"
        "v_perm_b32 v225, v217, v236, s61\n"
        "v_mfma_f32_16x16x32_bf16 v[48:51], v[112:115], v[200:203], v[48:51]\n"
        "ds_read_b32 v165, v225\n"
        "s_waitcnt lgkmcnt(15)\n"
        "v_mfma_f32_16x16x32_bf16 v[64:67], v[112:115], v[176:179], v[64:67]\n"
        "ds_read_b128 v[200:203], v230 offset:6144\n"
        "v_mfma_f32_16x16x32_bf16 v[64:67], v[116:119], v[180:183], v[64:67]\n"
        "ds_read_b128 v[204:207], v231 offset:6144\n"
        "v_perm_b32 v226, v217, v236, s62\n"
        "v_mfma_f32_16x16x32_bf16 v[68:71], v[124:127], v[180:183], v[68:71]\n"
        "ds_read_b32 v166, v226\n"
        "v_perm_b32 v227, v217, v236, s63\n"
        "v_mfma_f32_16x16x32_bf16 v[68:71], v[120:123], v[176:179], v[68:71]\n"
        "ds_read_b32 v167, v227\n"
        "v_perm_b32 v224, v220, v236, s60\n"
        "v_mfma_f32_16x16x32_bf16 v[72:75], v[128:131], v[176:179], v[72:75]\n"
        "ds_read_b32 v168, v224\n"
        "v_perm_b32 v225, v220, v236, s61\n"
        "v_mfma_f32_16x16x32_bf16 v[72:75], v[132:135], v[180:183], v[72:75]\n"
        "ds_read_b32 v169, v225\n"
        "v_perm_b32 v226, v220, v236, s62\n"
        "s_waitcnt lgkmcnt(15)\n"
        "v_mfma_f32_16x16x32_bf16 v[84:87], v[132:135], v[188:191], v[84:87]\n"
        "ds_read_b32 v170, v226\n"
        "v_perm_b32 v227, v220, v236, s63\n"
        "v_mfma_f32_16x16x32_bf16 v[84:87], v[128:131], v[184:187], v[84:87]\n"
        "ds_read_b32 v171, v227\n"
        "v_perm_b32 v224, v221, v236, s60\n"
        "v_mfma_f32_16x16x32_bf16 v[80:83], v[120:123], v[184:187], v[80:83]\n"
        "ds_read_b32 v172, v224\n"
        "v_perm_b32 v225, v221, v236, s61\n"
        "v_mfma_f32_16x16x32_bf16 v[80:83], v[124:127], v[188:191], v[80:83]\n"
        "ds_read_b32 v173, v225\n"
        "v_perm_b32 v226, v221, v236, s62\n"
        "v_mfma_f32_16x16x32_bf16 v[76:79], v[116:119], v[188:191], v[76:79]\n"
        "ds_read_b32 v174, v226\n"
        "v_perm_b32 v227, v221, v236, s63\n"
        "v_mfma_f32_16x16x32_bf16 v[76:79], v[112:115], v[184:187], v[76:79]\n"
        "ds_read_b32 v175, v227\n"
        "s_waitcnt lgkmcnt(10)\n"
        "s_barrier\n"
        "v_mfma_f32_16x16x32_bf16 v[88:91], v[112:115], v[192:195], v[88:91]\n"
        "ds_read_b128 v[208:211], v232 offset:0\n"
        "v_mfma_f32_16x16x32_bf16 v[88:91], v[116:119], v[196:199], v[88:91]\n"
        "ds_read_b128 v[212:215], v233 offset:0\n"
        "v_mfma_f32_16x16x32_bf16 v[92:95], v[124:127], v[196:199], v[92:95]\n"
        "v_mfma_f32_16x16x32_bf16 v[92:95], v[120:123], v[192:195], v[92:95]\n"
        "ds_read_b128 v[216:219], v234 offset:0\n"
        ".Ln_tp0:\n"
        "s_cbranch_execz .Ln_ret\n"
        "v_mfma_f32_16x16x32_bf16 v[96:99], v[128:131], v[192:195], v[96:99]\n"
        "ds_read_b128 v[220:223], v235 offset:0\n"
        "v_mfma_f32_16x16x32_bf16 v[96:99], v[132:135], v[196:199], v[96:99]\n"
        "v_mfma_f32_16x16x32_bf16 v[108:111], v[132:135], v[204:207], v[108:111]\n"
        "ds_read_b128 v[176:179], v228 offset:16384\n"
        "v_mfma_f32_16x16x32_bf16 v[108:111], v[128:131], v[200:203], v[108:111]\n"
        "ds_read_b128 v[180:183], v229 offset:16384\n"
        "v_mfma_f32_16x16x32_bf16 v[104:107], v[120:123], v[200:203], v[104:107]\n"
        "v_mfma_f32_16x16x32_bf16 v[104:107], v[124:127], v[204:207], v[104:107]\n"
        "ds_read_b128 v[184:187], v228 offset:18432\n"
        "v_mfma_f32_16x16x32_bf16 v[100:103], v[116:119], v[204:207], v[100:103]\n"
        "ds_read_b128 v[188:191], v229 offset:18432\n"
        "v_mfma_f32_16x16x32_bf16 v[100:103], v[112:115], v[200:203], v[100:103]\n"
        "s_waitcnt lgkmcnt(2)\n"
        "v_mfma_f32_16x16x32_bf16 v[0:3], v[144:147], v[176:179], v[0:3]\n"
        "ds_read_b128 v[192:195], v228 offset:20480\n"
        "v_bitop3_b32 v210, v210, v214, s34 bitop3:0x7e\n"
        "v_mfma_f32_16x16x32_bf16 v[0:3], v[148:151], v[180:183], v[0:3]\n"
        "ds_read_b128 v[196:199], v229 offset:20480\n"
        "v_bitop3_b32 v211, v211, v215, s39 bitop3:0x7e\n"
        "v_mfma_f32_16x16x32_bf16 v[4:7], v[156:159], v[180:183], v[4:7]\n"
        "ds_read_b128 v[200:203], v228 offset:22528\n"
        "v_bitop3_b32 v218, v218, v222, s34 bitop3:0x7e\n"
        "v_mfma_f32_16x16x32_bf16 v[4:7], v[152:155], v[176:179], v[4:7]\n"
        "ds_read_b128 v[204:207], v229 offset:22528\n"
        "v_perm_b32 v224, v208, v236, s60\n"
        "v_mfma_f32_16x16x32_bf16 v[8:11], v[160:163], v[176:179], v[8:11]\n"
        "ds_read_b32 v112, v224\n"
        "v_perm_b32 v225, v208, v236, s61\n"
        "v_mfma_f32_16x16x32_bf16 v[8:11], v[164:167], v[180:183], v[8:11]\n"
        "ds_read_b32 v113, v225\n"
        "v_perm_b32 v226, v208, v236, s62\n"
        "v_mfma_f32_16x16x32_bf16 v[12:15], v[172:175], v[180:183], v[12:15]\n"
        "ds_read_b32 v114, v226\n"
        "v_perm_b32 v227, v208, v236, s63\n"
        "v_mfma_f32_16x16x32_bf16 v[12:15], v[168:171], v[176:179], v[12:15]\n"
        "ds_read_b32 v115, v227\n"
        "v_bitop3_b32 v219, v219, v223, s39 bitop3:0x7e\n"
        "s_waitcnt lgkmcnt(8)\n"
        "v_mfma_f32_16x16x32_bf16 v[28:31], v[168:171], v[184:187], v[28:31]\n"
        "ds_read_b128 v[176:179], v230 offset:16384\n"
        "v_or3_b32 v210, v210, v211, v218\n"
        "v_mfma_f32_16x16x32_bf16 v[28:31], v[172:175], v[188:191], v[28:31]\n"
        "ds_read_b128 v[180:183], v231 offset:16384\n"
        "v_perm_b32 v224, v209, v236, s60\n"
        "v_mfma_f32_16x16x32_bf16 v[24:27], v[164:167], v[188:191], v[24:27]\n"
        "ds_read_b32 v116, v224\n"
        "v_perm_b32 v225, v209, v236, s61\n"
        "v_mfma_f32_16x16x32_bf16 v[24:27], v[160:163], v[184:187], v[24:27]\n"
        "ds_read_b32 v117, v225\n"
        "v_perm_b32 v226, v209, v236, s62\n"
        "v_mfma_f32_16x16x32_bf16 v[20:23], v[152:155], v[184:187], v[20:23]\n"
        "ds_read_b32 v118, v226\n"
        "v_perm_b32 v227, v209, v236, s63\n"
        "v_mfma_f32_16x16x32_bf16 v[20:23], v[156:159], v[188:191], v[20:23]\n"
        "ds_read_b32 v119, v227\n"
        "v_perm_b32 v224, v212, v236, s60\n"
        "v_mfma_f32_16x16x32_bf16 v[16:19], v[148:151], v[188:191], v[16:19]\n"
        "ds_read_b32 v120, v224\n"
        "v_perm_b32 v225, v212, v236, s61\n"
        "v_mfma_f32_16x16x32_bf16 v[16:19], v[144:147], v[184:187], v[16:19]\n"
        "ds_read_b32 v121, v225\n"
        "v_or_b32 v210, v210, v219\n"
        "s_waitcnt lgkmcnt(14)\n"
        "v_mfma_f32_16x16x32_bf16 v[32:35], v[144:147], v[192:195], v[32:35]\n"
        "ds_read_b128 v[184:187], v230 offset:18432\n"
        "v_cmp_ne_u32 vcc, 0, v210\n"
        "v_mfma_f32_16x16x32_bf16 v[32:35], v[148:151], v[196:199], v[32:35]\n"
        "ds_read_b128 v[188:191], v231 offset:18432\n"
        "v_perm_b32 v226, v212, v236, s62\n"
        "v_mfma_f32_16x16x32_bf16 v[36:39], v[156:159], v[196:199], v[36:39]\n"
        "ds_read_b32 v122, v226\n"
        "v_perm_b32 v227, v212, v236, s63\n"
        "v_mfma_f32_16x16x32_bf16 v[36:39], v[152:155], v[192:195], v[36:39]\n"
        "ds_read_b32 v123, v227\n"
        "v_perm_b32 v224, v213, v236, s60\n"
        "v_mfma_f32_16x16x32_bf16 v[40:43], v[160:163], v[192:195], v[40:43]\n"
        "ds_read_b32 v124, v224\n"
        "v_perm_b32 v225, v213, v236, s61\n"
        "v_mfma_f32_16x16x32_bf16 v[40:43], v[164:167], v[196:199], v[40:43]\n"
        "ds_read_b32 v125, v225\n"
        "v_perm_b32 v226, v213, v236, s62\n"
        "v_mfma_f32_16x16x32_bf16 v[44:47], v[172:175], v[196:199], v[44:47]\n"
        "ds_read_b32 v126, v226\n"
        "v_perm_b32 v227, v213, v236, s63\n"
        "v_mfma_f32_16x16x32_bf16 v[44:47], v[168:171], v[192:195], v[44:47]\n"
        "ds_read_b32 v127, v227\n"
        "s_waitcnt lgkmcnt(15)\n"
        "v_mfma_f32_16x16x32_bf16 v[60:63], v[168:171], v[200:203], v[60:63]\n"
        "ds_read_b128 v[192:195], v230 offset:20480\n"
        "s_cbranch_vccnz .Ln_f4\n"
        ".Ln_b4:\n"
        "v_mfma_f32_16x16x32_bf16 v[60:63], v[172:175], v[204:207], v[60:63]\n"
        "ds_read_b128 v[196:199], v231 offset:20480\n"
        "v_perm_b32 v224, v216, v236, s60\n"
        "v_mfma_f32_16x16x32_bf16 v[56:59], v[164:167], v[204:207], v[56:59]\n"
        "ds_read_b32 v128, v224\n"
        "v_perm_b32 v225, v216, v236, s61\n"
        "v_mfma_f32_16x16x32_bf16 v[56:59], v[160:163], v[200:203], v[56:59]\n"
        "ds_read_b32 v129, v225\n"
        "v_perm_b32 v226, v216, v236, s62\n"
        "v_mfma_f32_16x16x32_bf16 v[52:55], v[152:155], v[200:203], v[52:55]\n"
        "ds_read_b32 v130, v226\n"
        "v_perm_b32 v227, v216, v236, s63\n"
        "v_mfma_f32_16x16x32_bf16 v[52:55], v[156:159], v[204:207], v[52:55]\n"
        "ds_read_b32 v131, v227\n"
        "v_perm_b32 v224, v217, v236, s60\n"
        "v_mfma_f32_16x16x32_bf16 v[48:51], v[148:151], v[204:207], v[48:51]\n"
        "ds_read_b32 v132, v224\n"
        "v_perm_b32 v225, v217, v236, s61\n"
        "v_mfma_f32_16x16x32_bf16 v[48:51], v[144:147], v[200:203], v[48:51]\n"
        "ds_read_b32 v133, v225\n"
        "s_waitcnt lgkmcnt(15)\n"
        "v_mfma_f32_16x16x32_bf16 v[64:67], v[144:147], v[176:179], v[64:67]\n"
        "ds_read_b128 v[200:203], v230 offset:22528\n"
        "v_mfma_f32_16x16x32_bf16 v[64:67], v[148:151], v[180:183], v[64:67]\n"
        "ds_read_b128 v[204:207], v231 offset:22528\n"
        "v_perm_b32 v226, v217, v236, s62\n"
        "v_mfma_f32_16x16x32_bf16 v[68:71], v[156:159], v[180:183], v[68:71]\n"
        "ds_read_b32 v134, v226\n"
        "v_perm_b32 v227, v217, v236, s63\n"
        "v_mfma_f32_16x16x32_bf16 v[68:71], v[152:155], v[176:179], v[68:71]\n"
        "ds_read_b32 v135, v227\n"
        "v_perm_b32 v224, v220, v236, s60\n"
        "v_mfma_f32_16x16x32_bf16 v[72:75], v[160:163], v[176:179], v[72:75]\n"
        "ds_read_b32 v136, v224\n"
        "v_perm_b32 v225, v220, v236, s61\n"
        "v_mfma_f32_16x16x32_bf16 v[72:75], v[164:167], v[180:183], v[72:75]\n"
        "ds_read_b32 v137, v225\n"
        "v_perm_b32 v226, v220, v236, s62\n"
        "s_waitcnt lgkmcnt(15)\n"
        "v_mfma_f32_16x16x32_bf16 v[84:87], v[164:167], v[188:191], v[84:87]\n"
        "ds_read_b32 v138, v226\n"
        "v_perm_b32 v227, v220, v236, s63\n"
        "v_mfma_f32_16x16x32_bf16 v[84:87], v[160:163], v[184:187], v[84:87]\n"
        "ds_read_b32 v139, v227\n"
        "v_perm_b32 v224, v221, v236, s60\n"
        "v_mfma_f32_16x16x32_bf16 v[80:83], v[152:155], v[184:187], v[80:83]\n"
        "ds_read_b32 v140, v224\n"
        "v_perm_b32 v225, v221, v236, s61\n"
        "v_mfma_f32_16x16x32_bf16 v[80:83], v[156:159], v[188:191], v[80:83]\n"
        "ds_read_b32 v141, v225\n"
        "v_perm_b32 v226, v221, v236, s62\n"
        "v_mfma_f32_16x16x32_bf16 v[76:79], v[148:151], v[188:191], v[76:79]\n"
        "ds_read_b32 v142, v226\n"
        "v_perm_b32 v227, v221, v236, s63\n"
        "v_mfma_f32_16x16x32_bf16 v[76:79], v[144:147], v[184:187], v[76:79]\n"
        "ds_read_b32 v143, v227\n"
        "s_waitcnt lgkmcnt(10)\n"
        "s_barrier\n"
        "v_mfma_f32_16x16x32_bf16 v[88:91], v[144:147], v[192:195], v[88:91]\n"
        "ds_read_b128 v[208:211], v232 offset:16384\n"
        "v_mfma_f32_16x16x32_bf16 v[88:91], v[148:151], v[196:199], v[88:91]\n"
        "ds_read_b128 v[212:215], v233 offset:16384\n"
        "v_mfma_f32_16x16x32_bf16 v[92:95], v[156:159], v[196:199], v[92:95]\n"
        "v_mfma_f32_16x16x32_bf16 v[92:95], v[152:155], v[192:195], v[92:95]\n"
        "ds_read_b128 v[216:219], v234 offset:16384\n"
        "v_mfma_f32_16x16x32_bf16 v[96:99], v[160:163], v[192:195], v[96:99]\n"
        "ds_read_b128 v[220:223], v235 offset:16384\n"
        "v_mfma_f32_16x16x32_bf16 v[96:99], v[164:167], v[196:199], v[96:99]\n"
        "v_mfma_f32_16x16x32_bf16 v[108:111], v[164:167], v[204:207], v[108:111]\n"
        "ds_read_b128 v[176:179], v228 offset:32768\n"
        "v_mfma_f32_16x16x32_bf16 v[108:111], v[160:163], v[200:203], v[108:111]\n"
        "ds_read_b128 v[180:183], v229 offset:32768\n"
        "v_mfma_f32_16x16x32_bf16 v[104:107], v[152:155], v[200:203], v[104:107]\n"
        "v_mfma_f32_16x16x32_bf16 v[104:107], v[156:159], v[204:207], v[104:107]\n"
        "ds_read_b128 v[184:187], v228 offset:34816\n"
        "v_mfma_f32_16x16x32_bf16 v[100:103], v[148:151], v[204:207], v[100:103]\n"
        "ds_read_b128 v[188:191], v229 offset:34816\n"
        "v_mfma_f32_16x16x32_bf16 v[100:103], v[144:147], v[200:203], v[100:103]\n"
        "s_waitcnt lgkmcnt(2)\n"
        "v_mfma_f32_16x16x32_bf16 v[0:3], v[112:115], v[176:179], v[0:3]\n"
        "ds_read_b128 v[192:195], v228 offset:36864\n"
        "v_bitop3_b32 v210, v210, v214, s34 bitop3:0x7e\n"
        "v_mfma_f32_16x16x32_bf16 v[0:3], v[116:119], v[180:183], v[0:3]\n"
        "ds_read_b128 v[196:199], v229 offset:36864\n"
        "v_bitop3_b32 v211, v211, v215, s39 bitop3:0x7e\n"
        "v_mfma_f32_16x16x32_bf16 v[4:7], v[124:127], v[180:183], v[4:7]\n"
        "ds_read_b128 v[200:203], v228 offset:38912\n"
        "v_bitop3_b32 v218, v218, v222, s34 bitop3:0x7e\n"
        "v_mfma_f32_16x16x32_bf16 v[4:7], v[120:123], v[176:179], v[4:7]\n"
        "ds_read_b128 v[204:207], v229 offset:38912\n"
        "v_perm_b32 v224, v208, v236, s60\n"
        "v_mfma_f32_16x16x32_bf16 v[8:11], v[128:131], v[176:179], v[8:11]\n"
        "ds_read_b32 v144, v224\n"
        "v_perm_b32 v225, v208, v236, s61\n"
        "v_mfma_f32_16x16x32_bf16 v[8:11], v[132:135], v[180:183], v[8:11]\n"
        "ds_read_b32 v145, v225\n"
        "v_perm_b32 v226, v208, v236, s62\n"
        "v_mfma_f32_16x16x32_bf16 v[12:15], v[140:143], v[180:183], v[12:15]\n"
        "ds_read_b32 v146, v226\n"
        "v_perm_b32 v227, v208, v236, s63\n"
        "v_mfma_f32_16x16x32_bf16 v[12:15], v[136:139], v[176:179], v[12:15]\n"
        "ds_read_b32 v147, v227\n"
        "v_bitop3_b32 v219, v219, v223, s39 bitop3:0x7e\n"
        "s_waitcnt lgkmcnt(8)\n"
        "v_mfma_f32_16x16x32_bf16 v[28:31], v[136:139], v[184:187], v[28:31]\n"
        "ds_read_b128 v[176:179], v230 offset:32768\n"
        "v_or3_b32 v210, v210, v211, v218\n"
        "v_mfma_f32_16x16x32_bf16 v[28:31], v[140:143], v[188:191], v[28:31]\n"
        "ds_read_b128 v[180:183], v231 offset:32768\n"
        "v_perm_b32 v224, v209, v236, s60\n"
        "v_mfma_f32_16x16x32_bf16 v[24:27], v[132:135], v[188:191], v[24:27]\n"
        "ds_read_b32 v148, v224\n"
        "v_perm_b32 v225, v209, v236, s61\n"
        "v_mfma_f32_16x16x32_bf16 v[24:27], v[128:131], v[184:187], v[24:27]\n"
        "ds_read_b32 v149, v225\n"
        "v_perm_b32 v226, v209, v236, s62\n"
        "v_mfma_f32_16x16x32_bf16 v[20:23], v[120:123], v[184:187], v[20:23]\n"
        "ds_read_b32 v150, v226\n"
        "v_perm_b32 v227, v209, v236, s63\n"
        "v_mfma_f32_16x16x32_bf16 v[20:23], v[124:127], v[188:191], v[20:23]\n"
        "ds_read_b32 v151, v227\n"
        "v_perm_b32 v224, v212, v236, s60\n"
        "v_mfma_f32_16x16x32_bf16 v[16:19], v[116:119], v[188:191], v[16:19]\n"
        "ds_read_b32 v152, v224\n"
        "v_perm_b32 v225, v212, v236, s61\n"
        "v_mfma_f32_16x16x32_bf16 v[16:19], v[112:115], v[184:187], v[16:19]\n"
        "ds_read_b32 v153, v225\n"
        "v_or_b32 v210, v210, v219\n"
        "s_waitcnt lgkmcnt(14)\n"
        "v_mfma_f32_16x16x32_bf16 v[32:35], v[112:115], v[192:195], v[32:35]\n"
        "ds_read_b128 v[184:187], v230 offset:34816\n"
        "v_cmp_ne_u32 vcc, 0, v210\n"
        "v_mfma_f32_16x16x32_bf16 v[32:35], v[116:119], v[196:199], v[32:35]\n"
        "ds_read_b128 v[188:191], v231 offset:34816\n"
        "v_perm_b32 v226, v212, v236, s62\n"
        "v_mfma_f32_16x16x32_bf16 v[36:39], v[124:127], v[196:199], v[36:39]\n"
        "ds_read_b32 v154, v226\n"
        "v_perm_b32 v227, v212, v236, s63\n"
        "v_mfma_f32_16x16x32_bf16 v[36:39], v[120:123], v[192:195], v[36:39]\n"
        "ds_read_b32 v155, v227\n"
        "v_perm_b32 v224, v213, v236, s60\n"
        "v_mfma_f32_16x16x32_bf16 v[40:43], v[128:131], v[192:195], v[40:43]\n"
        "ds_read_b32 v156, v224\n"
        "v_perm_b32 v225, v213, v236, s61\n"
        "v_mfma_f32_16x16x32_bf16 v[40:43], v[132:135], v[196:199], v[40:43]\n"
        "ds_read_b32 v157, v225\n"
        "v_perm_b32 v226, v213, v236, s62\n"
        "v_mfma_f32_16x16x32_bf16 v[44:47], v[140:143], v[196:199], v[44:47]\n"
        "ds_read_b32 v158, v226\n"
        "v_perm_b32 v227, v213, v236, s63\n"
        "v_mfma_f32_16x16x32_bf16 v[44:47], v[136:139], v[192:195], v[44:47]\n"
        "ds_read_b32 v159, v227\n"
        "s_waitcnt lgkmcnt(15)\n"
        "v_mfma_f32_16x16x32_bf16 v[60:63], v[136:139], v[200:203], v[60:63]\n"
        "ds_read_b128 v[192:195], v230 offset:36864\n"
        "s_cbranch_vccnz .Ln_f5\n"
        ".Ln_b5:\n"
        "v_mfma_f32_16x16x32_bf16 v[60:63], v[140:143], v[204:207], v[60:63]\n"
        "ds_read_b128 v[196:199], v231 offset:36864\n"
        "v_perm_b32 v224, v216, v236, s60\n"
        "v_mfma_f32_16x16x32_bf16 v[56:59], v[132:135], v[204:207], v[56:59]\n"
        "ds_read_b32 v160, v224\n"
        "v_perm_b32 v225, v216, v236, s61\n"
        "v_mfma_f32_16x16x32_bf16 v[56:59], v[128:131], v[200:203], v[56:59]\n"
        "ds_read_b32 v161, v225\n"
        "v_perm_b32 v226, v216, v236, s62\n"
        "v_mfma_f32_16x16x32_bf16 v[52:55], v[120:123], v[200:203], v[52:55]\n"
        "ds_read_b32 v162, v226\n"
        "v_perm_b32 v227, v216, v236, s63\n"
        "v_mfma_f32_16x16x32_bf16 v[52:55], v[124:127], v[204:207], v[52:55]\n"
        "ds_read_b32 v163, v227\n"
        "v_perm_b32 v224, v217, v236, s60\n"
        "v_mfma_f32_16x16x32_bf16 v[48:51], v[116:119], v[204:207], v[48:51]\n"
        "ds_read_b32 v164, v224\n"
        "v_perm_b32 v225, v217, v236, s61\n"
        "v_mfma_f32_16x16x32_bf16 v[48:51], v[112:115], v[200:203], v[48:51]\n"
        "ds_read_b32 v165, v225\n"
        "s_waitcnt lgkmcnt(15)\n"
        "v_mfma_f32_16x16x32_bf16 v[64:67], v[112:115], v[176:179], v[64:67]\n"
        "ds_read_b128 v[200:203], v230 offset:38912\n"
        "v_mfma_f32_16x16x32_bf16 v[64:67], v[116:119], v[180:183], v[64:67]\n"
        "ds_read_b128 v[204:207], v231 offset:38912\n"
        "v_perm_b32 v226, v217, v236, s62\n"
        "v_mfma_f32_16x16x32_bf16 v[68:71], v[124:127], v[180:183], v[68:71]\n"
        "ds_read_b32 v166, v226\n"
        "v_perm_b32 v227, v217, v236, s63\n"
        "v_mfma_f32_16x16x32_bf16 v[68:71], v[120:123], v[176:179], v[68:71]\n"
        "ds_read_b32 v167, v227\n"
        "v_perm_b32 v224, v220, v236, s60\n"
        "v_mfma_f32_16x16x32_bf16 v[72:75], v[128:131], v[176:179], v[72:75]\n"
        "ds_read_b32 v168, v224\n"
        "v_perm_b32 v225, v220, v236, s61\n"
        "v_mfma_f32_16x16x32_bf16 v[72:75], v[132:135], v[180:183], v[72:75]\n"
        "ds_read_b32 v169, v225\n"
        "v_perm_b32 v226, v220, v236, s62\n"
        "s_waitcnt lgkmcnt(15)\n"
        "v_mfma_f32_16x16x32_bf16 v[84:87], v[132:135], v[188:191], v[84:87]\n"
        "ds_read_b32 v170, v226\n"
        "v_perm_b32 v227, v220, v236, s63\n"
        "v_mfma_f32_16x16x32_bf16 v[84:87], v[128:131], v[184:187], v[84:87]\n"
        "ds_read_b32 v171, v227\n"
        "v_perm_b32 v224, v221, v236, s60\n"
        "v_mfma_f32_16x16x32_bf16 v[80:83], v[120:123], v[184:187], v[80:83]\n"
        "ds_read_b32 v172, v224\n"
        "v_perm_b32 v225, v221, v236, s61\n"
        "v_mfma_f32_16x16x32_bf16 v[80:83], v[124:127], v[188:191], v[80:83]\n"
        "ds_read_b32 v173, v225\n"
        "v_perm_b32 v226, v221, v236, s62\n"
        "v_mfma_f32_16x16x32_bf16 v[76:79], v[116:119], v[188:191], v[76:79]\n"
        "ds_read_b32 v174, v226\n"
        "v_perm_b32 v227, v221, v236, s63\n"
        "v_mfma_f32_16x16x32_bf16 v[76:79], v[112:115], v[184:187], v[76:79]\n"
        "ds_read_b32 v175, v227\n"
        "s_waitcnt lgkmcnt(10)\n"
        "s_barrier\n"
        "v_mfma_f32_16x16x32_bf16 v[88:91], v[112:115], v[192:195], v[88:91]\n"
        "ds_read_b128 v[208:211], v232 offset:32768\n"
        "v_mfma_f32_16x16x32_bf16 v[88:91], v[116:119], v[196:199], v[88:91]\n"
        "ds_read_b128 v[212:215], v233 offset:32768\n"
        "v_mfma_f32_16x16x32_bf16 v[92:95], v[124:127], v[196:199], v[92:95]\n"
        "v_mfma_f32_16x16x32_bf16 v[92:95], v[120:123], v[192:195], v[92:95]\n"
        "ds_read_b128 v[216:219], v234 offset:32768\n"
        "v_mfma_f32_16x16x32_bf16 v[96:99], v[128:131], v[192:195], v[96:99]\n"
        "ds_read_b128 v[220:223], v235 offset:32768\n"
        "v_mfma_f32_16x16x32_bf16 v[96:99], v[132:135], v[196:199], v[96:99]\n"
        "v_mfma_f32_16x16x32_bf16 v[108:111], v[132:135], v[204:207], v[108:111]\n"
        "ds_read_b128 v[176:179], v228 offset:0\n"
        "v_mfma_f32_16x16x32_bf16 v[108:111], v[128:131], v[200:203], v[108:111]\n"
        "ds_read_b128 v[180:183], v229 offset:0\n"
        "v_mfma_f32_16x16x32_bf16 v[104:107], v[120:123], v[200:203], v[104:107]\n"
        "v_mfma_f32_16x16x32_bf16 v[104:107], v[124:127], v[204:207], v[104:107]\n"
        "ds_read_b128 v[184:187], v228 offset:2048\n"
        "v_mfma_f32_16x16x32_bf16 v[100:103], v[116:119], v[204:207], v[100:103]\n"
        "ds_read_b128 v[188:191], v229 offset:2048\n"
        "v_mfma_f32_16x16x32_bf16 v[100:103], v[112:115], v[200:203], v[100:103]\n"
        "s_waitcnt lgkmcnt(2)\n"
        "v_mfma_f32_16x16x32_bf16 v[0:3], v[144:147], v[176:179], v[0:3]\n"
        "ds_read_b128 v[192:195], v228 offset:4096\n"
        "v_bitop3_b32 v210, v210, v214, s34 bitop3:0x7e\n"
        "v_mfma_f32_16x16x32_bf16 v[0:3], v[148:151], v[180:183], v[0:3]\n"
        "ds_read_b128 v[196:199], v229 offset:4096\n"
        "v_bitop3_b32 v211, v211, v215, s39 bitop3:0x7e\n"
        "v_mfma_f32_16x16x32_bf16 v[4:7], v[156:159], v[180:183], v[4:7]\n"
        "ds_read_b128 v[200:203], v228 offset:6144\n"
        "v_bitop3_b32 v218, v218, v222, s34 bitop3:0x7e\n"
        "v_mfma_f32_16x16x32_bf16 v[4:7], v[152:155], v[176:179], v[4:7]\n"
        "ds_read_b128 v[204:207], v229 offset:6144\n"
        "v_perm_b32 v224, v208, v236, s60\n"
        "v_mfma_f32_16x16x32_bf16 v[8:11], v[160:163], v[176:179], v[8:11]\n"
        "ds_read_b32 v112, v224\n"
        "v_perm_b32 v225, v208, v236, s61\n"
        "v_mfma_f32_16x16x32_bf16 v[8:11], v[164:167], v[180:183], v[8:11]\n"
        "ds_read_b32 v113, v225\n"
        "v_perm_b32 v226, v208, v236, s62\n"
        "v_mfma_f32_16x16x32_bf16 v[12:15], v[172:175], v[180:183], v[12:15]\n"
        "ds_read_b32 v114, v226\n"
        "v_perm_b32 v227, v208, v236, s63\n"
        "v_mfma_f32_16x16x32_bf16 v[12:15], v[168:171], v[176:179], v[12:15]\n"
        "ds_read_b32 v115, v227\n"
        "v_bitop3_b32 v219, v219, v223, s39 bitop3:0x7e\n"
        "s_waitcnt lgkmcnt(8)\n"
        "v_mfma_f32_16x16x32_bf16 v[28:31], v[168:171], v[184:187], v[28:31]\n"
        "ds_read_b128 v[176:179], v230 offset:0\n"
        "v_or3_b32 v210, v210, v211, v218\n"
        "v_mfma_f32_16x16x32_bf16 v[28:31], v[172:175], v[188:191], v[28:31]\n"
        "ds_read_b128 v[180:183], v231 offset:0\n"
        "v_perm_b32 v224, v209, v236, s60\n"
        "v_mfma_f32_16x16x32_bf16 v[24:27], v[164:167], v[188:191], v[24:27]\n"
        "ds_read_b32 v116, v224\n"
        "v_perm_b32 v225, v209, v236, s61\n"
        "v_mfma_f32_16x16x32_bf16 v[24:27], v[160:163], v[184:187], v[24:27]\n"
        "ds_read_b32 v117, v225\n"
        "v_perm_b32 v226, v209, v236, s62\n"
        "v_mfma_f32_16x16x32_bf16 v[20:23], v[152:155], v[184:187], v[20:23]\n"
        "ds_read_b32 v118, v226\n"
        "v_perm_b32 v227, v209, v236, s63\n"
        "v_mfma_f32_16x16x32_bf16 v[20:23], v[156:159], v[188:191], v[20:23]\n"
        "ds_read_b32 v119, v227\n"
        "v_perm_b32 v224, v212, v236, s60\n"
        "v_mfma_f32_16x16x32_bf16 v[16:19], v[148:151], v[188:191], v[16:19]\n"
        "ds_read_b32 v120, v224\n"
        "v_perm_b32 v225, v212, v236, s61\n"
        "v_mfma_f32_16x16x32_bf16 v[16:19], v[144:147], v[184:187], v[16:19]\n"
        "ds_read_b32 v121, v225\n"
        "v_or_b32 v210, v210, v219\n"
        "s_waitcnt lgkmcnt(14)\n"
        "v_mfma_f32_16x16x32_bf16 v[32:35], v[144:147], v[192:195], v[32:35]\n"
        "ds_read_b128 v[184:187], v230 offset:2048\n"
        "v_cmp_ne_u32 vcc, 0, v210\n"
        "v_mfma_f32_16x16x32_bf16 v[32:35], v[148:151], v[196:199], v[32:35]\n"
        "ds_read_b128 v[188:191], v231 offset:2048\n"
        "v_perm_b32 v226, v212, v236, s62\n"
        "v_mfma_f32_16x16x32_bf16 v[36:39], v[156:159], v[196:199], v[36:39]\n"
        "ds_read_b32 v122, v226\n"
        "v_perm_b32 v227, v212, v236, s63\n"
        "v_mfma_f32_16x16x32_bf16 v[36:39], v[152:155], v[192:195], v[36:39]\n"
        "ds_read_b32 v123, v227\n"
        "v_perm_b32 v224, v213, v236, s60\n"
        "v_mfma_f32_16x16x32_bf16 v[40:43], v[160:163], v[192:195], v[40:43]\n"
        "ds_read_b32 v124, v224\n"
        "v_perm_b32 v225, v213, v236, s61\n"
        "v_mfma_f32_16x16x32_bf16 v[40:43], v[164:167], v[196:199], v[40:43]\n"
        "ds_read_b32 v125, v225\n"
        "v_perm_b32 v226, v213, v236, s62\n"
        "v_mfma_f32_16x16x32_bf16 v[44:47], v[172:175], v[196:199], v[44:47]\n"
        "ds_read_b32 v126, v226\n"
        "v_perm_b32 v227, v213, v236, s63\n"
        "v_mfma_f32_16x16x32_bf16 v[44:47], v[168:171], v[192:195], v[44:47]\n"
        "ds_read_b32 v127, v227\n"
        "s_waitcnt lgkmcnt(15)\n"
        "v_mfma_f32_16x16x32_bf16 v[60:63], v[168:171], v[200:203], v[60:63]\n"
        "ds_read_b128 v[192:195], v230 offset:4096\n"
        "s_cbranch_vccnz .Ln_f6\n"
        ".Ln_b6:\n"
        "v_mfma_f32_16x16x32_bf16 v[60:63], v[172:175], v[204:207], v[60:63]\n"
        "ds_read_b128 v[196:199], v231 offset:4096\n"
        "v_perm_b32 v224, v216, v236, s60\n"
        "v_mfma_f32_16x16x32_bf16 v[56:59], v[164:167], v[204:207], v[56:59]\n"
        "ds_read_b32 v128, v224\n"
        "v_perm_b32 v225, v216, v236, s61\n"
        "v_mfma_f32_16x16x32_bf16 v[56:59], v[160:163], v[200:203], v[56:59]\n"
        "ds_read_b32 v129, v225\n"
        "v_perm_b32 v226, v216, v236, s62\n"
        "v_mfma_f32_16x16x32_bf16 v[52:55], v[152:155], v[200:203], v[52:55]\n"
        "ds_read_b32 v130, v226\n"
        "v_perm_b32 v227, v216, v236, s63\n"
        "v_mfma_f32_16x16x32_bf16 v[52:55], v[156:159], v[204:207], v[52:55]\n"
        "ds_read_b32 v131, v227\n"
        "v_perm_b32 v224, v217, v236, s60\n"
        "v_mfma_f32_16x16x32_bf16 v[48:51], v[148:151], v[204:207], v[48:51]\n"
        "ds_read_b32 v132, v224\n"
        "v_perm_b32 v225, v217, v236, s61\n"
        "v_mfma_f32_16x16x32_bf16 v[48:51], v[144:147], v[200:203], v[48:51]\n"
        "ds_read_b32 v133, v225\n"
        "s_waitcnt lgkmcnt(15)\n"
        "v_mfma_f32_16x16x32_bf16 v[64:67], v[144:147], v[176:179], v[64:67]\n"
        "ds_read_b128 v[200:203], v230 offset:6144\n"
        "v_mfma_f32_16x16x32_bf16 v[64:67], v[148:151], v[180:183], v[64:67]\n"
        "ds_read_b128 v[204:207], v231 offset:6144\n"
        "v_perm_b32 v226, v217, v236, s62\n"
        "v_mfma_f32_16x16x32_bf16 v[68:71], v[156:159], v[180:183], v[68:71]\n"
        "ds_read_b32 v134, v226\n"
        "v_perm_b32 v227, v217, v236, s63\n"
        "v_mfma_f32_16x16x32_bf16 v[68:71], v[152:155], v[176:179], v[68:71]\n"
        "ds_read_b32 v135, v227\n"
        "v_perm_b32 v224, v220, v236, s60\n"
        "v_mfma_f32_16x16x32_bf16 v[72:75], v[160:163], v[176:179], v[72:75]\n"
        "ds_read_b32 v136, v224\n"
        "v_perm_b32 v225, v220, v236, s61\n"
        "v_mfma_f32_16x16x32_bf16 v[72:75], v[164:167], v[180:183], v[72:75]\n"
        "ds_read_b32 v137, v225\n"
        "v_perm_b32 v226, v220, v236, s62\n"
        "s_waitcnt lgkmcnt(15)\n"
        "v_mfma_f32_16x16x32_bf16 v[84:87], v[164:167], v[188:191], v[84:87]\n"
        "ds_read_b32 v138, v226\n"
        "v_perm_b32 v227, v220, v236, s63\n"
        "v_mfma_f32_16x16x32_bf16 v[84:87], v[160:163], v[184:187], v[84:87]\n"
        "ds_read_b32 v139, v227\n"
        "v_perm_b32 v224, v221, v236, s60\n"
        "v_mfma_f32_16x16x32_bf16 v[80:83], v[152:155], v[184:187], v[80:83]\n"
        "ds_read_b32 v140, v224\n"
        "v_perm_b32 v225, v221, v236, s61\n"
        "v_mfma_f32_16x16x32_bf16 v[80:83], v[156:159], v[188:191], v[80:83]\n"
        "ds_read_b32 v141, v225\n"
        "v_perm_b32 v226, v221, v236, s62\n"
        "v_mfma_f32_16x16x32_bf16 v[76:79], v[148:151], v[188:191], v[76:79]\n"
        "ds_read_b32 v142, v226\n"
        "v_perm_b32 v227, v221, v236, s63\n"
        "v_mfma_f32_16x16x32_bf16 v[76:79], v[144:147], v[184:187], v[76:79]\n"
        "ds_read_b32 v143, v227\n"
        "s_waitcnt lgkmcnt(10)\n"
        "s_barrier\n"
        "v_mfma_f32_16x16x32_bf16 v[88:91], v[144:147], v[192:195], v[88:91]\n"
        "ds_read_b128 v[208:211], v232 offset:0\n"
        "v_mfma_f32_16x16x32_bf16 v[88:91], v[148:151], v[196:199], v[88:91]\n"
        "ds_read_b128 v[212:215], v233 offset:0\n"
        "v_mfma_f32_16x16x32_bf16 v[92:95], v[156:159], v[196:199], v[92:95]\n"
        "v_mfma_f32_16x16x32_bf16 v[92:95], v[152:155], v[192:195], v[92:95]\n"
        "ds_read_b128 v[216:219], v234 offset:0\n"
        "v_mfma_f32_16x16x32_bf16 v[96:99], v[160:163], v[192:195], v[96:99]\n"
        "ds_read_b128 v[220:223], v235 offset:0\n"
        "v_mfma_f32_16x16x32_bf16 v[96:99], v[164:167], v[196:199], v[96:99]\n"
        "v_mfma_f32_16x16x32_bf16 v[108:111], v[164:167], v[204:207], v[108:111]\n"
        "ds_read_b128 v[176:179], v228 offset:16384\n"
        "v_mfma_f32_16x16x32_bf16 v[108:111], v[160:163], v[200:203], v[108:111]\n"
        "ds_read_b128 v[180:183], v229 offset:16384\n"
        "v_mfma_f32_16x16x32_bf16 v[104:107], v[152:155], v[200:203], v[104:107]\n"
        "v_mfma_f32_16x16x32_bf16 v[104:107], v[156:159], v[204:207], v[104:107]\n"
        "ds_read_b128 v[184:187], v228 offset:18432\n"
        "v_mfma_f32_16x16x32_bf16 v[100:103], v[148:151], v[204:207], v[100:103]\n"
        "ds_read_b128 v[188:191], v229 offset:18432\n"
        "v_mfma_f32_16x16x32_bf16 v[100:103], v[144:147], v[200:203], v[100:103]\n"
        "s_waitcnt lgkmcnt(2)\n"
        "v_mfma_f32_16x16x32_bf16 v[0:3], v[112:115], v[176:179], v[0:3]\n"
        "ds_read_b128 v[192:195], v228 offset:20480\n"
        "v_bitop3_b32 v210, v210, v214, s34 bitop3:0x7e\n"
        "v_mfma_f32_16x16x32_bf16 v[0:3], v[116:119], v[180:183], v[0:3]\n"
        "ds_read_b128 v[196:199], v229 offset:20480\n"
        "v_bitop3_b32 v211, v211, v215, s39 bitop3:0x7e\n"
        "v_mfma_f32_16x16x32_bf16 v[4:7], v[124:127], v[180:183], v[4:7]\n"
        "ds_read_b128 v[200:203], v228 offset:22528\n"
        "v_bitop3_b32 v218, v218, v222, s34 bitop3:0x7e\n"
        "v_mfma_f32_16x16x32_bf16 v[4:7], v[120:123], v[176:179], v[4:7]\n"
        "ds_read_b128 v[204:207], v229 offset:22528\n"
        "v_perm_b32 v224, v208, v236, s60\n"
        "v_mfma_f32_16x16x32_bf16 v[8:11], v[128:131], v[176:179], v[8:11]\n"
        "ds_read_b32 v144, v224\n"
        "v_perm_b32 v225, v208, v236, s61\n"
        "v_mfma_f32_16x16x32_bf16 v[8:11], v[132:135], v[180:183], v[8:11]\n"
        "ds_read_b32 v145, v225\n"
        "v_perm_b32 v226, v208, v236, s62\n"
        "v_mfma_f32_16x16x32_bf16 v[12:15], v[140:143], v[180:183], v[12:15]\n"
        "ds_read_b32 v146, v226\n"
        "v_perm_b32 v227, v208, v236, s63\n"
        "v_mfma_f32_16x16x32_bf16 v[12:15], v[136:139], v[176:179], v[12:15]\n"
        "ds_read_b32 v147, v227\n"
        "v_bitop3_b32 v219, v219, v223, s39 bitop3:0x7e\n"
        "s_waitcnt lgkmcnt(8)\n"
        "v_mfma_f32_16x16x32_bf16 v[28:31], v[136:139], v[184:187], v[28:31]\n"
        "ds_read_b128 v[176:179], v230 offset:16384\n"
        "v_or3_b32 v210, v210, v211, v218\n"
        "v_mfma_f32_16x16x32_bf16 v[28:31], v[140:143], v[188:191], v[28:31]\n"
        "ds_read_b128 v[180:183], v231 offset:16384\n"
        "v_perm_b32 v224, v209, v236, s60\n"
        "v_mfma_f32_16x16x32_bf16 v[24:27], v[132:135], v[188:191], v[24:27]\n"
        "ds_read_b32 v148, v224\n"
        "v_perm_b32 v225, v209, v236, s61\n"
        "v_mfma_f32_16x16x32_bf16 v[24:27], v[128:131], v[184:187], v[24:27]\n"
        "ds_read_b32 v149, v225\n"
        "v_perm_b32 v226, v209, v236, s62\n"
        "v_mfma_f32_16x16x32_bf16 v[20:23], v[120:123], v[184:187], v[20:23]\n"
        "ds_read_b32 v150, v226\n"
        "v_perm_b32 v227, v209, v236, s63\n"
        "v_mfma_f32_16x16x32_bf16 v[20:23], v[124:127], v[188:191], v[20:23]\n"
        "ds_read_b32 v151, v227\n"
        "v_perm_b32 v224, v212, v236, s60\n"
        "v_mfma_f32_16x16x32_bf16 v[16:19], v[116:119], v[188:191], v[16:19]\n"
        "ds_read_b32 v152, v224\n"
        "v_perm_b32 v225, v212, v236, s61\n"
        "v_mfma_f32_16x16x32_bf16 v[16:19], v[112:115], v[184:187], v[16:19]\n"
        "ds_read_b32 v153, v225\n"
        ".Ln_tp1:\n"
        "s_cbranch_execz .Ln_ret\n"
        "v_or_b32 v210, v210, v219\n"
        "s_waitcnt lgkmcnt(14)\n"
        "v_mfma_f32_16x16x32_bf16 v[32:35], v[112:115], v[192:195], v[32:35]\n"
        "ds_read_b128 v[184:187], v230 offset:18432\n"
        "v_cmp_ne_u32 vcc, 0, v210\n"
        "v_mfma_f32_16x16x32_bf16 v[32:35], v[116:119], v[196:199], v[32:35]\n"
        "ds_read_b128 v[188:191], v231 offset:18432\n"
        "v_perm_b32 v226, v212, v236, s62\n"
        "v_mfma_f32_16x16x32_bf16 v[36:39], v[124:127], v[196:199], v[36:39]\n"
        "ds_read_b32 v154, v226\n"
        "v_perm_b32 v227, v212, v236, s63\n"
        "v_mfma_f32_16x16x32_bf16 v[36:39], v[120:123], v[192:195], v[36:39]\n"
        "ds_read_b32 v155, v227\n"
        "v_perm_b32 v224, v213, v236, s60\n"
        "v_mfma_f32_16x16x32_bf16 v[40:43], v[128:131], v[192:195], v[40:43]\n"
        "ds_read_b32 v156, v224\n"
        "v_perm_b32 v225, v213, v236, s61\n"
        "v_mfma_f32_16x16x32_bf16 v[40:43], v[132:135], v[196:199], v[40:43]\n"
        "ds_read_b32 v157, v225\n"
        "v_perm_b32 v226, v213, v236, s62\n"
        "v_mfma_f32_16x16x32_bf16 v[44:47], v[140:143], v[196:199], v[44:47]\n"
        "ds_read_b32 v158, v226\n"
        "v_perm_b32 v227, v213, v236, s63\n"
        "v_mfma_f32_16x16x32_bf16 v[44:47], v[136:139], v[192:195], v[44:47]\n"
        "ds_read_b32 v159, v227\n"
        "s_waitcnt lgkmcnt(15)\n"
        "v_mfma_f32_16x16x32_bf16 v[60:63], v[136:139], v[200:203], v[60:63]\n"
        "ds_read_b128 v[192:195], v230 offset:20480\n"
        "s_cbranch_vccnz .Ln_f7\n"
        ".Ln_b7:\n"
        "v_mfma_f32_16x16x32_bf16 v[60:63], v[140:143], v[204:207], v[60:63]\n"
        "ds_read_b128 v[196:199], v231 offset:20480\n"
        "v_perm_b32 v224, v216, v236, s60\n"
        "v_mfma_f32_16x16x32_bf16 v[56:59], v[132:135], v[204:207], v[56:59]\n"
        "ds_read_b32 v160, v224\n"
        "v_perm_b32 v225, v216, v236, s61\n"
        "v_mfma_f32_16x16x32_bf16 v[56:59], v[128:131], v[200:203], v[56:59]\n"
        "ds_read_b32 v161, v225\n"
        "v_perm_b32 v226, v216, v236, s62\n"
        "v_mfma_f32_16x16x32_bf16 v[52:55], v[120:123], v[200:203], v[52:55]\n"
        "ds_read_b32 v162, v226\n"
        "v_perm_b32 v227, v216, v236, s63\n"
        "v_mfma_f32_16x16x32_bf16 v[52:55], v[124:127], v[204:207], v[52:55]\n"
        "ds_read_b32 v163, v227\n"
        "v_perm_b32 v224, v217, v236, s60\n"
        "v_mfma_f32_16x16x32_bf16 v[48:51], v[116:119], v[204:207], v[48:51]\n"
        "ds_read_b32 v164, v224\n"
        "v_perm_b32 v225, v217, v236, s61\n"
        "v_mfma_f32_16x16x32_bf16 v[48:51], v[112:115], v[200:203], v[48:51]\n"
        "ds_read_b32 v165, v225\n"
        "s_waitcnt lgkmcnt(15)\n"
        "v_mfma_f32_16x16x32_bf16 v[64:67], v[112:115], v[176:179], v[64:67]\n"
        "ds_read_b128 v[200:203], v230 offset:22528\n"
        "v_mfma_f32_16x16x32_bf16 v[64:67], v[116:119], v[180:183], v[64:67]\n"
        "ds_read_b128 v[204:207], v231 offset:22528\n"
        "v_perm_b32 v226, v217, v236, s62\n"
        "v_mfma_f32_16x16x32_bf16 v[68:71], v[124:127], v[180:183], v[68:71]\n"
        "ds_read_b32 v166, v226\n"
        "v_perm_b32 v227, v217, v236, s63\n"
        "v_mfma_f32_16x16x32_bf16 v[68:71], v[120:123], v[176:179], v[68:71]\n"
        "ds_read_b32 v167, v227\n"
        "v_perm_b32 v224, v220, v236, s60\n"
        "v_mfma_f32_16x16x32_bf16 v[72:75], v[128:131], v[176:179], v[72:75]\n"
        "ds_read_b32 v168, v224\n"
        "v_perm_b32 v225, v220, v236, s61\n"
        "v_mfma_f32_16x16x32_bf16 v[72:75], v[132:135], v[180:183], v[72:75]\n"
        "ds_read_b32 v169, v225\n"
        "v_perm_b32 v226, v220, v236, s62\n"
        "s_waitcnt lgkmcnt(15)\n"
        "v_mfma_f32_16x16x32_bf16 v[84:87], v[132:135], v[188:191], v[84:87]\n"
        "ds_read_b32 v170, v226\n"
        "v_perm_b32 v227, v220, v236, s63\n"
        "v_mfma_f32_16x16x32_bf16 v[84:87], v[128:131], v[184:187], v[84:87]\n"
        "ds_read_b32 v171, v227\n"
        "v_perm_b32 v224, v221, v236, s60\n"
        "v_mfma_f32_16x16x32_bf16 v[80:83], v[120:123], v[184:187], v[80:83]\n"
        "ds_read_b32 v172, v224\n"
        "v_perm_b32 v225, v221, v236, s61\n"
        "v_mfma_f32_16x16x32_bf16 v[80:83], v[124:127], v[188:191], v[80:83]\n"
        "ds_read_b32 v173, v225\n"
        "v_perm_b32 v226, v221, v236, s62\n"
        "v_mfma_f32_16x16x32_bf16 v[76:79], v[116:119], v[188:191], v[76:79]\n"
        "ds_read_b32 v174, v226\n"
        "v_perm_b32 v227, v221, v236, s63\n"
        "v_mfma_f32_16x16x32_bf16 v[76:79], v[112:115], v[184:187], v[76:79]\n"
        "ds_read_b32 v175, v227\n"
        "s_waitcnt lgkmcnt(10)\n"
        "s_barrier\n"
        "v_mfma_f32_16x16x32_bf16 v[88:91], v[112:115], v[192:195], v[88:91]\n"
        "ds_read_b128 v[208:211], v232 offset:16384\n"
        "v_mfma_f32_16x16x32_bf16 v[88:91], v[116:119], v[196:199], v[88:91]\n"
        "ds_read_b128 v[212:215], v233 offset:16384\n"
        "v_mfma_f32_16x16x32_bf16 v[92:95], v[124:127], v[196:199], v[92:95]\n"
        "v_mfma_f32_16x16x32_bf16 v[92:95], v[120:123], v[192:195], v[92:95]\n"
        "ds_read_b128 v[216:219], v234 offset:16384\n"
        "v_mfma_f32_16x16x32_bf16 v[96:99], v[128:131], v[192:195], v[96:99]\n"
        "ds_read_b128 v[220:223], v235 offset:16384\n"
        "v_mfma_f32_16x16x32_bf16 v[96:99], v[132:135], v[196:199], v[96:99]\n"
        "v_mfma_f32_16x16x32_bf16 v[108:111], v[132:135], v[204:207], v[108:111]\n"
        "ds_read_b128 v[176:179], v228 offset:32768\n"
        "v_mfma_f32_16x16x32_bf16 v[108:111], v[128:131], v[200:203], v[108:111]\n"
        "ds_read_b128 v[180:183], v229 offset:32768\n"
        "v_mfma_f32_16x16x32_bf16 v[104:107], v[120:123], v[200:203], v[104:107]\n"
        "v_mfma_f32_16x16x32_bf16 v[104:107], v[124:127], v[204:207], v[104:107]\n"
        "ds_read_b128 v[184:187], v228 offset:34816\n"
        "v_mfma_f32_16x16x32_bf16 v[100:103], v[116:119], v[204:207], v[100:103]\n"
        "ds_read_b128 v[188:191], v229 offset:34816\n"
        "v_mfma_f32_16x16x32_bf16 v[100:103], v[112:115], v[200:203], v[100:103]\n"
        "s_waitcnt lgkmcnt(2)\n"
        "v_mfma_f32_16x16x32_bf16 v[0:3], v[144:147], v[176:179], v[0:3]\n"
        "ds_read_b128 v[192:195], v228 offset:36864\n"
        "v_bitop3_b32 v210, v210, v214, s34 bitop3:0x7e\n"
        "v_mfma_f32_16x16x32_bf16 v[0:3], v[148:151], v[180:183], v[0:3]\n"
        "ds_read_b128 v[196:199], v229 offset:36864\n"
        "v_bitop3_b32 v211, v211, v215, s39 bitop3:0x7e\n"
        "v_mfma_f32_16x16x32_bf16 v[4:7], v[156:159], v[180:183], v[4:7]\n"
        "ds_read_b128 v[200:203], v228 offset:38912\n"
        "v_bitop3_b32 v218, v218, v222, s34 bitop3:0x7e\n"
        "v_mfma_f32_16x16x32_bf16 v[4:7], v[152:155], v[176:179], v[4:7]\n"
        "ds_read_b128 v[204:207], v229 offset:38912\n"
        "v_perm_b32 v224, v208, v236, s60\n"
        "v_mfma_f32_16x16x32_bf16 v[8:11], v[160:163], v[176:179], v[8:11]\n"
        "ds_read_b32 v112, v224\n"
        "v_perm_b32 v225, v208, v236, s61\n"
        "v_mfma_f32_16x16x32_bf16 v[8:11], v[164:167], v[180:183], v[8:11]\n"
        "ds_read_b32 v113, v225\n"
        "v_perm_b32 v226, v208, v236, s62\n"
        "v_mfma_f32_16x16x32_bf16 v[12:15], v[172:175], v[180:183], v[12:15]\n"
        "ds_read_b32 v114, v226\n"
        "v_perm_b32 v227, v208, v236, s63\n"
        "v_mfma_f32_16x16x32_bf16 v[12:15], v[168:171], v[176:179], v[12:15]\n"
        "ds_read_b32 v115, v227\n"
        "v_bitop3_b32 v219, v219, v223, s39 bitop3:0x7e\n"
        "s_waitcnt lgkmcnt(8)\n"
        "v_mfma_f32_16x16x32_bf16 v[28:31], v[168:171], v[184:187], v[28:31]\n"
        "ds_read_b128 v[176:179], v230 offset:32768\n"
        "v_or3_b32 v210, v210, v211, v218\n"
        "v_mfma_f32_16x16x32_bf16 v[28:31], v[172:175], v[188:191], v[28:31]\n"
        "ds_read_b128 v[180:183], v231 offset:32768\n"
        "v_perm_b32 v224, v209, v236, s60\n"
        "v_mfma_f32_16x16x32_bf16 v[24:27], v[164:167], v[188:191], v[24:27]\n"
        "ds_read_b32 v116, v224\n"
        "v_perm_b32 v225, v209, v236, s61\n"
        "v_mfma_f32_16x16x32_bf16 v[24:27], v[160:163], v[184:187], v[24:27]\n"
        "ds_read_b32 v117, v225\n"
        "v_perm_b32 v226, v209, v236, s62\n"
        "v_mfma_f32_16x16x32_bf16 v[20:23], v[152:155], v[184:187], v[20:23]\n"
        "ds_read_b32 v118, v226\n"
        "v_perm_b32 v227, v209, v236, s63\n"
        "v_mfma_f32_16x16x32_bf16 v[20:23], v[156:159], v[188:191], v[20:23]\n"
        "ds_read_b32 v119, v227\n"
        "v_perm_b32 v224, v212, v236, s60\n"
        "v_mfma_f32_16x16x32_bf16 v[16:19], v[148:151], v[188:191], v[16:19]\n"
        "ds_read_b32 v120, v224\n"
        "v_perm_b32 v225, v212, v236, s61\n"
        "v_mfma_f32_16x16x32_bf16 v[16:19], v[144:147], v[184:187], v[16:19]\n"
        "ds_read_b32 v121, v225\n"
        "v_or_b32 v210, v210, v219\n"
        "s_waitcnt lgkmcnt(14)\n"
        "v_mfma_f32_16x16x32_bf16 v[32:35], v[144:147], v[192:195], v[32:35]\n"
        "ds_read_b128 v[184:187], v230 offset:34816\n"
        "v_cmp_ne_u32 vcc, 0, v210\n"
        "v_mfma_f32_16x16x32_bf16 v[32:35], v[148:151], v[196:199], v[32:35]\n"
        "ds_read_b128 v[188:191], v231 offset:34816\n"
        "v_perm_b32 v226, v212, v236, s62\n"
        "v_mfma_f32_16x16x32_bf16 v[36:39], v[156:159], v[196:199], v[36:39]\n"
        "ds_read_b32 v122, v226\n"
        "v_perm_b32 v227, v212, v236, s63\n"
        "v_mfma_f32_16x16x32_bf16 v[36:39], v[152:155], v[192:195], v[36:39]\n"
        "ds_read_b32 v123, v227\n"
        "v_perm_b32 v224, v213, v236, s60\n"
        "v_mfma_f32_16x16x32_bf16 v[40:43], v[160:163], v[192:195], v[40:43]\n"
        "ds_read_b32 v124, v224\n"
        "v_perm_b32 v225, v213, v236, s61\n"
        "v_mfma_f32_16x16x32_bf16 v[40:43], v[164:167], v[196:199], v[40:43]\n"
        "ds_read_b32 v125, v225\n"
        "v_perm_b32 v226, v213, v236, s62\n"
        "v_mfma_f32_16x16x32_bf16 v[44:47], v[172:175], v[196:199], v[44:47]\n"
        "ds_read_b32 v126, v226\n"
        "v_perm_b32 v227, v213, v236, s63\n"
        "v_mfma_f32_16x16x32_bf16 v[44:47], v[168:171], v[192:195], v[44:47]\n"
        "ds_read_b32 v127, v227\n"
        "s_waitcnt lgkmcnt(15)\n"
        "v_mfma_f32_16x16x32_bf16 v[60:63], v[168:171], v[200:203], v[60:63]\n"
        "ds_read_b128 v[192:195], v230 offset:36864\n"
        "s_cbranch_vccnz .Ln_f8\n"
        ".Ln_b8:\n"
        "v_mfma_f32_16x16x32_bf16 v[60:63], v[172:175], v[204:207], v[60:63]\n"
        "ds_read_b128 v[196:199], v231 offset:36864\n"
        "v_perm_b32 v224, v216, v236, s60\n"
        "v_mfma_f32_16x16x32_bf16 v[56:59], v[164:167], v[204:207], v[56:59]\n"
        "ds_read_b32 v128, v224\n"
        "v_perm_b32 v225, v216, v236, s61\n"
        "v_mfma_f32_16x16x32_bf16 v[56:59], v[160:163], v[200:203], v[56:59]\n"
        "ds_read_b32 v129, v225\n"
        "v_perm_b32 v226, v216, v236, s62\n"
        "v_mfma_f32_16x16x32_bf16 v[52:55], v[152:155], v[200:203], v[52:55]\n"
        "ds_read_b32 v130, v226\n"
        "v_perm_b32 v227, v216, v236, s63\n"
        "v_mfma_f32_16x16x32_bf16 v[52:55], v[156:159], v[204:207], v[52:55]\n"
        "ds_read_b32 v131, v227\n"
        "v_perm_b32 v224, v217, v236, s60\n"
        "v_mfma_f32_16x16x32_bf16 v[48:51], v[148:151], v[204:207], v[48:51]\n"
        "ds_read_b32 v132, v224\n"
        "v_perm_b32 v225, v217, v236, s61\n"
        "v_mfma_f32_16x16x32_bf16 v[48:51], v[144:147], v[200:203], v[48:51]\n"
        "ds_read_b32 v133, v225\n"
        "s_waitcnt lgkmcnt(15)\n"
        "v_mfma_f32_16x16x32_bf16 v[64:67], v[144:147], v[176:179], v[64:67]\n"
        "ds_read_b128 v[200:203], v230 offset:38912\n"
        "v_mfma_f32_16x16x32_bf16 v[64:67], v[148:151], v[180:183], v[64:67]\n"
        "ds_read_b128 v[204:207], v231 offset:38912\n"
        "v_perm_b32 v226, v217, v236, s62\n"
        "v_mfma_f32_16x16x32_bf16 v[68:71], v[156:159], v[180:183], v[68:71]\n"
        "ds_read_b32 v134, v226\n"
        "v_perm_b32 v227, v217, v236, s63\n"
        "v_mfma_f32_16x16x32_bf16 v[68:71], v[152:155], v[176:179], v[68:71]\n"
        "ds_read_b32 v135, v227\n"
        "v_perm_b32 v224, v220, v236, s60\n"
        "v_mfma_f32_16x16x32_bf16 v[72:75], v[160:163], v[176:179], v[72:75]\n"
        "ds_read_b32 v136, v224\n"
        "v_perm_b32 v225, v220, v236, s61\n"
        "v_mfma_f32_16x16x32_bf16 v[72:75], v[164:167], v[180:183], v[72:75]\n"
        "ds_read_b32 v137, v225\n"
        "v_perm_b32 v226, v220, v236, s62\n"
        "s_waitcnt lgkmcnt(15)\n"
        "v_mfma_f32_16x16x32_bf16 v[84:87], v[164:167], v[188:191], v[84:87]\n"
        "ds_read_b32 v138, v226\n"
        "v_perm_b32 v227, v220, v236, s63\n"
        "v_mfma_f32_16x16x32_bf16 v[84:87], v[160:163], v[184:187], v[84:87]\n"
        "ds_read_b32 v139, v227\n"
        "v_perm_b32 v224, v221, v236, s60\n"
        "v_mfma_f32_16x16x32_bf16 v[80:83], v[152:155], v[184:187], v[80:83]\n"
        "ds_read_b32 v140, v224\n"
        "v_perm_b32 v225, v221, v236, s61\n"
        "v_mfma_f32_16x16x32_bf16 v[80:83], v[156:159], v[188:191], v[80:83]\n"
        "ds_read_b32 v141, v225\n"
        "v_perm_b32 v226, v221, v236, s62\n"
        "v_mfma_f32_16x16x32_bf16 v[76:79], v[148:151], v[188:191], v[76:79]\n"
        "ds_read_b32 v142, v226\n"
        "v_perm_b32 v227, v221, v236, s63\n"
        "v_mfma_f32_16x16x32_bf16 v[76:79], v[144:147], v[184:187], v[76:79]\n"
        "ds_read_b32 v143, v227\n"
        "s_waitcnt lgkmcnt(10)\n"
        "s_barrier\n"
        "v_mfma_f32_16x16x32_bf16 v[88:91], v[144:147], v[192:195], v[88:91]\n"
        "ds_read_b128 v[208:211], v232 offset:32768\n"
        "v_mfma_f32_16x16x32_bf16 v[88:91], v[148:151], v[196:199], v[88:91]\n"
        "ds_read_b128 v[212:215], v233 offset:32768\n"
        "v_mfma_f32_16x16x32_bf16 v[92:95], v[156:159], v[196:199], v[92:95]\n"
        "v_mfma_f32_16x16x32_bf16 v[92:95], v[152:155], v[192:195], v[92:95]\n"
        "ds_read_b128 v[216:219], v234 offset:32768\n"
        "v_mfma_f32_16x16x32_bf16 v[96:99], v[160:163], v[192:195], v[96:99]\n"
        "ds_read_b128 v[220:223], v235 offset:32768\n"
        "v_mfma_f32_16x16x32_bf16 v[96:99], v[164:167], v[196:199], v[96:99]\n"
        "v_mfma_f32_16x16x32_bf16 v[108:111], v[164:167], v[204:207], v[108:111]\n"
        "ds_read_b128 v[176:179], v228 offset:0\n"
        "v_mfma_f32_16x16x32_bf16 v[108:111], v[160:163], v[200:203], v[108:111]\n"
        "ds_read_b128 v[180:183], v229 offset:0\n"
        "v_mfma_f32_16x16x32_bf16 v[104:107], v[152:155], v[200:203], v[104:107]\n"
        "v_mfma_f32_16x16x32_bf16 v[104:107], v[156:159], v[204:207], v[104:107]\n"
        "ds_read_b128 v[184:187], v228 offset:2048\n"
        "v_mfma_f32_16x16x32_bf16 v[100:103], v[148:151], v[204:207], v[100:103]\n"
        "ds_read_b128 v[188:191], v229 offset:2048\n"
        "v_mfma_f32_16x16x32_bf16 v[100:103], v[144:147], v[200:203], v[100:103]\n"
        "s_waitcnt lgkmcnt(2)\n"
        "v_mfma_f32_16x16x32_bf16 v[0:3], v[112:115], v[176:179], v[0:3]\n"
        "ds_read_b128 v[192:195], v228 offset:4096\n"
        "v_bitop3_b32 v210, v210, v214, s34 bitop3:0x7e\n"
        "v_mfma_f32_16x16x32_bf16 v[0:3], v[116:119], v[180:183], v[0:3]\n"
        "ds_read_b128 v[196:199], v229 offset:4096\n"
        "v_bitop3_b32 v211, v211, v215, s39 bitop3:0x7e\n"
        "v_mfma_f32_16x16x32_bf16 v[4:7], v[124:127], v[180:183], v[4:7]\n"
        "ds_read_b128 v[200:203], v228 offset:6144\n"
        "v_bitop3_b32 v218, v218, v222, s34 bitop3:0x7e\n"
        "v_mfma_f32_16x16x32_bf16 v[4:7], v[120:123], v[176:179], v[4:7]\n"
        "ds_read_b128 v[204:207], v229 offset:6144\n"
        "v_perm_b32 v224, v208, v236, s60\n"
        "v_mfma_f32_16x16x32_bf16 v[8:11], v[128:131], v[176:179], v[8:11]\n"
        "ds_read_b32 v144, v224\n"
        "v_perm_b32 v225, v208, v236, s61\n"
        "v_mfma_f32_16x16x32_bf16 v[8:11], v[132:135], v[180:183], v[8:11]\n"
        "ds_read_b32 v145, v225\n"
        "v_perm_b32 v226, v208, v236, s62\n"
        "v_mfma_f32_16x16x32_bf16 v[12:15], v[140:143], v[180:183], v[12:15]\n"
        "ds_read_b32 v146, v226\n"
        "v_perm_b32 v227, v208, v236, s63\n"
        "v_mfma_f32_16x16x32_bf16 v[12:15], v[136:139], v[176:179], v[12:15]\n"
        "ds_read_b32 v147, v227\n"
        "v_bitop3_b32 v219, v219, v223, s39 bitop3:0x7e\n"
        "s_waitcnt lgkmcnt(8)\n"
        "v_mfma_f32_16x16x32_bf16 v[28:31], v[136:139], v[184:187], v[28:31]\n"
        "ds_read_b128 v[176:179], v230 offset:0\n"
        "v_or3_b32 v210, v210, v211, v218\n"
        "v_mfma_f32_16x16x32_bf16 v[28:31], v[140:143], v[188:191], v[28:31]\n"
        "ds_read_b128 v[180:183], v231 offset:0\n"
        "v_perm_b32 v224, v209, v236, s60\n"
        "v_mfma_f32_16x16x32_bf16 v[24:27], v[132:135], v[188:191], v[24:27]\n"
        "ds_read_b32 v148, v224\n"
        "v_perm_b32 v225, v209, v236, s61\n"
        "v_mfma_f32_16x16x32_bf16 v[24:27], v[128:131], v[184:187], v[24:27]\n"
        "ds_read_b32 v149, v225\n"
        "v_perm_b32 v226, v209, v236, s62\n"
        "v_mfma_f32_16x16x32_bf16 v[20:23], v[120:123], v[184:187], v[20:23]\n"
        "ds_read_b32 v150, v226\n"
        "v_perm_b32 v227, v209, v236, s63\n"
        "v_mfma_f32_16x16x32_bf16 v[20:23], v[124:127], v[188:191], v[20:23]\n"
        "ds_read_b32 v151, v227\n"
        "v_perm_b32 v224, v212, v236, s60\n"
        "v_mfma_f32_16x16x32_bf16 v[16:19], v[116:119], v[188:191], v[16:19]\n"
        "ds_read_b32 v152, v224\n"
        "v_perm_b32 v225, v212, v236, s61\n"
        "v_mfma_f32_16x16x32_bf16 v[16:19], v[112:115], v[184:187], v[16:19]\n"
        "ds_read_b32 v153, v225\n"
        "v_or_b32 v210, v210, v219\n"
        "s_waitcnt lgkmcnt(14)\n"
        "v_mfma_f32_16x16x32_bf16 v[32:35], v[112:115], v[192:195], v[32:35]\n"
        "ds_read_b128 v[184:187], v230 offset:2048\n"
        "v_cmp_ne_u32 vcc, 0, v210\n"
        "v_mfma_f32_16x16x32_bf16 v[32:35], v[116:119], v[196:199], v[32:35]\n"
        "ds_read_b128 v[188:191], v231 offset:2048\n"
        "v_perm_b32 v226, v212, v236, s62\n"
        "v_mfma_f32_16x16x32_bf16 v[36:39], v[124:127], v[196:199], v[36:39]\n"
        "ds_read_b32 v154, v226\n"
        "v_perm_b32 v227, v212, v236, s63\n"
        "v_mfma_f32_16x16x32_bf16 v[36:39], v[120:123], v[192:195], v[36:39]\n"
        "ds_read_b32 v155, v227\n"
        "v_perm_b32 v224, v213, v236, s60\n"
        "v_mfma_f32_16x16x32_bf16 v[40:43], v[128:131], v[192:195], v[40:43]\n"
        "ds_read_b32 v156, v224\n"
        "v_perm_b32 v225, v213, v236, s61\n"
        "v_mfma_f32_16x16x32_bf16 v[40:43], v[132:135], v[196:199], v[40:43]\n"
        "ds_read_b32 v157, v225\n"
        "v_perm_b32 v226, v213, v236, s62\n"
        "v_mfma_f32_16x16x32_bf16 v[44:47], v[140:143], v[196:199], v[44:47]\n"
        "ds_read_b32 v158, v226\n"
        "v_perm_b32 v227, v213, v236, s63\n"
        "v_mfma_f32_16x16x32_bf16 v[44:47], v[136:139], v[192:195], v[44:47]\n"
        "ds_read_b32 v159, v227\n"
        "s_waitcnt lgkmcnt(15)\n"
        "v_mfma_f32_16x16x32_bf16 v[60:63], v[136:139], v[200:203], v[60:63]\n"
        "ds_read_b128 v[192:195], v230 offset:4096\n"
        "s_cbranch_vccnz .Ln_f9\n"
        ".Ln_b9:\n"
        "v_mfma_f32_16x16x32_bf16 v[60:63], v[140:143], v[204:207], v[60:63]\n"
        "ds_read_b128 v[196:199], v231 offset:4096\n"
        "v_perm_b32 v224, v216, v236, s60\n"
        "v_mfma_f32_16x16x32_bf16 v[56:59], v[132:135], v[204:207], v[56:59]\n"
        "ds_read_b32 v160, v224\n"
        "v_perm_b32 v225, v216, v236, s61\n"
        "v_mfma_f32_16x16x32_bf16 v[56:59], v[128:131], v[200:203], v[56:59]\n"
        "ds_read_b32 v161, v225\n"
        "v_perm_b32 v226, v216, v236, s62\n"
        "v_mfma_f32_16x16x32_bf16 v[52:55], v[120:123], v[200:203], v[52:55]\n"
        "ds_read_b32 v162, v226\n"
        "v_perm_b32 v227, v216, v236, s63\n"
        "v_mfma_f32_16x16x32_bf16 v[52:55], v[124:127], v[204:207], v[52:55]\n"
        "ds_read_b32 v163, v227\n"
        "v_perm_b32 v224, v217, v236, s60\n"
        "v_mfma_f32_16x16x32_bf16 v[48:51], v[116:119], v[204:207], v[48:51]\n"
        "ds_read_b32 v164, v224\n"
        "v_perm_b32 v225, v217, v236, s61\n"
        "v_mfma_f32_16x16x32_bf16 v[48:51], v[112:115], v[200:203], v[48:51]\n"
        "ds_read_b32 v165, v225\n"
        "s_waitcnt lgkmcnt(15)\n"
        "v_mfma_f32_16x16x32_bf16 v[64:67], v[112:115], v[176:179], v[64:67]\n"
        "ds_read_b128 v[200:203], v230 offset:6144\n"
        "v_mfma_f32_16x16x32_bf16 v[64:67], v[116:119], v[180:183], v[64:67]\n"
        "ds_read_b128 v[204:207], v231 offset:6144\n"
        "v_perm_b32 v226, v217, v236, s62\n"
        "v_mfma_f32_16x16x32_bf16 v[68:71], v[124:127], v[180:183], v[68:71]\n"
        "ds_read_b32 v166, v226\n"
        "v_perm_b32 v227, v217, v236, s63\n"
        "v_mfma_f32_16x16x32_bf16 v[68:71], v[120:123], v[176:179], v[68:71]\n"
        "ds_read_b32 v167, v227\n"
        "v_perm_b32 v224, v220, v236, s60\n"
        "v_mfma_f32_16x16x32_bf16 v[72:75], v[128:131], v[176:179], v[72:75]\n"
        "ds_read_b32 v168, v224\n"
        "v_perm_b32 v225, v220, v236, s61\n"
        "v_mfma_f32_16x16x32_bf16 v[72:75], v[132:135], v[180:183], v[72:75]\n"
        "ds_read_b32 v169, v225\n"
        "v_perm_b32 v226, v220, v236, s62\n"
        "s_waitcnt lgkmcnt(15)\n"
        "v_mfma_f32_16x16x32_bf16 v[84:87], v[132:135], v[188:191], v[84:87]\n"
        "ds_read_b32 v170, v226\n"
        "v_perm_b32 v227, v220, v236, s63\n"
        "v_mfma_f32_16x16x32_bf16 v[84:87], v[128:131], v[184:187], v[84:87]\n"
        "ds_read_b32 v171, v227\n"
        "v_perm_b32 v224, v221, v236, s60\n"
        "v_mfma_f32_16x16x32_bf16 v[80:83], v[120:123], v[184:187], v[80:83]\n"
        "ds_read_b32 v172, v224\n"
        "v_perm_b32 v225, v221, v236, s61\n"
        "v_mfma_f32_16x16x32_bf16 v[80:83], v[124:127], v[188:191], v[80:83]\n"
        "ds_read_b32 v173, v225\n"
        "v_perm_b32 v226, v221, v236, s62\n"
        "v_mfma_f32_16x16x32_bf16 v[76:79], v[116:119], v[188:191], v[76:79]\n"
        "ds_read_b32 v174, v226\n"
        "v_perm_b32 v227, v221, v236, s63\n"
        "v_mfma_f32_16x16x32_bf16 v[76:79], v[112:115], v[184:187], v[76:79]\n"
        "ds_read_b32 v175, v227\n"
        "s_waitcnt lgkmcnt(10)\n"
        "s_barrier\n"
        "v_mfma_f32_16x16x32_bf16 v[88:91], v[112:115], v[192:195], v[88:91]\n"
        "ds_read_b128 v[208:211], v232 offset:0\n"
        "v_mfma_f32_16x16x32_bf16 v[88:91], v[116:119], v[196:199], v[88:91]\n"
        "ds_read_b128 v[212:215], v233 offset:0\n"
        "v_mfma_f32_16x16x32_bf16 v[92:95], v[124:127], v[196:199], v[92:95]\n"
        "v_mfma_f32_16x16x32_bf16 v[92:95], v[120:123], v[192:195], v[92:95]\n"
        "ds_read_b128 v[216:219], v234 offset:0\n"
        "v_mfma_f32_16x16x32_bf16 v[96:99], v[128:131], v[192:195], v[96:99]\n"
        "ds_read_b128 v[220:223], v235 offset:0\n"
        "v_mfma_f32_16x16x32_bf16 v[96:99], v[132:135], v[196:199], v[96:99]\n"
        "v_mfma_f32_16x16x32_bf16 v[108:111], v[132:135], v[204:207], v[108:111]\n"
        "ds_read_b128 v[176:179], v228 offset:16384\n"
        "v_mfma_f32_16x16x32_bf16 v[108:111], v[128:131], v[200:203], v[108:111]\n"
        "ds_read_b128 v[180:183], v229 offset:16384\n"
        "v_mfma_f32_16x16x32_bf16 v[104:107], v[120:123], v[200:203], v[104:107]\n"
        "v_mfma_f32_16x16x32_bf16 v[104:107], v[124:127], v[204:207], v[104:107]\n"
        "ds_read_b128 v[184:187], v228 offset:18432\n"
        "v_mfma_f32_16x16x32_bf16 v[100:103], v[116:119], v[204:207], v[100:103]\n"
        "ds_read_b128 v[188:191], v229 offset:18432\n"
        "v_mfma_f32_16x16x32_bf16 v[100:103], v[112:115], v[200:203], v[100:103]\n"
        "s_waitcnt lgkmcnt(2)\n"
        "v_mfma_f32_16x16x32_bf16 v[0:3], v[144:147], v[176:179], v[0:3]\n"
        "ds_read_b128 v[192:195], v228 offset:20480\n"
        "v_bitop3_b32 v210, v210, v214, s34 bitop3:0x7e\n"
        "v_mfma_f32_16x16x32_bf16 v[0:3], v[148:151], v[180:183], v[0:3]\n"
        "ds_read_b128 v[196:199], v229 offset:20480\n"
        "v_bitop3_b32 v211, v211, v215, s39 bitop3:0x7e\n"
        "v_mfma_f32_16x16x32_bf16 v[4:7], v[156:159], v[180:183], v[4:7]\n"
        "ds_read_b128 v[200:203], v228 offset:22528\n"
        "v_bitop3_b32 v218, v218, v222, s34 bitop3:0x7e\n"
        "v_mfma_f32_16x16x32_bf16 v[4:7], v[152:155], v[176:179], v[4:7]\n"
        "ds_read_b128 v[204:207], v229 offset:22528\n"
        "v_perm_b32 v224, v208, v236, s60\n"
        "v_mfma_f32_16x16x32_bf16 v[8:11], v[160:163], v[176:179], v[8:11]\n"
        "ds_read_b32 v112, v224\n"
        "v_perm_b32 v225, v208, v236, s61\n"
        "v_mfma_f32_16x16x32_bf16 v[8:11], v[164:167], v[180:183], v[8:11]\n"
        "ds_read_b32 v113, v225\n"
        "v_perm_b32 v226, v208, v236, s62\n"
        "v_mfma_f32_16x16x32_bf16 v[12:15], v[172:175], v[180:183], v[12:15]\n"
        "ds_read_b32 v114, v226\n"
        "v_perm_b32 v227, v208, v236, s63\n"
        "v_mfma_f32_16x16x32_bf16 v[12:15], v[168:171], v[176:179], v[12:15]\n"
        "ds_read_b32 v115, v227\n"
        "v_bitop3_b32 v219, v219, v223, s39 bitop3:0x7e\n"
        "s_waitcnt lgkmcnt(8)\n"
        "v_mfma_f32_16x16x32_bf16 v[28:31], v[168:171], v[184:187], v[28:31]\n"
        "ds_read_b128 v[176:179], v230 offset:16384\n"
        "v_or3_b32 v210, v210, v211, v218\n"
        "v_mfma_f32_16x16x32_bf16 v[28:31], v[172:175], v[188:191], v[28:31]\n"
        "ds_read_b128 v[180:183], v231 offset:16384\n"
        "v_perm_b32 v224, v209, v236, s60\n"
        "v_mfma_f32_16x16x32_bf16 v[24:27], v[164:167], v[188:191], v[24:27]\n"
        "ds_read_b32 v116, v224\n"
        "v_perm_b32 v225, v209, v236, s61\n"
        "v_mfma_f32_16x16x32_bf16 v[24:27], v[160:163], v[184:187], v[24:27]\n"
        "ds_read_b32 v117, v225\n"
        "v_perm_b32 v226, v209, v236, s62\n"
        "v_mfma_f32_16x16x32_bf16 v[20:23], v[152:155], v[184:187], v[20:23]\n"
        "ds_read_b32 v118, v226\n"
        "v_perm_b32 v227, v209, v236, s63\n"
        "v_mfma_f32_16x16x32_bf16 v[20:23], v[156:159], v[188:191], v[20:23]\n"
        "ds_read_b32 v119, v227\n"
        "v_perm_b32 v224, v212, v236, s60\n"
        "v_mfma_f32_16x16x32_bf16 v[16:19], v[148:151], v[188:191], v[16:19]\n"
        "ds_read_b32 v120, v224\n"
        "v_perm_b32 v225, v212, v236, s61\n"
        "v_mfma_f32_16x16x32_bf16 v[16:19], v[144:147], v[184:187], v[16:19]\n"
        "ds_read_b32 v121, v225\n"
        "v_or_b32 v210, v210, v219\n"
        "s_waitcnt lgkmcnt(14)\n"
        "v_mfma_f32_16x16x32_bf16 v[32:35], v[144:147], v[192:195], v[32:35]\n"
        "ds_read_b128 v[184:187], v230 offset:18432\n"
        "v_cmp_ne_u32 vcc, 0, v210\n"
        "v_mfma_f32_16x16x32_bf16 v[32:35], v[148:151], v[196:199], v[32:35]\n"
        "ds_read_b128 v[188:191], v231 offset:18432\n"
        "v_perm_b32 v226, v212, v236, s62\n"
        "v_mfma_f32_16x16x32_bf16 v[36:39], v[156:159], v[196:199], v[36:39]\n"
        "ds_read_b32 v122, v226\n"
        "v_perm_b32 v227, v212, v236, s63\n"
        "v_mfma_f32_16x16x32_bf16 v[36:39], v[152:155], v[192:195], v[36:39]\n"
        "ds_read_b32 v123, v227\n"
        "v_perm_b32 v224, v213, v236, s60\n"
        "v_mfma_f32_16x16x32_bf16 v[40:43], v[160:163], v[192:195], v[40:43]\n"
        "ds_read_b32 v124, v224\n"
        "v_perm_b32 v225, v213, v236, s61\n"
        "v_mfma_f32_16x16x32_bf16 v[40:43], v[164:167], v[196:199], v[40:43]\n"
        "ds_read_b32 v125, v225\n"
        "v_perm_b32 v226, v213, v236, s62\n"
        "v_mfma_f32_16x16x32_bf16 v[44:47], v[172:175], v[196:199], v[44:47]\n"
        "ds_read_b32 v126, v226\n"
        "v_perm_b32 v227, v213, v236, s63\n"
        "v_mfma_f32_16x16x32_bf16 v[44:47], v[168:171], v[192:195], v[44:47]\n"
        "ds_read_b32 v127, v227\n"
        "s_waitcnt lgkmcnt(15)\n"
        "v_mfma_f32_16x16x32_bf16 v[60:63], v[168:171], v[200:203], v[60:63]\n"
        "ds_read_b128 v[192:195], v230 offset:20480\n"
        "s_cbranch_vccnz .Ln_f10\n"
        ".Ln_b10:\n"
        "v_mfma_f32_16x16x32_bf16 v[60:63], v[172:175], v[204:207], v[60:63]\n"
        "ds_read_b128 v[196:199], v231 offset:20480\n"
        "v_perm_b32 v224, v216, v236, s60\n"
        "v_mfma_f32_16x16x32_bf16 v[56:59], v[164:167], v[204:207], v[56:59]\n"
        "ds_read_b32 v128, v224\n"
        "v_perm_b32 v225, v216, v236, s61\n"
        "v_mfma_f32_16x16x32_bf16 v[56:59], v[160:163], v[200:203], v[56:59]\n"
        "ds_read_b32 v129, v225\n"
        "v_perm_b32 v226, v216, v236, s62\n"
        "v_mfma_f32_16x16x32_bf16 v[52:55], v[152:155], v[200:203], v[52:55]\n"
        "ds_read_b32 v130, v226\n"
        "v_perm_b32 v227, v216, v236, s63\n"
        "v_mfma_f32_16x16x32_bf16 v[52:55], v[156:159], v[204:207], v[52:55]\n"
        "ds_read_b32 v131, v227\n"
        "v_perm_b32 v224, v217, v236, s60\n"
        "v_mfma_f32_16x16x32_bf16 v[48:51], v[148:151], v[204:207], v[48:51]\n"
        "ds_read_b32 v132, v224\n"
        "v_perm_b32 v225, v217, v236, s61\n"
        "v_mfma_f32_16x16x32_bf16 v[48:51], v[144:147], v[200:203], v[48:51]\n"
        "ds_read_b32 v133, v225\n"
        "s_waitcnt lgkmcnt(15)\n"
        "v_mfma_f32_16x16x32_bf16 v[64:67], v[144:147], v[176:179], v[64:67]\n"
        "ds_read_b128 v[200:203], v230 offset:22528\n"
        "v_mfma_f32_16x16x32_bf16 v[64:67], v[148:151], v[180:183], v[64:67]\n"
        "ds_read_b128 v[204:207], v231 offset:22528\n"
        "v_perm_b32 v226, v217, v236, s62\n"
        "v_mfma_f32_16x16x32_bf16 v[68:71], v[156:159], v[180:183], v[68:71]\n"
        "ds_read_b32 v134, v226\n"
        "v_perm_b32 v227, v217, v236, s63\n"
        "v_mfma_f32_16x16x32_bf16 v[68:71], v[152:155], v[176:179], v[68:71]\n"
        "ds_read_b32 v135, v227\n"
        "v_perm_b32 v224, v220, v236, s60\n"
        "v_mfma_f32_16x16x32_bf16 v[72:75], v[160:163], v[176:179], v[72:75]\n"
        "ds_read_b32 v136, v224\n"
        ".Ln_tp2:\n"
        "s_cbranch_execz .Ln_ret\n"
        "v_perm_b32 v225, v220, v236, s61\n"
        "v_mfma_f32_16x16x32_bf16 v[72:75], v[164:167], v[180:183], v[72:75]\n"
        "ds_read_b32 v137, v225\n"
        "v_perm_b32 v226, v220, v236, s62\n"
        "s_waitcnt lgkmcnt(15)\n"
        "v_mfma_f32_16x16x32_bf16 v[84:87], v[164:167], v[188:191], v[84:87]\n"
        "ds_read_b32 v138, v226\n"
        "v_perm_b32 v227, v220, v236, s63\n"
        "v_mfma_f32_16x16x32_bf16 v[84:87], v[160:163], v[184:187], v[84:87]\n"
        "ds_read_b32 v139, v227\n"
        "v_perm_b32 v224, v221, v236, s60\n"
        "v_mfma_f32_16x16x32_bf16 v[80:83], v[152:155], v[184:187], v[80:83]\n"
        "ds_read_b32 v140, v224\n"
        "v_perm_b32 v225, v221, v236, s61\n"
        "v_mfma_f32_16x16x32_bf16 v[80:83], v[156:159], v[188:191], v[80:83]\n"
        "ds_read_b32 v141, v225\n"
        "v_perm_b32 v226, v221, v236, s62\n"
        "v_mfma_f32_16x16x32_bf16 v[76:79], v[148:151], v[188:191], v[76:79]\n"
        "ds_read_b32 v142, v226\n"
        "v_perm_b32 v227, v221, v236, s63\n"
        "v_mfma_f32_16x16x32_bf16 v[76:79], v[144:147], v[184:187], v[76:79]\n"
        "ds_read_b32 v143, v227\n"
        "s_waitcnt lgkmcnt(10)\n"
        "s_barrier\n"
        "v_mfma_f32_16x16x32_bf16 v[88:91], v[144:147], v[192:195], v[88:91]\n"
        "ds_read_b128 v[208:211], v232 offset:16384\n"
        "v_mfma_f32_16x16x32_bf16 v[88:91], v[148:151], v[196:199], v[88:91]\n"
        "ds_read_b128 v[212:215], v233 offset:16384\n"
        "v_mfma_f32_16x16x32_bf16 v[92:95], v[156:159], v[196:199], v[92:95]\n"
        "v_mfma_f32_16x16x32_bf16 v[92:95], v[152:155], v[192:195], v[92:95]\n"
        "ds_read_b128 v[216:219], v234 offset:16384\n"
        "v_mfma_f32_16x16x32_bf16 v[96:99], v[160:163], v[192:195], v[96:99]\n"
        "ds_read_b128 v[220:223], v235 offset:16384\n"
        "v_mfma_f32_16x16x32_bf16 v[96:99], v[164:167], v[196:199], v[96:99]\n"
        "v_mfma_f32_16x16x32_bf16 v[108:111], v[164:167], v[204:207], v[108:111]\n"
        "ds_read_b128 v[176:179], v228 offset:32768\n"
        "v_mfma_f32_16x16x32_bf16 v[108:111], v[160:163], v[200:203], v[108:111]\n"
        "ds_read_b128 v[180:183], v229 offset:32768\n"
        "v_mfma_f32_16x16x32_bf16 v[104:107], v[152:155], v[200:203], v[104:107]\n"
        "v_mfma_f32_16x16x32_bf16 v[104:107], v[156:159], v[204:207], v[104:107]\n"
        "ds_read_b128 v[184:187], v228 offset:34816\n"
        "v_mfma_f32_16x16x32_bf16 v[100:103], v[148:151], v[204:207], v[100:103]\n"
        "ds_read_b128 v[188:191], v229 offset:34816\n"
        "v_mfma_f32_16x16x32_bf16 v[100:103], v[144:147], v[200:203], v[100:103]\n"
        "s_waitcnt lgkmcnt(2)\n"
        "v_mfma_f32_16x16x32_bf16 v[0:3], v[112:115], v[176:179], v[0:3]\n"
        "ds_read_b128 v[192:195], v228 offset:36864\n"
        "v_bitop3_b32 v210, v210, v214, s34 bitop3:0x7e\n"
        "v_mfma_f32_16x16x32_bf16 v[0:3], v[116:119], v[180:183], v[0:3]\n"
        "ds_read_b128 v[196:199], v229 offset:36864\n"
        "v_bitop3_b32 v211, v211, v215, s39 bitop3:0x7e\n"
        "v_mfma_f32_16x16x32_bf16 v[4:7], v[124:127], v[180:183], v[4:7]\n"
        "ds_read_b128 v[200:203], v228 offset:38912\n"
        "v_bitop3_b32 v218, v218, v222, s34 bitop3:0x7e\n"
        "v_mfma_f32_16x16x32_bf16 v[4:7], v[120:123], v[176:179], v[4:7]\n"
        "ds_read_b128 v[204:207], v229 offset:38912\n"
        "v_perm_b32 v224, v208, v236, s60\n"
        "v_mfma_f32_16x16x32_bf16 v[8:11], v[128:131], v[176:179], v[8:11]\n"
        "ds_read_b32 v144, v224\n"
        "v_perm_b32 v225, v208, v236, s61\n"
        "v_mfma_f32_16x16x32_bf16 v[8:11], v[132:135], v[180:183], v[8:11]\n"
        "ds_read_b32 v145, v225\n"
        "v_perm_b32 v226, v208, v236, s62\n"
        "v_mfma_f32_16x16x32_bf16 v[12:15], v[140:143], v[180:183], v[12:15]\n"
        "ds_read_b32 v146, v226\n"
        "v_perm_b32 v227, v208, v236, s63\n"
        "v_mfma_f32_16x16x32_bf16 v[12:15], v[136:139], v[176:179], v[12:15]\n"
        "ds_read_b32 v147, v227\n"
        "v_bitop3_b32 v219, v219, v223, s39 bitop3:0x7e\n"
        "s_waitcnt lgkmcnt(8)\n"
        "v_mfma_f32_16x16x32_bf16 v[28:31], v[136:139], v[184:187], v[28:31]\n"
        "ds_read_b128 v[176:179], v230 offset:32768\n"
        "v_or3_b32 v210, v210, v211, v218\n"
        "v_mfma_f32_16x16x32_bf16 v[28:31], v[140:143], v[188:191], v[28:31]\n"
        "ds_read_b128 v[180:183], v231 offset:32768\n"
        "v_perm_b32 v224, v209, v236, s60\n"
        "v_mfma_f32_16x16x32_bf16 v[24:27], v[132:135], v[188:191], v[24:27]\n"
        "ds_read_b32 v148, v224\n"
        "v_perm_b32 v225, v209, v236, s61\n"
        "v_mfma_f32_16x16x32_bf16 v[24:27], v[128:131], v[184:187], v[24:27]\n"
        "ds_read_b32 v149, v225\n"
        "v_perm_b32 v226, v209, v236, s62\n"
        "v_mfma_f32_16x16x32_bf16 v[20:23], v[120:123], v[184:187], v[20:23]\n"
        "ds_read_b32 v150, v226\n"
        "v_perm_b32 v227, v209, v236, s63\n"
        "v_mfma_f32_16x16x32_bf16 v[20:23], v[124:127], v[188:191], v[20:23]\n"
        "ds_read_b32 v151, v227\n"
        "v_perm_b32 v224, v212, v236, s60\n"
        "v_mfma_f32_16x16x32_bf16 v[16:19], v[116:119], v[188:191], v[16:19]\n"
        "ds_read_b32 v152, v224\n"
        "v_perm_b32 v225, v212, v236, s61\n"
        "v_mfma_f32_16x16x32_bf16 v[16:19], v[112:115], v[184:187], v[16:19]\n"
        "ds_read_b32 v153, v225\n"
        "v_or_b32 v210, v210, v219\n"
        "s_waitcnt lgkmcnt(14)\n"
        "v_mfma_f32_16x16x32_bf16 v[32:35], v[112:115], v[192:195], v[32:35]\n"
        "ds_read_b128 v[184:187], v230 offset:34816\n"
        "v_cmp_ne_u32 vcc, 0, v210\n"
        "v_mfma_f32_16x16x32_bf16 v[32:35], v[116:119], v[196:199], v[32:35]\n"
        "ds_read_b128 v[188:191], v231 offset:34816\n"
        "v_perm_b32 v226, v212, v236, s62\n"
        "v_mfma_f32_16x16x32_bf16 v[36:39], v[124:127], v[196:199], v[36:39]\n"
        "ds_read_b32 v154, v226\n"
        "v_perm_b32 v227, v212, v236, s63\n"
        "v_mfma_f32_16x16x32_bf16 v[36:39], v[120:123], v[192:195], v[36:39]\n"
        "ds_read_b32 v155, v227\n"
        "v_perm_b32 v224, v213, v236, s60\n"
        "v_mfma_f32_16x16x32_bf16 v[40:43], v[128:131], v[192:195], v[40:43]\n"
        "ds_read_b32 v156, v224\n"
        "v_perm_b32 v225, v213, v236, s61\n"
        "v_mfma_f32_16x16x32_bf16 v[40:43], v[132:135], v[196:199], v[40:43]\n"
        "ds_read_b32 v157, v225\n"
        "v_perm_b32 v226, v213, v236, s62\n"
        "v_mfma_f32_16x16x32_bf16 v[44:47], v[140:143], v[196:199], v[44:47]\n"
        "ds_read_b32 v158, v226\n"
        "v_perm_b32 v227, v213, v236, s63\n"
        "v_mfma_f32_16x16x32_bf16 v[44:47], v[136:139], v[192:195], v[44:47]\n"
        "ds_read_b32 v159, v227\n"
        "s_waitcnt lgkmcnt(15)\n"
        "v_mfma_f32_16x16x32_bf16 v[60:63], v[136:139], v[200:203], v[60:63]\n"
        "ds_read_b128 v[192:195], v230 offset:36864\n"
        "s_cbranch_vccnz .Ln_f11\n"
        ".Ln_b11:\n"
        "v_mfma_f32_16x16x32_bf16 v[60:63], v[140:143], v[204:207], v[60:63]\n"
        "ds_read_b128 v[196:199], v231 offset:36864\n"
        "v_perm_b32 v224, v216, v236, s60\n"
        "v_mfma_f32_16x16x32_bf16 v[56:59], v[132:135], v[204:207], v[56:59]\n"
        "ds_read_b32 v160, v224\n"
        "v_perm_b32 v225, v216, v236, s61\n"
        "v_mfma_f32_16x16x32_bf16 v[56:59], v[128:131], v[200:203], v[56:59]\n"
        "ds_read_b32 v161, v225\n"
        "v_perm_b32 v226, v216, v236, s62\n"
        "v_mfma_f32_16x16x32_bf16 v[52:55], v[120:123], v[200:203], v[52:55]\n"
        "ds_read_b32 v162, v226\n"
        "v_perm_b32 v227, v216, v236, s63\n"
        "v_mfma_f32_16x16x32_bf16 v[52:55], v[124:127], v[204:207], v[52:55]\n"
        "ds_read_b32 v163, v227\n"
        "v_perm_b32 v224, v217, v236, s60\n"
        "v_mfma_f32_16x16x32_bf16 v[48:51], v[116:119], v[204:207], v[48:51]\n"
        "ds_read_b32 v164, v224\n"
        "v_perm_b32 v225, v217, v236, s61\n"
        "v_mfma_f32_16x16x32_bf16 v[48:51], v[112:115], v[200:203], v[48:51]\n"
        "ds_read_b32 v165, v225\n"
        "s_waitcnt lgkmcnt(15)\n"
        "v_mfma_f32_16x16x32_bf16 v[64:67], v[112:115], v[176:179], v[64:67]\n"
        "ds_read_b128 v[200:203], v230 offset:38912\n"
        "v_mfma_f32_16x16x32_bf16 v[64:67], v[116:119], v[180:183], v[64:67]\n"
        "ds_read_b128 v[204:207], v231 offset:38912\n"
        "v_perm_b32 v226, v217, v236, s62\n"
        "v_mfma_f32_16x16x32_bf16 v[68:71], v[124:127], v[180:183], v[68:71]\n"
        "ds_read_b32 v166, v226\n"
        "v_perm_b32 v227, v217, v236, s63\n"
        "v_mfma_f32_16x16x32_bf16 v[68:71], v[120:123], v[176:179], v[68:71]\n"
        "ds_read_b32 v167, v227\n"
        "v_perm_b32 v224, v220, v236, s60\n"
        "v_mfma_f32_16x16x32_bf16 v[72:75], v[128:131], v[176:179], v[72:75]\n"
        "ds_read_b32 v168, v224\n"
        "v_perm_b32 v225, v220, v236, s61\n"
        "v_mfma_f32_16x16x32_bf16 v[72:75], v[132:135], v[180:183], v[72:75]\n"
        "ds_read_b32 v169, v225\n"
        "v_perm_b32 v226, v220, v236, s62\n"
        "s_waitcnt lgkmcnt(15)\n"
        "v_mfma_f32_16x16x32_bf16 v[84:87], v[132:135], v[188:191], v[84:87]\n"
        "ds_read_b32 v170, v226\n"
        "v_perm_b32 v227, v220, v236, s63\n"
        "v_mfma_f32_16x16x32_bf16 v[84:87], v[128:131], v[184:187], v[84:87]\n"
        "ds_read_b32 v171, v227\n"
        "v_perm_b32 v224, v221, v236, s60\n"
        "v_mfma_f32_16x16x32_bf16 v[80:83], v[120:123], v[184:187], v[80:83]\n"
        "ds_read_b32 v172, v224\n"
        "v_perm_b32 v225, v221, v236, s61\n"
        "v_mfma_f32_16x16x32_bf16 v[80:83], v[124:127], v[188:191], v[80:83]\n"
        "ds_read_b32 v173, v225\n"
        "v_perm_b32 v226, v221, v236, s62\n"
        "v_mfma_f32_16x16x32_bf16 v[76:79], v[116:119], v[188:191], v[76:79]\n"
        "ds_read_b32 v174, v226\n"
        "v_perm_b32 v227, v221, v236, s63\n"
        "v_mfma_f32_16x16x32_bf16 v[76:79], v[112:115], v[184:187], v[76:79]\n"
        "ds_read_b32 v175, v227\n"
        "s_waitcnt lgkmcnt(10)\n"
        "s_barrier\n"
        "v_mfma_f32_16x16x32_bf16 v[88:91], v[112:115], v[192:195], v[88:91]\n"
        "ds_read_b128 v[208:211], v232 offset:32768\n"
        "v_mfma_f32_16x16x32_bf16 v[88:91], v[116:119], v[196:199], v[88:91]\n"
        "ds_read_b128 v[212:215], v233 offset:32768\n"
        "v_mfma_f32_16x16x32_bf16 v[92:95], v[124:127], v[196:199], v[92:95]\n"
        "v_mfma_f32_16x16x32_bf16 v[92:95], v[120:123], v[192:195], v[92:95]\n"
        "ds_read_b128 v[216:219], v234 offset:32768\n"
        "v_mfma_f32_16x16x32_bf16 v[96:99], v[128:131], v[192:195], v[96:99]\n"
        "ds_read_b128 v[220:223], v235 offset:32768\n"
        "v_mfma_f32_16x16x32_bf16 v[96:99], v[132:135], v[196:199], v[96:99]\n"
        "v_mfma_f32_16x16x32_bf16 v[108:111], v[132:135], v[204:207], v[108:111]\n"
        "ds_read_b128 v[176:179], v228 offset:0\n"
        "v_mfma_f32_16x16x32_bf16 v[108:111], v[128:131], v[200:203], v[108:111]\n"
        "ds_read_b128 v[180:183], v229 offset:0\n"
        "v_mfma_f32_16x16x32_bf16 v[104:107], v[120:123], v[200:203], v[104:107]\n"
        "v_mfma_f32_16x16x32_bf16 v[104:107], v[124:127], v[204:207], v[104:107]\n"
        "ds_read_b128 v[184:187], v228 offset:2048\n"
        "v_mfma_f32_16x16x32_bf16 v[100:103], v[116:119], v[204:207], v[100:103]\n"
        "ds_read_b128 v[188:191], v229 offset:2048\n"
        "v_mfma_f32_16x16x32_bf16 v[100:103], v[112:115], v[200:203], v[100:103]\n"
        "s_waitcnt lgkmcnt(2)\n"
        "v_mfma_f32_16x16x32_bf16 v[0:3], v[144:147], v[176:179], v[0:3]\n"
        "ds_read_b128 v[192:195], v228 offset:4096\n"
        "v_bitop3_b32 v210, v210, v214, s34 bitop3:0x7e\n"
        "v_mfma_f32_16x16x32_bf16 v[0:3], v[148:151], v[180:183], v[0:3]\n"
        "ds_read_b128 v[196:199], v229 offset:4096\n"
        "v_bitop3_b32 v211, v211, v215, s39 bitop3:0x7e\n"
        "v_mfma_f32_16x16x32_bf16 v[4:7], v[156:159], v[180:183], v[4:7]\n"
        "ds_read_b128 v[200:203], v228 offset:6144\n"
        "v_bitop3_b32 v218, v218, v222, s34 bitop3:0x7e\n"
        "v_mfma_f32_16x16x32_bf16 v[4:7], v[152:155], v[176:179], v[4:7]\n"
        "ds_read_b128 v[204:207], v229 offset:6144\n"
        "v_perm_b32 v224, v208, v236, s60\n"
        "v_mfma_f32_16x16x32_bf16 v[8:11], v[160:163], v[176:179], v[8:11]\n"
        "ds_read_b32 v112, v224\n"
        "v_perm_b32 v225, v208, v236, s61\n"
        "v_mfma_f32_16x16x32_bf16 v[8:11], v[164:167], v[180:183], v[8:11]\n"
        "ds_read_b32 v113, v225\n"
        "v_perm_b32 v226, v208, v236, s62\n"
        "v_mfma_f32_16x16x32_bf16 v[12:15], v[172:175], v[180:183], v[12:15]\n"
        "ds_read_b32 v114, v226\n"
        "v_perm_b32 v227, v208, v236, s63\n"
        "v_mfma_f32_16x16x32_bf16 v[12:15], v[168:171], v[176:179], v[12:15]\n"
        "ds_read_b32 v115, v227\n"
        "v_bitop3_b32 v219, v219, v223, s39 bitop3:0x7e\n"
        "s_waitcnt lgkmcnt(8)\n"
        "v_mfma_f32_16x16x32_bf16 v[28:31], v[168:171], v[184:187], v[28:31]\n"
        "ds_read_b128 v[176:179], v230 offset:0\n"
        "v_or3_b32 v210, v210, v211, v218\n"
        "v_mfma_f32_16x16x32_bf16 v[28:31], v[172:175], v[188:191], v[28:31]\n"
        "ds_read_b128 v[180:183], v231 offset:0\n"
        "v_perm_b32 v224, v209, v236, s60\n"
        "v_mfma_f32_16x16x32_bf16 v[24:27], v[164:167], v[188:191], v[24:27]\n"
        "ds_read_b32 v116, v224\n"
        "v_perm_b32 v225, v209, v236, s61\n"
        "v_mfma_f32_16x16x32_bf16 v[24:27], v[160:163], v[184:187], v[24:27]\n"
        "ds_read_b32 v117, v225\n"
        "v_perm_b32 v226, v209, v236, s62\n"
        "v_mfma_f32_16x16x32_bf16 v[20:23], v[152:155], v[184:187], v[20:23]\n"
        "ds_read_b32 v118, v226\n"
        "v_perm_b32 v227, v209, v236, s63\n"
        "v_mfma_f32_16x16x32_bf16 v[20:23], v[156:159], v[188:191], v[20:23]\n"
        "ds_read_b32 v119, v227\n"
        "v_perm_b32 v224, v212, v236, s60\n"
        "v_mfma_f32_16x16x32_bf16 v[16:19], v[148:151], v[188:191], v[16:19]\n"
        "ds_read_b32 v120, v224\n"
        "v_perm_b32 v225, v212, v236, s61\n"
        "v_mfma_f32_16x16x32_bf16 v[16:19], v[144:147], v[184:187], v[16:19]\n"
        "ds_read_b32 v121, v225\n"
        "v_or_b32 v210, v210, v219\n"
        "s_waitcnt lgkmcnt(14)\n"
        "v_mfma_f32_16x16x32_bf16 v[32:35], v[144:147], v[192:195], v[32:35]\n"
        "ds_read_b128 v[184:187], v230 offset:2048\n"
        "v_cmp_ne_u32 vcc, 0, v210\n"
        "v_mfma_f32_16x16x32_bf16 v[32:35], v[148:151], v[196:199], v[32:35]\n"
        "ds_read_b128 v[188:191], v231 offset:2048\n"
        "v_perm_b32 v226, v212, v236, s62\n"
        "v_mfma_f32_16x16x32_bf16 v[36:39], v[156:159], v[196:199], v[36:39]\n"
        "ds_read_b32 v122, v226\n"
        "v_perm_b32 v227, v212, v236, s63\n"
        "v_mfma_f32_16x16x32_bf16 v[36:39], v[152:155], v[192:195], v[36:39]\n"
        "ds_read_b32 v123, v227\n"
        "v_perm_b32 v224, v213, v236, s60\n"
        "v_mfma_f32_16x16x32_bf16 v[40:43], v[160:163], v[192:195], v[40:43]\n"
        "ds_read_b32 v124, v224\n"
        "v_perm_b32 v225, v213, v236, s61\n"
        "v_mfma_f32_16x16x32_bf16 v[40:43], v[164:167], v[196:199], v[40:43]\n"
        "ds_read_b32 v125, v225\n"
        "v_perm_b32 v226, v213, v236, s62\n"
        "v_mfma_f32_16x16x32_bf16 v[44:47], v[172:175], v[196:199], v[44:47]\n"
        "ds_read_b32 v126, v226\n"
        "v_perm_b32 v227, v213, v236, s63\n"
        "v_mfma_f32_16x16x32_bf16 v[44:47], v[168:171], v[192:195], v[44:47]\n"
        "ds_read_b32 v127, v227\n"
        "s_waitcnt lgkmcnt(15)\n"
        "v_mfma_f32_16x16x32_bf16 v[60:63], v[168:171], v[200:203], v[60:63]\n"
        "ds_read_b128 v[192:195], v230 offset:4096\n"
        "s_cbranch_vccnz .Ln_f12\n"
        ".Ln_b12:\n"
        "v_mfma_f32_16x16x32_bf16 v[60:63], v[172:175], v[204:207], v[60:63]\n"
        "ds_read_b128 v[196:199], v231 offset:4096\n"
        "v_perm_b32 v224, v216, v236, s60\n"
        "v_mfma_f32_16x16x32_bf16 v[56:59], v[164:167], v[204:207], v[56:59]\n"
        "ds_read_b32 v128, v224\n"
        "v_perm_b32 v225, v216, v236, s61\n"
        "v_mfma_f32_16x16x32_bf16 v[56:59], v[160:163], v[200:203], v[56:59]\n"
        "ds_read_b32 v129, v225\n"
        "v_perm_b32 v226, v216, v236, s62\n"
        "v_mfma_f32_16x16x32_bf16 v[52:55], v[152:155], v[200:203], v[52:55]\n"
        "ds_read_b32 v130, v226\n"
        "v_perm_b32 v227, v216, v236, s63\n"
        "v_mfma_f32_16x16x32_bf16 v[52:55], v[156:159], v[204:207], v[52:55]\n"
        "ds_read_b32 v131, v227\n"
        "v_perm_b32 v224, v217, v236, s60\n"
        "v_mfma_f32_16x16x32_bf16 v[48:51], v[148:151], v[204:207], v[48:51]\n"
        "ds_read_b32 v132, v224\n"
        "v_perm_b32 v225, v217, v236, s61\n"
        "v_mfma_f32_16x16x32_bf16 v[48:51], v[144:147], v[200:203], v[48:51]\n"
        "ds_read_b32 v133, v225\n"
        "s_waitcnt lgkmcnt(15)\n"
        "v_mfma_f32_16x16x32_bf16 v[64:67], v[144:147], v[176:179], v[64:67]\n"
        "ds_read_b128 v[200:203], v230 offset:6144\n"
        "v_mfma_f32_16x16x32_bf16 v[64:67], v[148:151], v[180:183], v[64:67]\n"
        "ds_read_b128 v[204:207], v231 offset:6144\n"
        "v_perm_b32 v226, v217, v236, s62\n"
        "v_mfma_f32_16x16x32_bf16 v[68:71], v[156:159], v[180:183], v[68:71]\n"
        "ds_read_b32 v134, v226\n"
        "v_perm_b32 v227, v217, v236, s63\n"
        "v_mfma_f32_16x16x32_bf16 v[68:71], v[152:155], v[176:179], v[68:71]\n"
        "ds_read_b32 v135, v227\n"
        "v_perm_b32 v224, v220, v236, s60\n"
        "v_mfma_f32_16x16x32_bf16 v[72:75], v[160:163], v[176:179], v[72:75]\n"
        "ds_read_b32 v136, v224\n"
        "v_perm_b32 v225, v220, v236, s61\n"
        "v_mfma_f32_16x16x32_bf16 v[72:75], v[164:167], v[180:183], v[72:75]\n"
        "ds_read_b32 v137, v225\n"
        "v_perm_b32 v226, v220, v236, s62\n"
        "s_waitcnt lgkmcnt(15)\n"
        "v_mfma_f32_16x16x32_bf16 v[84:87], v[164:167], v[188:191], v[84:87]\n"
        "ds_read_b32 v138, v226\n"
        "v_perm_b32 v227, v220, v236, s63\n"
        "v_mfma_f32_16x16x32_bf16 v[84:87], v[160:163], v[184:187], v[84:87]\n"
        "ds_read_b32 v139, v227\n"
        "v_perm_b32 v224, v221, v236, s60\n"
        "v_mfma_f32_16x16x32_bf16 v[80:83], v[152:155], v[184:187], v[80:83]\n"
        "ds_read_b32 v140, v224\n"
        "v_perm_b32 v225, v221, v236, s61\n"
        "v_mfma_f32_16x16x32_bf16 v[80:83], v[156:159], v[188:191], v[80:83]\n"
        "ds_read_b32 v141, v225\n"
        "v_perm_b32 v226, v221, v236, s62\n"
        "v_mfma_f32_16x16x32_bf16 v[76:79], v[148:151], v[188:191], v[76:79]\n"
        "ds_read_b32 v142, v226\n"
        "v_perm_b32 v227, v221, v236, s63\n"
        "v_mfma_f32_16x16x32_bf16 v[76:79], v[144:147], v[184:187], v[76:79]\n"
        "ds_read_b32 v143, v227\n"
        "s_waitcnt lgkmcnt(10)\n"
        "s_barrier\n"
        "v_mfma_f32_16x16x32_bf16 v[88:91], v[144:147], v[192:195], v[88:91]\n"
        "ds_read_b128 v[208:211], v232 offset:0\n"
        "v_mfma_f32_16x16x32_bf16 v[88:91], v[148:151], v[196:199], v[88:91]\n"
        "ds_read_b128 v[212:215], v233 offset:0\n"
        "v_mfma_f32_16x16x32_bf16 v[92:95], v[156:159], v[196:199], v[92:95]\n"
        "v_mfma_f32_16x16x32_bf16 v[92:95], v[152:155], v[192:195], v[92:95]\n"
        "ds_read_b128 v[216:219], v234 offset:0\n"
        "v_mfma_f32_16x16x32_bf16 v[96:99], v[160:163], v[192:195], v[96:99]\n"
        "ds_read_b128 v[220:223], v235 offset:0\n"
        "v_mfma_f32_16x16x32_bf16 v[96:99], v[164:167], v[196:199], v[96:99]\n"
        "v_mfma_f32_16x16x32_bf16 v[108:111], v[164:167], v[204:207], v[108:111]\n"
        "ds_read_b128 v[176:179], v228 offset:16384\n"
        "v_mfma_f32_16x16x32_bf16 v[108:111], v[160:163], v[200:203], v[108:111]\n"
        "ds_read_b128 v[180:183], v229 offset:16384\n"
        "v_mfma_f32_16x16x32_bf16 v[104:107], v[152:155], v[200:203], v[104:107]\n"
        "v_mfma_f32_16x16x32_bf16 v[104:107], v[156:159], v[204:207], v[104:107]\n"
        "ds_read_b128 v[184:187], v228 offset:18432\n"
        "v_mfma_f32_16x16x32_bf16 v[100:103], v[148:151], v[204:207], v[100:103]\n"
        "ds_read_b128 v[188:191], v229 offset:18432\n"
        "v_mfma_f32_16x16x32_bf16 v[100:103], v[144:147], v[200:203], v[100:103]\n"
        "s_waitcnt lgkmcnt(2)\n"
        "v_mfma_f32_16x16x32_bf16 v[0:3], v[112:115], v[176:179], v[0:3]\n"
        "ds_read_b128 v[192:195], v228 offset:20480\n"
        "v_bitop3_b32 v210, v210, v214, s34 bitop3:0x7e\n"
        "v_mfma_f32_16x16x32_bf16 v[0:3], v[116:119], v[180:183], v[0:3]\n"
        "ds_read_b128 v[196:199], v229 offset:20480\n"
        "v_bitop3_b32 v211, v211, v215, s39 bitop3:0x7e\n"
        "v_mfma_f32_16x16x32_bf16 v[4:7], v[124:127], v[180:183], v[4:7]\n"
        "ds_read_b128 v[200:203], v228 offset:22528\n"
        "v_bitop3_b32 v218, v218, v222, s34 bitop3:0x7e\n"
        "v_mfma_f32_16x16x32_bf16 v[4:7], v[120:123], v[176:179], v[4:7]\n"
        "ds_read_b128 v[204:207], v229 offset:22528\n"
        "v_perm_b32 v224, v208, v236, s60\n"
        "v_mfma_f32_16x16x32_bf16 v[8:11], v[128:131], v[176:179], v[8:11]\n"
        "ds_read_b32 v144, v224\n"
        "v_perm_b32 v225, v208, v236, s61\n"
        "v_mfma_f32_16x16x32_bf16 v[8:11], v[132:135], v[180:183], v[8:11]\n"
        "ds_read_b32 v145, v225\n"
        "v_perm_b32 v226, v208, v236, s62\n"
        "v_mfma_f32_16x16x32_bf16 v[12:15], v[140:143], v[180:183], v[12:15]\n"
        "ds_read_b32 v146, v226\n"
        "v_perm_b32 v227, v208, v236, s63\n"
        "v_mfma_f32_16x16x32_bf16 v[12:15], v[136:139], v[176:179], v[12:15]\n"
        "ds_read_b32 v147, v227\n"
        "v_bitop3_b32 v219, v219, v223, s39 bitop3:0x7e\n"
        "s_waitcnt lgkmcnt(8)\n"
        "v_mfma_f32_16x16x32_bf16 v[28:31], v[136:139], v[184:187], v[28:31]\n"
        "ds_read_b128 v[176:179], v230 offset:16384\n"
        "v_or3_b32 v210, v210, v211, v218\n"
        "v_mfma_f32_16x16x32_bf16 v[28:31], v[140:143], v[188:191], v[28:31]\n"
        "ds_read_b128 v[180:183], v231 offset:16384\n"
        "v_perm_b32 v224, v209, v236, s60\n"
        "v_mfma_f32_16x16x32_bf16 v[24:27], v[132:135], v[188:191], v[24:27]\n"
        "ds_read_b32 v148, v224\n"
        "v_perm_b32 v225, v209, v236, s61\n"
        "v_mfma_f32_16x16x32_bf16 v[24:27], v[128:131], v[184:187], v[24:27]\n"
        "ds_read_b32 v149, v225\n"
        "v_perm_b32 v226, v209, v236, s62\n"
        "v_mfma_f32_16x16x32_bf16 v[20:23], v[120:123], v[184:187], v[20:23]\n"
        "ds_read_b32 v150, v226\n"
        "v_perm_b32 v227, v209, v236, s63\n"
        "v_mfma_f32_16x16x32_bf16 v[20:23], v[124:127], v[188:191], v[20:23]\n"
        "ds_read_b32 v151, v227\n"
        "v_perm_b32 v224, v212, v236, s60\n"
        "v_mfma_f32_16x16x32_bf16 v[16:19], v[116:119], v[188:191], v[16:19]\n"
        "ds_read_b32 v152, v224\n"
        "v_perm_b32 v225, v212, v236, s61\n"
        "v_mfma_f32_16x16x32_bf16 v[16:19], v[112:115], v[184:187], v[16:19]\n"
        "ds_read_b32 v153, v225\n"
        "v_or_b32 v210, v210, v219\n"
        "s_waitcnt lgkmcnt(14)\n"
        "v_mfma_f32_16x16x32_bf16 v[32:35], v[112:115], v[192:195], v[32:35]\n"
        "ds_read_b128 v[184:187], v230 offset:18432\n"
        "v_cmp_ne_u32 vcc, 0, v210\n"
        "v_mfma_f32_16x16x32_bf16 v[32:35], v[116:119], v[196:199], v[32:35]\n"
        "ds_read_b128 v[188:191], v231 offset:18432\n"
        "v_perm_b32 v226, v212, v236, s62\n"
        "v_mfma_f32_16x16x32_bf16 v[36:39], v[124:127], v[196:199], v[36:39]\n"
        "ds_read_b32 v154, v226\n"
        "v_perm_b32 v227, v212, v236, s63\n"
        "v_mfma_f32_16x16x32_bf16 v[36:39], v[120:123], v[192:195], v[36:39]\n"
        "ds_read_b32 v155, v227\n"
        "v_perm_b32 v224, v213, v236, s60\n"
        "v_mfma_f32_16x16x32_bf16 v[40:43], v[128:131], v[192:195], v[40:43]\n"
        "ds_read_b32 v156, v224\n"
        "v_perm_b32 v225, v213, v236, s61\n"
        "v_mfma_f32_16x16x32_bf16 v[40:43], v[132:135], v[196:199], v[40:43]\n"
        "ds_read_b32 v157, v225\n"
        "v_perm_b32 v226, v213, v236, s62\n"
        "v_mfma_f32_16x16x32_bf16 v[44:47], v[140:143], v[196:199], v[44:47]\n"
        "ds_read_b32 v158, v226\n"
        "v_perm_b32 v227, v213, v236, s63\n"
        "v_mfma_f32_16x16x32_bf16 v[44:47], v[136:139], v[192:195], v[44:47]\n"
        "ds_read_b32 v159, v227\n"
        "s_waitcnt lgkmcnt(15)\n"
        "v_mfma_f32_16x16x32_bf16 v[60:63], v[136:139], v[200:203], v[60:63]\n"
        "ds_read_b128 v[192:195], v230 offset:20480\n"
        "s_cbranch_vccnz .Ln_f13\n"
        ".Ln_b13:\n"
        "v_mfma_f32_16x16x32_bf16 v[60:63], v[140:143], v[204:207], v[60:63]\n"
        "ds_read_b128 v[196:199], v231 offset:20480\n"
        "v_perm_b32 v224, v216, v236, s60\n"
        "v_mfma_f32_16x16x32_bf16 v[56:59], v[132:135], v[204:207], v[56:59]\n"
        "ds_read_b32 v160, v224\n"
        "v_perm_b32 v225, v216, v236, s61\n"
        "v_mfma_f32_16x16x32_bf16 v[56:59], v[128:131], v[200:203], v[56:59]\n"
        "ds_read_b32 v161, v225\n"
        "v_perm_b32 v226, v216, v236, s62\n"
        "v_mfma_f32_16x16x32_bf16 v[52:55], v[120:123], v[200:203], v[52:55]\n"
        "ds_read_b32 v162, v226\n"
        "v_perm_b32 v227, v216, v236, s63\n"
        "v_mfma_f32_16x16x32_bf16 v[52:55], v[124:127], v[204:207], v[52:55]\n"
        "ds_read_b32 v163, v227\n"
        "v_perm_b32 v224, v217, v236, s60\n"
        "v_mfma_f32_16x16x32_bf16 v[48:51], v[116:119], v[204:207], v[48:51]\n"
        "ds_read_b32 v164, v224\n"
        "v_perm_b32 v225, v217, v236, s61\n"
        "v_mfma_f32_16x16x32_bf16 v[48:51], v[112:115], v[200:203], v[48:51]\n"
        "ds_read_b32 v165, v225\n"
        "s_waitcnt lgkmcnt(15)\n"
        "v_mfma_f32_16x16x32_bf16 v[64:67], v[112:115], v[176:179], v[64:67]\n"
        "ds_read_b128 v[200:203], v230 offset:22528\n"
        "v_mfma_f32_16x16x32_bf16 v[64:67], v[116:119], v[180:183], v[64:67]\n"
        "ds_read_b128 v[204:207], v231 offset:22528\n"
        "v_perm_b32 v226, v217, v236, s62\n"
        "v_mfma_f32_16x16x32_bf16 v[68:71], v[124:127], v[180:183], v[68:71]\n"
        "ds_read_b32 v166, v226\n"
        "v_perm_b32 v227, v217, v236, s63\n"
        "v_mfma_f32_16x16x32_bf16 v[68:71], v[120:123], v[176:179], v[68:71]\n"
        "ds_read_b32 v167, v227\n"
        "v_perm_b32 v224, v220, v236, s60\n"
        "v_mfma_f32_16x16x32_bf16 v[72:75], v[128:131], v[176:179], v[72:75]\n"
        "ds_read_b32 v168, v224\n"
        "v_perm_b32 v225, v220, v236, s61\n"
        "v_mfma_f32_16x16x32_bf16 v[72:75], v[132:135], v[180:183], v[72:75]\n"
        "ds_read_b32 v169, v225\n"
        "v_perm_b32 v226, v220, v236, s62\n"
        "s_waitcnt lgkmcnt(15)\n"
        "v_mfma_f32_16x16x32_bf16 v[84:87], v[132:135], v[188:191], v[84:87]\n"
        "ds_read_b32 v170, v226\n"
        "v_perm_b32 v227, v220, v236, s63\n"
        "v_mfma_f32_16x16x32_bf16 v[84:87], v[128:131], v[184:187], v[84:87]\n"
        "ds_read_b32 v171, v227\n"
        "v_perm_b32 v224, v221, v236, s60\n"
        "v_mfma_f32_16x16x32_bf16 v[80:83], v[120:123], v[184:187], v[80:83]\n"
        "ds_read_b32 v172, v224\n"
        "v_perm_b32 v225, v221, v236, s61\n"
        "v_mfma_f32_16x16x32_bf16 v[80:83], v[124:127], v[188:191], v[80:83]\n"
        "ds_read_b32 v173, v225\n"
        "v_perm_b32 v226, v221, v236, s62\n"
        "v_mfma_f32_16x16x32_bf16 v[76:79], v[116:119], v[188:191], v[76:79]\n"
        "ds_read_b32 v174, v226\n"
        "v_perm_b32 v227, v221, v236, s63\n"
        "v_mfma_f32_16x16x32_bf16 v[76:79], v[112:115], v[184:187], v[76:79]\n"
        "ds_read_b32 v175, v227\n"
        "s_waitcnt lgkmcnt(10)\n"
        "s_barrier\n"
        "v_mfma_f32_16x16x32_bf16 v[88:91], v[112:115], v[192:195], v[88:91]\n"
        "ds_read_b128 v[208:211], v232 offset:16384\n"
        "v_mfma_f32_16x16x32_bf16 v[88:91], v[116:119], v[196:199], v[88:91]\n"
        "ds_read_b128 v[212:215], v233 offset:16384\n"
        "v_mfma_f32_16x16x32_bf16 v[92:95], v[124:127], v[196:199], v[92:95]\n"
        "v_mfma_f32_16x16x32_bf16 v[92:95], v[120:123], v[192:195], v[92:95]\n"
        "ds_read_b128 v[216:219], v234 offset:16384\n"
        "v_mfma_f32_16x16x32_bf16 v[96:99], v[128:131], v[192:195], v[96:99]\n"
        "ds_read_b128 v[220:223], v235 offset:16384\n"
        "v_mfma_f32_16x16x32_bf16 v[96:99], v[132:135], v[196:199], v[96:99]\n"
        "v_mfma_f32_16x16x32_bf16 v[108:111], v[132:135], v[204:207], v[108:111]\n"
        "ds_read_b128 v[176:179], v228 offset:32768\n"
        "v_mfma_f32_16x16x32_bf16 v[108:111], v[128:131], v[200:203], v[108:111]\n"
        "ds_read_b128 v[180:183], v229 offset:32768\n"
        "v_mfma_f32_16x16x32_bf16 v[104:107], v[120:123], v[200:203], v[104:107]\n"
        "v_mfma_f32_16x16x32_bf16 v[104:107], v[124:127], v[204:207], v[104:107]\n"
        "ds_read_b128 v[184:187], v228 offset:34816\n"
        "v_mfma_f32_16x16x32_bf16 v[100:103], v[116:119], v[204:207], v[100:103]\n"
        "ds_read_b128 v[188:191], v229 offset:34816\n"
        "v_mfma_f32_16x16x32_bf16 v[100:103], v[112:115], v[200:203], v[100:103]\n"
        "s_waitcnt lgkmcnt(2)\n"
        "v_mfma_f32_16x16x32_bf16 v[0:3], v[144:147], v[176:179], v[0:3]\n"
        "ds_read_b128 v[192:195], v228 offset:36864\n"
        "v_bitop3_b32 v210, v210, v214, s34 bitop3:0x7e\n"
        "v_mfma_f32_16x16x32_bf16 v[0:3], v[148:151], v[180:183], v[0:3]\n"
        "ds_read_b128 v[196:199], v229 offset:36864\n"
        "v_bitop3_b32 v211, v211, v215, s39 bitop3:0x7e\n"
        "v_mfma_f32_16x16x32_bf16 v[4:7], v[156:159], v[180:183], v[4:7]\n"
        "ds_read_b128 v[200:203], v228 offset:38912\n"
        "v_bitop3_b32 v218, v218, v222, s34 bitop3:0x7e\n"
        "v_mfma_f32_16x16x32_bf16 v[4:7], v[152:155], v[176:179], v[4:7]\n"
        "ds_read_b128 v[204:207], v229 offset:38912\n"
        "v_perm_b32 v224, v208, v236, s60\n"
        "v_mfma_f32_16x16x32_bf16 v[8:11], v[160:163], v[176:179], v[8:11]\n"
        "ds_read_b32 v112, v224\n"
        "v_perm_b32 v225, v208, v236, s61\n"
        "v_mfma_f32_16x16x32_bf16 v[8:11], v[164:167], v[180:183], v[8:11]\n"
        "ds_read_b32 v113, v225\n"
        ".Ln_tp3:\n"
        "s_cbranch_execz .Ln_ret\n"
        "v_perm_b32 v226, v208, v236, s62\n"
        "v_mfma_f32_16x16x32_bf16 v[12:15], v[172:175], v[180:183], v[12:15]\n"
        "ds_read_b32 v114, v226\n"
        "v_perm_b32 v227, v208, v236, s63\n"
        "v_mfma_f32_16x16x32_bf16 v[12:15], v[168:171], v[176:179], v[12:15]\n"
        "ds_read_b32 v115, v227\n"
        "v_bitop3_b32 v219, v219, v223, s39 bitop3:0x7e\n"
        "s_waitcnt lgkmcnt(8)\n"
        "v_mfma_f32_16x16x32_bf16 v[28:31], v[168:171], v[184:187], v[28:31]\n"
        "ds_read_b128 v[176:179], v230 offset:32768\n"
        "v_or3_b32 v210, v210, v211, v218\n"
        "v_mfma_f32_16x16x32_bf16 v[28:31], v[172:175], v[188:191], v[28:31]\n"
        "ds_read_b128 v[180:183], v231 offset:32768\n"
        "v_perm_b32 v224, v209, v236, s60\n"
        "v_mfma_f32_16x16x32_bf16 v[24:27], v[164:167], v[188:191], v[24:27]\n"
        "ds_read_b32 v116, v224\n"
        "v_perm_b32 v225, v209, v236, s61\n"
        "v_mfma_f32_16x16x32_bf16 v[24:27], v[160:163], v[184:187], v[24:27]\n"
        "ds_read_b32 v117, v225\n"
        "v_perm_b32 v226, v209, v236, s62\n"
        "v_mfma_f32_16x16x32_bf16 v[20:23], v[152:155], v[184:187], v[20:23]\n"
        "ds_read_b32 v118, v226\n"
        "v_perm_b32 v227, v209, v236, s63\n"
        "v_mfma_f32_16x16x32_bf16 v[20:23], v[156:159], v[188:191], v[20:23]\n"
        "ds_read_b32 v119, v227\n"
        "v_perm_b32 v224, v212, v236, s60\n"
        "v_mfma_f32_16x16x32_bf16 v[16:19], v[148:151], v[188:191], v[16:19]\n"
        "ds_read_b32 v120, v224\n"
        "v_perm_b32 v225, v212, v236, s61\n"
        "v_mfma_f32_16x16x32_bf16 v[16:19], v[144:147], v[184:187], v[16:19]\n"
        "ds_read_b32 v121, v225\n"
        "v_or_b32 v210, v210, v219\n"
        "s_waitcnt lgkmcnt(14)\n"
        "v_mfma_f32_16x16x32_bf16 v[32:35], v[144:147], v[192:195], v[32:35]\n"
        "ds_read_b128 v[184:187], v230 offset:34816\n"
        "v_cmp_ne_u32 vcc, 0, v210\n"
        "v_mfma_f32_16x16x32_bf16 v[32:35], v[148:151], v[196:199], v[32:35]\n"
        "ds_read_b128 v[188:191], v231 offset:34816\n"
        "v_perm_b32 v226, v212, v236, s62\n"
        "v_mfma_f32_16x16x32_bf16 v[36:39], v[156:159], v[196:199], v[36:39]\n"
        "ds_read_b32 v122, v226\n"
        "v_perm_b32 v227, v212, v236, s63\n"
        "v_mfma_f32_16x16x32_bf16 v[36:39], v[152:155], v[192:195], v[36:39]\n"
        "ds_read_b32 v123, v227\n"
        "v_perm_b32 v224, v213, v236, s60\n"
        "v_mfma_f32_16x16x32_bf16 v[40:43], v[160:163], v[192:195], v[40:43]\n"
        "ds_read_b32 v124, v224\n"
        "v_perm_b32 v225, v213, v236, s61\n"
        "v_mfma_f32_16x16x32_bf16 v[40:43], v[164:167], v[196:199], v[40:43]\n"
        "ds_read_b32 v125, v225\n"
        "v_perm_b32 v226, v213, v236, s62\n"
        "v_mfma_f32_16x16x32_bf16 v[44:47], v[172:175], v[196:199], v[44:47]\n"
        "ds_read_b32 v126, v226\n"
        "v_perm_b32 v227, v213, v236, s63\n"
        "v_mfma_f32_16x16x32_bf16 v[44:47], v[168:171], v[192:195], v[44:47]\n"
        "ds_read_b32 v127, v227\n"
        "s_waitcnt lgkmcnt(15)\n"
        "v_mfma_f32_16x16x32_bf16 v[60:63], v[168:171], v[200:203], v[60:63]\n"
        "ds_read_b128 v[192:195], v230 offset:36864\n"
        "s_cbranch_vccnz .Ln_f14\n"
        ".Ln_b14:\n"
        "v_mfma_f32_16x16x32_bf16 v[60:63], v[172:175], v[204:207], v[60:63]\n"
        "ds_read_b128 v[196:199], v231 offset:36864\n"
        "v_perm_b32 v224, v216, v236, s60\n"
        "v_mfma_f32_16x16x32_bf16 v[56:59], v[164:167], v[204:207], v[56:59]\n"
        "ds_read_b32 v128, v224\n"
        "v_perm_b32 v225, v216, v236, s61\n"
        "v_mfma_f32_16x16x32_bf16 v[56:59], v[160:163], v[200:203], v[56:59]\n"
        "ds_read_b32 v129, v225\n"
        "v_perm_b32 v226, v216, v236, s62\n"
        "v_mfma_f32_16x16x32_bf16 v[52:55], v[152:155], v[200:203], v[52:55]\n"
        "ds_read_b32 v130, v226\n"
        "v_perm_b32 v227, v216, v236, s63\n"
        "v_mfma_f32_16x16x32_bf16 v[52:55], v[156:159], v[204:207], v[52:55]\n"
        "ds_read_b32 v131, v227\n"
        "v_perm_b32 v224, v217, v236, s60\n"
        "v_mfma_f32_16x16x32_bf16 v[48:51], v[148:151], v[204:207], v[48:51]\n"
        "ds_read_b32 v132, v224\n"
        "v_perm_b32 v225, v217, v236, s61\n"
        "v_mfma_f32_16x16x32_bf16 v[48:51], v[144:147], v[200:203], v[48:51]\n"
        "ds_read_b32 v133, v225\n"
        "s_waitcnt lgkmcnt(15)\n"
        "v_mfma_f32_16x16x32_bf16 v[64:67], v[144:147], v[176:179], v[64:67]\n"
        "ds_read_b128 v[200:203], v230 offset:38912\n"
        "v_mfma_f32_16x16x32_bf16 v[64:67], v[148:151], v[180:183], v[64:67]\n"
        "ds_read_b128 v[204:207], v231 offset:38912\n"
        "v_perm_b32 v226, v217, v236, s62\n"
        "v_mfma_f32_16x16x32_bf16 v[68:71], v[156:159], v[180:183], v[68:71]\n"
        "ds_read_b32 v134, v226\n"
        "v_perm_b32 v227, v217, v236, s63\n"
        "v_mfma_f32_16x16x32_bf16 v[68:71], v[152:155], v[176:179], v[68:71]\n"
        "ds_read_b32 v135, v227\n"
        "v_perm_b32 v224, v220, v236, s60\n"
        "v_mfma_f32_16x16x32_bf16 v[72:75], v[160:163], v[176:179], v[72:75]\n"
        "ds_read_b32 v136, v224\n"
        "v_perm_b32 v225, v220, v236, s61\n"
        "v_mfma_f32_16x16x32_bf16 v[72:75], v[164:167], v[180:183], v[72:75]\n"
        "ds_read_b32 v137, v225\n"
        "v_perm_b32 v226, v220, v236, s62\n"
        "s_waitcnt lgkmcnt(15)\n"
        "v_mfma_f32_16x16x32_bf16 v[84:87], v[164:167], v[188:191], v[84:87]\n"
        "ds_read_b32 v138, v226\n"
        "v_perm_b32 v227, v220, v236, s63\n"
        "v_mfma_f32_16x16x32_bf16 v[84:87], v[160:163], v[184:187], v[84:87]\n"
        "ds_read_b32 v139, v227\n"
        "v_perm_b32 v224, v221, v236, s60\n"
        "v_mfma_f32_16x16x32_bf16 v[80:83], v[152:155], v[184:187], v[80:83]\n"
        "ds_read_b32 v140, v224\n"
        "v_perm_b32 v225, v221, v236, s61\n"
        "v_mfma_f32_16x16x32_bf16 v[80:83], v[156:159], v[188:191], v[80:83]\n"
        "ds_read_b32 v141, v225\n"
        "v_perm_b32 v226, v221, v236, s62\n"
        "v_mfma_f32_16x16x32_bf16 v[76:79], v[148:151], v[188:191], v[76:79]\n"
        "ds_read_b32 v142, v226\n"
        "v_perm_b32 v227, v221, v236, s63\n"
        "v_mfma_f32_16x16x32_bf16 v[76:79], v[144:147], v[184:187], v[76:79]\n"
        "ds_read_b32 v143, v227\n"
        "s_waitcnt lgkmcnt(10)\n"
        "s_barrier\n"
        "v_mfma_f32_16x16x32_bf16 v[88:91], v[144:147], v[192:195], v[88:91]\n"
        "ds_read_b128 v[208:211], v232 offset:32768\n"
        "v_mfma_f32_16x16x32_bf16 v[88:91], v[148:151], v[196:199], v[88:91]\n"
        "ds_read_b128 v[212:215], v233 offset:32768\n"
        "v_mfma_f32_16x16x32_bf16 v[92:95], v[156:159], v[196:199], v[92:95]\n"
        "v_mfma_f32_16x16x32_bf16 v[92:95], v[152:155], v[192:195], v[92:95]\n"
        "ds_read_b128 v[216:219], v234 offset:32768\n"
        "v_mfma_f32_16x16x32_bf16 v[96:99], v[160:163], v[192:195], v[96:99]\n"
        "ds_read_b128 v[220:223], v235 offset:32768\n"
        "v_mfma_f32_16x16x32_bf16 v[96:99], v[164:167], v[196:199], v[96:99]\n"
        "v_mfma_f32_16x16x32_bf16 v[108:111], v[164:167], v[204:207], v[108:111]\n"
        "ds_read_b128 v[176:179], v228 offset:0\n"
        "v_mfma_f32_16x16x32_bf16 v[108:111], v[160:163], v[200:203], v[108:111]\n"
        "ds_read_b128 v[180:183], v229 offset:0\n"
        "v_mfma_f32_16x16x32_bf16 v[104:107], v[152:155], v[200:203], v[104:107]\n"
        "v_mfma_f32_16x16x32_bf16 v[104:107], v[156:159], v[204:207], v[104:107]\n"
        "ds_read_b128 v[184:187], v228 offset:2048\n"
        "v_mfma_f32_16x16x32_bf16 v[100:103], v[148:151], v[204:207], v[100:103]\n"
        "ds_read_b128 v[188:191], v229 offset:2048\n"
        "v_mfma_f32_16x16x32_bf16 v[100:103], v[144:147], v[200:203], v[100:103]\n"
        "s_waitcnt lgkmcnt(2)\n"
        "v_perm_b32 v224, v208, v236, s60\n"
        "v_mfma_f32_16x16x32_bf16 v[0:3], v[112:115], v[176:179], v[0:3]\n"
        "ds_read_b32 v144, v224\n"
        "ds_read_b128 v[192:195], v228 offset:16384\n"
        "v_perm_b32 v225, v208, v236, s61\n"
        "v_mfma_f32_16x16x32_bf16 v[0:3], v[116:119], v[180:183], v[0:3]\n"
        "ds_read_b32 v145, v225\n"
        "ds_read_b128 v[196:199], v229 offset:16384\n"
        "v_perm_b32 v226, v208, v236, s62\n"
        "v_mfma_f32_16x16x32_bf16 v[4:7], v[120:123], v[176:179], v[4:7]\n"
        "ds_read_b32 v146, v226\n"
        "ds_read_b128 v[200:203], v228 offset:18432\n"
        "v_perm_b32 v227, v208, v236, s63\n"
        "v_mfma_f32_16x16x32_bf16 v[4:7], v[124:127], v[180:183], v[4:7]\n"
        "ds_read_b32 v147, v227\n"
        "ds_read_b128 v[204:207], v229 offset:18432\n"
        "v_perm_b32 v224, v209, v236, s60\n"
        "v_mfma_f32_16x16x32_bf16 v[8:11], v[128:131], v[176:179], v[8:11]\n"
        "ds_read_b32 v148, v224\n"
        "v_perm_b32 v225, v209, v236, s61\n"
        "v_bitop3_b32 v210, v210, v214, s34 bitop3:0x7e\n"
        "v_mfma_f32_16x16x32_bf16 v[8:11], v[132:135], v[180:183], v[8:11]\n"
        "ds_read_b32 v149, v225\n"
        "v_perm_b32 v226, v209, v236, s62\n"
        "v_bitop3_b32 v211, v211, v215, s39 bitop3:0x7e\n"
        "v_mfma_f32_16x16x32_bf16 v[12:15], v[136:139], v[176:179], v[12:15]\n"
        "ds_read_b32 v150, v226\n"
        "v_perm_b32 v227, v209, v236, s63\n"
        "v_bitop3_b32 v218, v218, v222, s34 bitop3:0x7e\n"
        "v_mfma_f32_16x16x32_bf16 v[12:15], v[140:143], v[180:183], v[12:15]\n"
        "ds_read_b32 v151, v227\n"
        "v_perm_b32 v224, v212, v236, s60\n"
        "v_bitop3_b32 v219, v219, v223, s39 bitop3:0x7e\n"
        "s_waitcnt lgkmcnt(12)\n"
        "s_barrier\n"
        "v_mfma_f32_16x16x32_bf16 v[16:19], v[112:115], v[184:187], v[16:19]\n"
        "ds_read_b32 v152, v224\n"
        "ds_read_b128 v[176:179], v228 offset:4096\n"
        "v_perm_b32 v225, v212, v236, s61\n"
        "v_mfma_f32_16x16x32_bf16 v[16:19], v[116:119], v[188:191], v[16:19]\n"
        "ds_read_b32 v153, v225\n"
        "ds_read_b128 v[180:183], v229 offset:4096\n"
        "v_perm_b32 v226, v212, v236, s62\n"
        "v_mfma_f32_16x16x32_bf16 v[20:23], v[120:123], v[184:187], v[20:23]\n"
        "ds_read_b32 v154, v226\n"
        "v_perm_b32 v227, v212, v236, s63\n"
        "v_or3_b32 v210, v210, v211, v218\n"
        "v_mfma_f32_16x16x32_bf16 v[20:23], v[124:127], v[188:191], v[20:23]\n"
        "ds_read_b32 v155, v227\n"
        "v_perm_b32 v224, v213, v236, s60\n"
        "v_or_b32 v210, v210, v219\n"
        "v_mfma_f32_16x16x32_bf16 v[24:27], v[128:131], v[184:187], v[24:27]\n"
        "ds_read_b32 v156, v224\n"
        "v_perm_b32 v225, v213, v236, s61\n"
        "v_cmp_ne_u32 vcc, 0, v210\n"
        "v_mfma_f32_16x16x32_bf16 v[24:27], v[132:135], v[188:191], v[24:27]\n"
        "ds_read_b32 v157, v225\n"
        "v_perm_b32 v226, v213, v236, s62\n"
        "v_mfma_f32_16x16x32_bf16 v[28:31], v[136:139], v[184:187], v[28:31]\n"
        "ds_read_b32 v158, v226\n"
        "s_cbranch_vccnz .Ln_f15\n"
        ".Ln_b15:\n"
        "v_perm_b32 v227, v213, v236, s63\n"
        "v_mfma_f32_16x16x32_bf16 v[28:31], v[140:143], v[188:191], v[28:31]\n"
        "ds_read_b32 v159, v227\n"
        "v_perm_b32 v224, v216, v236, s60\n"
        "v_lshlrev_b32 v208, 16, v238\n"
        "s_waitcnt lgkmcnt(15)\n"
        "v_mfma_f32_16x16x32_bf16 v[0:3], v[144:147], v[192:195], v[0:3]\n"
        "ds_read_b32 v160, v224\n"
        "ds_read_b128 v[184:187], v228 offset:20480\n"
        "v_perm_b32 v225, v216, v236, s61\n"
        "s_waitcnt lgkmcnt(12)\n"
        "v_mfma_f32_16x16x32_bf16 v[0:3], v[148:151], v[196:199], v[0:3]\n"
        "ds_read_b32 v161, v225\n"
        "ds_read_b128 v[188:191], v229 offset:20480\n"
        "v_perm_b32 v226, v216, v236, s62\n"
        "v_mfma_f32_16x16x32_bf16 v[16:19], v[144:147], v[200:203], v[16:19]\n"
        "ds_read_b32 v162, v226\n"
        "v_perm_b32 v227, v216, v236, s63\n"
        "v_and_b32 v209, 0xffff0000, v238\n"
        "v_mfma_f32_16x16x32_bf16 v[16:19], v[148:151], v[204:207], v[16:19]\n"
        "ds_read_b32 v163, v227\n"
        "v_perm_b32 v224, v217, v236, s60\n"
        "v_lshlrev_b32 v210, 16, v239\n"
        "s_waitcnt lgkmcnt(10)\n"
        "v_mfma_f32_16x16x32_bf16 v[4:7], v[152:155], v[192:195], v[4:7]\n"
        "ds_read_b32 v164, v224\n"
        "v_perm_b32 v225, v217, v236, s61\n"
        "v_and_b32 v211, 0xffff0000, v239\n"
        "s_waitcnt lgkmcnt(7)\n"
        "v_mfma_f32_16x16x32_bf16 v[4:7], v[156:159], v[196:199], v[4:7]\n"
        "ds_read_b32 v165, v225\n"
        "v_perm_b32 v226, v217, v236, s62\n"
        "v_lshlrev_b32 v212, 16, v240\n"
        "v_mfma_f32_16x16x32_bf16 v[20:23], v[152:155], v[200:203], v[20:23]\n"
        "ds_read_b32 v166, v226\n"
        "v_perm_b32 v227, v217, v236, s63\n"
        "v_and_b32 v213, 0xffff0000, v240\n"
        "v_mfma_f32_16x16x32_bf16 v[20:23], v[156:159], v[204:207], v[20:23]\n"
        "ds_read_b32 v167, v227\n"
        "v_perm_b32 v224, v220, v236, s60\n"
        "v_lshlrev_b32 v214, 16, v241\n"
        "v_mfma_f32_16x16x32_bf16 v[32:35], v[112:115], v[176:179], v[32:35]\n"
        "ds_read_b32 v168, v224\n"
        "v_perm_b32 v225, v220, v236, s61\n"
        "v_and_b32 v215, 0xffff0000, v241\n"
        "v_mfma_f32_16x16x32_bf16 v[32:35], v[116:119], v[180:183], v[32:35]\n"
        "ds_read_b32 v169, v225\n"
        "v_perm_b32 v226, v220, v236, s62\n"
        "v_mul_f32 v0, v0, v208\n"
        "v_mfma_f32_16x16x32_bf16 v[36:39], v[120:123], v[176:179], v[36:39]\n"
        "ds_read_b32 v170, v226\n"
        "v_perm_b32 v227, v220, v236, s63\n"
        "v_mul_f32 v1, v1, v210\n"
        "v_mfma_f32_16x16x32_bf16 v[36:39], v[124:127], v[180:183], v[36:39]\n"
        "ds_read_b32 v171, v227\n"
        "v_perm_b32 v224, v221, v236, s60\n"
        "v_mul_f32 v2, v2, v212\n"
        "v_mfma_f32_16x16x32_bf16 v[40:43], v[128:131], v[176:179], v[40:43]\n"
        "ds_read_b32 v172, v224\n"
        "v_perm_b32 v225, v221, v236, s61\n"
        "v_mul_f32 v3, v3, v214\n"
        "v_mfma_f32_16x16x32_bf16 v[40:43], v[132:135], v[180:183], v[40:43]\n"
        "ds_read_b32 v173, v225\n"
        "v_perm_b32 v226, v221, v236, s62\n"
        "v_mul_f32 v4, v4, v209\n"
        "v_mfma_f32_16x16x32_bf16 v[44:47], v[136:139], v[176:179], v[44:47]\n"
        "ds_read_b32 v174, v226\n"
        "v_perm_b32 v227, v221, v236, s63\n"
        "v_cvt_pk_bf16_f32 v0, v0, v4\n"
        "v_mfma_f32_16x16x32_bf16 v[44:47], v[140:143], v[180:183], v[44:47]\n"
        "ds_read_b32 v175, v227\n"
        "v_mul_f32 v5, v5, v211\n"
        "v_cvt_pk_bf16_f32 v1, v1, v5\n"
        "s_waitcnt lgkmcnt(12)\n"
        "v_mfma_f32_16x16x32_bf16 v[8:11], v[160:163], v[192:195], v[8:11]\n"
        "ds_read_b128 v[176:179], v228 offset:6144\n"
        "v_mul_f32 v6, v6, v213\n"
        "v_cvt_pk_bf16_f32 v2, v2, v6\n"
        "s_waitcnt lgkmcnt(9)\n"
        "v_mfma_f32_16x16x32_bf16 v[8:11], v[164:167], v[196:199], v[8:11]\n"
        "ds_read_b128 v[180:183], v229 offset:6144\n"
        "v_mul_f32 v7, v7, v215\n"
        "v_cvt_pk_bf16_f32 v3, v3, v7\n"
        "v_mfma_f32_16x16x32_bf16 v[24:27], v[160:163], v[200:203], v[24:27]\n"
        "ds_write_b128 v246, v[0:3] offset:0\n"
        "ds_write_b32 v237, v237 offset:0\n"
        "v_mul_f32 v16, v16, v208\n"
        "v_mfma_f32_16x16x32_bf16 v[24:27], v[164:167], v[204:207], v[24:27]\n"
        "v_mul_f32 v17, v17, v210\n"
        "v_mul_f32 v18, v18, v212\n"
        "v_mul_f32 v19, v19, v214\n"
        "s_waitcnt lgkmcnt(8)\n"
        "v_mfma_f32_16x16x32_bf16 v[12:15], v[168:171], v[192:195], v[12:15]\n"
        "v_mul_f32 v20, v20, v209\n"
        "v_cvt_pk_bf16_f32 v16, v16, v20\n"
        "v_mul_f32 v21, v21, v211\n"
        "s_waitcnt lgkmcnt(4)\n"
        "v_mfma_f32_16x16x32_bf16 v[12:15], v[172:175], v[196:199], v[12:15]\n"
        "v_cvt_pk_bf16_f32 v17, v17, v21\n"
        "v_mul_f32 v22, v22, v213\n"
        "v_cvt_pk_bf16_f32 v18, v18, v22\n"
        "v_mfma_f32_16x16x32_bf16 v[28:31], v[168:171], v[200:203], v[28:31]\n"
        "ds_read_b128 v[192:195], v228 offset:22528\n"
        "v_mul_f32 v23, v23, v215\n"
        "v_cvt_pk_bf16_f32 v19, v19, v23\n"
        "v_mfma_f32_16x16x32_bf16 v[28:31], v[172:175], v[204:207], v[28:31]\n"
        "ds_read_b128 v[196:199], v229 offset:22528\n"
        "ds_write_b128 v246, v[16:19] offset:8192\n"
        "ds_write_b32 v237, v237 offset:256\n"
        "v_mfma_f32_16x16x32_bf16 v[32:35], v[144:147], v[184:187], v[32:35]\n"
        "ds_read_b128 v[200:203], v230 offset:0\n"
        "v_lshlrev_b32 v216, 16, v242\n"
        "v_and_b32 v217, 0xffff0000, v242\n"
        "v_mfma_f32_16x16x32_bf16 v[32:35], v[148:151], v[188:191], v[32:35]\n"
        "ds_read_b128 v[204:207], v231 offset:0\n"
        "v_lshlrev_b32 v218, 16, v243\n"
        "v_and_b32 v219, 0xffff0000, v243\n"
        "v_mfma_f32_16x16x32_bf16 v[36:39], v[152:155], v[184:187], v[36:39]\n"
        "v_lshlrev_b32 v220, 16, v244\n"
        "v_and_b32 v221, 0xffff0000, v244\n"
        "v_lshlrev_b32 v222, 16, v245\n"
        "v_mfma_f32_16x16x32_bf16 v[36:39], v[156:159], v[188:191], v[36:39]\n"
        "v_and_b32 v223, 0xffff0000, v245\n"
        "v_mul_f32 v8, v8, v216\n"
        "v_mul_f32 v9, v9, v217\n"
        "v_mfma_f32_16x16x32_bf16 v[40:43], v[160:163], v[184:187], v[40:43]\n"
        "v_mul_f32 v10, v10, v218\n"
        "v_mul_f32 v11, v11, v219\n"
        "v_cvt_pk_bf16_f32 v8, v8, v9\n"
        "v_mfma_f32_16x16x32_bf16 v[40:43], v[164:167], v[188:191], v[40:43]\n"
        "v_cvt_pk_bf16_f32 v9, v10, v11\n"
        "v_mfma_f32_16x16x32_bf16 v[44:47], v[168:171], v[184:187], v[44:47]\n"
        "ds_write_b64 v247, v[8:9] offset:0\n"
        "v_mul_f32 v12, v12, v220\n"
        "v_mul_f32 v13, v13, v221\n"
        "v_mfma_f32_16x16x32_bf16 v[44:47], v[172:175], v[188:191], v[44:47]\n"
        "v_mul_f32 v14, v14, v222\n"
        "v_mul_f32 v15, v15, v223\n"
        "v_cvt_pk_bf16_f32 v12, v12, v13\n"
        "s_waitcnt lgkmcnt(9)\n"
        "v_mfma_f32_16x16x32_bf16 v[48:51], v[112:115], v[176:179], v[48:51]\n"
        "ds_read_b128 v[184:187], v230 offset:16384\n"
        "v_cvt_pk_bf16_f32 v13, v14, v15\n"
        "v_mfma_f32_16x16x32_bf16 v[48:51], v[116:119], v[180:183], v[48:51]\n"
        "ds_read_b128 v[188:191], v231 offset:16384\n"
        "ds_write_b64 v248, v[12:13] offset:0\n"
        "ds_write_b32 v237, v237 offset:512\n"
        "s_waitcnt lgkmcnt(9)\n"
        "v_mfma_f32_16x16x32_bf16 v[48:51], v[144:147], v[192:195], v[48:51]\n"
        "v_mul_f32 v24, v24, v216\n"
        "v_mul_f32 v25, v25, v217\n"
        "v_mul_f32 v26, v26, v218\n"
        "v_mfma_f32_16x16x32_bf16 v[48:51], v[148:151], v[196:199], v[48:51]\n"
        "v_mul_f32 v27, v27, v219\n"
        "v_cvt_pk_bf16_f32 v24, v24, v25\n"
        "v_cvt_pk_bf16_f32 v25, v26, v27\n"
        "v_mfma_f32_16x16x32_bf16 v[52:55], v[120:123], v[176:179], v[52:55]\n"
        "ds_write_b64 v247, v[24:25] offset:8192\n"
        "v_mul_f32 v28, v28, v220\n"
        "v_mul_f32 v29, v29, v221\n"
        "v_mfma_f32_16x16x32_bf16 v[52:55], v[124:127], v[180:183], v[52:55]\n"
        "v_mul_f32 v30, v30, v222\n"
        "v_mul_f32 v31, v31, v223\n"
        "v_cvt_pk_bf16_f32 v28, v28, v29\n"
        "v_mfma_f32_16x16x32_bf16 v[52:55], v[152:155], v[192:195], v[52:55]\n"
        "v_cvt_pk_bf16_f32 v29, v30, v31\n"
        "v_mfma_f32_16x16x32_bf16 v[52:55], v[156:159], v[196:199], v[52:55]\n"
        "ds_write_b64 v248, v[28:29] offset:8192\n"
        "ds_write_b32 v237, v237 offset:768\n"
        "v_mul_f32 v32, v32, v208\n"
        "v_mfma_f32_16x16x32_bf16 v[56:59], v[128:131], v[176:179], v[56:59]\n"
        "v_mul_f32 v33, v33, v210\n"
        "v_mul_f32 v34, v34, v212\n"
        "v_mul_f32 v35, v35, v214\n"
        "v_mfma_f32_16x16x32_bf16 v[56:59], v[132:135], v[180:183], v[56:59]\n"
        "v_mul_f32 v36, v36, v209\n"
        "v_cvt_pk_bf16_f32 v32, v32, v36\n"
        "v_mul_f32 v37, v37, v211\n"
        "v_mfma_f32_16x16x32_bf16 v[56:59], v[160:163], v[192:195], v[56:59]\n"
        "v_cvt_pk_bf16_f32 v33, v33, v37\n"
        "v_mul_f32 v38, v38, v213\n"
        "v_cvt_pk_bf16_f32 v34, v34, v38\n"
        "v_mfma_f32_16x16x32_bf16 v[56:59], v[164:167], v[196:199], v[56:59]\n"
        "v_mul_f32 v39, v39, v215\n"
        "v_cvt_pk_bf16_f32 v35, v35, v39\n"
        "v_mfma_f32_16x16x32_bf16 v[60:63], v[136:139], v[176:179], v[60:63]\n"
        "ds_write_b128 v246, v[32:35] offset:16384\n"
        "v_mul_f32 v40, v40, v216\n"
        "v_mul_f32 v41, v41, v217\n"
        "v_mfma_f32_16x16x32_bf16 v[60:63], v[140:143], v[180:183], v[60:63]\n"
        "v_mul_f32 v42, v42, v218\n"
        "v_mul_f32 v43, v43, v219\n"
        "v_cvt_pk_bf16_f32 v40, v40, v41\n"
        "v_mfma_f32_16x16x32_bf16 v[60:63], v[168:171], v[192:195], v[60:63]\n"
        "ds_read_b128 v[176:179], v230 offset:2048\n"
        "v_cvt_pk_bf16_f32 v41, v42, v43\n"
        "v_mfma_f32_16x16x32_bf16 v[60:63], v[172:175], v[196:199], v[60:63]\n"
        "ds_read_b128 v[180:183], v231 offset:2048\n"
        "ds_write_b64 v247, v[40:41] offset:16384\n"
        "v_mul_f32 v44, v44, v220\n"
        "s_waitcnt lgkmcnt(12)\n"
        "v_mfma_f32_16x16x32_bf16 v[64:67], v[112:115], v[200:203], v[64:67]\n"
        "ds_read_b128 v[192:195], v230 offset:18432\n"
        "v_mul_f32 v45, v45, v221\n"
        "v_mul_f32 v46, v46, v222\n"
        "v_mfma_f32_16x16x32_bf16 v[64:67], v[116:119], v[204:207], v[64:67]\n"
        "ds_read_b128 v[196:199], v231 offset:18432\n"
        "v_mul_f32 v47, v47, v223\n"
        "v_cvt_pk_bf16_f32 v44, v44, v45\n"
        "s_waitcnt lgkmcnt(11)\n"
        "v_mfma_f32_16x16x32_bf16 v[64:67], v[144:147], v[184:187], v[64:67]\n"
        "v_cvt_pk_bf16_f32 v45, v46, v47\n"
        "v_mfma_f32_16x16x32_bf16 v[64:67], v[148:151], v[188:191], v[64:67]\n"
        "ds_write_b64 v248, v[44:45] offset:16384\n"
        "ds_write_b32 v237, v237 offset:1024\n"
        "v_mul_f32 v48, v48, v208\n"
        "v_mfma_f32_16x16x32_bf16 v[68:71], v[120:123], v[200:203], v[68:71]\n"
        "v_mul_f32 v49, v49, v210\n"
        "v_mul_f32 v50, v50, v212\n"
        "v_mul_f32 v51, v51, v214\n"
        "v_mfma_f32_16x16x32_bf16 v[68:71], v[124:127], v[204:207], v[68:71]\n"
        "v_mul_f32 v52, v52, v209\n"
        "v_cvt_pk_bf16_f32 v48, v48, v52\n"
        "v_mul_f32 v53, v53, v211\n"
        "v_mfma_f32_16x16x32_bf16 v[68:71], v[152:155], v[184:187], v[68:71]\n"
        "v_cvt_pk_bf16_f32 v49, v49, v53\n"
        "v_mul_f32 v54, v54, v213\n"
        "v_cvt_pk_bf16_f32 v50, v50, v54\n"
        "v_mfma_f32_16x16x32_bf16 v[68:71], v[156:159], v[188:191], v[68:71]\n"
        "v_mul_f32 v55, v55, v215\n"
        "v_cvt_pk_bf16_f32 v51, v51, v55\n"
        "v_mfma_f32_16x16x32_bf16 v[72:75], v[128:131], v[200:203], v[72:75]\n"
        "ds_write_b128 v246, v[48:51] offset:24576\n"
        "v_mul_f32 v56, v56, v216\n"
        "v_mul_f32 v57, v57, v217\n"
        "v_mfma_f32_16x16x32_bf16 v[72:75], v[132:135], v[204:207], v[72:75]\n"
        "v_mul_f32 v58, v58, v218\n"
        "v_mul_f32 v59, v59, v219\n"
        "v_cvt_pk_bf16_f32 v56, v56, v57\n"
        "v_mfma_f32_16x16x32_bf16 v[72:75], v[160:163], v[184:187], v[72:75]\n"
        "ds_read_b128 v[200:203], v230 offset:4096\n"
        "v_cvt_pk_bf16_f32 v57, v58, v59\n"
        "v_mfma_f32_16x16x32_bf16 v[72:75], v[164:167], v[188:191], v[72:75]\n"
        "ds_read_b128 v[204:207], v231 offset:4096\n"
        "ds_write_b64 v247, v[56:57] offset:24576\n"
        "v_mul_f32 v60, v60, v220\n"
        "s_waitcnt lgkmcnt(9)\n"
        "v_mfma_f32_16x16x32_bf16 v[76:79], v[112:115], v[176:179], v[76:79]\n"
        "ds_read_b128 v[184:187], v230 offset:20480\n"
        "v_mul_f32 v61, v61, v221\n"
        "v_mul_f32 v62, v62, v222\n"
        "v_mfma_f32_16x16x32_bf16 v[76:79], v[116:119], v[180:183], v[76:79]\n"
        "ds_read_b128 v[188:191], v231 offset:20480\n"
        "v_mul_f32 v63, v63, v223\n"
        "v_cvt_pk_bf16_f32 v60, v60, v61\n"
        "s_waitcnt lgkmcnt(8)\n"
        "v_mfma_f32_16x16x32_bf16 v[76:79], v[144:147], v[192:195], v[76:79]\n"
        "v_cvt_pk_bf16_f32 v61, v62, v63\n"
        "v_mfma_f32_16x16x32_bf16 v[76:79], v[148:151], v[196:199], v[76:79]\n"
        "ds_write_b64 v248, v[60:61] offset:24576\n"
        "ds_write_b32 v237, v237 offset:1280\n"
        "v_mul_f32 v64, v64, v208\n"
        "v_mfma_f32_16x16x32_bf16 v[80:83], v[120:123], v[176:179], v[80:83]\n"
        "v_mul_f32 v65, v65, v210\n"
        "v_mul_f32 v66, v66, v212\n"
        "v_mul_f32 v67, v67, v214\n"
        "v_mfma_f32_16x16x32_bf16 v[80:83], v[124:127], v[180:183], v[80:83]\n"
        "v_mul_f32 v68, v68, v209\n"
        "v_cvt_pk_bf16_f32 v64, v64, v68\n"
        "v_mul_f32 v69, v69, v211\n"
        "v_mfma_f32_16x16x32_bf16 v[80:83], v[152:155], v[192:195], v[80:83]\n"
        "v_cvt_pk_bf16_f32 v65, v65, v69\n"
        "v_mul_f32 v70, v70, v213\n"
        "v_cvt_pk_bf16_f32 v66, v66, v70\n"
        "v_mfma_f32_16x16x32_bf16 v[80:83], v[156:159], v[196:199], v[80:83]\n"
        "v_mul_f32 v71, v71, v215\n"
        "v_cvt_pk_bf16_f32 v67, v67, v71\n"
        "v_mfma_f32_16x16x32_bf16 v[84:87], v[128:131], v[176:179], v[84:87]\n"
        "ds_write_b128 v246, v[64:67] offset:32768\n"
        "v_mul_f32 v72, v72, v216\n"
        "v_mul_f32 v73, v73, v217\n"
        "v_mfma_f32_16x16x32_bf16 v[84:87], v[132:135], v[180:183], v[84:87]\n"
        "v_mul_f32 v74, v74, v218\n"
        "v_mul_f32 v75, v75, v219\n"
        "v_cvt_pk_bf16_f32 v72, v72, v73\n"
        "v_mfma_f32_16x16x32_bf16 v[84:87], v[160:163], v[192:195], v[84:87]\n"
        "ds_read_b128 v[176:179], v230 offset:6144\n"
        "v_cvt_pk_bf16_f32 v73, v74, v75\n"
        "v_mfma_f32_16x16x32_bf16 v[84:87], v[164:167], v[196:199], v[84:87]\n"
        "ds_read_b128 v[180:183], v231 offset:6144\n"
        "ds_write_b64 v247, v[72:73] offset:32768\n"
        "ds_write_b32 v237, v237 offset:1536\n"
        "s_waitcnt lgkmcnt(10)\n"
        "v_mfma_f32_16x16x32_bf16 v[88:91], v[112:115], v[200:203], v[88:91]\n"
        "ds_read_b128 v[192:195], v230 offset:22528\n"
        "v_mul_f32 v76, v76, v208\n"
        "v_mul_f32 v77, v77, v210\n"
        "v_mfma_f32_16x16x32_bf16 v[88:91], v[116:119], v[204:207], v[88:91]\n"
        "ds_read_b128 v[196:199], v231 offset:22528\n"
        "v_mul_f32 v78, v78, v212\n"
        "v_mul_f32 v79, v79, v214\n"
        "s_waitcnt lgkmcnt(9)\n"
        "v_mfma_f32_16x16x32_bf16 v[88:91], v[144:147], v[184:187], v[88:91]\n"
        "v_mul_f32 v80, v80, v209\n"
        "v_cvt_pk_bf16_f32 v76, v76, v80\n"
        "v_mul_f32 v81, v81, v211\n"
        "v_mfma_f32_16x16x32_bf16 v[88:91], v[148:151], v[188:191], v[88:91]\n"
        "v_cvt_pk_bf16_f32 v77, v77, v81\n"
        "v_mul_f32 v82, v82, v213\n"
        "v_cvt_pk_bf16_f32 v78, v78, v82\n"
        "v_mfma_f32_16x16x32_bf16 v[92:95], v[120:123], v[200:203], v[92:95]\n"
        "v_mul_f32 v83, v83, v215\n"
        "v_cvt_pk_bf16_f32 v79, v79, v83\n"
        "v_mfma_f32_16x16x32_bf16 v[92:95], v[124:127], v[204:207], v[92:95]\n"
        "ds_write_b128 v246, v[76:79] offset:40960\n"
        "v_mul_f32 v84, v84, v216\n"
        "v_mul_f32 v85, v85, v217\n"
        "v_mfma_f32_16x16x32_bf16 v[92:95], v[152:155], v[184:187], v[92:95]\n"
        "v_mul_f32 v86, v86, v218\n"
        "v_mul_f32 v87, v87, v219\n"
        "v_cvt_pk_bf16_f32 v84, v84, v85\n"
        "v_mfma_f32_16x16x32_bf16 v[92:95], v[156:159], v[188:191], v[92:95]\n"
        "v_cvt_pk_bf16_f32 v85, v86, v87\n"
        "v_mfma_f32_16x16x32_bf16 v[96:99], v[128:131], v[200:203], v[96:99]\n"
        "ds_write_b64 v247, v[84:85] offset:40960\n"
        "ds_write_b32 v237, v237 offset:1792\n"
        "v_mul_f32 v88, v88, v208\n"
        "v_mfma_f32_16x16x32_bf16 v[96:99], v[132:135], v[204:207], v[96:99]\n"
        "v_mul_f32 v89, v89, v210\n"
        "v_mul_f32 v90, v90, v212\n"
        "v_mul_f32 v91, v91, v214\n"
        "v_mfma_f32_16x16x32_bf16 v[96:99], v[160:163], v[184:187], v[96:99]\n"
        "v_mul_f32 v92, v92, v209\n"
        "v_cvt_pk_bf16_f32 v88, v88, v92\n"
        "v_mul_f32 v93, v93, v211\n"
        "v_mfma_f32_16x16x32_bf16 v[96:99], v[164:167], v[188:191], v[96:99]\n"
        "v_cvt_pk_bf16_f32 v89, v89, v93\n"
        "v_mul_f32 v94, v94, v213\n"
        "v_cvt_pk_bf16_f32 v90, v90, v94\n"
        "s_waitcnt lgkmcnt(7)\n"
        "v_mfma_f32_16x16x32_bf16 v[100:103], v[112:115], v[176:179], v[100:103]\n"
        "v_mul_f32 v95, v95, v215\n"
        "v_cvt_pk_bf16_f32 v91, v91, v95\n"
        "v_mfma_f32_16x16x32_bf16 v[100:103], v[116:119], v[180:183], v[100:103]\n"
        "ds_write_b128 v246, v[88:91] offset:49152\n"
        "v_mul_f32 v96, v96, v216\n"
        "v_mul_f32 v97, v97, v217\n"
        "s_waitcnt lgkmcnt(4)\n"
        "v_mfma_f32_16x16x32_bf16 v[100:103], v[144:147], v[192:195], v[100:103]\n"
        "v_mul_f32 v98, v98, v218\n"
        "v_mul_f32 v99, v99, v219\n"
        "v_cvt_pk_bf16_f32 v96, v96, v97\n"
        "v_mfma_f32_16x16x32_bf16 v[100:103], v[148:151], v[196:199], v[100:103]\n"
        "v_cvt_pk_bf16_f32 v97, v98, v99\n"
        "v_mfma_f32_16x16x32_bf16 v[104:107], v[120:123], v[176:179], v[104:107]\n"
        "ds_write_b64 v247, v[96:97] offset:49152\n"
        "ds_write_b32 v237, v237 offset:2048\n"
        "v_mfma_f32_16x16x32_bf16 v[104:107], v[124:127], v[180:183], v[104:107]\n"
        "v_mfma_f32_16x16x32_bf16 v[104:107], v[152:155], v[192:195], v[104:107]\n"
        "v_mfma_f32_16x16x32_bf16 v[104:107], v[156:159], v[196:199], v[104:107]\n"
        "v_mfma_f32_16x16x32_bf16 v[108:111], v[128:131], v[176:179], v[108:111]\n"
        "v_mul_f32 v100, v100, v208\n"
        "v_mul_f32 v101, v101, v210\n"
        "v_mul_f32 v102, v102, v212\n"
        "v_mfma_f32_16x16x32_bf16 v[108:111], v[132:135], v[180:183], v[108:111]\n"
        "v_mul_f32 v103, v103, v214\n"
        "v_mfma_f32_16x16x32_bf16 v[108:111], v[160:163], v[192:195], v[108:111]\n"
        "v_mfma_f32_16x16x32_bf16 v[108:111], v[164:167], v[196:199], v[108:111]\n"
        "v_mul_f32 v104, v104, v209\n"
        "v_cvt_pk_bf16_f32 v100, v100, v104\n"
        "v_mul_f32 v105, v105, v211\n"
        "s_nop 7\n"
        "v_cvt_pk_bf16_f32 v101, v101, v105\n"
        "v_mul_f32 v106, v106, v213\n"
        "v_cvt_pk_bf16_f32 v102, v102, v106\n"
        "v_mul_f32 v107, v107, v215\n"
        "v_cvt_pk_bf16_f32 v103, v103, v107\n"
        "ds_write_b128 v246, v[100:103] offset:57344\n"
        "v_mul_f32 v108, v108, v216\n"
        "v_mul_f32 v109, v109, v217\n"
        "v_mul_f32 v110, v110, v218\n"
        "v_mul_f32 v111, v111, v219\n"
        "v_cvt_pk_bf16_f32 v108, v108, v109\n"
        "v_cvt_pk_bf16_f32 v109, v110, v111\n"
        "ds_write_b64 v247, v[108:109] offset:57344\n"
        "ds_write_b32 v237, v237 offset:2304\n"
        "s_endpgm\n"
        ".Ln_f1:\n"
        "s_movk_i32 s77, 1\n"
        "s_getpc_b64 s[70:71]\n"
        "s_branch .Ln_rec1\n"
        "s_branch .Ln_b1\n"
        ".Ln_f2:\n"
        "s_movk_i32 s77, 2\n"
        "s_getpc_b64 s[70:71]\n"
        "s_branch .Ln_rec0\n"
        "s_branch .Ln_b2\n"
        ".Ln_f3:\n"
        "s_movk_i32 s77, 3\n"
        "s_getpc_b64 s[70:71]\n"
        "s_branch .Ln_rec1\n"
        "s_branch .Ln_b3\n"
        ".Ln_f4:\n"
        "s_movk_i32 s77, 4\n"
        "s_getpc_b64 s[70:71]\n"
        "s_branch .Ln_rec0\n"
        "s_branch .Ln_b4\n"
        ".Ln_f5:\n"
        "s_movk_i32 s77, 5\n"
        "s_getpc_b64 s[70:71]\n"
        "s_branch .Ln_rec1\n"
        "s_branch .Ln_b5\n"
        ".Ln_f6:\n"
        "s_movk_i32 s77, 6\n"
        "s_getpc_b64 s[70:71]\n"
        "s_branch .Ln_rec0\n"
        "s_branch .Ln_b6\n"
        ".Ln_f7:\n"
        "s_movk_i32 s77, 7\n"
        "s_getpc_b64 s[70:71]\n"
        "s_branch .Ln_rec1\n"
        "s_branch .Ln_b7\n"
        ".Ln_f8:\n"
        "s_movk_i32 s77, 8\n"
        "s_getpc_b64 s[70:71]\n"
        "s_branch .Ln_rec0\n"
        "s_branch .Ln_b8\n"
        ".Ln_f9:\n"
        "s_movk_i32 s77, 9\n"
        "s_getpc_b64 s[70:71]\n"
        "s_branch .Ln_rec1\n"
        "s_branch .Ln_b9\n"
        ".Ln_f10:\n"
        "s_movk_i32 s77, 10\n"
        "s_getpc_b64 s[70:71]\n"
        "s_branch .Ln_rec0\n"
        "s_branch .Ln_b10\n"
        ".Ln_f11:\n"
        "s_movk_i32 s77, 11\n"
        "s_getpc_b64 s[70:71]\n"
        "s_branch .Ln_rec1\n"
        "s_branch .Ln_b11\n"
        ".Ln_f12:\n"
        "s_movk_i32 s77, 12\n"
        "s_getpc_b64 s[70:71]\n"
        "s_branch .Ln_rec0\n"
        "s_branch .Ln_b12\n"
        ".Ln_f13:\n"
        "s_movk_i32 s77, 13\n"
        "s_getpc_b64 s[70:71]\n"
        "s_branch .Ln_rec1\n"
        "s_branch .Ln_b13\n"
        ".Ln_f14:\n"
        "s_movk_i32 s77, 14\n"
        "s_getpc_b64 s[70:71]\n"
        "s_branch .Ln_rec0\n"
        "s_branch .Ln_b14\n"
        ".Ln_f15:\n"
        "s_movk_i32 s77, 15\n"
        "s_getpc_b64 s[70:71]\n"
        "s_branch .Ln_rec1\n"
        "s_branch .Ln_b15\n"
        ".Ln_f0:\n"
        "s_movk_i32 s77, 0\n"
        "s_waitcnt vmcnt(0)\n"
        "v_mov_b32 v144, v208\n"
        "v_mov_b32 v145, v209\n"
        "v_mov_b32 v146, v210\n"
        "v_mov_b32 v147, v211\n"
        "v_mov_b32 v148, v212\n"
        "v_mov_b32 v149, v213\n"
        "v_mov_b32 v150, v214\n"
        "v_mov_b32 v151, v215\n"
        "v_mov_b32 v152, v216\n"
        "v_mov_b32 v153, v217\n"
        "v_mov_b32 v154, v218\n"
        "v_mov_b32 v155, v219\n"
        "v_mov_b32 v156, v220\n"
        "v_mov_b32 v157, v221\n"
        "v_mov_b32 v158, v222\n"
        "v_mov_b32 v159, v223\n"
        "s_getpc_b64 s[70:71]\n"
        "s_branch .Ln_rec0\n"
        "v_mov_b32 v64, v208\n"
        "v_mov_b32 v208, v144\n"
        "v_mov_b32 v65, v209\n"
        "v_mov_b32 v209, v145\n"
        "v_mov_b32 v66, v210\n"
        "v_mov_b32 v210, v146\n"
        "v_mov_b32 v67, v211\n"
        "v_mov_b32 v211, v147\n"
        "v_mov_b32 v68, v212\n"
        "v_mov_b32 v212, v148\n"
        "v_mov_b32 v69, v213\n"
        "v_mov_b32 v213, v149\n"
        "v_mov_b32 v70, v214\n"
        "v_mov_b32 v214, v150\n"
        "v_mov_b32 v71, v215\n"
        "v_mov_b32 v215, v151\n"
        "v_mov_b32 v72, v216\n"
        "v_mov_b32 v216, v152\n"
        "v_mov_b32 v73, v217\n"
        "v_mov_b32 v217, v153\n"
        "v_mov_b32 v74, v218\n"
        "v_mov_b32 v218, v154\n"
        "v_mov_b32 v75, v219\n"
        "v_mov_b32 v219, v155\n"
        "v_mov_b32 v76, v220\n"
        "v_mov_b32 v220, v156\n"
        "v_mov_b32 v77, v221\n"
        "v_mov_b32 v221, v157\n"
        "v_mov_b32 v78, v222\n"
        "v_mov_b32 v222, v158\n"
        "v_mov_b32 v79, v223\n"
        "v_mov_b32 v223, v159\n"
        "s_branch .Ln_b0\n"
        ".Ln_rec0:\n"
        "s_waitcnt lgkmcnt(0)\n"
        "s_lshl_b32 s76, s77, 14\n"
        "s_add_u32 s76, s76, s64\n"
        "3:\n"
        "s_add_u32 s73, s73, 1\n"
        "s_cmp_lt_u32 s73, s74\n"
        "s_cbranch_scc1 2f\n"
        "s_mov_b32 s74, 0\n"
        "s_mov_b32 s78, 0\n"
        "1:\n"
        "s_lshl_b32 s72, s66, 2\n"
        "s_add_u32 s72, s72, 2\n"
        "s_cmp_eq_u32 s78, 0\n"
        "s_cselect_b32 s72, s68, s72\n"
        "s_cmp_eq_u32 s78, 2\n"
        "s_cselect_b32 s72, 3, s72\n"
        "s_lshl_b32 s35, s77, 1\n"
        "s_lshr_b32 s36, s33, 3\n"
        "s_add_u32 s35, s35, s36\n"
        "s_mul_i32 s35, s35, 7\n"
        "s_add_u32 s72, s72, s35\n"
        "s_mul_i32 s35, s72, 9363\n"
        "s_lshr_b32 s35, s35, 16\n"
        "s_mul_i32 s36, s35, 7\n"
        "s_sub_u32 s36, s72, s36\n"
        "s_and_b32 s79, s35, 1\n"
        "s_lshl_b32 s79, s79, 3\n"
        "s_and_b32 s37, s33, 7\n"
        "s_or_b32 s79, s79, s37\n"
        "s_lshr_b32 s35, s35, 1\n"
        "s_mul_i32 s40, s35, 917504\n"
        "s_mul_i32 s37, s79, 896\n"
        "s_lshl_b32 s38, s36, 7\n"
        "s_add_u32 s40, s40, s37\n"
        "s_add_u32 s40, s40, s38\n"
        "s_add_u32 s41, s40, 14336\n"
        "s_add_u32 s42, s41, 14336\n"
        "s_add_u32 s43, s42, 14336\n"
        "s_add_u32 s44, s43, 14336\n"
        "s_add_u32 s45, s44, 14336\n"
        "s_add_u32 s46, s45, 14336\n"
        "s_add_u32 s47, s46, 14336\n"
        "s_lshl_b32 s75, s79, 4\n"
        "s_add_u32 s75, s75, s35\n"
        "s_lshl_b32 s75, s75, 3\n"
        "s_add_u32 s75, s75, s36\n"
        "s_lshl_b32 s75, s75, 11\n"
        "s_add_u32 s75, s75, 114688\n"
        "buffer_load_dwordx4 v[112:115], v249, s[8:11], s40 offen\n"
        "buffer_load_dwordx4 v[116:119], v249, s[8:11], s41 offen\n"
        "buffer_load_dwordx4 v[120:123], v249, s[8:11], s42 offen\n"
        "buffer_load_dwordx4 v[124:127], v249, s[8:11], s43 offen\n"
        "buffer_load_dwordx4 v[128:131], v249, s[8:11], s44 offen\n"
        "buffer_load_dwordx4 v[132:135], v249, s[8:11], s45 offen\n"
        "buffer_load_dwordx4 v[136:139], v249, s[8:11], s46 offen\n"
        "buffer_load_dwordx4 v[140:143], v249, s[8:11], s47 offen\n"
        "v_mov_b32 v210, s34\n"
        "v_mov_b32 v211, s39\n"
        "v_mov_b32 v214, s34\n"
        "v_mov_b32 v215, s39\n"
        "s_waitcnt vmcnt(0)\n"
        "v_lshl_add_u64 v[140:141], v[140:141], 4, v[136:137]\n"
        "v_lshl_add_u64 v[142:143], v[142:143], 4, v[138:139]\n"
        "v_lshl_add_u64 v[140:141], v[140:141], 4, v[132:133]\n"
        "v_lshl_add_u64 v[142:143], v[142:143], 4, v[134:135]\n"
        "v_lshl_add_u64 v[140:141], v[140:141], 4, v[128:129]\n"
        "v_lshl_add_u64 v[142:143], v[142:143], 4, v[130:131]\n"
        "v_lshl_add_u64 v[140:141], v[140:141], 4, v[124:125]\n"
        "v_lshl_add_u64 v[142:143], v[142:143], 4, v[126:127]\n"
        "v_lshl_add_u64 v[140:141], v[140:141], 4, v[120:121]\n"
        "v_lshl_add_u64 v[142:143], v[142:143], 4, v[122:123]\n"
        "v_lshl_add_u64 v[140:141], v[140:141], 4, v[116:117]\n"
        "v_lshl_add_u64 v[142:143], v[142:143], 4, v[118:119]\n"
        "v_lshl_add_u64 v[208:209], v[140:141], 4, v[112:113]\n"
        "v_lshl_add_u64 v[212:213], v[142:143], 4, v[114:115]\n"
        "s_nop 0\n"
        "v_permlane32_swap_b32 v208, v209\n"
        "v_permlane32_swap_b32 v212, v213\n"
        "buffer_store_dwordx4 v[208:211], v250, s[16:19], s75 offen sc1\n"
        "buffer_store_dwordx4 v[212:215], v251, s[16:19], s75 offen offset:1024 sc1\n"
        "s_waitcnt vmcnt(0)\n"
        "s_add_u32 s78, s78, 1\n"
        "s_cmp_lt_u32 s78, 3\n"
        "s_cbranch_scc1 1b\n"
        "2:\n"
        "s_sleep 1\n"
        "buffer_load_dwordx4 v[208:211], v232, s[16:19], s76 offen sc1\n"
        "buffer_load_dwordx4 v[212:215], v233, s[16:19], s76 offen sc1\n"
        "buffer_load_dwordx4 v[216:219], v234, s[16:19], s76 offen sc1\n"
        "buffer_load_dwordx4 v[220:223], v235, s[16:19], s76 offen sc1\n"
        "s_waitcnt vmcnt(0)\n"
        "v_bitop3_b32 v210, v210, v214, s34 bitop3:0x7e\n"
        "v_bitop3_b32 v211, v211, v215, s39 bitop3:0x7e\n"
        "v_bitop3_b32 v218, v218, v222, s34 bitop3:0x7e\n"
        "v_bitop3_b32 v219, v219, v223, s39 bitop3:0x7e\n"
        "v_or3_b32 v210, v210, v211, v218\n"
        "v_or_b32 v210, v210, v219\n"
        "v_cmp_ne_u32 vcc, 0, v210\n"
        "s_cbranch_vccnz 3b\n"
        "4:\n"
        "v_perm_b32 v224, v208, v236, s60\n"
        "v_perm_b32 v225, v208, v236, s61\n"
        "v_perm_b32 v226, v208, v236, s62\n"
        "v_perm_b32 v227, v208, v236, s63\n"
        "ds_read_b32 v112, v224\n"
        "v_perm_b32 v224, v209, v236, s60\n"
        "ds_read_b32 v113, v225\n"
        "v_perm_b32 v225, v209, v236, s61\n"
        "ds_read_b32 v114, v226\n"
        "v_perm_b32 v226, v209, v236, s62\n"
        "ds_read_b32 v115, v227\n"
        "v_perm_b32 v227, v209, v236, s63\n"
        "ds_read_b32 v116, v224\n"
        "v_perm_b32 v224, v212, v236, s60\n"
        "ds_read_b32 v117, v225\n"
        "v_perm_b32 v225, v212, v236, s61\n"
        "ds_read_b32 v118, v226\n"
        "v_perm_b32 v226, v212, v236, s62\n"
        "ds_read_b32 v119, v227\n"
        "v_perm_b32 v227, v212, v236, s63\n"
        "ds_read_b32 v120, v224\n"
        "v_perm_b32 v224, v213, v236, s60\n"
        "ds_read_b32 v121, v225\n"
        "v_perm_b32 v225, v213, v236, s61\n"
        "ds_read_b32 v122, v226\n"
        "v_perm_b32 v226, v213, v236, s62\n"
        "ds_read_b32 v123, v227\n"
        "v_perm_b32 v227, v213, v236, s63\n"
        "ds_read_b32 v124, v224\n"
        "v_perm_b32 v224, v216, v236, s60\n"
        "ds_read_b32 v125, v225\n"
        "v_perm_b32 v225, v216, v236, s61\n"
        "ds_read_b32 v126, v226\n"
        "v_perm_b32 v226, v216, v236, s62\n"
        "ds_read_b32 v127, v227\n"
        "v_perm_b32 v227, v216, v236, s63\n"
        "ds_read_b32 v128, v224\n"
        "v_perm_b32 v224, v217, v236, s60\n"
        "ds_read_b32 v129, v225\n"
        "v_perm_b32 v225, v217, v236, s61\n"
        "ds_read_b32 v130, v226\n"
        "v_perm_b32 v226, v217, v236, s62\n"
        "ds_read_b32 v131, v227\n"
        "v_perm_b32 v227, v217, v236, s63\n"
        "ds_read_b32 v132, v224\n"
        "v_perm_b32 v224, v220, v236, s60\n"
        "ds_read_b32 v133, v225\n"
        "v_perm_b32 v225, v220, v236, s61\n"
        "ds_read_b32 v134, v226\n"
        "v_perm_b32 v226, v220, v236, s62\n"
        "ds_read_b32 v135, v227\n"
        "v_perm_b32 v227, v220, v236, s63\n"
        "ds_read_b32 v136, v224\n"
        "v_perm_b32 v224, v221, v236, s60\n"
        "ds_read_b32 v137, v225\n"
        "v_perm_b32 v225, v221, v236, s61\n"
        "ds_read_b32 v138, v226\n"
        "v_perm_b32 v226, v221, v236, s62\n"
        "ds_read_b32 v139, v227\n"
        "v_perm_b32 v227, v221, v236, s63\n"
        "ds_read_b32 v140, v224\n"
        "ds_read_b32 v141, v225\n"
        "ds_read_b32 v142, v226\n"
        "ds_read_b32 v143, v227\n"
        "s_waitcnt lgkmcnt(0)\n"
        "s_add_u32 s70, s70, 4\n"
        "s_addc_u32 s71, s71, 0\n"
        "s_setpc_b64 s[70:71]\n"
        ".Ln_rec1:\n"
        "s_waitcnt lgkmcnt(0)\n"
        "s_lshl_b32 s76, s77, 14\n"
        "s_add_u32 s76, s76, s64\n"
        "3:\n"
        "s_add_u32 s73, s73, 1\n"
        "s_cmp_lt_u32 s73, s74\n"
        "s_cbranch_scc1 2f\n"
        "s_mov_b32 s74, 0\n"
        "s_mov_b32 s78, 0\n"
        "1:\n"
        "s_lshl_b32 s72, s66, 2\n"
        "s_add_u32 s72, s72, 2\n"
        "s_cmp_eq_u32 s78, 0\n"
        "s_cselect_b32 s72, s68, s72\n"
        "s_cmp_eq_u32 s78, 2\n"
        "s_cselect_b32 s72, 3, s72\n"
        "s_lshl_b32 s35, s77, 1\n"
        "s_lshr_b32 s36, s33, 3\n"
        "s_add_u32 s35, s35, s36\n"
        "s_mul_i32 s35, s35, 7\n"
        "s_add_u32 s72, s72, s35\n"
        "s_mul_i32 s35, s72, 9363\n"
        "s_lshr_b32 s35, s35, 16\n"
        "s_mul_i32 s36, s35, 7\n"
        "s_sub_u32 s36, s72, s36\n"
        "s_and_b32 s79, s35, 1\n"
        "s_lshl_b32 s79, s79, 3\n"
        "s_and_b32 s37, s33, 7\n"
        "s_or_b32 s79, s79, s37\n"
        "s_lshr_b32 s35, s35, 1\n"
        "s_mul_i32 s40, s35, 917504\n"
        "s_mul_i32 s37, s79, 896\n"
        "s_lshl_b32 s38, s36, 7\n"
        "s_add_u32 s40, s40, s37\n"
        "s_add_u32 s40, s40, s38\n"
        "s_add_u32 s41, s40, 14336\n"
        "s_add_u32 s42, s41, 14336\n"
        "s_add_u32 s43, s42, 14336\n"
        "s_add_u32 s44, s43, 14336\n"
        "s_add_u32 s45, s44, 14336\n"
        "s_add_u32 s46, s45, 14336\n"
        "s_add_u32 s47, s46, 14336\n"
        "s_lshl_b32 s75, s79, 4\n"
        "s_add_u32 s75, s75, s35\n"
        "s_lshl_b32 s75, s75, 3\n"
        "s_add_u32 s75, s75, s36\n"
        "s_lshl_b32 s75, s75, 11\n"
        "s_add_u32 s75, s75, 114688\n"
        "buffer_load_dwordx4 v[144:147], v249, s[8:11], s40 offen\n"
        "buffer_load_dwordx4 v[148:151], v249, s[8:11], s41 offen\n"
        "buffer_load_dwordx4 v[152:155], v249, s[8:11], s42 offen\n"
        "buffer_load_dwordx4 v[156:159], v249, s[8:11], s43 offen\n"
        "buffer_load_dwordx4 v[160:163], v249, s[8:11], s44 offen\n"
        "buffer_load_dwordx4 v[164:167], v249, s[8:11], s45 offen\n"
        "buffer_load_dwordx4 v[168:171], v249, s[8:11], s46 offen\n"
        "buffer_load_dwordx4 v[172:175], v249, s[8:11], s47 offen\n"
        "v_mov_b32 v210, s34\n"
        "v_mov_b32 v211, s39\n"
        "v_mov_b32 v214, s34\n"
        "v_mov_b32 v215, s39\n"
        "s_waitcnt vmcnt(0)\n"
        "v_lshl_add_u64 v[172:173], v[172:173], 4, v[168:169]\n"
        "v_lshl_add_u64 v[174:175], v[174:175], 4, v[170:171]\n"
        "v_lshl_add_u64 v[172:173], v[172:173], 4, v[164:165]\n"
        "v_lshl_add_u64 v[174:175], v[174:175], 4, v[166:167]\n"
        "v_lshl_add_u64 v[172:173], v[172:173], 4, v[160:161]\n"
        "v_lshl_add_u64 v[174:175], v[174:175], 4, v[162:163]\n"
        "v_lshl_add_u64 v[172:173], v[172:173], 4, v[156:157]\n"
        "v_lshl_add_u64 v[174:175], v[174:175], 4, v[158:159]\n"
        "v_lshl_add_u64 v[172:173], v[172:173], 4, v[152:153]\n"
        "v_lshl_add_u64 v[174:175], v[174:175], 4, v[154:155]\n"
        "v_lshl_add_u64 v[172:173], v[172:173], 4, v[148:149]\n"
        "v_lshl_add_u64 v[174:175], v[174:175], 4, v[150:151]\n"
        "v_lshl_add_u64 v[208:209], v[172:173], 4, v[144:145]\n"
        "v_lshl_add_u64 v[212:213], v[174:175], 4, v[146:147]\n"
        "s_nop 0\n"
        "v_permlane32_swap_b32 v208, v209\n"
        "v_permlane32_swap_b32 v212, v213\n"
        "buffer_store_dwordx4 v[208:211], v250, s[16:19], s75 offen sc1\n"
        "buffer_store_dwordx4 v[212:215], v251, s[16:19], s75 offen offset:1024 sc1\n"
        "s_waitcnt vmcnt(0)\n"
        "s_add_u32 s78, s78, 1\n"
        "s_cmp_lt_u32 s78, 3\n"
        "s_cbranch_scc1 1b\n"
        "2:\n"
        "s_sleep 1\n"
        "buffer_load_dwordx4 v[208:211], v232, s[16:19], s76 offen sc1\n"
        "buffer_load_dwordx4 v[212:215], v233, s[16:19], s76 offen sc1\n"
        "buffer_load_dwordx4 v[216:219], v234, s[16:19], s76 offen sc1\n"
        "buffer_load_dwordx4 v[220:223], v235, s[16:19], s76 offen sc1\n"
        "s_waitcnt vmcnt(0)\n"
        "v_bitop3_b32 v210, v210, v214, s34 bitop3:0x7e\n"
        "v_bitop3_b32 v211, v211, v215, s39 bitop3:0x7e\n"
        "v_bitop3_b32 v218, v218, v222, s34 bitop3:0x7e\n"
        "v_bitop3_b32 v219, v219, v223, s39 bitop3:0x7e\n"
        "v_or3_b32 v210, v210, v211, v218\n"
        "v_or_b32 v210, v210, v219\n"
        "v_cmp_ne_u32 vcc, 0, v210\n"
        "s_cbranch_vccnz 3b\n"
        "4:\n"
        "v_perm_b32 v224, v208, v236, s60\n"
        "v_perm_b32 v225, v208, v236, s61\n"
        "v_perm_b32 v226, v208, v236, s62\n"
        "v_perm_b32 v227, v208, v236, s63\n"
        "ds_read_b32 v144, v224\n"
        "v_perm_b32 v224, v209, v236, s60\n"
        "ds_read_b32 v145, v225\n"
        "v_perm_b32 v225, v209, v236, s61\n"
        "ds_read_b32 v146, v226\n"
        "v_perm_b32 v226, v209, v236, s62\n"
        "ds_read_b32 v147, v227\n"
        "v_perm_b32 v227, v209, v236, s63\n"
        "ds_read_b32 v148, v224\n"
        "v_perm_b32 v224, v212, v236, s60\n"
        "ds_read_b32 v149, v225\n"
        "v_perm_b32 v225, v212, v236, s61\n"
        "ds_read_b32 v150, v226\n"
        "v_perm_b32 v226, v212, v236, s62\n"
        "ds_read_b32 v151, v227\n"
        "v_perm_b32 v227, v212, v236, s63\n"
        "ds_read_b32 v152, v224\n"
        "v_perm_b32 v224, v213, v236, s60\n"
        "ds_read_b32 v153, v225\n"
        "v_perm_b32 v225, v213, v236, s61\n"
        "ds_read_b32 v154, v226\n"
        "v_perm_b32 v226, v213, v236, s62\n"
        "ds_read_b32 v155, v227\n"
        "v_perm_b32 v227, v213, v236, s63\n"
        "ds_read_b32 v156, v224\n"
        "v_perm_b32 v224, v216, v236, s60\n"
        "ds_read_b32 v157, v225\n"
        "v_perm_b32 v225, v216, v236, s61\n"
        "ds_read_b32 v158, v226\n"
        "v_perm_b32 v226, v216, v236, s62\n"
        "ds_read_b32 v159, v227\n"
        "v_perm_b32 v227, v216, v236, s63\n"
        "ds_read_b32 v160, v224\n"
        "v_perm_b32 v224, v217, v236, s60\n"
        "ds_read_b32 v161, v225\n"
        "v_perm_b32 v225, v217, v236, s61\n"
        "ds_read_b32 v162, v226\n"
        "v_perm_b32 v226, v217, v236, s62\n"
        "ds_read_b32 v163, v227\n"
        "v_perm_b32 v227, v217, v236, s63\n"
        "ds_read_b32 v164, v224\n"
        "v_perm_b32 v224, v220, v236, s60\n"
        "ds_read_b32 v165, v225\n"
        "v_perm_b32 v225, v220, v236, s61\n"
        "ds_read_b32 v166, v226\n"
        "v_perm_b32 v226, v220, v236, s62\n"
        "ds_read_b32 v167, v227\n"
        "v_perm_b32 v227, v220, v236, s63\n"
        "ds_read_b32 v168, v224\n"
        "v_perm_b32 v224, v221, v236, s60\n"
        "ds_read_b32 v169, v225\n"
        "v_perm_b32 v225, v221, v236, s61\n"
        "ds_read_b32 v170, v226\n"
        "v_perm_b32 v226, v221, v236, s62\n"
        "ds_read_b32 v171, v227\n"
        "v_perm_b32 v227, v221, v236, s63\n"
        "ds_read_b32 v172, v224\n"
        "ds_read_b32 v173, v225\n"
        "ds_read_b32 v174, v226\n"
        "ds_read_b32 v175, v227\n"
        "s_waitcnt lgkmcnt(0)\n"
        "s_add_u32 s70, s70, 4\n"
        "s_addc_u32 s71, s71, 0\n"
        "s_setpc_b64 s[70:71]\n"
        ".Ln_ret:\n"
        "s_setpc_b64 s[70:71]\n"
        ".Ln_end:\n"
        :: "{s[0:1]}"((u64)__builtin_amdgcn_dispatch_ptr()), "{s[2:3]}"((u64)__builtin_amdgcn_kernarg_segment_ptr()), "{s[4:5]}"(llvm_dispatch_id()),
           "{s6}"(blockIdx.x), "{v0}"(threadIdx.x), "s"((unsigned)(size_t)(__attribute__((address_space(3))) unsigned char*)smem)
        : "v1", "v2", "v3", "v4", "v5", "v6", "v7", "v8", "v9", "v10", "v11", "v12", "v13", "v14", "v15", "v16", "v17", "v18", "v19", "v20", "v21", "v22", "v23", "v24", "v25", "v26", "v27", "v28", "v29", "v30", "v31", "v32", "v33", "v34", "v35", "v36", "v37", "v38", "v39", "v40", "v41", "v42", "v43", "v44", "v45", "v46", "v47", "v48", "v49", "v50", "v51", "v52", "v53", "v54", "v55", "v56", "v57", "v58", "v59", "v60", "v61", "v62", "v63", "v64", "v65", "v66", "v67", "v68", "v69", "v70", "v71", "v72", "v73", "v74", "v75", "v76", "v77", "v78", "v79", "v80", "v81", "v82", "v83", "v84", "v85", "v86", "v87", "v88", "v89", "v90", "v91", "v92", "v93", "v94", "v95", "v96", "v97", "v98", "v99", "v100", "v101", "v102", "v103", "v104", "v105", "v106", "v107", "v108", "v109", "v110", "v111", "v112", "v113", "v114", "v115", "v116", "v117", "v118", "v119", "v120", "v121", "v122", "v123", "v124", "v125", "v126", "v127", "v128", "v129", "v130", "v131", "v132", "v133", "v134", "v135", "v136", "v137", "v138", "v139", "v140", "v141", "v142", "v143", "v144", "v145", "v146", "v147", "v148", "v149", "v150", "v151", "v152", "v153", "v154", "v155", "v156", "v157", "v158", "v159", "v160", "v161", "v162", "v163", "v164", "v165", "v166", "v167", "v168", "v169", "v170", "v171", "v172", "v173", "v174", "v175", "v176", "v177", "v178", "v179", "v180", "v181", "v182", "v183", "v184", "v185", "v186", "v187", "v188", "v189", "v190", "v191", "v192", "v193", "v194", "v195", "v196", "v197", "v198", "v199", "v200", "v201", "v202", "v203", "v204", "v205", "v206", "v207", "v208", "v209", "v210", "v211", "v212", "v213", "v214", "v215", "v216", "v217", "v218", "v219", "v220", "v221", "v222", "v223", "v224", "v225", "v226", "v227", "v228", "v229", "v230", "v231", "v232", "v233", "v234", "v235", "v236", "v237", "v238", "v239", "v240", "v241", "v242", "v243", "v244", "v245", "v246", "v247", "v248", "v249", "v250", "v251", "v252", "v253", "v254", "v255", "s7", "s8", "s9", "s10", "s11", "s12", "s13", "s14", "s15", "s16", "s17", "s18", "s19", "s20", "s21", "s22", "s23", "s24", "s25", "s26", "s27", "s28", "s29", "s30", "s31", "s32", "s33", "s34", "s35", "s36", "s37", "s38", "s39", "s40", "s41", "s42", "s43", "s44", "s45", "s46", "s47", "s48", "s49", "s50", "s51", "s52", "s53", "s54", "s55", "s56", "s57", "s58", "s59", "s60", "s61", "s62", "s63", "s64", "s65", "s66", "s67", "s68", "s69", "s70", "s71", "s72", "s73", "s74", "s75", "s76", "s77", "s78", "s79", "s80", "s81", "s82", "s83", "s84", "s85", "s86", "s87", "s88", "s89", "s90", "s91", "s92", "s93", "s94", "s95", "vcc", "scc", "memory");
}
}

extern "C" void kernel_launch(void* const* d_in, const int* in_sizes, int n_in, void* d_out, int out_size, void* d_ws, size_t ws_size, hipStream_t stream) {
    const u64 fl = (u64)0x00020000 << 32;
    nf4_gemm_kernel<<<dim3(256), dim3(512), 0, stream>>>(d_in[1], fl | (1024u * 3584u * 4u), d_in[0], fl | (2048u * 1024u * 2u), (char*)d_ws + (32u << 20), fl | ((4u << 20) + 114688u),
                                                         d_out, fl | (2048u * 3584u * 2u), d_in[2], d_in[3], d_ws);
}
```

```cpp
#include <hip/hip_runtime.h>
#include <stdint.h>

extern "C" __device__ unsigned long long llvm_dispatch_id() __asm("llvm.amdgcn.dispatch.id");

namespace {
typedef unsigned long long u64;
__global__ __launch_bounds__(512, 2)
void nf4_gemm_kernel(const void* q, u64 qd, const void* x, u64 xd, void* w, u64 wd, void* o, u64 od, const void* lut, const void* scales, void* dbg) {
    __shared__ __attribute__((aligned(16))) unsigned char smem[163840];
    asm volatile(
        ".Ln_start:\n"
        "s_load_dwordx16 s[8:23], s[2:3], 0x0\n"
        "s_load_dwordx4 s[24:27], s[2:3], 0x40\n"
        "v_readfirstlane_b32 s30, v0\n"
        "v_and_b32 v252, 63, v0\n"
        "s_getpc_b64 s[86:87]\n"
        ".Ln_here:\n"
        "s_cmp_lt_u32 s30, 448\n"
        "s_cbranch_scc1 1f\n"
        "s_lshr_b32 s35, s6, 3\n"
        "s_lshl_b32 s35, s35, 7\n"
        "v_lshl_add_u32 v3, v252, 12, s35\n"
        "v_min_u32 v3, .Ln_end-.Ln_here, v3\n"
        "global_load_dword v110, v3, s[86:87]\n"
        "1:\n"
        "s_and_b32 s32, s6, 3\n"
        "s_lshl_b32 s32, s32, 2\n"
        "s_bfe_u32 s35, s6, 0x20003\n"
        "s_or_b32 s32, s32, s35\n"
        "s_and_b32 s33, s6, 4\n"
        "s_lshl_b32 s33, s33, 1\n"
        "s_lshr_b32 s35, s6, 5\n"
        "s_or_b32 s33, s33, s35\n"
        "s_lshr_b32 s30, s30, 6\n"
        "s_and_b32 s31, s30, 3\n"
        "s_and_b32 s35, s6, 7\n"
        "s_lshl_b32 s35, s35, 2\n"
        "s_lshr_b32 s69, 0x54763210, s35\n"
        "s_and_b32 s69, s69, 7\n"
        "s_lshl_b32 s69, s69, 2\n"
        "s_and_b32 s35, s32, 3\n"
        "s_or_b32 s69, s69, s35\n"
        "v_lshrrev_b32 v1, 3, v252\n"
        "v_and_b32 v2, 7, v252\n"
        "v_mul_u32_u24 v4, 0x1c000, v1\n"
        "v_lshl_add_u32 v249, v2, 4, v4\n"
        "s_cmp_ge_u32 s30, 4\n"
        "s_cbranch_scc1 .Ln_helper\n"
        "s_lshl_b32 s72, s69, 1\n"
        "s_add_u32 s72, s72, s30\n"
        "s_mul_i32 s35, s72, 9363\n"
        "s_lshr_b32 s35, s35, 16\n"
        "s_mul_i32 s36, s35, 7\n"
        "s_sub_u32 s36, s72, s36\n"
        "s_and_b32 s79, s35, 1\n"
        "s_lshl_b32 s79, s79, 3\n"
        "s_and_b32 s37, s33, 7\n"
        "s_or_b32 s79, s79, s37\n"
        "s_lshr_b32 s35, s35, 1\n"
        "s_mul_i32 s40, s35, 917504\n"
        "s_mul_i32 s37, s79, 896\n"
        "s_lshl_b32 s38, s36, 7\n"
        "s_add_u32 s40, s40, s37\n"
        "s_add_u32 s40, s40, s38\n"
        "s_add_u32 s41, s40, 14336\n"
        "s_add_u32 s42, s41, 14336\n"
        "s_add_u32 s43, s42, 14336\n"
        "s_add_u32 s44, s43, 14336\n"
        "s_add_u32 s45, s44, 14336\n"
        "s_add_u32 s46, s45, 14336\n"
        "s_add_u32 s47, s46, 14336\n"
        "s_cmp_ge_u32 s30, 2\n"
        "s_cbranch_scc1 2f\n"
        "s_waitcnt lgkmcnt(0)\n"
        "buffer_load_dwordx4 v[112:115], v249, s[8:11], s40 offen\n"
        "buffer_load_dwordx4 v[116:119], v249, s[8:11], s41 offen\n"
        "buffer_load_dwordx4 v[120:123], v249, s[8:11], s42 offen\n"
        "buffer_load_dwordx4 v[124:127], v249, s[8:11], s43 offen\n"
        "buffer_load_dwordx4 v[128:131], v249, s[8:11], s44 offen\n"
        "buffer_load_dwordx4 v[132:135], v249, s[8:11], s45 offen\n"
        "buffer_load_dwordx4 v[136:139], v249, s[8:11], s46 offen\n"
        "buffer_load_dwordx4 v[140:143], v249, s[8:11], s47 offen\n"
        "2:\n"
        "s_waitcnt lgkmcnt(0)\n"
        "s_lshl_b32 s37, s31, 3\n"
        "s_load_dwordx8 s[48:55], s[24:25], 0x0\n"
        "s_load_dwordx2 s[56:57], s[24:25], s37\n"
        "s_lshl_b32 s75, s79, 4\n"
        "s_add_u32 s75, s75, s35\n"
        "s_lshl_b32 s75, s75, 3\n"
        "s_add_u32 s75, s75, s36\n"
        "s_lshl_b32 s75, s75, 11\n"
        "s_add_u32 s75, s75, 114688\n"
        "s_mul_i32 s34, s4, 0x7f4a7c15\n"
        "s_mul_hi_u32 s39, s4, 0x7f4a7c15\n"
        "s_mul_i32 s35, s4, 0x9e3779b9\n"
        "s_mul_i32 s36, s5, 0x7f4a7c15\n"
        "s_add_u32 s39, s39, s35\n"
        "s_add_u32 s39, s39, s36\n"
        "s_xor_b32 s34, s34, s0\n"
        "s_xor_b32 s39, s39, s1\n"
        "s_xor_b32 s34, s34, 0x5bd1e995\n"
        "s_mov_b32 s73, 0\n"
        "s_mov_b32 s74, 20000\n"
        "s_lshr_b32 s66, s31, 1\n"
        "s_and_b32 s67, s31, 1\n"
        "s_lshl_b32 s68, s66, 2\n"
        "s_add_u32 s68, s68, s67\n"
        "s_lshl_b32 s64, s33, 18\n"
        "s_add_u32 s65, s64, 0x4000\n"
        "v_and_b32 v184, 15, v252\n"
        "v_lshrrev_b32 v185, 4, v252\n"
        "v_and_b32 v186, 3, v1\n"
        "v_lshlrev_b32 v186, 8, v186\n"
        "v_lshl_add_u32 v186, v2, 4, v186\n"
        "v_lshrrev_b32 v250, 5, v252\n"
        "v_lshl_add_u32 v250, v250, 7, v186\n"
        "v_xor_b32 v251, 0xc0, v250\n"
        "v_mov_b32 v178, s34\n"
        "v_mov_b32 v179, s39\n"
        "v_mov_b32 v182, s34\n"
        "v_mov_b32 v183, s39\n"
        "v_and_b32 v192, 3, v1\n"
        "v_mul_u32_u24 v193, 0x202, v192\n"
        "v_add_u32 v193, 0x0c0c0100, v193\n"
        "v_lshlrev_b32 v192, 8, v192\n"
        "v_lshrrev_b32 v144, 5, v252\n"
        "v_lshl_add_u32 v192, v144, 12, v192\n"
        "v_mul_u32_u24 v144, 0x202, v144\n"
        "v_lshlrev_b32 v144, 16, v144\n"
        "v_add_u32 v144, 0x01000c0c, v144\n"
        "v_lshl_add_u32 v192, v2, 4, v192\n"
        "s_lshl_b32 s35, s31, 14\n"
        "v_add_u32 v192, s35, v192\n"
        "v_and_b32 v186, 1, v184\n"
        "v_lshrrev_b32 v187, 1, v184\n"
        "v_lshl_add_u32 v187, v185, 4, v187\n"
        "v_mul_u32_u24 v188, 0x4c, v186\n"
        "v_xor_b32 v187, v187, v188\n"
        "s_lshl_b32 s36, s68, 11\n"
        "s_add_u32 s36, s36, 114688\n"
        "v_lshl_add_u32 v232, v187, 4, s36\n"
        "v_xor_b32 v233, 0x80, v232\n"
        "v_lshrrev_b32 v187, 2, v184\n"
        "v_lshl_add_u32 v187, v185, 4, v187\n"
        "v_lshl_add_u32 v187, v186, 3, v187\n"
        "v_bfe_u32 v188, v184, 1, 1\n"
        "v_mul_u32_u24 v188, 0x4c, v188\n"
        "s_lshl_b32 s36, s67, 2\n"
        "v_xor_b32 v188, s36, v188\n"
        "v_xor_b32 v187, v188, v187\n"
        "s_mov_b32 s36, 120832\n"
        "v_lshl_add_u32 v235, v187, 4, s36\n"
        "s_lshl_b32 s35, s66, 13\n"
        "s_sub_u32 s35, s35, 2048\n"
        "v_add_u32 v234, s35, v235\n"
        "v_and_b32 v236, 31, v252\n"
        "v_lshlrev_b32 v236, 2, v236\n"
        "v_bfe_u32 v186, v184, 1, 3\n"
        "v_xor_b32 v186, v186, v185\n"
        "v_lshlrev_b32 v186, 4, v186\n"
        "v_lshl_add_u32 v186, v184, 7, v186\n"
        "s_lshl_b32 s35, s66, 13\n"
        "s_add_u32 s35, s35, 65536\n"
        "v_add_u32 v228, s35, v186\n"
        "v_xor_b32 v229, 64, v228\n"
        "v_xor_b32 v230, 0x2000, v228\n"
        "v_xor_b32 v231, 0x2000, v229\n"
        "s_waitcnt lgkmcnt(0)\n"
        "v_mov_b32 v194, s48\n"
        "v_mov_b32 v195, s50\n"
        "v_mov_b32 v196, s52\n"
        "v_mov_b32 v197, s54\n"
        "v_perm_b32 v194, s49, v194, v193\n"
        "v_perm_b32 v195, s51, v195, v193\n"
        "v_perm_b32 v196, s53, v196, v193\n"
        "v_perm_b32 v197, s55, v197, v193\n"
        "v_perm_b32 v198, s56, s56, v144\n"
        "v_perm_b32 v199, s57, s57, v144\n"
        "v_or_b32 v144, v194, v198\n"
        "v_or_b32 v145, v194, v198\n"
        "v_or_b32 v146, v194, v198\n"
        "v_or_b32 v147, v194, v198\n"
        "ds_write_b128 v192, v[144:147] offset:0\n"
        "v_or_b32 v148, v195, v198\n"
        "v_or_b32 v149, v195, v198\n"
        "v_or_b32 v150, v195, v198\n"
        "v_or_b32 v151, v195, v198\n"
        "ds_write_b128 v192, v[148:151] offset:1024\n"
        "v_or_b32 v152, v196, v198\n"
        "v_or_b32 v153, v196, v198\n"
        "v_or_b32 v154, v196, v198\n"
        "v_or_b32 v155, v196, v198\n"
        "ds_write_b128 v192, v[152:155] offset:2048\n"
        "v_or_b32 v156, v197, v198\n"
        "v_or_b32 v157, v197, v198\n"
        "v_or_b32 v158, v197, v198\n"
        "v_or_b32 v159, v197, v198\n"
        "ds_write_b128 v192, v[156:159] offset:3072\n"
        "v_or_b32 v160, v194, v199\n"
        "v_or_b32 v161, v194, v199\n"
        "v_or_b32 v162, v194, v199\n"
        "v_or_b32 v163, v194, v199\n"
        "ds_write_b128 v192, v[160:163] offset:8192\n"
        "v_or_b32 v164, v195, v199\n"
        "v_or_b32 v165, v195, v199\n"
        "v_or_b32 v166, v195, v199\n"
        "v_or_b32 v167, v195, v199\n"
        "ds_write_b128 v192, v[164:167] offset:9216\n"
        "v_or_b32 v168, v196, v199\n"
        "v_or_b32 v169, v196, v199\n"
        "v_or_b32 v170, v196, v199\n"
        "v_or_b32 v171, v196, v199\n"
        "ds_write_b128 v192, v[168:171] offset:10240\n"
        "v_or_b32 v172, v197, v199\n"
        "v_or_b32 v173, v197, v199\n"
        "v_or_b32 v174, v197, v199\n"
        "v_or_b32 v175, v197, v199\n"
        "ds_write_b128 v192, v[172:175] offset:11264\n"
        "s_mul_i32 s35, s33, 448\n"
        "s_lshl_b32 s36, s68, 6\n"
        "s_add_u32 s36, s36, s35\n"
        "v_lshl_add_u32 v108, v185, 4, s36\n"
        "s_lshl_b32 s36, s67, 5\n"
        "s_add_u32 s35, s35, s36\n"
        "v_lshl_add_u32 v109, v185, 3, s35\n"
        "s_lshl_b32 s36, s66, 8\n"
        "v_add_u32 v110, s36, v109\n"
        "v_and_b32 v186, 7, v184\n"
        "s_lshl_b32 s35, s66, 12\n"
        "s_add_u32 s35, s35, 98304\n"
        "v_lshl_add_u32 v187, v184, 8, s35\n"
        "s_add_u32 s36, s66, s67\n"
        "s_lshl_b32 s36, s36, 2\n"
        "v_add_u32 v188, s36, v185\n"
        "v_xor_b32 v188, v188, v186\n"
        "v_lshl_add_u32 v246, v188, 4, v187\n"
        "v_and_b32 v188, 1, v185\n"
        "v_lshl_add_u32 v187, v188, 3, v187\n"
        "v_lshrrev_b32 v185, 1, v185\n"
        "s_lshl_b32 s35, s67, 1\n"
        "s_lshl_b32 s36, s66, 2\n"
        "s_add_u32 s36, s36, s35\n"
        "s_add_u32 s36, s36, 8\n"
        "v_add_u32 v188, s36, v185\n"
        "v_xor_b32 v188, v188, v186\n"
        "v_lshl_add_u32 v247, v188, 4, v187\n"
        "s_mul_i32 s36, s66, 12\n"
        "s_sub_u32 s36, s35, s36\n"
        "s_add_u32 s36, s36, 12\n"
        "v_add_u32 v188, s36, v185\n"
        "v_xor_b32 v188, v188, v186\n"
        "v_lshl_add_u32 v248, v188, 4, v187\n"
        "s_lshl_b32 s35, s31, 2\n"
        "s_add_u32 s35, s35, 128\n"
        "v_mov_b32 v237, s35\n"
        "s_mov_b32 s60, 0x0c0c0400\n"
        "s_mov_b32 s61, 0x0c0c0500\n"
        "s_mov_b32 s62, 0x0c0c0600\n"
        "s_mov_b32 s63, 0x0c0c0700\n"
        "global_load_dwordx4 v[238:241], v108, s[26:27]\n"
        "global_load_dwordx2 v[242:243], v110, s[26:27] offset:128\n"
        "global_load_dwordx2 v[244:245], v109, s[26:27] offset:192\n"
        "s_cmp_ge_u32 s30, 2\n"
        "s_cbranch_scc1 .Ln_none\n"
        "s_waitcnt vmcnt(3)\n"
        "v_lshl_add_u64 v[140:141], v[140:141], 4, v[136:137]\n"
        "v_lshl_add_u64 v[142:143], v[142:143], 4, v[138:139]\n"
        "v_lshl_add_u64 v[140:141], v[140:141], 4, v[132:133]\n"
        "v_lshl_add_u64 v[142:143], v[142:143], 4, v[134:135]\n"
        "v_lshl_add_u64 v[140:141], v[140:141], 4, v[128:129]\n"
        "v_lshl_add_u64 v[142:143], v[142:143], 4, v[130:131]\n"
        "v_lshl_add_u64 v[140:141], v[140:141], 4, v[124:125]\n"
        "v_lshl_add_u64 v[142:143], v[142:143], 4, v[126:127]\n"
        "v_lshl_add_u64 v[140:141], v[140:141], 4, v[120:121]\n"
        "v_lshl_add_u64 v[142:143], v[142:143], 4, v[122:123]\n"
        "v_lshl_add_u64 v[140:141], v[140:141], 4, v[116:117]\n"
        "v_lshl_add_u64 v[142:143], v[142:143], 4, v[118:119]\n"
        "v_lshl_add_u64 v[176:177], v[140:141], 4, v[112:113]\n"
        "v_lshl_add_u64 v[180:181], v[142:143], 4, v[114:115]\n"
        "s_nop 0\n"
        "v_permlane32_swap_b32 v176, v177\n"
        "v_permlane32_swap_b32 v180, v181\n"
        "buffer_store_dwordx4 v[176:179], v250, s[16:19], s75 offen sc1\n"
        "buffer_store_dwordx4 v[180:183], v251, s[16:19], s75 offen offset:1024 sc1\n"
        ".Ln_none:\n"
        "s_waitcnt lgkmcnt(0)\n"
        "s_barrier\n"
        "s_cmp_lt_u32 s69, 16\n"
        "s_cbranch_scc0 1f\n"
        "s_sleep 4\n"
        "1:\n"
        "s_sleep 6\n"
        "buffer_load_dwordx4 v[64:67], v232, s[16:19], s64 offen sc1\n"
        "buffer_load_dwordx4 v[68:71], v233, s[16:19], s64 offen sc1\n"
        "buffer_load_dwordx4 v[72:75], v234, s[16:19], s64 offen sc1\n"
        "buffer_load_dwordx4 v[76:79], v235, s[16:19], s64 offen sc1\n"
        "buffer_load_dwordx4 v[208:211], v232, s[16:19], s65 offen sc1\n"
        "buffer_load_dwordx4 v[212:215], v233, s[16:19], s65 offen sc1\n"
        "buffer_load_dwordx4 v[216:219], v234, s[16:19], s65 offen sc1\n"
        "buffer_load_dwordx4 v[220:223], v235, s[16:19], s65 offen sc1\n"
        "3:\n"
        "ds_read_b128 v[176:179], v228 offset:16384\n"
        "ds_read_b128 v[180:183], v229 offset:16384\n"
        "ds_read_b128 v[184:187], v228 offset:18432\n"
        "ds_read_b128 v[188:191], v229 offset:18432\n"
        "ds_read_b128 v[192:195], v228 offset:20480\n"
        "ds_read_b128 v[196:199], v229 offset:20480\n"
        "ds_read_b128 v[200:203], v228 offset:22528\n"
        "ds_read_b128 v[204:207], v229 offset:22528\n"
        "ds_read_b128 v[80:83], v230 offset:16384\n"
        "ds_read_b128 v[84:87], v231 offset:16384\n"
        "ds_read_b128 v[88:91], v230 offset:18432\n"
        "ds_read_b128 v[92:95], v231 offset:18432\n"
        "ds_read_b128 v[100:103], v230 offset:20480\n"
        "ds_read_b128 v[104:107], v231 offset:20480\n"
        "s_branch .Ln_go\n"
        ".Ln_helper:\n"
        "s_lshl_b32 s35, s32, 2\n"
        "s_add_u32 s35, s35, s31\n"
        "s_lshl_b32 s35, s35, 16\n"
        "v_lshrrev_b32 v80, 4, v252\n"
        "v_xor_b32 v81, v80, v2\n"
        "v_lshl_add_u32 v82, v1, 11, s35\n"
        "v_lshl_add_u32 v8, v81, 4, v82\n"
        "v_xor_b32 v9, 64, v8\n"
        "v_add_u32 v9, 0x3c00, v9\n"
        "v_add_u32 v10, 0x7800, v8\n"
        "v_add_u32 v11, 0x7800, v9\n"
        "s_lshl_b32 s58, s31, 12\n"
        "s_add_u32 s58, s58, 65536\n"
        "s_waitcnt lgkmcnt(0)\n"
        "s_add_u32 m0, s58, 16384\n"
        "s_movk_i32 s59, 0\n"
        "buffer_load_dwordx4 v8, s[12:15], s59 offen offset:0 lds\n"
        "buffer_load_dwordx4 v9, s[12:15], s59 offen offset:1024 lds\n"
        "buffer_load_dwordx4 v10, s[12:15], s59 offen offset:2048 lds\n"
        "buffer_load_dwordx4 v11, s[12:15], s59 offen offset:3072 lds\n"
        "s_mul_i32 s34, s4, 0x7f4a7c15\n"
        "s_mul_hi_u32 s39, s4, 0x7f4a7c15\n"
        "s_mul_i32 s35, s4, 0x9e3779b9\n"
        "s_mul_i32 s36, s5, 0x7f4a7c15\n"
        "s_add_u32 s39, s39, s35\n"
        "s_add_u32 s39, s39, s36\n"
        "s_xor_b32 s34, s34, s0\n"
        "s_xor_b32 s39, s39, s1\n"
        "s_xor_b32 s34, s34, 0x5bd1e995\n"
        "s_cmp_lt_u32 s31, 3\n"
        "s_cselect_b32 s35, 0, 0x7f000000\n"
        "s_lshl_b32 s64, s33, 18\n"
        "s_add_u32 s36, s58, 49152\n"
        "v_lshl_add_u32 v12, v252, 4, s36\n"
        "v_or_b32 v13, s35, v12\n"
        "v_and_b32 v83, 3, v1\n"
        "v_lshlrev_b32 v83, 8, v83\n"
        "v_lshl_add_u32 v83, v2, 4, v83\n"
        "v_lshrrev_b32 v250, 5, v252\n"
        "v_lshl_add_u32 v250, v250, 7, v83\n"
        "v_xor_b32 v251, 0xc0, v250\n"
        "v_mov_b32 v74, s34\n"
        "v_mov_b32 v75, s39\n"
        "v_mov_b32 v78, s34\n"
        "v_mov_b32 v79, s39\n"
        "v_mov_b32 v83, 0\n"
        "s_lshl_b32 s35, s31, 2\n"
        "s_add_u32 s35, s35, 128\n"
        "v_lshl_add_u32 v84, v252, 8, s35\n"
        "ds_write_b32 v84, v83\n"
        "v_and_b32 v81, 15, v252\n"
        "s_lshr_b32 s66, s31, 1\n"
        "s_and_b32 s67, s31, 1\n"
        "v_xor_b32 v82, v80, v81\n"
        "v_lshlrev_b32 v82, 4, v82\n"
        "v_lshl_add_u32 v82, v80, 8, v82\n"
        "s_lshl_b32 s37, s31, 11\n"
        "s_add_u32 s37, s37, 98304\n"
        "v_add_u32 v88, s37, v82\n"
        "v_xor_b32 v89, 64, v88\n"
        "s_lshl_b32 s35, s32, 4\n"
        "s_add_u32 s35, s35, s67\n"
        "s_mul_i32 s35, s35, 57344\n"
        "s_mul_i32 s36, s33, 448\n"
        "s_add_u32 s35, s35, s36\n"
        "s_mul_i32 s36, s66, 192\n"
        "s_add_u32 s35, s35, s36\n"
        "v_mul_u32_u24 v82, 0x1c00, v80\n"
        "v_lshl_add_u32 v82, v81, 4, v82\n"
        "v_add_u32 v90, s35, v82\n"
        "s_lshl_b32 s36, s66, 2\n"
        "v_add_u32 v100, s36, v2\n"
        "v_add_u32 v101, 8, v100\n"
        "v_and_b32 v101, 15, v101\n"
        "v_mul_u32_u24 v82, 0x1c00, v1\n"
        "v_add_u32 v82, s35, v82\n"
        "v_lshl_add_u32 v102, v100, 4, v82\n"
        "v_lshl_add_u32 v103, v101, 4, v82\n"
        "v_lshl_add_u32 v82, v1, 8, s37\n"
        "v_xor_b32 v100, v1, v100\n"
        "v_xor_b32 v101, v1, v101\n"
        "v_lshl_add_u32 v100, v100, 4, v82\n"
        "v_lshl_add_u32 v101, v101, 4, v82\n"
        "v_subrev_u32 v81, s36, v81\n"
        "v_cmp_gt_u32 vcc, 12, v81\n"
        "v_mov_b32 v82, 0x7f000000\n"
        "s_lshl_b32 s35, s66, 3\n"
        "v_cndmask_b32 v91, v82, v90, vcc\n"
        "s_add_u32 s88, s35, 128\n"
        "s_add_u32 s89, s88, 4\n"
        "v_mov_b32 v92, s88\n"
        "s_mul_i32 s84, s66, 458752\n"
        "s_sub_u32 s85, 458752, s84\n"
        "s_cmp_eq_u32 s31, 1\n"
        "s_cbranch_scc1 .Ln_h1\n"
        "s_cmp_eq_u32 s31, 2\n"
        "s_cbranch_scc1 .Ln_h2\n"
        "s_cmp_eq_u32 s31, 3\n"
        "s_cbranch_scc1 .Ln_h3\n"
        ".Ln_h0:\n"
        "s_add_u32 s72, s69, 64\n"
        "s_mul_i32 s35, s72, 9363\n"
        "s_lshr_b32 s35, s35, 16\n"
        "s_mul_i32 s36, s35, 7\n"
        "s_sub_u32 s36, s72, s36\n"
        "s_and_b32 s79, s35, 1\n"
        "s_lshl_b32 s79, s79, 3\n"
        "s_and_b32 s37, s33, 7\n"
        "s_or_b32 s79, s79, s37\n"
        "s_lshr_b32 s35, s35, 1\n"
        "s_mul_i32 s40, s35, 917504\n"
        "s_mul_i32 s37, s79, 896\n"
        "s_lshl_b32 s38, s36, 7\n"
        "s_add_u32 s40, s40, s37\n"
        "s_add_u32 s40, s40, s38\n"
        "s_add_u32 s41, s40, 14336\n"
        "s_add_u32 s42, s41, 14336\n"
        "s_add_u32 s43, s42, 14336\n"
        "s_add_u32 s44, s43, 14336\n"
        "s_add_u32 s45, s44, 14336\n"
        "s_add_u32 s46, s45, 14336\n"
        "s_add_u32 s47, s46, 14336\n"
        "s_lshl_b32 s75, s79, 4\n"
        "s_add_u32 s75, s75, s35\n"
        "s_lshl_b32 s75, s75, 3\n"
        "s_add_u32 s75, s75, s36\n"
        "s_lshl_b32 s75, s75, 11\n"
        "s_add_u32 s75, s75, 114688\n"
        "buffer_load_dwordx4 v[40:43], v249, s[8:11], s40 offen\n"
        "buffer_load_dwordx4 v[44:47], v249, s[8:11], s41 offen\n"
        "buffer_load_dwordx4 v[48:51], v249, s[8:11], s42 offen\n"
        "buffer_load_dwordx4 v[52:55], v249, s[8:11], s43 offen\n"
        "buffer_load_dwordx4 v[56:59], v249, s[8:11], s44 offen\n"
        "buffer_load_dwordx4 v[60:63], v249, s[8:11], s45 offen\n"
        "buffer_load_dwordx4 v[64:67], v249, s[8:11], s46 offen\n"
        "buffer_load_dwordx4 v[68:71], v249, s[8:11], s47 offen\n"
        "s_waitcnt vmcnt(8)\n"
        "s_waitcnt lgkmcnt(0)\n"
        "s_barrier\n"
        "s_cmp_lt_u32 s69, 16\n"
        "s_cbranch_scc0 1f\n"
        "s_sleep 4\n"
        "1:\n"
        "s_sleep 10\n"
        "s_add_u32 m0, s58, 32768\n"
        "s_movk_i32 s59, 128\n"
        "buffer_load_dwordx4 v8, s[12:15], s59 offen offset:0 lds\n"
        "buffer_load_dwordx4 v9, s[12:15], s59 offen offset:1024 lds\n"
        "buffer_load_dwordx4 v10, s[12:15], s59 offen offset:2048 lds\n"
        "buffer_load_dwordx4 v11, s[12:15], s59 offen offset:3072 lds\n"
        "s_add_u32 m0, s58, 65536\n"
        "s_add_u32 s76, s64, 32768\n"
        "buffer_load_dwordx4 v12, s[16:19], s76 offen offset:0 sc1 lds\n"
        "buffer_load_dwordx4 v12, s[16:19], s76 offen offset:1024 sc1 lds\n"
        "buffer_load_dwordx4 v12, s[16:19], s76 offen offset:2048 sc1 lds\n"
        "buffer_load_dwordx4 v12, s[16:19], s76 offen offset:3072 sc1 lds\n"
        "s_waitcnt vmcnt(8)\n"
        "v_lshl_add_u64 v[68:69], v[68:69], 4, v[64:65]\n"
        "v_lshl_add_u64 v[70:71], v[70:71], 4, v[66:67]\n"
        "v_lshl_add_u64 v[68:69], v[68:69], 4, v[60:61]\n"
        "v_lshl_add_u64 v[70:71], v[70:71], 4, v[62:63]\n"
        "v_lshl_add_u64 v[68:69], v[68:69], 4, v[56:57]\n"
        "v_lshl_add_u64 v[70:71], v[70:71], 4, v[58:59]\n"
        "v_lshl_add_u64 v[68:69], v[68:69], 4, v[52:53]\n"
        "v_lshl_add_u64 v[70:71], v[70:71], 4, v[54:55]\n"
        "v_lshl_add_u64 v[68:69], v[68:69], 4, v[48:49]\n"
        "v_lshl_add_u64 v[70:71], v[70:71], 4, v[50:51]\n"
        "v_lshl_add_u64 v[68:69], v[68:69], 4, v[44:45]\n"
        "v_lshl_add_u64 v[70:71], v[70:71], 4, v[46:47]\n"
        "v_lshl_add_u64 v[72:73], v[68:69], 4, v[40:41]\n"
        "v_lshl_add_u64 v[76:77], v[70:71], 4, v[42:43]\n"
        "s_nop 0\n"
        "v_permlane32_swap_b32 v72, v73\n"
        "v_permlane32_swap_b32 v76, v77\n"
        "buffer_store_dwordx4 v[72:75], v250, s[16:19], s75 offen sc1\n"
        "buffer_store_dwordx4 v[76:79], v251, s[16:19], s75 offen offset:1024 sc1\n"
        "s_add_u32 s72, s69, 192\n"
        "s_mul_i32 s35, s72, 9363\n"
        "s_lshr_b32 s35, s35, 16\n"
        "s_mul_i32 s36, s35, 7\n"
        "s_sub_u32 s36, s72, s36\n"
        "s_and_b32 s79, s35, 1\n"
        "s_lshl_b32 s79, s79, 3\n"
        "s_and_b32 s37, s33, 7\n"
        "s_or_b32 s79, s79, s37\n"
        "s_lshr_b32 s35, s35, 1\n"
        "s_mul_i32 s40, s35, 917504\n"
        "s_mul_i32 s37, s79, 896\n"
        "s_lshl_b32 s38, s36, 7\n"
        "s_add_u32 s40, s40, s37\n"
        "s_add_u32 s40, s40, s38\n"
        "s_add_u32 s41, s40, 14336\n"
        "s_add_u32 s42, s41, 14336\n"
        "s_add_u32 s43, s42, 14336\n"
        "s_add_u32 s44, s43, 14336\n"
        "s_add_u32 s45, s44, 14336\n"
        "s_add_u32 s46, s45, 14336\n"
        "s_add_u32 s47, s46, 14336\n"
        "s_lshl_b32 s75, s79, 4\n"
        "s_add_u32 s75, s75, s35\n"
        "s_lshl_b32 s75, s75, 3\n"
        "s_add_u32 s75, s75, s36\n"
        "s_lshl_b32 s75, s75, 11\n"
        "s_add_u32 s75, s75, 114688\n"
        "v_mov_b32 v84, 128\n"
        "s_movk_i32 s73, 64\n"
        "1:\n"
        "s_sub_u32 s73, s73, 1\n"
        "s_cbranch_scc1 2f\n"
        "s_sleep 1\n"
        "ds_read_b128 v[96:99], v84 offset:4096\n"
        "s_waitcnt lgkmcnt(0)\n"
        "v_min3_u32 v96, v96, v97, v98\n"
        "v_min_u32 v96, v96, v99\n"
        "v_cmp_eq_u32 vcc, 0, v96\n"
        "s_cbranch_vccnz 1b\n"
        "2:\n"
        "s_add_u32 m0, s58, 0\n"
        "s_movk_i32 s59, 256\n"
        "buffer_load_dwordx4 v8, s[12:15], s59 offen offset:0 lds\n"
        "buffer_load_dwordx4 v9, s[12:15], s59 offen offset:1024 lds\n"
        "buffer_load_dwordx4 v10, s[12:15], s59 offen offset:2048 lds\n"
        "buffer_load_dwordx4 v11, s[12:15], s59 offen offset:3072 lds\n"
        "s_add_u32 m0, s58, 81920\n"
        "s_add_u32 s76, s64, 49152\n"
        "buffer_load_dwordx4 v12, s[16:19], s76 offen offset:0 sc1 lds\n"
        "buffer_load_dwordx4 v12, s[16:19], s76 offen offset:1024 sc1 lds\n"
        "buffer_load_dwordx4 v12, s[16:19], s76 offen offset:2048 sc1 lds\n"
        "buffer_load_dwordx4 v12, s[16:19], s76 offen offset:3072 sc1 lds\n"
        "s_mov_b64 exec, 0\n"
        "s_add_u32 s94, s86, .Ln_tp0-.Ln_here\n"
        "s_addc_u32 s95, s87, 0\n"
        "s_swappc_b64 s[70:71], s[94:95]\n"
        "s_mov_b64 exec, -1\n"
        "s_waitcnt vmcnt(10)\n"
        "s_barrier\n"
        "s_add_u32 m0, s58, 16384\n"
        "s_movk_i32 s59, 384\n"
        "buffer_load_dwordx4 v8, s[12:15], s59 offen offset:0 lds\n"
        "buffer_load_dwordx4 v9, s[12:15], s59 offen offset:1024 lds\n"
        "buffer_load_dwordx4 v10, s[12:15], s59 offen offset:2048 lds\n"
        "buffer_load_dwordx4 v11, s[12:15], s59 offen offset:3072 lds\n"
        "s_add_u32 m0, s58, 49152\n"
        "s_add_u32 s76, s64, 65536\n"
        "buffer_load_dwordx4 v12, s[16:19], s76 offen offset:0 sc1 lds\n"
        "buffer_load_dwordx4 v12, s[16:19], s76 offen offset:1024 sc1 lds\n"
        "buffer_load_dwordx4 v12, s[16:19], s76 offen offset:2048 sc1 lds\n"
        "buffer_load_dwordx4 v12, s[16:19], s76 offen offset:3072 sc1 lds\n"
        "s_waitcnt vmcnt(8)\n"
        "s_barrier\n"
        "s_add_u32 m0, s58, 65536\n"
        "s_add_u32 s76, s64, 81920\n"
        "buffer_load_dwordx4 v12, s[16:19], s76 offen offset:0 sc1 lds\n"
        "buffer_load_dwordx4 v12, s[16:19], s76 offen offset:1024 sc1 lds\n"
        "buffer_load_dwordx4 v12, s[16:19], s76 offen offset:2048 sc1 lds\n"
        "buffer_load_dwordx4 v12, s[16:19], s76 offen offset:3072 sc1 lds\n"
        "s_add_u32 m0, s58, 32768\n"
        "s_movk_i32 s59, 512\n"
        "buffer_load_dwordx4 v8, s[12:15], s59 offen offset:0 lds\n"
        "buffer_load_dwordx4 v9, s[12:15], s59 offen offset:1024 lds\n"
        "buffer_load_dwordx4 v10, s[12:15], s59 offen offset:2048 lds\n"
        "buffer_load_dwordx4 v11, s[12:15], s59 offen offset:3072 lds\n"
        "s_waitcnt vmcnt(8)\n"
        "s_barrier\n"
        "s_add_u32 m0, s58, 81920\n"
        "s_add_u32 s76, s64, 98304\n"
        "buffer_load_dwordx4 v12, s[16:19], s76 offen offset:0 sc1 lds\n"
        "buffer_load_dwordx4 v12, s[16:19], s76 offen offset:1024 sc1 lds\n"
        "buffer_load_dwordx4 v12, s[16:19], s76 offen offset:2048 sc1 lds\n"
        "buffer_load_dwordx4 v12, s[16:19], s76 offen offset:3072 sc1 lds\n"
        "s_add_u32 m0, s58, 0\n"
        "s_movk_i32 s59, 640\n"
        "buffer_load_dwordx4 v8, s[12:15], s59 offen offset:0 lds\n"
        "buffer_load_dwordx4 v9, s[12:15], s59 offen offset:1024 lds\n"
        "buffer_load_dwordx4 v10, s[12:15], s59 offen offset:2048 lds\n"
        "buffer_load_dwordx4 v11, s[12:15], s59 offen offset:3072 lds\n"
        "buffer_load_dwordx4 v[40:43], v249, s[8:11], s40 offen\n"
        "buffer_load_dwordx4 v[44:47], v249, s[8:11], s41 offen\n"
        "buffer_load_dwordx4 v[48:51], v249, s[8:11], s42 offen\n"
        "buffer_load_dwordx4 v[52:55], v249, s[8:11], s43 offen\n"
        "buffer_load_dwordx4 v[56:59], v249, s[8:11], s44 offen\n"
        "buffer_load_dwordx4 v[60:63], v249, s[8:11], s45 offen\n"
        "buffer_load_dwordx4 v[64:67], v249, s[8:11], s46 offen\n"
        "buffer_load_dwordx4 v[68:71], v249, s[8:11], s47 offen\n"
        "s_waitcnt vmcnt(16)\n"
        "s_barrier\n"
        "s_add_u32 m0, s58, 49152\n"
        "s_add_u32 s76, s64, 114688\n"
        "buffer_load_dwordx4 v12, s[16:19], s76 offen offset:0 sc1 lds\n"
        "buffer_load_dwordx4 v12, s[16:19], s76 offen offset:1024 sc1 lds\n"
        "buffer_load_dwordx4 v12, s[16:19], s76 offen offset:2048 sc1 lds\n"
        "buffer_load_dwordx4 v12, s[16:19], s76 offen offset:3072 sc1 lds\n"
        "s_add_u32 m0, s58, 16384\n"
        "s_movk_i32 s59, 768\n"
        "buffer_load_dwordx4 v8, s[12:15], s59 offen offset:0 lds\n"
        "buffer_load_dwordx4 v9, s[12:15], s59 offen offset:1024 lds\n"
        "buffer_load_dwordx4 v10, s[12:15], s59 offen offset:2048 lds\n"
        "buffer_load_dwordx4 v11, s[12:15], s59 offen offset:3072 lds\n"
        "s_waitcnt vmcnt(16)\n"
        "s_barrier\n"
        "s_add_u32 m0, s58, 65536\n"
        "s_add_u32 s76, s64, 131072\n"
        "buffer_load_dwordx4 v12, s[16:19], s76 offen offset:0 sc1 lds\n"
        "buffer_load_dwordx4 v12, s[16:19], s76 offen offset:1024 sc1 lds\n"
        "buffer_load_dwordx4 v12, s[16:19], s76 offen offset:2048 sc1 lds\n"
        "buffer_load_dwordx4 v12, s[16:19], s76 offen offset:3072 sc1 lds\n"
        "s_add_u32 m0, s58, 32768\n"
        "s_movk_i32 s59, 896\n"
        "buffer_load_dwordx4 v8, s[12:15], s59 offen offset:0 lds\n"
        "buffer_load_dwordx4 v9, s[12:15], s59 offen offset:1024 lds\n"
        "buffer_load_dwordx4 v10, s[12:15], s59 offen offset:2048 lds\n"
        "buffer_load_dwordx4 v11, s[12:15], s59 offen offset:3072 lds\n"
        "s_waitcnt vmcnt(16)\n"
        "v_lshl_add_u64 v[68:69], v[68:69], 4, v[64:65]\n"
        "v_lshl_add_u64 v[70:71], v[70:71], 4, v[66:67]\n"
        "v_lshl_add_u64 v[68:69], v[68:69], 4, v[60:61]\n"
        "v_lshl_add_u64 v[70:71], v[70:71], 4, v[62:63]\n"
        "v_lshl_add_u64 v[68:69], v[68:69], 4, v[56:57]\n"
        "v_lshl_add_u64 v[70:71], v[70:71], 4, v[58:59]\n"
        "v_lshl_add_u64 v[68:69], v[68:69], 4, v[52:53]\n"
        "v_lshl_add_u64 v[70:71], v[70:71], 4, v[54:55]\n"
        "v_lshl_add_u64 v[68:69], v[68:69], 4, v[48:49]\n"
        "v_lshl_add_u64 v[70:71], v[70:71], 4, v[50:51]\n"
        "v_lshl_add_u64 v[68:69], v[68:69], 4, v[44:45]\n"
        "v_lshl_add_u64 v[70:71], v[70:71], 4, v[46:47]\n"
        "v_lshl_add_u64 v[72:73], v[68:69], 4, v[40:41]\n"
        "v_lshl_add_u64 v[76:77], v[70:71], 4, v[42:43]\n"
        "s_nop 0\n"
        "v_permlane32_swap_b32 v72, v73\n"
        "v_permlane32_swap_b32 v76, v77\n"
        "buffer_store_dwordx4 v[72:75], v250, s[16:19], s75 offen sc1\n"
        "buffer_store_dwordx4 v[76:79], v251, s[16:19], s75 offen offset:1024 sc1\n"
        "s_waitcnt vmcnt(10)\n"
        "s_barrier\n"
        "s_add_u32 m0, s58, 81920\n"
        "s_add_u32 s76, s64, 147456\n"
        "buffer_load_dwordx4 v12, s[16:19], s76 offen offset:0 sc1 lds\n"
        "buffer_load_dwordx4 v12, s[16:19], s76 offen offset:1024 sc1 lds\n"
        "buffer_load_dwordx4 v12, s[16:19], s76 offen offset:2048 sc1 lds\n"
        "buffer_load_dwordx4 v12, s[16:19], s76 offen offset:3072 sc1 lds\n"
        "s_add_u32 m0, s58, 0\n"
        "s_movk_i32 s59, 1024\n"
        "buffer_load_dwordx4 v8, s[12:15], s59 offen offset:0 lds\n"
        "buffer_load_dwordx4 v9, s[12:15], s59 offen offset:1024 lds\n"
        "buffer_load_dwordx4 v10, s[12:15], s59 offen offset:2048 lds\n"
        "buffer_load_dwordx4 v11, s[12:15], s59 offen offset:3072 lds\n"
        "s_waitcnt vmcnt(10)\n"
        "s_barrier\n"
        "s_add_u32 m0, s58, 49152\n"
        "s_add_u32 s76, s64, 163840\n"
        "buffer_load_dwordx4 v12, s[16:19], s76 offen offset:0 sc1 lds\n"
        "buffer_load_dwordx4 v12, s[16:19], s76 offen offset:1024 sc1 lds\n"
        "buffer_load_dwordx4 v12, s[16:19], s76 offen offset:2048 sc1 lds\n"
        "buffer_load_dwordx4 v12, s[16:19], s76 offen offset:3072 sc1 lds\n"
        "s_add_u32 m0, s58, 16384\n"
        "s_movk_i32 s59, 1152\n"
        "buffer_load_dwordx4 v8, s[12:15], s59 offen offset:0 lds\n"
        "buffer_load_dwordx4 v9, s[12:15], s59 offen offset:1024 lds\n"
        "buffer_load_dwordx4 v10, s[12:15], s59 offen offset:2048 lds\n"
        "buffer_load_dwordx4 v11, s[12:15], s59 offen offset:3072 lds\n"
        "s_waitcnt vmcnt(8)\n"
        "s_barrier\n"
        "s_add_u32 m0, s58, 65536\n"
        "s_add_u32 s76, s64, 180224\n"
        "buffer_load_dwordx4 v12, s[16:19], s76 offen offset:0 sc1 lds\n"
        "buffer_load_dwordx4 v12, s[16:19], s76 offen offset:1024 sc1 lds\n"
        "buffer_load_dwordx4 v12, s[16:19], s76 offen offset:2048 sc1 lds\n"
        "buffer_load_dwordx4 v12, s[16:19], s76 offen offset:3072 sc1 lds\n"
        "s_add_u32 m0, s58, 32768\n"
        "s_movk_i32 s59, 1280\n"
        "buffer_load_dwordx4 v8, s[12:15], s59 offen offset:0 lds\n"
        "buffer_load_dwordx4 v9, s[12:15], s59 offen offset:1024 lds\n"
        "buffer_load_dwordx4 v10, s[12:15], s59 offen offset:2048 lds\n"
        "buffer_load_dwordx4 v11, s[12:15], s59 offen offset:3072 lds\n"
        "s_waitcnt vmcnt(8)\n"
        "s_barrier\n"
        "s_add_u32 m0, s58, 81920\n"
        "s_add_u32 s76, s64, 196608\n"
        "buffer_load_dwordx4 v12, s[16:19], s76 offen offset:0 sc1 lds\n"
        "buffer_load_dwordx4 v12, s[16:19], s76 offen offset:1024 sc1 lds\n"
        "buffer_load_dwordx4 v12, s[16:19], s76 offen offset:2048 sc1 lds\n"
        "buffer_load_dwordx4 v12, s[16:19], s76 offen offset:3072 sc1 lds\n"
        "s_add_u32 m0, s58, 0\n"
        "s_movk_i32 s59, 1408\n"
        "buffer_load_dwordx4 v8, s[12:15], s59 offen offset:0 lds\n"
        "buffer_load_dwordx4 v9, s[12:15], s59 offen offset:1024 lds\n"
        "buffer_load_dwordx4 v10, s[12:15], s59 offen offset:2048 lds\n"
        "buffer_load_dwordx4 v11, s[12:15], s59 offen offset:3072 lds\n"
        "s_waitcnt vmcnt(8)\n"
        "s_barrier\n"
        "s_add_u32 m0, s58, 49152\n"
        "s_add_u32 s76, s64, 212992\n"
        "buffer_load_dwordx4 v12, s[16:19], s76 offen offset:0 sc1 lds\n"
        "buffer_load_dwordx4 v12, s[16:19], s76 offen offset:1024 sc1 lds\n"
        "buffer_load_dwordx4 v12, s[16:19], s76 offen offset:2048 sc1 lds\n"
        "buffer_load_dwordx4 v12, s[16:19], s76 offen offset:3072 sc1 lds\n"
        "s_add_u32 m0, s58, 16384\n"
        "s_movk_i32 s59, 1536\n"
        "buffer_load_dwordx4 v8, s[12:15], s59 offen offset:0 lds\n"
        "buffer_load_dwordx4 v9, s[12:15], s59 offen offset:1024 lds\n"
        "buffer_load_dwordx4 v10, s[12:15], s59 offen offset:2048 lds\n"
        "buffer_load_dwordx4 v11, s[12:15], s59 offen offset:3072 lds\n"
        "s_waitcnt vmcnt(8)\n"
        "s_barrier\n"
        "s_add_u32 m0, s58, 65536\n"
        "s_add_u32 s76, s64, 229376\n"
        "buffer_load_dwordx4 v12, s[16:19], s76 offen offset:0 sc1 lds\n"
        "buffer_load_dwordx4 v12, s[16:19], s76 offen offset:1024 sc1 lds\n"
        "buffer_load_dwordx4 v12, s[16:19], s76 offen offset:2048 sc1 lds\n"
        "buffer_load_dwordx4 v12, s[16:19], s76 offen offset:3072 sc1 lds\n"
        "s_add_u32 m0, s58, 32768\n"
        "s_movk_i32 s59, 1664\n"
        "buffer_load_dwordx4 v8, s[12:15], s59 offen offset:0 lds\n"
        "buffer_load_dwordx4 v9, s[12:15], s59 offen offset:1024 lds\n"
        "buffer_load_dwordx4 v10, s[12:15], s59 offen offset:2048 lds\n"
        "buffer_load_dwordx4 v11, s[12:15], s59 offen offset:3072 lds\n"
        "s_waitcnt vmcnt(8)\n"
        "s_barrier\n"
        "s_add_u32 m0, s58, 81920\n"
        "s_add_u32 s76, s64, 245760\n"
        "buffer_load_dwordx4 v12, s[16:19], s76 offen offset:0 sc1 lds\n"
        "buffer_load_dwordx4 v12, s[16:19], s76 offen offset:1024 sc1 lds\n"
        "buffer_load_dwordx4 v12, s[16:19], s76 offen offset:2048 sc1 lds\n"
        "buffer_load_dwordx4 v12, s[16:19], s76 offen offset:3072 sc1 lds\n"
        "s_add_u32 m0, s58, 0\n"
        "s_movk_i32 s59, 1792\n"
        "buffer_load_dwordx4 v8, s[12:15], s59 offen offset:0 lds\n"
        "buffer_load_dwordx4 v9, s[12:15], s59 offen offset:1024 lds\n"
        "buffer_load_dwordx4 v10, s[12:15], s59 offen offset:2048 lds\n"
        "buffer_load_dwordx4 v11, s[12:15], s59 offen offset:3072 lds\n"
        "s_waitcnt vmcnt(8)\n"
        "s_barrier\n"
        "s_add_u32 m0, s58, 16384\n"
        "s_movk_i32 s59, 1920\n"
        "buffer_load_dwordx4 v8, s[12:15], s59 offen offset:0 lds\n"
        "buffer_load_dwordx4 v9, s[12:15], s59 offen offset:1024 lds\n"
        "buffer_load_dwordx4 v10, s[12:15], s59 offen offset:2048 lds\n"
        "buffer_load_dwordx4 v11, s[12:15], s59 offen offset:3072 lds\n"
        "s_waitcnt vmcnt(0)\n"
        "s_barrier\n"
        "s_branch .Ln_ht\n"
        ".Ln_h1:\n"
        "s_add_u32 s72, s69, 96\n"
        "s_mul_i32 s35, s72, 9363\n"
        "s_lshr_b32 s35, s35, 16\n"
        "s_mul_i32 s36, s35, 7\n"
        "s_sub_u32 s36, s72, s36\n"
        "s_and_b32 s79, s35, 1\n"
        "s_lshl_b32 s79, s79, 3\n"
        "s_and_b32 s37, s33, 7\n"
        "s_or_b32 s79, s79, s37\n"
        "s_lshr_b32 s35, s35, 1\n"
        "s_mul_i32 s40, s35, 917504\n"
        "s_mul_i32 s37, s79, 896\n"
        "s_lshl_b32 s38, s36, 7\n"
        "s_add_u32 s40, s40, s37\n"
        "s_add_u32 s40, s40, s38\n"
        "s_add_u32 s41, s40, 14336\n"
        "s_add_u32 s42, s41, 14336\n"
        "s_add_u32 s43, s42, 14336\n"
        "s_add_u32 s44, s43, 14336\n"
        "s_add_u32 s45, s44, 14336\n"
        "s_add_u32 s46, s45, 14336\n"
        "s_add_u32 s47, s46, 14336\n"
        "s_lshl_b32 s75, s79, 4\n"
        "s_add_u32 s75, s75, s35\n"
        "s_lshl_b32 s75, s75, 3\n"
        "s_add_u32 s75, s75, s36\n"
        "s_lshl_b32 s75, s75, 11\n"
        "s_add_u32 s75, s75, 114688\n"
        "s_waitcnt vmcnt(0)\n"
        "s_waitcnt lgkmcnt(0)\n"
        "s_barrier\n"
        "s_cmp_lt_u32 s69, 16\n"
        "s_cbranch_scc0 1f\n"
        "s_sleep 4\n"
        "1:\n"
        "s_sleep 10\n"
        "s_add_u32 m0, s58, 32768\n"
        "s_movk_i32 s59, 128\n"
        "buffer_load_dwordx4 v8, s[12:15], s59 offen offset:0 lds\n"
        "buffer_load_dwordx4 v9, s[12:15], s59 offen offset:1024 lds\n"
        "buffer_load_dwordx4 v10, s[12:15], s59 offen offset:2048 lds\n"
        "buffer_load_dwordx4 v11, s[12:15], s59 offen offset:3072 lds\n"
        "s_add_u32 m0, s58, 65536\n"
        "s_add_u32 s76, s64, 32768\n"
        "buffer_load_dwordx4 v12, s[16:19], s76 offen offset:0 sc1 lds\n"
        "buffer_load_dwordx4 v12, s[16:19], s76 offen offset:1024 sc1 lds\n"
        "buffer_load_dwordx4 v12, s[16:19], s76 offen offset:2048 sc1 lds\n"
        "buffer_load_dwordx4 v12, s[16:19], s76 offen offset:3072 sc1 lds\n"
        "buffer_load_dwordx4 v[40:43], v249, s[8:11], s40 offen\n"
        "buffer_load_dwordx4 v[44:47], v249, s[8:11], s41 offen\n"
        "buffer_load_dwordx4 v[48:51], v249, s[8:11], s42 offen\n"
        "buffer_load_dwordx4 v[52:55], v249, s[8:11], s43 offen\n"
        "buffer_load_dwordx4 v[56:59], v249, s[8:11], s44 offen\n"
        "buffer_load_dwordx4 v[60:63], v249, s[8:11], s45 offen\n"
        "buffer_load_dwordx4 v[64:67], v249, s[8:11], s46 offen\n"
        "buffer_load_dwordx4 v[68:71], v249, s[8:11], s47 offen\n"
        "v_mov_b32 v84, 128\n"
        "s_movk_i32 s73, 64\n"
        "1:\n"
        "s_sub_u32 s73, s73, 1\n"
        "s_cbranch_scc1 2f\n"
        "s_sleep 1\n"
        "ds_read_b128 v[96:99], v84 offset:4096\n"
        "s_waitcnt lgkmcnt(0)\n"
        "v_min3_u32 v96, v96, v97, v98\n"
        "v_min_u32 v96, v96, v99\n"
        "v_cmp_eq_u32 vcc, 0, v96\n"
        "s_cbranch_vccnz 1b\n"
        "2:\n"
        "s_add_u32 m0, s58, 0\n"
        "s_movk_i32 s59, 256\n"
        "buffer_load_dwordx4 v8, s[12:15], s59 offen offset:0 lds\n"
        "buffer_load_dwordx4 v9, s[12:15], s59 offen offset:1024 lds\n"
        "buffer_load_dwordx4 v10, s[12:15], s59 offen offset:2048 lds\n"
        "buffer_load_dwordx4 v11, s[12:15], s59 offen offset:3072 lds\n"
        "s_add_u32 m0, s58, 81920\n"
        "s_add_u32 s76, s64, 49152\n"
        "buffer_load_dwordx4 v12, s[16:19], s76 offen offset:0 sc1 lds\n"
        "buffer_load_dwordx4 v12, s[16:19], s76 offen offset:1024 sc1 lds\n"
        "buffer_load_dwordx4 v12, s[16:19], s76 offen offset:2048 sc1 lds\n"
        "buffer_load_dwordx4 v12, s[16:19], s76 offen offset:3072 sc1 lds\n"
        "s_mov_b64 exec, 0\n"
        "s_add_u32 s94, s86, .Ln_tp1-.Ln_here\n"
        "s_addc_u32 s95, s87, 0\n"
        "s_swappc_b64 s[70:71], s[94:95]\n"
        "s_mov_b64 exec, -1\n"
        "s_waitcnt vmcnt(16)\n"
        "s_barrier\n"
        "s_add_u32 m0, s58, 16384\n"
        "s_movk_i32 s59, 384\n"
        "buffer_load_dwordx4 v8, s[12:15], s59 offen offset:0 lds\n"
        "buffer_load_dwordx4 v9, s[12:15], s59 offen offset:1024 lds\n"
        "buffer_load_dwordx4 v10, s[12:15], s59 offen offset:2048 lds\n"
        "buffer_load_dwordx4 v11, s[12:15], s59 offen offset:3072 lds\n"
        "s_add_u32 m0, s58, 49152\n"
        "s_add_u32 s76, s64, 65536\n"
        "buffer_load_dwordx4 v12, s[16:19], s76 offen offset:0 sc1 lds\n"
        "buffer_load_dwordx4 v12, s[16:19], s76 offen offset:1024 sc1 lds\n"
        "buffer_load_dwordx4 v12, s[16:19], s76 offen offset:2048 sc1 lds\n"
        "buffer_load_dwordx4 v12, s[16:19], s76 offen offset:3072 sc1 lds\n"
        "s_waitcnt vmcnt(16)\n"
        "v_lshl_add_u64 v[68:69], v[68:69], 4, v[64:65]\n"
        "v_lshl_add_u64 v[70:71], v[70:71], 4, v[66:67]\n"
        "v_lshl_add_u64 v[68:69], v[68:69], 4, v[60:61]\n"
        "v_lshl_add_u64 v[70:71], v[70:71], 4, v[62:63]\n"
        "v_lshl_add_u64 v[68:69], v[68:69], 4, v[56:57]\n"
        "v_lshl_add_u64 v[70:71], v[70:71], 4, v[58:59]\n"
        "v_lshl_add_u64 v[68:69], v[68:69], 4, v[52:53]\n"
        "v_lshl_add_u64 v[70:71], v[70:71], 4, v[54:55]\n"
        "v_lshl_add_u64 v[68:69], v[68:69], 4, v[48:49]\n"
        "v_lshl_add_u64 v[70:71], v[70:71], 4, v[50:51]\n"
        "v_lshl_add_u64 v[68:69], v[68:69], 4, v[44:45]\n"
        "v_lshl_add_u64 v[70:71], v[70:71], 4, v[46:47]\n"
        "v_lshl_add_u64 v[72:73], v[68:69], 4, v[40:41]\n"
        "v_lshl_add_u64 v[76:77], v[70:71], 4, v[42:43]\n"
        "s_nop 0\n"
        "v_permlane32_swap_b32 v72, v73\n"
        "v_permlane32_swap_b32 v76, v77\n"
        "buffer_store_dwordx4 v[72:75], v250, s[16:19], s75 offen sc1\n"
        "buffer_store_dwordx4 v[76:79], v251, s[16:19], s75 offen offset:1024 sc1\n"
        "s_waitcnt vmcnt(10)\n"
        "s_barrier\n"
        "s_add_u32 m0, s58, 65536\n"
        "s_add_u32 s76, s64, 81920\n"
        "buffer_load_dwordx4 v12, s[16:19], s76 offen offset:0 sc1 lds\n"
        "buffer_load_dwordx4 v12, s[16:19], s76 offen offset:1024 sc1 lds\n"
        "buffer_load_dwordx4 v12, s[16:19], s76 offen offset:2048 sc1 lds\n"
        "buffer_load_dwordx4 v12, s[16:19], s76 offen offset:3072 sc1 lds\n"
        "s_add_u32 m0, s58, 32768\n"
        "s_movk_i32 s59, 512\n"
        "buffer_load_dwordx4 v8, s[12:15], s59 offen offset:0 lds\n"
        "buffer_load_dwordx4 v9, s[12:15], s59 offen offset:1024 lds\n"
        "buffer_load_dwordx4 v10, s[12:15], s59 offen offset:2048 lds\n"
        "buffer_load_dwordx4 v11, s[12:15], s59 offen offset:3072 lds\n"
        "s_waitcnt vmcnt(10)\n"
        "s_barrier\n"
        "s_add_u32 m0, s58, 81920\n"
        "s_add_u32 s76, s64, 98304\n"
        "buffer_load_dwordx4 v12, s[16:19], s76 offen offset:0 sc1 lds\n"
        "buffer_load_dwordx4 v12, s[16:19], s76 offen offset:1024 sc1 lds\n"
        "buffer_load_dwordx4 v12, s[16:19], s76 offen offset:2048 sc1 lds\n"
        "buffer_load_dwordx4 v12, s[16:19], s76 offen offset:3072 sc1 lds\n"
        "s_add_u32 m0, s58, 0\n"
        "s_movk_i32 s59, 640\n"
        "buffer_load_dwordx4 v8, s[12:15], s59 offen offset:0 lds\n"
        "buffer_load_dwordx4 v9, s[12:15], s59 offen offset:1024 lds\n"
        "buffer_load_dwordx4 v10, s[12:15], s59 offen offset:2048 lds\n"
        "buffer_load_dwordx4 v11, s[12:15], s59 offen offset:3072 lds\n"
        "s_waitcnt vmcnt(8)\n"
        "s_barrier\n"
        "s_add_u32 m0, s58, 49152\n"
        "s_add_u32 s76, s64, 114688\n"
        "buffer_load_dwordx4 v12, s[16:19], s76 offen offset:0 sc1 lds\n"
        "buffer_load_dwordx4 v12, s[16:19], s76 offen offset:1024 sc1 lds\n"
        "buffer_load_dwordx4 v12, s[16:19], s76 offen offset:2048 sc1 lds\n"
        "buffer_load_dwordx4 v12, s[16:19], s76 offen offset:3072 sc1 lds\n"
        "s_add_u32 m0, s58, 16384\n"
        "s_movk_i32 s59, 768\n"
        "buffer_load_dwordx4 v8, s[12:15], s59 offen offset:0 lds\n"
        "buffer_load_dwordx4 v9, s[12:15], s59 offen offset:1024 lds\n"
        "buffer_load_dwordx4 v10, s[12:15], s59 offen offset:2048 lds\n"
        "buffer_load_dwordx4 v11, s[12:15], s59 offen offset:3072 lds\n"
        "s_waitcnt vmcnt(8)\n"
        "s_barrier\n"
        "s_add_u32 m0, s58, 65536\n"
        "s_add_u32 s76, s64, 131072\n"
        "buffer_load_dwordx4 v12, s[16:19], s76 offen offset:0 sc1 lds\n"
        "buffer_load_dwordx4 v12, s[16:19], s76 offen offset:1024 sc1 lds\n"
        "buffer_load_dwordx4 v12, s[16:19], s76 offen offset:2048 sc1 lds\n"
        "buffer_load_dwordx4 v12, s[16:19], s76 offen offset:3072 sc1 lds\n"
        "s_add_u32 m0, s58, 32768\n"
        "s_movk_i32 s59, 896\n"
        "buffer_load_dwordx4 v8, s[12:15], s59 offen offset:0 lds\n"
        "buffer_load_dwordx4 v9, s[12:15], s59 offen offset:1024 lds\n"
        "buffer_load_dwordx4 v10, s[12:15], s59 offen offset:2048 lds\n"
        "buffer_load_dwordx4 v11, s[12:15], s59 offen offset:3072 lds\n"
        "s_waitcnt vmcnt(8)\n"
        "s_barrier\n"
        "s_add_u32 m0, s58, 81920\n"
        "s_add_u32 s76, s64, 147456\n"
        "buffer_load_dwordx4 v12, s[16:19], s76 offen offset:0 sc1 lds\n"
        "buffer_load_dwordx4 v12, s[16:19], s76 offen offset:1024 sc1 lds\n"
        "buffer_load_dwordx4 v12, s[16:19], s76 offen offset:2048 sc1 lds\n"
        "buffer_load_dwordx4 v12, s[16:19], s76 offen offset:3072 sc1 lds\n"
        "s_add_u32 m0, s58, 0\n"
        "s_movk_i32 s59, 1024\n"
        "buffer_load_dwordx4 v8, s[12:15], s59 offen offset:0 lds\n"
        "buffer_load_dwordx4 v9, s[12:15], s59 offen offset:1024 lds\n"
        "buffer_load_dwordx4 v10, s[12:15], s59 offen offset:2048 lds\n"
        "buffer_load_dwordx4 v11, s[12:15], s59 offen offset:3072 lds\n"
        "s_waitcnt vmcnt(8)\n"
        "s_barrier\n"
        "s_add_u32 m0, s58, 49152\n"
        "s_add_u32 s76, s64, 163840\n"
        "buffer_load_dwordx4 v12, s[16:19], s76 offen offset:0 sc1 lds\n"
        "buffer_load_dwordx4 v12, s[16:19], s76 offen offset:1024 sc1 lds\n"
        "buffer_load_dwordx4 v12, s[16:19], s76 offen offset:2048 sc1 lds\n"
        "buffer_load_dwordx4 v12, s[16:19], s76 offen offset:3072 sc1 lds\n"
        "s_add_u32 m0, s58, 16384\n"
        "s_movk_i32 s59, 1152\n"
        "buffer_load_dwordx4 v8, s[12:15], s59 offen offset:0 lds\n"
        "buffer_load_dwordx4 v9, s[12:15], s59 offen offset:1024 lds\n"
        "buffer_load_dwordx4 v10, s[12:15], s59 offen offset:2048 lds\n"
        "buffer_load_dwordx4 v11, s[12:15], s59 offen offset:3072 lds\n"
        "s_waitcnt vmcnt(8)\n"
        "s_barrier\n"
        "s_add_u32 m0, s58, 65536\n"
        "s_add_u32 s76, s64, 180224\n"
        "buffer_load_dwordx4 v12, s[16:19], s76 offen offset:0 sc1 lds\n"
        "buffer_load_dwordx4 v12, s[16:19], s76 offen offset:1024 sc1 lds\n"
        "buffer_load_dwordx4 v12, s[16:19], s76 offen offset:2048 sc1 lds\n"
        "buffer_load_dwordx4 v12, s[16:19], s76 offen offset:3072 sc1 lds\n"
        "s_add_u32 m0, s58, 32768\n"
        "s_movk_i32 s59, 1280\n"
        "buffer_load_dwordx4 v8, s[12:15], s59 offen offset:0 lds\n"
        "buffer_load_dwordx4 v9, s[12:15], s59 offen offset:1024 lds\n"
        "buffer_load_dwordx4 v10, s[12:15], s59 offen offset:2048 lds\n"
        "buffer_load_dwordx4 v11, s[12:15], s59 offen offset:3072 lds\n"
        "s_waitcnt vmcnt(8)\n"
        "s_barrier\n"
        "s_add_u32 m0, s58, 81920\n"
        "s_add_u32 s76, s64, 196608\n"
        "buffer_load_dwordx4 v12, s[16:19], s76 offen offset:0 sc1 lds\n"
        "buffer_load_dwordx4 v12, s[16:19], s76 offen offset:1024 sc1 lds\n"
        "buffer_load_dwordx4 v12, s[16:19], s76 offen offset:2048 sc1 lds\n"
        "buffer_load_dwordx4 v12, s[16:19], s76 offen offset:3072 sc1 lds\n"
        "s_add_u32 m0, s58, 0\n"
        "s_movk_i32 s59, 1408\n"
        "buffer_load_dwordx4 v8, s[12:15], s59 offen offset:0 lds\n"
        "buffer_load_dwordx4 v9, s[12:15], s59 offen offset:1024 lds\n"
        "buffer_load_dwordx4 v10, s[12:15], s59 offen offset:2048 lds\n"
        "buffer_load_dwordx4 v11, s[12:15], s59 offen offset:3072 lds\n"
        "s_waitcnt vmcnt(8)\n"
        "s_barrier\n"
        "s_add_u32 m0, s58, 49152\n"
        "s_add_u32 s76, s64, 212992\n"
        "buffer_load_dwordx4 v12, s[16:19], s76 offen offset:0 sc1 lds\n"
        "buffer_load_dwordx4 v12, s[16:19], s76 offen offset:1024 sc1 lds\n"
        "buffer_load_dwordx4 v12, s[16:19], s76 offen offset:2048 sc1 lds\n"
        "buffer_load_dwordx4 v12, s[16:19], s76 offen offset:3072 sc1 lds\n"
        "s_add_u32 m0, s58, 16384\n"
        "s_movk_i32 s59, 1536\n"
        "buffer_load_dwordx4 v8, s[12:15], s59 offen offset:0 lds\n"
        "buffer_load_dwordx4 v9, s[12:15], s59 offen offset:1024 lds\n"
        "buffer_load_dwordx4 v10, s[12:15], s59 offen offset:2048 lds\n"
        "buffer_load_dwordx4 v11, s[12:15], s59 offen offset:3072 lds\n"
        "s_waitcnt vmcnt(8)\n"
        "s_barrier\n"
        "s_add_u32 m0, s58, 65536\n"
        "s_add_u32 s76, s64, 229376\n"
        "buffer_load_dwordx4 v12, s[16:19], s76 offen offset:0 sc1 lds\n"
        "buffer_load_dwordx4 v12, s[16:19], s76 offen offset:1024 sc1 lds\n"
        "buffer_load_dwordx4 v12, s[16:19], s76 offen offset:2048 sc1 lds\n"
        "buffer_load_dwordx4 v12, s[16:19], s76 offen offset:3072 sc1 lds\n"
        "s_add_u32 m0, s58, 32768\n"
        "s_movk_i32 s59, 1664\n"
        "buffer_load_dwordx4 v8, s[12:15], s59 offen offset:0 lds\n"
        "buffer_load_dwordx4 v9, s[12:15], s59 offen offset:1024 lds\n"
        "buffer_load_dwordx4 v10, s[12:15], s59 offen offset:2048 lds\n"
        "buffer_load_dwordx4 v11, s[12:15], s59 offen offset:3072 lds\n"
        "s_waitcnt vmcnt(8)\n"
        "s_barrier\n"
        "s_add_u32 m0, s58, 81920\n"
        "s_add_u32 s76, s64, 245760\n"
        "buffer_load_dwordx4 v12, s[16:19], s76 offen offset:0 sc1 lds\n"
        "buffer_load_dwordx4 v12, s[16:19], s76 offen offset:1024 sc1 lds\n"
        "buffer_load_dwordx4 v12, s[16:19], s76 offen offset:2048 sc1 lds\n"
        "buffer_load_dwordx4 v12, s[16:19], s76 offen offset:3072 sc1 lds\n"
        "s_add_u32 m0, s58, 0\n"
        "s_movk_i32 s59, 1792\n"
        "buffer_load_dwordx4 v8, s[12:15], s59 offen offset:0 lds\n"
        "buffer_load_dwordx4 v9, s[12:15], s59 offen offset:1024 lds\n"
        "buffer_load_dwordx4 v10, s[12:15], s59 offen offset:2048 lds\n"
        "buffer_load_dwordx4 v11, s[12:15], s59 offen offset:3072 lds\n"
        "s_waitcnt vmcnt(8)\n"
        "s_barrier\n"
        "s_add_u32 m0, s58, 16384\n"
        "s_movk_i32 s59, 1920\n"
        "buffer_load_dwordx4 v8, s[12:15], s59 offen offset:0 lds\n"
        "buffer_load_dwordx4 v9, s[12:15], s59 offen offset:1024 lds\n"
        "buffer_load_dwordx4 v10, s[12:15], s59 offen offset:2048 lds\n"
        "buffer_load_dwordx4 v11, s[12:15], s59 offen offset:3072 lds\n"
        "s_waitcnt vmcnt(0)\n"
        "s_barrier\n"
        "s_branch .Ln_ht\n"
        ".Ln_h2:\n"
        "s_add_u32 s72, s69, 128\n"
        "s_mul_i32 s35, s72, 9363\n"
        "s_lshr_b32 s35, s35, 16\n"
        "s_mul_i32 s36, s35, 7\n"
        "s_sub_u32 s36, s72, s36\n"
        "s_and_b32 s79, s35, 1\n"
        "s_lshl_b32 s79, s79, 3\n"
        "s_and_b32 s37, s33, 7\n"
        "s_or_b32 s79, s79, s37\n"
        "s_lshr_b32 s35, s35, 1\n"
        "s_mul_i32 s40, s35, 917504\n"
        "s_mul_i32 s37, s79, 896\n"
        "s_lshl_b32 s38, s36, 7\n"
        "s_add_u32 s40, s40, s37\n"
        "s_add_u32 s40, s40, s38\n"
        "s_add_u32 s41, s40, 14336\n"
        "s_add_u32 s42, s41, 14336\n"
        "s_add_u32 s43, s42, 14336\n"
        "s_add_u32 s44, s43, 14336\n"
        "s_add_u32 s45, s44, 14336\n"
        "s_add_u32 s46, s45, 14336\n"
        "s_add_u32 s47, s46, 14336\n"
        "s_lshl_b32 s75, s79, 4\n"
        "s_add_u32 s75, s75, s35\n"
        "s_lshl_b32 s75, s75, 3\n"
        "s_add_u32 s75, s75, s36\n"
        "s_lshl_b32 s75, s75, 11\n"
        "s_add_u32 s75, s75, 114688\n"
        "s_waitcnt vmcnt(0)\n"
        "s_waitcnt lgkmcnt(0)\n"
        "s_barrier\n"
        "s_cmp_lt_u32 s69, 16\n"
        "s_cbranch_scc0 1f\n"
        "s_sleep 4\n"
        "1:\n"
        "s_sleep 10\n"
        "s_add_u32 m0, s58, 32768\n"
        "s_movk_i32 s59, 128\n"
        "buffer_load_dwordx4 v8, s[12:15], s59 offen offset:0 lds\n"
        "buffer_load_dwordx4 v9, s[12:15], s59 offen offset:1024 lds\n"
        "buffer_load_dwordx4 v10, s[12:15], s59 offen offset:2048 lds\n"
        "buffer_load_dwordx4 v11, s[12:15], s59 offen offset:3072 lds\n"
        "s_add_u32 m0, s58, 65536\n"
        "s_add_u32 s76, s64, 32768\n"
        "buffer_load_dwordx4 v12, s[16:19], s76 offen offset:0 sc1 lds\n"
        "buffer_load_dwordx4 v12, s[16:19], s76 offen offset:1024 sc1 lds\n"
        "buffer_load_dwordx4 v12, s[16:19], s76 offen offset:2048 sc1 lds\n"
        "buffer_load_dwordx4 v12, s[16:19], s76 offen offset:3072 sc1 lds\n"
        "v_mov_b32 v84, 128\n"
        "s_movk_i32 s73, 64\n"
        "1:\n"
        "s_sub_u32 s73, s73, 1\n"
        "s_cbranch_scc1 2f\n"
        "s_sleep 1\n"
        "ds_read_b128 v[96:99], v84 offset:4096\n"
        "s_waitcnt lgkmcnt(0)\n"
        "v_min3_u32 v96, v96, v97, v98\n"
        "v_min_u32 v96, v96, v99\n"
        "v_cmp_eq_u32 vcc, 0, v96\n"
        "s_cbranch_vccnz 1b\n"
        "2:\n"
        "s_add_u32 m0, s58, 0\n"
        "s_movk_i32 s59, 256\n"
        "buffer_load_dwordx4 v8, s[12:15], s59 offen offset:0 lds\n"
        "buffer_load_dwordx4 v9, s[12:15], s59 offen offset:1024 lds\n"
        "buffer_load_dwordx4 v10, s[12:15], s59 offen offset:2048 lds\n"
        "buffer_load_dwordx4 v11, s[12:15], s59 offen offset:3072 lds\n"
        "s_add_u32 m0, s58, 81920\n"
        "s_add_u32 s76, s64, 49152\n"
        "buffer_load_dwordx4 v12, s[16:19], s76 offen offset:0 sc1 lds\n"
        "buffer_load_dwordx4 v12, s[16:19], s76 offen offset:1024 sc1 lds\n"
        "buffer_load_dwordx4 v12, s[16:19], s76 offen offset:2048 sc1 lds\n"
        "buffer_load_dwordx4 v12, s[16:19], s76 offen offset:3072 sc1 lds\n"
        "s_mov_b64 exec, 0\n"
        "s_add_u32 s94, s86, .Ln_tp2-.Ln_here\n"
        "s_addc_u32 s95, s87, 0\n"
        "s_swappc_b64 s[70:71], s[94:95]\n"
        "s_mov_b64 exec, -1\n"
        "s_waitcnt vmcnt(8)\n"
        "s_barrier\n"
        "s_add_u32 m0, s58, 16384\n"
        "s_movk_i32 s59, 384\n"
        "buffer_load_dwordx4 v8, s[12:15], s59 offen offset:0 lds\n"
        "buffer_load_dwordx4 v9, s[12:15], s59 offen offset:1024 lds\n"
        "buffer_load_dwordx4 v10, s[12:15], s59 offen offset:2048 lds\n"
        "buffer_load_dwordx4 v11, s[12:15], s59 offen offset:3072 lds\n"
        "s_add_u32 m0, s58, 49152\n"
        "s_add_u32 s76, s64, 65536\n"
        "buffer_load_dwordx4 v12, s[16:19], s76 offen offset:0 sc1 lds\n"
        "buffer_load_dwordx4 v12, s[16:19], s76 offen offset:1024 sc1 lds\n"
        "buffer_load_dwordx4 v12, s[16:19], s76 offen offset:2048 sc1 lds\n"
        "buffer_load_dwordx4 v12, s[16:19], s76 offen offset:3072 sc1 lds\n"
        "buffer_load_dwordx4 v[40:43], v249, s[8:11], s40 offen\n"
        "buffer_load_dwordx4 v[44:47], v249, s[8:11], s41 offen\n"
        "buffer_load_dwordx4 v[48:51], v249, s[8:11], s42 offen\n"
        "buffer_load_dwordx4 v[52:55], v249, s[8:11], s43 offen\n"
        "buffer_load_dwordx4 v[56:59], v249, s[8:11], s44 offen\n"
        "buffer_load_dwordx4 v[60:63], v249, s[8:11], s45 offen\n"
        "buffer_load_dwordx4 v[64:67], v249, s[8:11], s46 offen\n"
        "buffer_load_dwordx4 v[68:71], v249, s[8:11], s47 offen\n"
        "s_waitcnt vmcnt(16)\n"
        "s_barrier\n"
        "s_add_u32 m0, s58, 65536\n"
        "s_add_u32 s76, s64, 81920\n"
        "buffer_load_dwordx4 v12, s[16:19], s76 offen offset:0 sc1 lds\n"
        "buffer_load_dwordx4 v12, s[16:19], s76 offen offset:1024 sc1 lds\n"
        "buffer_load_dwordx4 v12, s[16:19], s76 offen offset:2048 sc1 lds\n"
        "buffer_load_dwordx4 v12, s[16:19], s76 offen offset:3072 sc1 lds\n"
        "s_add_u32 m0, s58, 32768\n"
        "s_movk_i32 s59, 512\n"
        "buffer_load_dwordx4 v8, s[12:15], s59 offen offset:0 lds\n"
        "buffer_load_dwordx4 v9, s[12:15], s59 offen offset:1024 lds\n"
        "buffer_load_dwordx4 v10, s[12:15], s59 offen offset:2048 lds\n"
        "buffer_load_dwordx4 v11, s[12:15], s59 offen offset:3072 lds\n"
        "s_waitcnt vmcnt(16)\n"
        "s_barrier\n"
        "s_add_u32 m0, s58, 81920\n"
        "s_add_u32 s76, s64, 98304\n"
        "buffer_load_dwordx4 v12, s[16:19], s76 offen offset:0 sc1 lds\n"
        "buffer_load_dwordx4 v12, s[16:19], s76 offen offset:1024 sc1 lds\n"
        "buffer_load_dwordx4 v12, s[16:19], s76 offen offset:2048 sc1 lds\n"
        "buffer_load_dwordx4 v12, s[16:19], s76 offen offset:3072 sc1 lds\n"
        "s_add_u32 m0, s58, 0\n"
        "s_movk_i32 s59, 640\n"
        "buffer_load_dwordx4 v8, s[12:15], s59 offen offset:0 lds\n"
        "buffer_load_dwordx4 v9, s[12:15], s59 offen offset:1024 lds\n"
        "buffer_load_dwordx4 v10, s[12:15], s59 offen offset:2048 lds\n"
        "buffer_load_dwordx4 v11, s[12:15], s59 offen offset:3072 lds\n"
        "s_waitcnt vmcnt(16)\n"
        "v_lshl_add_u64 v[68:69], v[68:69], 4, v[64:65]\n"
        "v_lshl_add_u64 v[70:71], v[70:71], 4, v[66:67]\n"
        "v_lshl_add_u64 v[68:69], v[68:69], 4, v[60:61]\n"
        "v_lshl_add_u64 v[70:71], v[70:71], 4, v[62:63]\n"
        "v_lshl_add_u64 v[68:69], v[68:69], 4, v[56:57]\n"
        "v_lshl_add_u64 v[70:71], v[70:71], 4, v[58:59]\n"
        "v_lshl_add_u64 v[68:69], v[68:69], 4, v[52:53]\n"
        "v_lshl_add_u64 v[70:71], v[70:71], 4, v[54:55]\n"
        "v_lshl_add_u64 v[68:69], v[68:69], 4, v[48:49]\n"
        "v_lshl_add_u64 v[70:71], v[70:71], 4, v[50:51]\n"
        "v_lshl_add_u64 v[68:69], v[68:69], 4, v[44:45]\n"
        "v_lshl_add_u64 v[70:71], v[70:71], 4, v[46:47]\n"
        "v_lshl_add_u64 v[72:73], v[68:69], 4, v[40:41]\n"
        "v_lshl_add_u64 v[76:77], v[70:71], 4, v[42:43]\n"
        "s_nop 0\n"
        "v_permlane32_swap_b32 v72, v73\n"
        "v_permlane32_swap_b32 v76, v77\n"
        "buffer_store_dwordx4 v[72:75], v250, s[16:19], s75 offen sc1\n"
        "buffer_store_dwordx4 v[76:79], v251, s[16:19], s75 offen offset:1024 sc1\n"
        "s_waitcnt vmcnt(10)\n"
        "s_barrier\n"
        "s_add_u32 m0, s58, 49152\n"
        "s_add_u32 s76, s64, 114688\n"
        "buffer_load_dwordx4 v12, s[16:19], s76 offen offset:0 sc1 lds\n"
        "buffer_load_dwordx4 v12, s[16:19], s76 offen offset:1024 sc1 lds\n"
        "buffer_load_dwordx4 v12, s[16:19], s76 offen offset:2048 sc1 lds\n"
        "buffer_load_dwordx4 v12, s[16:19], s76 offen offset:3072 sc1 lds\n"
        "s_add_u32 m0, s58, 16384\n"
        "s_movk_i32 s59, 768\n"
        "buffer_load_dwordx4 v8, s[12:15], s59 offen offset:0 lds\n"
        "buffer_load_dwordx4 v9, s[12:15], s59 offen offset:1024 lds\n"
        "buffer_load_dwordx4 v10, s[12:15], s59 offen offset:2048 lds\n"
        "buffer_load_dwordx4 v11, s[12:15], s59 offen offset:3072 lds\n"
        "s_waitcnt vmcnt(10)\n"
        "s_barrier\n"
        "s_add_u32 m0, s58, 65536\n"
        "s_add_u32 s76, s64, 131072\n"
        "buffer_load_dwordx4 v12, s[16:19], s76 offen offset:0 sc1 lds\n"
        "buffer_load_dwordx4 v12, s[16:19], s76 offen offset:1024 sc1 lds\n"
        "buffer_load_dwordx4 v12, s[16:19], s76 offen offset:2048 sc1 lds\n"
        "buffer_load_dwordx4 v12, s[16:19], s76 offen offset:3072 sc1 lds\n"
        "s_add_u32 m0, s58, 32768\n"
        "s_movk_i32 s59, 896\n"
        "buffer_load_dwordx4 v8, s[12:15], s59 offen offset:0 lds\n"
        "buffer_load_dwordx4 v9, s[12:15], s59 offen offset:1024 lds\n"
        "buffer_load_dwordx4 v10, s[12:15], s59 offen offset:2048 lds\n"
        "buffer_load_dwordx4 v11, s[12:15], s59 offen offset:3072 lds\n"
        "s_waitcnt vmcnt(8)\n"
        "s_barrier\n"
        "s_add_u32 m0, s58, 81920\n"
        "s_add_u32 s76, s64, 147456\n"
        "buffer_load_dwordx4 v12, s[16:19], s76 offen offset:0 sc1 lds\n"
        "buffer_load_dwordx4 v12, s[16:19], s76 offen offset:1024 sc1 lds\n"
        "buffer_load_dwordx4 v12, s[16:19], s76 offen offset:2048 sc1 lds\n"
        "buffer_load_dwordx4 v12, s[16:19], s76 offen offset:3072 sc1 lds\n"
        "s_add_u32 m0, s58, 0\n"
        "s_movk_i32 s59, 1024\n"
        "buffer_load_dwordx4 v8, s[12:15], s59 offen offset:0 lds\n"
        "buffer_load_dwordx4 v9, s[12:15], s59 offen offset:1024 lds\n"
        "buffer_load_dwordx4 v10, s[12:15], s59 offen offset:2048 lds\n"
        "buffer_load_dwordx4 v11, s[12:15], s59 offen offset:3072 lds\n"
        "s_waitcnt vmcnt(8)\n"
        "s_barrier\n"
        "s_add_u32 m0, s58, 49152\n"
        "s_add_u32 s76, s64, 163840\n"
        "buffer_load_dwordx4 v12, s[16:19], s76 offen offset:0 sc1 lds\n"
        "buffer_load_dwordx4 v12, s[16:19], s76 offen offset:1024 sc1 lds\n"
        "buffer_load_dwordx4 v12, s[16:19], s76 offen offset:2048 sc1 lds\n"
        "buffer_load_dwordx4 v12, s[16:19], s76 offen offset:3072 sc1 lds\n"
        "s_add_u32 m0, s58, 16384\n"
        "s_movk_i32 s59, 1152\n"
        "buffer_load_dwordx4 v8, s[12:15], s59 offen offset:0 lds\n"
        "buffer_load_dwordx4 v9, s[12:15], s59 offen offset:1024 lds\n"
        "buffer_load_dwordx4 v10, s[12:15], s59 offen offset:2048 lds\n"
        "buffer_load_dwordx4 v11, s[12:15], s59 offen offset:3072 lds\n"
        "s_waitcnt vmcnt(8)\n"
        "s_barrier\n"
        "s_add_u32 m0, s58, 65536\n"
        "s_add_u32 s76, s64, 180224\n"
        "buffer_load_dwordx4 v12, s[16:19], s76 offen offset:0 sc1 lds\n"
        "buffer_load_dwordx4 v12, s[16:19], s76 offen offset:1024 sc1 lds\n"
        "buffer_load_dwordx4 v12, s[16:19], s76 offen offset:2048 sc1 lds\n"
        "buffer_load_dwordx4 v12, s[16:19], s76 offen offset:3072 sc1 lds\n"
        "s_add_u32 m0, s58, 32768\n"
        "s_movk_i32 s59, 1280\n"
        "buffer_load_dwordx4 v8, s[12:15], s59 offen offset:0 lds\n"
        "buffer_load_dwordx4 v9, s[12:15], s59 offen offset:1024 lds\n"
        "buffer_load_dwordx4 v10, s[12:15], s59 offen offset:2048 lds\n"
        "buffer_load_dwordx4 v11, s[12:15], s59 offen offset:3072 lds\n"
        "s_waitcnt vmcnt(8)\n"
        "s_barrier\n"
        "s_add_u32 m0, s58, 81920\n"
        "s_add_u32 s76, s64, 196608\n"
        "buffer_load_dwordx4 v12, s[16:19], s76 offen offset:0 sc1 lds\n"
        "buffer_load_dwordx4 v12, s[16:19], s76 offen offset:1024 sc1 lds\n"
        "buffer_load_dwordx4 v12, s[16:19], s76 offen offset:2048 sc1 lds\n"
        "buffer_load_dwordx4 v12, s[16:19], s76 offen offset:3072 sc1 lds\n"
        "s_add_u32 m0, s58, 0\n"
        "s_movk_i32 s59, 1408\n"
        "buffer_load_dwordx4 v8, s[12:15], s59 offen offset:0 lds\n"
        "buffer_load_dwordx4 v9, s[12:15], s59 offen offset:1024 lds\n"
        "buffer_load_dwordx4 v10, s[12:15], s59 offen offset:2048 lds\n"
        "buffer_load_dwordx4 v11, s[12:15], s59 offen offset:3072 lds\n"
        "s_waitcnt vmcnt(8)\n"
        "s_barrier\n"
        "s_add_u32 m0, s58, 49152\n"
        "s_add_u32 s76, s64, 212992\n"
        "buffer_load_dwordx4 v12, s[16:19], s76 offen offset:0 sc1 lds\n"
        "buffer_load_dwordx4 v12, s[16:19], s76 offen offset:1024 sc1 lds\n"
        "buffer_load_dwordx4 v12, s[16:19], s76 offen offset:2048 sc1 lds\n"
        "buffer_load_dwordx4 v12, s[16:19], s76 offen offset:3072 sc1 lds\n"
        "s_add_u32 m0, s58, 16384\n"
        "s_movk_i32 s59, 1536\n"
        "buffer_load_dwordx4 v8, s[12:15], s59 offen offset:0 lds\n"
        "buffer_load_dwordx4 v9, s[12:15], s59 offen offset:1024 lds\n"
        "buffer_load_dwordx4 v10, s[12:15], s59 offen offset:2048 lds\n"
        "buffer_load_dwordx4 v11, s[12:15], s59 offen offset:3072 lds\n"
        "s_waitcnt vmcnt(8)\n"
        "s_barrier\n"
        "s_add_u32 m0, s58, 65536\n"
        "s_add_u32 s76, s64, 229376\n"
        "buffer_load_dwordx4 v12, s[16:19], s76 offen offset:0 sc1 lds\n"
        "buffer_load_dwordx4 v12, s[16:19], s76 offen offset:1024 sc1 lds\n"
        "buffer_load_dwordx4 v12, s[16:19], s76 offen offset:2048 sc1 lds\n"
        "buffer_load_dwordx4 v12, s[16:19], s76 offen offset:3072 sc1 lds\n"
        "s_add_u32 m0, s58, 32768\n"
        "s_movk_i32 s59, 1664\n"
        "buffer_load_dwordx4 v8, s[12:15], s59 offen offset:0 lds\n"
        "buffer_load_dwordx4 v9, s[12:15], s59 offen offset:1024 lds\n"
        "buffer_load_dwordx4 v10, s[12:15], s59 offen offset:2048 lds\n"
        "buffer_load_dwordx4 v11, s[12:15], s59 offen offset:3072 lds\n"
        "s_waitcnt vmcnt(8)\n"
        "s_barrier\n"
        "s_add_u32 m0, s58, 81920\n"
        "s_add_u32 s76, s64, 245760\n"
        "buffer_load_dwordx4 v12, s[16:19], s76 offen offset:0 sc1 lds\n"
        "buffer_load_dwordx4 v12, s[16:19], s76 offen offset:1024 sc1 lds\n"
        "buffer_load_dwordx4 v12, s[16:19], s76 offen offset:2048 sc1 lds\n"
        "buffer_load_dwordx4 v12, s[16:19], s76 offen offset:3072 sc1 lds\n"
        "s_add_u32 m0, s58, 0\n"
        "s_movk_i32 s59, 1792\n"
        "buffer_load_dwordx4 v8, s[12:15], s59 offen offset:0 lds\n"
        "buffer_load_dwordx4 v9, s[12:15], s59 offen offset:1024 lds\n"
        "buffer_load_dwordx4 v10, s[12:15], s59 offen offset:2048 lds\n"
        "buffer_load_dwordx4 v11, s[12:15], s59 offen offset:3072 lds\n"
        "s_waitcnt vmcnt(8)\n"
        "s_barrier\n"
        "s_add_u32 m0, s58, 16384\n"
        "s_movk_i32 s59, 1920\n"
        "buffer_load_dwordx4 v8, s[12:15], s59 offen offset:0 lds\n"
        "buffer_load_dwordx4 v9, s[12:15], s59 offen offset:1024 lds\n"
        "buffer_load_dwordx4 v10, s[12:15], s59 offen offset:2048 lds\n"
        "buffer_load_dwordx4 v11, s[12:15], s59 offen offset:3072 lds\n"
        "s_waitcnt vmcnt(0)\n"
        "s_barrier\n"
        "s_branch .Ln_ht\n"
        ".Ln_h3:\n"
        "s_add_u32 s72, s69, 160\n"
        "s_mul_i32 s35, s72, 9363\n"
        "s_lshr_b32 s35, s35, 16\n"
        "s_mul_i32 s36, s35, 7\n"
        "s_sub_u32 s36, s72, s36\n"
        "s_and_b32 s79, s35, 1\n"
        "s_lshl_b32 s79, s79, 3\n"
        "s_and_b32 s37, s33, 7\n"
        "s_or_b32 s79, s79, s37\n"
        "s_lshr_b32 s35, s35, 1\n"
        "s_mul_i32 s40, s35, 917504\n"
        "s_mul_i32 s37, s79, 896\n"
        "s_lshl_b32 s38, s36, 7\n"
        "s_add_u32 s40, s40, s37\n"
        "s_add_u32 s40, s40, s38\n"
        "s_add_u32 s41, s40, 14336\n"
        "s_add_u32 s42, s41, 14336\n"
        "s_add_u32 s43, s42, 14336\n"
        "s_add_u32 s44, s43, 14336\n"
        "s_add_u32 s45, s44, 14336\n"
        "s_add_u32 s46, s45, 14336\n"
        "s_add_u32 s47, s46, 14336\n"
        "s_lshl_b32 s75, s79, 4\n"
        "s_add_u32 s75, s75, s35\n"
        "s_lshl_b32 s75, s75, 3\n"
        "s_add_u32 s75, s75, s36\n"
        "s_lshl_b32 s75, s75, 11\n"
        "s_add_u32 s75, s75, 114688\n"
        "s_waitcnt vmcnt(0)\n"
        "s_waitcnt lgkmcnt(0)\n"
        "s_barrier\n"
        "s_cmp_lt_u32 s69, 16\n"
        "s_cbranch_scc0 1f\n"
        "s_sleep 4\n"
        "1:\n"
        "s_sleep 10\n"
        "s_add_u32 m0, s58, 32768\n"
        "s_movk_i32 s59, 128\n"
        "buffer_load_dwordx4 v8, s[12:15], s59 offen offset:0 lds\n"
        "buffer_load_dwordx4 v9, s[12:15], s59 offen offset:1024 lds\n"
        "buffer_load_dwordx4 v10, s[12:15], s59 offen offset:2048 lds\n"
        "buffer_load_dwordx4 v11, s[12:15], s59 offen offset:3072 lds\n"
        "s_add_u32 m0, s58, 65536\n"
        "s_add_u32 s76, s64, 32768\n"
        "buffer_load_dwordx4 v12, s[16:19], s76 offen offset:0 sc1 lds\n"
        "buffer_load_dwordx4 v12, s[16:19], s76 offen offset:1024 sc1 lds\n"
        "v_mov_b32 v84, 128\n"
        "s_movk_i32 s73, 64\n"
        "1:\n"
        "s_sub_u32 s73, s73, 1\n"
        "s_cbranch_scc1 2f\n"
        "s_sleep 1\n"
        "ds_read_b128 v[96:99], v84 offset:4096\n"
        "s_waitcnt lgkmcnt(0)\n"
        "v_min3_u32 v96, v96, v97, v98\n"
        "v_min_u32 v96, v96, v99\n"
        "v_cmp_eq_u32 vcc, 0, v96\n"
        "s_cbranch_vccnz 1b\n"
        "2:\n"
        "s_add_u32 m0, s58, 0\n"
        "s_movk_i32 s59, 256\n"
        "buffer_load_dwordx4 v8, s[12:15], s59 offen offset:0 lds\n"
        "buffer_load_dwordx4 v9, s[12:15], s59 offen offset:1024 lds\n"
        "buffer_load_dwordx4 v10, s[12:15], s59 offen offset:2048 lds\n"
        "buffer_load_dwordx4 v11, s[12:15], s59 offen offset:3072 lds\n"
        "s_add_u32 m0, s58, 81920\n"
        "s_add_u32 s76, s64, 49152\n"
        "buffer_load_dwordx4 v12, s[16:19], s76 offen offset:0 sc1 lds\n"
        "buffer_load_dwordx4 v12, s[16:19], s76 offen offset:1024 sc1 lds\n"
        "s_mov_b64 exec, 0\n"
        "s_add_u32 s94, s86, .Ln_tp3-.Ln_here\n"
        "s_addc_u32 s95, s87, 0\n"
        "s_swappc_b64 s[70:71], s[94:95]\n"
        "s_mov_b64 exec, -1\n"
        "s_waitcnt vmcnt(6)\n"
        "s_barrier\n"
        "s_add_u32 m0, s58, 16384\n"
        "s_movk_i32 s59, 384\n"
        "buffer_load_dwordx4 v8, s[12:15], s59 offen offset:0 lds\n"
        "buffer_load_dwordx4 v9, s[12:15], s59 offen offset:1024 lds\n"
        "buffer_load_dwordx4 v10, s[12:15], s59 offen offset:2048 lds\n"
        "buffer_load_dwordx4 v11, s[12:15], s59 offen offset:3072 lds\n"
        "s_add_u32 m0, s58, 49152\n"
        "s_add_u32 s76, s64, 65536\n"
        "buffer_load_dwordx4 v12, s[16:19], s76 offen offset:0 sc1 lds\n"
        "buffer_load_dwordx4 v12, s[16:19], s76 offen offset:1024 sc1 lds\n"
        "s_waitcnt vmcnt(6)\n"
        "s_barrier\n"
        "s_add_u32 m0, s58, 65536\n"
        "s_add_u32 s76, s64, 81920\n"
        "buffer_load_dwordx4 v12, s[16:19], s76 offen offset:0 sc1 lds\n"
        "buffer_load_dwordx4 v12, s[16:19], s76 offen offset:1024 sc1 lds\n"
        "s_add_u32 m0, s58, 32768\n"
        "s_movk_i32 s59, 512\n"
        "buffer_load_dwordx4 v8, s[12:15], s59 offen offset:0 lds\n"
        "buffer_load_dwordx4 v9, s[12:15], s59 offen offset:1024 lds\n"
        "buffer_load_dwordx4 v10, s[12:15], s59 offen offset:2048 lds\n"
        "buffer_load_dwordx4 v11, s[12:15], s59 offen offset:3072 lds\n"
        "buffer_load_dwordx4 v[40:43], v249, s[8:11], s40 offen\n"
        "buffer_load_dwordx4 v[44:47], v249, s[8:11], s41 offen\n"
        "buffer_load_dwordx4 v[48:51], v249, s[8:11], s42 offen\n"
        "buffer_load_dwordx4 v[52:55], v249, s[8:11], s43 offen\n"
        "buffer_load_dwordx4 v[56:59], v249, s[8:11], s44 offen\n"
        "buffer_load_dwordx4 v[60:63], v249, s[8:11], s45 offen\n"
        "buffer_load_dwordx4 v[64:67], v249, s[8:11], s46 offen\n"
        "buffer_load_dwordx4 v[68:71], v249, s[8:11], s47 offen\n"
        "s_waitcnt vmcnt(14)\n"
        "s_barrier\n"
        "s_add_u32 m0, s58, 81920\n"
        "s_add_u32 s76, s64, 98304\n"
        "buffer_load_dwordx4 v12, s[16:19], s76 offen offset:0 sc1 lds\n"
        "buffer_load_dwordx4 v12, s[16:19], s76 offen offset:1024 sc1 lds\n"
        "s_add_u32 m0, s58, 0\n"
        "s_movk_i32 s59, 640\n"
        "buffer_load_dwordx4 v8, s[12:15], s59 offen offset:0 lds\n"
        "buffer_load_dwordx4 v9, s[12:15], s59 offen offset:1024 lds\n"
        "buffer_load_dwordx4 v10, s[12:15], s59 offen offset:2048 lds\n"
        "buffer_load_dwordx4 v11, s[12:15], s59 offen offset:3072 lds\n"
        "s_waitcnt vmcnt(14)\n"
        "s_barrier\n"
        "s_add_u32 m0, s58, 49152\n"
        "s_add_u32 s76, s64, 114688\n"
        "buffer_load_dwordx4 v12, s[16:19], s76 offen offset:0 sc1 lds\n"
        "buffer_load_dwordx4 v12, s[16:19], s76 offen offset:1024 sc1 lds\n"
        "s_add_u32 m0, s58, 16384\n"
        "s_movk_i32 s59, 768\n"
        "buffer_load_dwordx4 v8, s[12:15], s59 offen offset:0 lds\n"
        "buffer_load_dwordx4 v9, s[12:15], s59 offen offset:1024 lds\n"
        "buffer_load_dwordx4 v10, s[12:15], s59 offen offset:2048 lds\n"
        "buffer_load_dwordx4 v11, s[12:15], s59 offen offset:3072 lds\n"
        "s_waitcnt vmcnt(12)\n"
        "v_lshl_add_u64 v[68:69], v[68:69], 4, v[64:65]\n"
        "v_lshl_add_u64 v[70:71], v[70:71], 4, v[66:67]\n"
        "v_lshl_add_u64 v[68:69], v[68:69], 4, v[60:61]\n"
        "v_lshl_add_u64 v[70:71], v[70:71], 4, v[62:63]\n"
        "v_lshl_add_u64 v[68:69], v[68:69], 4, v[56:57]\n"
        "v_lshl_add_u64 v[70:71], v[70:71], 4, v[58:59]\n"
        "v_lshl_add_u64 v[68:69], v[68:69], 4, v[52:53]\n"
        "v_lshl_add_u64 v[70:71], v[70:71], 4, v[54:55]\n"
        "v_lshl_add_u64 v[68:69], v[68:69], 4, v[48:49]\n"
        "v_lshl_add_u64 v[70:71], v[70:71], 4, v[50:51]\n"
        "v_lshl_add_u64 v[68:69], v[68:69], 4, v[44:45]\n"
        "v_lshl_add_u64 v[70:71], v[70:71], 4, v[46:47]\n"
        "v_lshl_add_u64 v[72:73], v[68:69], 4, v[40:41]\n"
        "v_lshl_add_u64 v[76:77], v[70:71], 4, v[42:43]\n"
        "s_nop 0\n"
        "v_permlane32_swap_b32 v72, v73\n"
        "v_permlane32_swap_b32 v76, v77\n"
        "buffer_store_dwordx4 v[72:75], v250, s[16:19], s75 offen sc1\n"
        "buffer_store_dwordx4 v[76:79], v251, s[16:19], s75 offen offset:1024 sc1\n"
        "s_waitcnt vmcnt(8)\n"
        "s_barrier\n"
        "s_add_u32 m0, s58, 65536\n"
        "s_add_u32 s76, s64, 131072\n"
        "buffer_load_dwordx4 v12, s[16:19], s76 offen offset:0 sc1 lds\n"
        "buffer_load_dwordx4 v12, s[16:19], s76 offen offset:1024 sc1 lds\n"
        "s_add_u32 m0, s58, 32768\n"
        "s_movk_i32 s59, 896\n"
        "buffer_load_dwordx4 v8, s[12:15], s59 offen offset:0 lds\n"
        "buffer_load_dwordx4 v9, s[12:15], s59 offen offset:1024 lds\n"
        "buffer_load_dwordx4 v10, s[12:15], s59 offen offset:2048 lds\n"
        "buffer_load_dwordx4 v11, s[12:15], s59 offen offset:3072 lds\n"
        "s_waitcnt vmcnt(8)\n"
        "s_barrier\n"
        "s_add_u32 m0, s58, 81920\n"
        "s_add_u32 s76, s64, 147456\n"
        "buffer_load_dwordx4 v12, s[16:19], s76 offen offset:0 sc1 lds\n"
        "buffer_load_dwordx4 v12, s[16:19], s76 offen offset:1024 sc1 lds\n"
        "s_add_u32 m0, s58, 0\n"
        "s_movk_i32 s59, 1024\n"
        "buffer_load_dwordx4 v8, s[12:15], s59 offen offset:0 lds\n"
        "buffer_load_dwordx4 v9, s[12:15], s59 offen offset:1024 lds\n"
        "buffer_load_dwordx4 v10, s[12:15], s59 offen offset:2048 lds\n"
        "buffer_load_dwordx4 v11, s[12:15], s59 offen offset:3072 lds\n"
        "s_waitcnt vmcnt(6)\n"
        "s_barrier\n"
        "s_add_u32 m0, s58, 49152\n"
        "s_add_u32 s76, s64, 163840\n"
        "buffer_load_dwordx4 v12, s[16:19], s76 offen offset:0 sc1 lds\n"
        "buffer_load_dwordx4 v12, s[16:19], s76 offen offset:1024 sc1 lds\n"
        "s_add_u32 m0, s58, 16384\n"
        "s_movk_i32 s59, 1152\n"
        "buffer_load_dwordx4 v8, s[12:15], s59 offen offset:0 lds\n"
        "buffer_load_dwordx4 v9, s[12:15], s59 offen offset:1024 lds\n"
        "buffer_load_dwordx4 v10, s[12:15], s59 offen offset:2048 lds\n"
        "buffer_load_dwordx4 v11, s[12:15], s59 offen offset:3072 lds\n"
        "s_waitcnt vmcnt(6)\n"
        "s_barrier\n"
        "s_add_u32 m0, s58, 65536\n"
        "s_add_u32 s76, s64, 180224\n"
        "buffer_load_dwordx4 v12, s[16:19], s76 offen offset:0 sc1 lds\n"
        "buffer_load_dwordx4 v12, s[16:19], s76 offen offset:1024 sc1 lds\n"
        "s_add_u32 m0, s58, 32768\n"
        "s_movk_i32 s59, 1280\n"
        "buffer_load_dwordx4 v8, s[12:15], s59 offen offset:0 lds\n"
        "buffer_load_dwordx4 v9, s[12:15], s59 offen offset:1024 lds\n"
        "buffer_load_dwordx4 v10, s[12:15], s59 offen offset:2048 lds\n"
        "buffer_load_dwordx4 v11, s[12:15], s59 offen offset:3072 lds\n"
        "s_waitcnt vmcnt(6)\n"
        "s_barrier\n"
        "s_add_u32 m0, s58, 81920\n"
        "s_add_u32 s76, s64, 196608\n"
        "buffer_load_dwordx4 v12, s[16:19], s76 offen offset:0 sc1 lds\n"
        "buffer_load_dwordx4 v12, s[16:19], s76 offen offset:1024 sc1 lds\n"
        "s_add_u32 m0, s58, 0\n"
        "s_movk_i32 s59, 1408\n"
        "buffer_load_dwordx4 v8, s[12:15], s59 offen offset:0 lds\n"
        "buffer_load_dwordx4 v9, s[12:15], s59 offen offset:1024 lds\n"
        "buffer_load_dwordx4 v10, s[12:15], s59 offen offset:2048 lds\n"
        "buffer_load_dwordx4 v11, s[12:15], s59 offen offset:3072 lds\n"
        "s_waitcnt vmcnt(6)\n"
        "s_barrier\n"
        "s_add_u32 m0, s58, 49152\n"
        "s_add_u32 s76, s64, 212992\n"
        "buffer_load_dwordx4 v12, s[16:19], s76 offen offset:0 sc1 lds\n"
        "buffer_load_dwordx4 v12, s[16:19], s76 offen offset:1024 sc1 lds\n"
        "s_add_u32 m0, s58, 16384\n"
        "s_movk_i32 s59, 1536\n"
        "buffer_load_dwordx4 v8, s[12:15], s59 offen offset:0 lds\n"
        "buffer_load_dwordx4 v9, s[12:15], s59 offen offset:1024 lds\n"
        "buffer_load_dwordx4 v10, s[12:15], s59 offen offset:2048 lds\n"
        "buffer_load_dwordx4 v11, s[12:15], s59 offen offset:3072 lds\n"
        "s_waitcnt vmcnt(6)\n"
        "s_barrier\n"
        "s_add_u32 m0, s58, 65536\n"
        "s_add_u32 s76, s64, 229376\n"
        "buffer_load_dwordx4 v12, s[16:19], s76 offen offset:0 sc1 lds\n"
        "buffer_load_dwordx4 v12, s[16:19], s76 offen offset:1024 sc1 lds\n"
        "s_add_u32 m0, s58, 32768\n"
        "s_movk_i32 s59, 1664\n"
        "buffer_load_dwordx4 v8, s[12:15], s59 offen offset:0 lds\n"
        "buffer_load_dwordx4 v9, s[12:15], s59 offen offset:1024 lds\n"
        "buffer_load_dwordx4 v10, s[12:15], s59 offen offset:2048 lds\n"
        "buffer_load_dwordx4 v11, s[12:15], s59 offen offset:3072 lds\n"
        "s_waitcnt vmcnt(6)\n"
        "s_barrier\n"
        "s_add_u32 m0, s58, 81920\n"
        "s_add_u32 s76, s64, 245760\n"
        "buffer_load_dwordx4 v12, s[16:19], s76 offen offset:0 sc1 lds\n"
        "buffer_load_dwordx4 v12, s[16:19], s76 offen offset:1024 sc1 lds\n"
        "s_add_u32 m0, s58, 0\n"
        "s_movk_i32 s59, 1792\n"
        "buffer_load_dwordx4 v8, s[12:15], s59 offen offset:0 lds\n"
        "buffer_load_dwordx4 v9, s[12:15], s59 offen offset:1024 lds\n"
        "buffer_load_dwordx4 v10, s[12:15], s59 offen offset:2048 lds\n"
        "buffer_load_dwordx4 v11, s[12:15], s59 offen offset:3072 lds\n"
        "s_waitcnt vmcnt(6)\n"
        "s_barrier\n"
        "s_add_u32 m0, s58, 16384\n"
        "s_movk_i32 s59, 1920\n"
        "buffer_load_dwordx4 v8, s[12:15], s59 offen offset:0 lds\n"
        "buffer_load_dwordx4 v9, s[12:15], s59 offen offset:1024 lds\n"
        "buffer_load_dwordx4 v10, s[12:15], s59 offen offset:2048 lds\n"
        "buffer_load_dwordx4 v11, s[12:15], s59 offen offset:3072 lds\n"
        "s_waitcnt vmcnt(0)\n"
        "s_barrier\n"
        ".Ln_ht:\n"
        "s_barrier\n"
        "1:\n"
        "s_sleep 1\n"
        "ds_read_b64 v[80:81], v92 offset:0\n"
        "s_waitcnt lgkmcnt(0)\n"
        "v_cmp_ne_u64 vcc, s[88:89], v[80:81]\n"
        "s_cbranch_vccnz 1b\n"
        "ds_read_b128 v[168:171], v100 offset:0\n"
        "s_add_u32 s35, s84, 0\n"
        "s_waitcnt lgkmcnt(0)\n"
        "buffer_store_dwordx4 v[168:171], v102, s[20:23], s35 offen nt\n"
        "1:\n"
        "s_sleep 1\n"
        "ds_read_b64 v[80:81], v92 offset:256\n"
        "s_waitcnt lgkmcnt(0)\n"
        "v_cmp_ne_u64 vcc, s[88:89], v[80:81]\n"
        "s_cbranch_vccnz 1b\n"
        "ds_read_b128 v[176:179], v100 offset:8192\n"
        "s_add_u32 s35, s84, 114688\n"
        "s_waitcnt lgkmcnt(0)\n"
        "buffer_store_dwordx4 v[176:179], v102, s[20:23], s35 offen nt\n"
        "1:\n"
        "s_sleep 1\n"
        "ds_read_b64 v[80:81], v92 offset:512\n"
        "s_waitcnt lgkmcnt(0)\n"
        "v_cmp_ne_u64 vcc, s[88:89], v[80:81]\n"
        "s_cbranch_vccnz 1b\n"
        "ds_read_b128 v[184:187], v101 offset:0\n"
        "s_add_u32 s35, s84, 0\n"
        "s_waitcnt lgkmcnt(0)\n"
        "buffer_store_dwordx4 v[184:187], v103, s[20:23], s35 offen nt\n"
        "1:\n"
        "s_sleep 1\n"
        "ds_read_b64 v[80:81], v92 offset:768\n"
        "s_waitcnt lgkmcnt(0)\n"
        "v_cmp_ne_u64 vcc, s[88:89], v[80:81]\n"
        "s_cbranch_vccnz 1b\n"
        "ds_read_b128 v[192:195], v101 offset:8192\n"
        "s_add_u32 s35, s84, 114688\n"
        "s_waitcnt lgkmcnt(0)\n"
        "buffer_store_dwordx4 v[192:195], v103, s[20:23], s35 offen nt\n"
        "1:\n"
        "s_sleep 1\n"
        "ds_read_b64 v[80:81], v92 offset:1024\n"
        "s_waitcnt lgkmcnt(0)\n"
        "v_cmp_ne_u64 vcc, s[88:89], v[80:81]\n"
        "s_cbranch_vccnz 1b\n"
        "ds_read_b128 v[200:203], v88 offset:16384\n"
        "ds_read_b128 v[204:207], v89 offset:17408\n"
        "s_add_u32 s35, s84, 229376\n"
        "s_add_u32 s36, s35, 28672\n"
        "s_waitcnt lgkmcnt(1)\n"
        "buffer_store_dwordx4 v[200:203], v90, s[20:23], s35 offen nt\n"
        "s_waitcnt lgkmcnt(0)\n"
        "buffer_store_dwordx4 v[204:207], v90, s[20:23], s36 offen nt\n"
        "1:\n"
        "s_sleep 1\n"
        "ds_read_b64 v[80:81], v92 offset:1280\n"
        "s_waitcnt lgkmcnt(0)\n"
        "v_cmp_ne_u64 vcc, s[88:89], v[80:81]\n"
        "s_cbranch_vccnz 1b\n"
        "ds_read_b128 v[208:211], v88 offset:24576\n"
        "ds_read_b128 v[212:215], v89 offset:25600\n"
        "s_add_u32 s35, s84, 344064\n"
        "s_add_u32 s36, s35, 28672\n"
        "s_waitcnt lgkmcnt(1)\n"
        "buffer_store_dwordx4 v[208:211], v90, s[20:23], s35 offen nt\n"
        "s_waitcnt lgkmcnt(0)\n"
        "buffer_store_dwordx4 v[212:215], v90, s[20:23], s36 offen nt\n"
        "1:\n"
        "s_sleep 1\n"
        "ds_read_b64 v[80:81], v92 offset:1536\n"
        "s_waitcnt lgkmcnt(0)\n"
        "v_cmp_ne_u64 vcc, s[88:89], v[80:81]\n"
        "s_cbranch_vccnz 1b\n"
        "ds_read_b128 v[216:219], v88 offset:32768\n"
        "ds_read_b128 v[220:223], v89 offset:33792\n"
        "s_add_u32 s35, s85, 0\n"
        "s_add_u32 s36, s35, 28672\n"
        "s_waitcnt lgkmcnt(1)\n"
        "buffer_store_dwordx4 v[216:219], v91, s[20:23], s35 offen nt\n"
        "s_waitcnt lgkmcnt(0)\n"
        "buffer_store_dwordx4 v[220:223], v91, s[20:23], s36 offen nt\n"
        "1:\n"
        "s_sleep 1\n"
        "ds_read_b64 v[80:81], v92 offset:1792\n"
        "s_waitcnt lgkmcnt(0)\n"
        "v_cmp_ne_u64 vcc, s[88:89], v[80:81]\n"
        "s_cbranch_vccnz 1b\n"
        "ds_read_b128 v[224:227], v88 offset:40960\n"
        "ds_read_b128 v[228:231], v89 offset:41984\n"
        "s_add_u32 s35, s85, 114688\n"
        "s_add_u32 s36, s35, 28672\n"
        "s_waitcnt lgkmcnt(1)\n"
        "buffer_store_dwordx4 v[224:227], v91, s[20:23], s35 offen nt\n"
        "s_waitcnt lgkmcnt(0)\n"
        "buffer_store_dwordx4 v[228:231], v91, s[20:23], s36 offen nt\n"
        "1:\n"
        "s_sleep 1\n"
        "ds_read_b64 v[80:81], v92 offset:2048\n"
        "s_waitcnt lgkmcnt(0)\n"
        "v_cmp_ne_u64 vcc, s[88:89], v[80:81]\n"
        "s_cbranch_vccnz 1b\n"
        "ds_read_b128 v[232:235], v88 offset:49152\n"
        "ds_read_b128 v[236:239], v89 offset:50176\n"
        "s_add_u32 s35, s85, 229376\n"
        "s_add_u32 s36, s35, 28672\n"
        "s_waitcnt lgkmcnt(1)\n"
        "buffer_store_dwordx4 v[232:235], v91, s[20:23], s35 offen nt\n"
        "s_waitcnt lgkmcnt(0)\n"
        "buffer_store_dwordx4 v[236:239], v91, s[20:23], s36 offen nt\n"
        "1:\n"
        "s_sleep 1\n"
        "ds_read_b64 v[80:81], v92 offset:2304\n"
        "s_waitcnt lgkmcnt(0)\n"
        "v_cmp_ne_u64 vcc, s[88:89], v[80:81]\n"
        "s_cbranch_vccnz 1b\n"
        "ds_read_b128 v[240:243], v88 offset:57344\n"
        "ds_read_b128 v[244:247], v89 offset:58368\n"
        "s_add_u32 s35, s85, 344064\n"
        "s_add_u32 s36, s35, 28672\n"
        "s_waitcnt lgkmcnt(1)\n"
        "buffer_store_dwordx4 v[240:243], v91, s[20:23], s35 offen nt\n"
        "s_waitcnt lgkmcnt(0)\n"
        "buffer_store_dwordx4 v[244:247], v91, s[20:23], s36 offen nt\n"
        "s_endpgm\n"
        ".p2align 12\n"
        ".Ln_r0:\n"
        "s_add_u32 s73, s73, 1\n"
        "s_cmp_lt_u32 s73, s74\n"
        "s_cbranch_scc0 .Ln_f0\n"
        "buffer_load_dwordx4 v[64:67], v232, s[16:19], s64 offen sc1\n"
        "buffer_load_dwordx4 v[68:71], v233, s[16:19], s64 offen sc1\n"
        "buffer_load_dwordx4 v[72:75], v234, s[16:19], s64 offen sc1\n"
        "buffer_load_dwordx4 v[76:79], v235, s[16:19], s64 offen sc1\n"
        "buffer_load_dwordx4 v[208:211], v232, s[16:19], s65 offen sc1\n"
        "buffer_load_dwordx4 v[212:215], v233, s[16:19], s65 offen sc1\n"
        "buffer_load_dwordx4 v[216:219], v234, s[16:19], s65 offen sc1\n"
        "buffer_load_dwordx4 v[220:223], v235, s[16:19], s65 offen sc1\n"
        "s_waitcnt vmcnt(0)\n"
        ".Ln_go:\n"
        "s_waitcnt vmcnt(4)\n"
        "v_bitop3_b32 v66, v66, v70, s34 bitop3:0x7e\n"
        "v_bitop3_b32 v67, v67, v71, s39 bitop3:0x7e\n"
        "v_bitop3_b32 v74, v74, v78, s34 bitop3:0x7e\n"
        "v_bitop3_b32 v75, v75, v79, s39 bitop3:0x7e\n"
        "v_or3_b32 v66, v66, v67, v74\n"
        "v_or_b32 v66, v66, v75\n"
        "v_cmp_ne_u32 vcc, 0, v66\n"
        "s_cbranch_vccnz .Ln_r0\n"
        ".Ln_b0:\n"
        "v_perm_b32 v224, v64, v236, s60\n"
        "v_perm_b32 v225, v64, v236, s61\n"
        "ds_read_b32 v112, v224\n"
        "v_perm_b32 v226, v64, v236, s62\n"
        "ds_read_b32 v113, v225\n"
        "v_perm_b32 v227, v64, v236, s63\n"
        "ds_read_b32 v114, v226\n"
        "v_perm_b32 v224, v65, v236, s60\n"
        "ds_read_b32 v115, v227\n"
        "v_perm_b32 v225, v65, v236, s61\n"
        "ds_read_b32 v116, v224\n"
        "v_perm_b32 v226, v65, v236, s62\n"
        "ds_read_b32 v117, v225\n"
        "v_perm_b32 v227, v65, v236, s63\n"
        "ds_read_b32 v118, v226\n"
        "v_perm_b32 v224, v68, v236, s60\n"
        "ds_read_b32 v119, v227\n"
        "v_perm_b32 v225, v68, v236, s61\n"
        "ds_read_b32 v120, v224\n"
        "ds_read_b32 v121, v225\n"
        "ds_write_b32 v237, v237 offset:4096\n"
        "v_perm_b32 v226, v68, v236, s62\n"
        "s_waitcnt lgkmcnt(7)\n"
        "v_mfma_f32_16x16x32_bf16 v[0:3], v[112:115], v[176:179], 0\n"
        "ds_read_b32 v122, v226\n"
        "v_perm_b32 v227, v68, v236, s63\n"
        "v_perm_b32 v224, v69, v236, s60\n"
        "s_waitcnt lgkmcnt(4)\n"
        "v_mfma_f32_16x16x32_bf16 v[0:3], v[116:119], v[180:183], v[0:3]\n"
        "ds_read_b32 v123, v227\n"
        "ds_read_b32 v124, v224\n"
        "v_perm_b32 v225, v69, v236, s61\n"
        "v_mfma_f32_16x16x32_bf16 v[16:19], v[112:115], v[184:187], 0\n"
        "ds_read_b32 v125, v225\n"
        "v_perm_b32 v226, v69, v236, s62\n"
        "v_perm_b32 v227, v69, v236, s63\n"
        "v_mfma_f32_16x16x32_bf16 v[16:19], v[116:119], v[188:191], v[16:19]\n"
        "ds_read_b32 v126, v226\n"
        "ds_read_b32 v127, v227\n"
        "v_perm_b32 v224, v72, v236, s60\n"
        "v_mfma_f32_16x16x32_bf16 v[32:35], v[112:115], v[192:195], 0\n"
        "ds_read_b32 v128, v224\n"
        "v_perm_b32 v225, v72, v236, s61\n"
        "v_perm_b32 v226, v72, v236, s62\n"
        "v_mfma_f32_16x16x32_bf16 v[32:35], v[116:119], v[196:199], v[32:35]\n"
        "ds_read_b32 v129, v225\n"
        "ds_read_b32 v130, v226\n"
        "v_perm_b32 v227, v72, v236, s63\n"
        "v_mfma_f32_16x16x32_bf16 v[48:51], v[112:115], v[200:203], 0\n"
        "ds_read_b32 v131, v227\n"
        "v_perm_b32 v224, v73, v236, s60\n"
        "v_perm_b32 v225, v73, v236, s61\n"
        "s_waitcnt vmcnt(0)\n"
        "v_mfma_f32_16x16x32_bf16 v[48:51], v[116:119], v[204:207], v[48:51]\n"
        "ds_read_b32 v132, v224\n"
        "ds_read_b32 v133, v225\n"
        "v_perm_b32 v226, v73, v236, s62\n"
        "v_bitop3_b32 v210, v210, v214, s34 bitop3:0x7e\n"
        "s_waitcnt lgkmcnt(10)\n"
        "v_mfma_f32_16x16x32_bf16 v[4:7], v[120:123], v[176:179], 0\n"
        "ds_read_b32 v134, v226\n"
        "v_perm_b32 v227, v73, v236, s63\n"
        "v_perm_b32 v224, v76, v236, s60\n"
        "v_bitop3_b32 v211, v211, v215, s39 bitop3:0x7e\n"
        "s_waitcnt lgkmcnt(7)\n"
        "v_mfma_f32_16x16x32_bf16 v[4:7], v[124:127], v[180:183], v[4:7]\n"
        "ds_read_b32 v135, v227\n"
        "ds_read_b32 v136, v224\n"
        "v_perm_b32 v225, v76, v236, s61\n"
        "v_bitop3_b32 v218, v218, v222, s34 bitop3:0x7e\n"
        "v_mfma_f32_16x16x32_bf16 v[20:23], v[120:123], v[184:187], 0\n"
        "ds_read_b32 v137, v225\n"
        "v_perm_b32 v226, v76, v236, s62\n"
        "v_perm_b32 v227, v76, v236, s63\n"
        "v_bitop3_b32 v219, v219, v223, s39 bitop3:0x7e\n"
        "v_mfma_f32_16x16x32_bf16 v[20:23], v[124:127], v[188:191], v[20:23]\n"
        "ds_read_b32 v138, v226\n"
        "ds_read_b32 v139, v227\n"
        "v_perm_b32 v224, v77, v236, s60\n"
        "v_or3_b32 v210, v210, v211, v218\n"
        "v_mfma_f32_16x16x32_bf16 v[36:39], v[120:123], v[192:195], 0\n"
        "ds_read_b32 v140, v224\n"
        "v_perm_b32 v225, v77, v236, s61\n"
        "v_perm_b32 v226, v77, v236, s62\n"
        "v_or_b32 v210, v210, v219\n"
        "v_mfma_f32_16x16x32_bf16 v[36:39], v[124:127], v[196:199], v[36:39]\n"
        "ds_read_b32 v141, v225\n"
        "ds_read_b32 v142, v226\n"
        "v_perm_b32 v227, v77, v236, s63\n"
        "v_cmp_ne_u32 vcc, 0, v210\n"
        "v_mfma_f32_16x16x32_bf16 v[52:55], v[120:123], v[200:203], 0\n"
        "ds_read_b32 v143, v227\n"
        "s_cbranch_vccnz .Ln_f1\n"
        ".Ln_b1:\n"
        "v_perm_b32 v224, v208, v236, s60\n"
        "v_perm_b32 v225, v208, v236, s61\n"
        "v_mfma_f32_16x16x32_bf16 v[52:55], v[124:127], v[204:207], v[52:55]\n"
        "ds_read_b32 v144, v224\n"
        "ds_read_b32 v145, v225\n"
        "v_perm_b32 v226, v208, v236, s62\n"
        "s_waitcnt lgkmcnt(14)\n"
        "v_mfma_f32_16x16x32_bf16 v[8:11], v[128:131], v[176:179], 0\n"
        "ds_read_b32 v146, v226\n"
        "v_perm_b32 v227, v208, v236, s63\n"
        "v_perm_b32 v224, v209, v236, s60\n"
        "s_waitcnt lgkmcnt(11)\n"
        "v_mfma_f32_16x16x32_bf16 v[8:11], v[132:135], v[180:183], v[8:11]\n"
        "ds_read_b32 v147, v227\n"
        "ds_read_b32 v148, v224\n"
        "v_perm_b32 v225, v209, v236, s61\n"
        "v_mfma_f32_16x16x32_bf16 v[24:27], v[128:131], v[184:187], 0\n"
        "ds_read_b32 v149, v225\n"
        "v_perm_b32 v226, v209, v236, s62\n"
        "v_perm_b32 v227, v209, v236, s63\n"
        "v_mfma_f32_16x16x32_bf16 v[24:27], v[132:135], v[188:191], v[24:27]\n"
        "ds_read_b32 v150, v226\n"
        "ds_read_b32 v151, v227\n"
        "v_perm_b32 v224, v212, v236, s60\n"
        "v_mfma_f32_16x16x32_bf16 v[40:43], v[128:131], v[192:195], 0\n"
        "ds_read_b32 v152, v224\n"
        "v_perm_b32 v225, v212, v236, s61\n"
        "v_perm_b32 v226, v212, v236, s62\n"
        "v_mfma_f32_16x16x32_bf16 v[40:43], v[132:135], v[196:199], v[40:43]\n"
        "ds_read_b32 v153, v225\n"
        "ds_read_b32 v154, v226\n"
        "v_perm_b32 v227, v212, v236, s63\n"
        "v_mfma_f32_16x16x32_bf16 v[56:59], v[128:131], v[200:203], 0\n"
        "ds_read_b32 v155, v227\n"
        "v_perm_b32 v224, v213, v236, s60\n"
        "v_perm_b32 v225, v213, v236, s61\n"
        "v_mfma_f32_16x16x32_bf16 v[56:59], v[132:135], v[204:207], v[56:59]\n"
        "ds_read_b32 v156, v224\n"
        "ds_read_b32 v157, v225\n"
        "v_perm_b32 v226, v213, v236, s62\n"
        "s_waitcnt lgkmcnt(15)\n"
        "v_mfma_f32_16x16x32_bf16 v[12:15], v[136:139], v[176:179], 0\n"
        "ds_read_b32 v158, v226\n"
        "v_perm_b32 v227, v213, v236, s63\n"
        "s_waitcnt lgkmcnt(15)\n"
        "v_mfma_f32_16x16x32_bf16 v[12:15], v[140:143], v[180:183], v[12:15]\n"
        "ds_read_b32 v159, v227\n"
        "v_perm_b32 v224, v216, v236, s60\n"
        "v_mfma_f32_16x16x32_bf16 v[28:31], v[136:139], v[184:187], 0\n"
        "ds_read_b32 v160, v224\n"
        "v_perm_b32 v225, v216, v236, s61\n"
        "v_mfma_f32_16x16x32_bf16 v[28:31], v[140:143], v[188:191], v[28:31]\n"
        "ds_read_b32 v161, v225\n"
        "v_perm_b32 v226, v216, v236, s62\n"
        "v_mfma_f32_16x16x32_bf16 v[44:47], v[136:139], v[192:195], 0\n"
        "ds_read_b32 v162, v226\n"
        "v_perm_b32 v227, v216, v236, s63\n"
        "v_mfma_f32_16x16x32_bf16 v[44:47], v[140:143], v[196:199], v[44:47]\n"
        "ds_read_b32 v163, v227\n"
        "v_perm_b32 v224, v217, v236, s60\n"
        "v_mfma_f32_16x16x32_bf16 v[60:63], v[136:139], v[200:203], 0\n"
        "ds_read_b32 v164, v224\n"
        "v_perm_b32 v225, v217, v236, s61\n"
        "v_mfma_f32_16x16x32_bf16 v[60:63], v[140:143], v[204:207], v[60:63]\n"
        "ds_read_b32 v165, v225\n"
        "v_mfma_f32_16x16x32_bf16 v[64:67], v[112:115], v[80:83], 0\n"
        "ds_read_b128 v[200:203], v230 offset:22528\n"
        "v_mfma_f32_16x16x32_bf16 v[64:67], v[116:119], v[84:87], v[64:67]\n"
        "ds_read_b128 v[204:207], v231 offset:22528\n"
        "v_perm_b32 v226, v217, v236, s62\n"
        "v_mfma_f32_16x16x32_bf16 v[68:71], v[120:123], v[80:83], 0\n"
        "ds_read_b32 v166, v226\n"
        "v_perm_b32 v227, v217, v236, s63\n"
        "v_mfma_f32_16x16x32_bf16 v[68:71], v[124:127], v[84:87], v[68:71]\n"
        "ds_read_b32 v167, v227\n"
        "v_perm_b32 v224, v220, v236, s60\n"
        "v_mfma_f32_16x16x32_bf16 v[72:75], v[128:131], v[80:83], 0\n"
        "ds_read_b32 v168, v224\n"
        "v_perm_b32 v225, v220, v236, s61\n"
        "v_mfma_f32_16x16x32_bf16 v[72:75], v[132:135], v[84:87], v[72:75]\n"
        "ds_read_b32 v169, v225\n"
        "v_perm_b32 v226, v220, v236, s62\n"
        "v_mfma_f32_16x16x32_bf16 v[76:79], v[112:115], v[88:91], 0\n"
        "ds_read_b32 v170, v226\n"
        "v_perm_b32 v227, v220, v236, s63\n"
        "v_mfma_f32_16x16x32_bf16 v[76:79], v[116:119], v[92:95], v[76:79]\n"
        "ds_read_b32 v171, v227\n"
        "v_perm_b32 v224, v221, v236, s60\n"
        "v_mfma_f32_16x16x32_bf16 v[80:83], v[120:123], v[88:91], 0\n"
        "ds_read_b32 v172, v224\n"
        "v_perm_b32 v225, v221, v236, s61\n"
        "v_mfma_f32_16x16x32_bf16 v[80:83], v[124:127], v[92:95], v[80:83]\n"
        "ds_read_b32 v173, v225\n"
        "v_perm_b32 v226, v221, v236, s62\n"
        "v_mfma_f32_16x16x32_bf16 v[84:87], v[128:131], v[88:91], 0\n"
        "ds_read_b32 v174, v226\n"
        "v_perm_b32 v227, v221, v236, s63\n"
        "v_mfma_f32_16x16x32_bf16 v[84:87], v[132:135], v[92:95], v[84:87]\n"
        "ds_read_b32 v175, v227\n"
        "s_waitcnt lgkmcnt(10)\n"
        "s_barrier\n"
        "v_mfma_f32_16x16x32_bf16 v[88:91], v[112:115], v[100:103], 0\n"
        "ds_read_b128 v[208:211], v232 offset:16384\n"
        "v_mfma_f32_16x16x32_bf16 v[88:91], v[116:119], v[104:107], v[88:91]\n"
        "ds_read_b128 v[212:215], v233 offset:16384\n"
        "v_mfma_f32_16x16x32_bf16 v[92:95], v[120:123], v[100:103], 0\n"
        "v_mfma_f32_16x16x32_bf16 v[92:95], v[124:127], v[104:107], v[92:95]\n"
        "ds_read_b128 v[216:219], v234 offset:16384\n"
        "v_mfma_f32_16x16x32_bf16 v[96:99], v[128:131], v[100:103], 0\n"
        "ds_read_b128 v[220:223], v235 offset:16384\n"
        "v_mfma_f32_16x16x32_bf16 v[96:99], v[132:135], v[104:107], v[96:99]\n"
        "v_mfma_f32_16x16x32_bf16 v[100:103], v[112:115], v[200:203], 0\n"
        "ds_read_b128 v[176:179], v228 offset:32768\n"
        "v_mfma_f32_16x16x32_bf16 v[100:103], v[116:119], v[204:207], v[100:103]\n"
        "ds_read_b128 v[180:183], v229 offset:32768\n"
        "v_mfma_f32_16x16x32_bf16 v[104:107], v[120:123], v[200:203], 0\n"
        "v_mfma_f32_16x16x32_bf16 v[104:107], v[124:127], v[204:207], v[104:107]\n"
        "ds_read_b128 v[184:187], v228 offset:34816\n"
        "v_mfma_f32_16x16x32_bf16 v[108:111], v[128:131], v[200:203], 0\n"
        "ds_read_b128 v[188:191], v229 offset:34816\n"
        "v_mfma_f32_16x16x32_bf16 v[108:111], v[132:135], v[204:207], v[108:111]\n"
        "s_waitcnt lgkmcnt(2)\n"
        "v_mfma_f32_16x16x32_bf16 v[0:3], v[144:147], v[176:179], v[0:3]\n"
        "ds_read_b128 v[192:195], v228 offset:36864\n"
        "v_bitop3_b32 v210, v210, v214, s34 bitop3:0x7e\n"
        "v_mfma_f32_16x16x32_bf16 v[0:3], v[148:151], v[180:183], v[0:3]\n"
        "ds_read_b128 v[196:199], v229 offset:36864\n"
        "v_bitop3_b32 v211, v211, v215, s39 bitop3:0x7e\n"
        "v_mfma_f32_16x16x32_bf16 v[4:7], v[156:159], v[180:183], v[4:7]\n"
        "ds_read_b128 v[200:203], v228 offset:38912\n"
        "v_bitop3_b32 v218, v218, v222, s34 bitop3:0x7e\n"
        "v_mfma_f32_16x16x32_bf16 v[4:7], v[152:155], v[176:179], v[4:7]\n"
        "ds_read_b128 v[204:207], v229 offset:38912\n"
        "v_perm_b32 v224, v208, v236, s60\n"
        "v_mfma_f32_16x16x32_bf16 v[8:11], v[160:163], v[176:179], v[8:11]\n"
        "ds_read_b32 v112, v224\n"
        "v_perm_b32 v225, v208, v236, s61\n"
        "v_mfma_f32_16x16x32_bf16 v[8:11], v[164:167], v[180:183], v[8:11]\n"
        "ds_read_b32 v113, v225\n"
        "v_perm_b32 v226, v208, v236, s62\n"
        "v_mfma_f32_16x16x32_bf16 v[12:15], v[172:175], v[180:183], v[12:15]\n"
        "ds_read_b32 v114, v226\n"
        "v_perm_b32 v227, v208, v236, s63\n"
        "v_mfma_f32_16x16x32_bf16 v[12:15], v[168:171], v[176:179], v[12:15]\n"
        "ds_read_b32 v115, v227\n"
        "v_bitop3_b32 v219, v219, v223, s39 bitop3:0x7e\n"
        "s_waitcnt lgkmcnt(8)\n"
        "v_mfma_f32_16x16x32_bf16 v[28:31], v[168:171], v[184:187], v[28:31]\n"
        "ds_read_b128 v[176:179], v230 offset:32768\n"
        "v_or3_b32 v210, v210, v211, v218\n"
        "v_mfma_f32_16x16x32_bf16 v[28:31], v[172:175], v[188:191], v[28:31]\n"
        "ds_read_b128 v[180:183], v231 offset:32768\n"
        "v_perm_b32 v224, v209, v236, s60\n"
        "v_mfma_f32_16x16x32_bf16 v[24:27], v[164:167], v[188:191], v[24:27]\n"
        "ds_read_b32 v116, v224\n"
        "v_perm_b32 v225, v209, v236, s61\n"
        "v_mfma_f32_16x16x32_bf16 v[24:27], v[160:163], v[184:187], v[24:27]\n"
        "ds_read_b32 v117, v225\n"
        "v_perm_b32 v226, v209, v236, s62\n"
        "v_mfma_f32_16x16x32_bf16 v[20:23], v[152:155], v[184:187], v[20:23]\n"
        "ds_read_b32 v118, v226\n"
        "v_perm_b32 v227, v209, v236, s63\n"
        "v_mfma_f32_16x16x32_bf16 v[20:23], v[156:159], v[188:191], v[20:23]\n"
        "ds_read_b32 v119, v227\n"
        "v_perm_b32 v224, v212, v236, s60\n"
        "v_mfma_f32_16x16x32_bf16 v[16:19], v[148:151], v[188:191], v[16:19]\n"
        "ds_read_b32 v120, v224\n"
        "v_perm_b32 v225, v212, v236, s61\n"
        "v_mfma_f32_16x16x32_bf16 v[16:19], v[144:147], v[184:187], v[16:19]\n"
        "ds_read_b32 v121, v225\n"
        "v_or_b32 v210, v210, v219\n"
        "s_waitcnt lgkmcnt(14)\n"
        "v_mfma_f32_16x16x32_bf16 v[32:35], v[144:147], v[192:195], v[32:35]\n"
        "ds_read_b128 v[184:187], v230 offset:34816\n"
        "v_cmp_ne_u32 vcc, 0, v210\n"
        "v_mfma_f32_16x16x32_bf16 v[32:35], v[148:151], v[196:199], v[32:35]\n"
        "ds_read_b128 v[188:191], v231 offset:34816\n"
        "v_perm_b32 v226, v212, v236, s62\n"
        "v_mfma_f32_16x16x32_bf16 v[36:39], v[156:159], v[196:199], v[36:39]\n"
        "ds_read_b32 v122, v226\n"
        "v_perm_b32 v227, v212, v236, s63\n"
        "v_mfma_f32_16x16x32_bf16 v[36:39], v[152:155], v[192:195], v[36:39]\n"
        "ds_read_b32 v123, v227\n"
        "v_perm_b32 v224, v213, v236, s60\n"
        "v_mfma_f32_16x16x32_bf16 v[40:43], v[160:163], v[192:195], v[40:43]\n"
        "ds_read_b32 v124, v224\n"
        "v_perm_b32 v225, v213, v236, s61\n"
        "v_mfma_f32_16x16x32_bf16 v[40:43], v[164:167], v[196:199], v[40:43]\n"
        "ds_read_b32 v125, v225\n"
        "v_perm_b32 v226, v213, v236, s62\n"
        "v_mfma_f32_16x16x32_bf16 v[44:47], v[172:175], v[196:199], v[44:47]\n"
        "ds_read_b32 v126, v226\n"
        "v_perm_b32 v227, v213, v236, s63\n"
        "v_mfma_f32_16x16x32_bf16 v[44:47], v[168:171], v[192:195], v[44:47]\n"
        "ds_read_b32 v127, v227\n"
        "s_waitcnt lgkmcnt(15)\n"
        "v_mfma_f32_16x16x32_bf16 v[60:63], v[168:171], v[200:203], v[60:63]\n"
        "ds_read_b128 v[192:195], v230 offset:36864\n"
        "s_cbranch_vccnz .Ln_f2\n"
        ".Ln_b2:\n"
        "v_mfma_f32_16x16x32_bf16 v[60:63], v[172:175], v[204:207], v[60:63]\n"
        "ds_read_b128 v[196:199], v231 offset:36864\n"
        "v_perm_b32 v224, v216, v236, s60\n"
        "v_mfma_f32_16x16x32_bf16 v[56:59], v[164:167], v[204:207], v[56:59]\n"
        "ds_read_b32 v128, v224\n"
        "v_perm_b32 v225, v216, v236, s61\n"
        "v_mfma_f32_16x16x32_bf16 v[56:59], v[160:163], v[200:203], v[56:59]\n"
        "ds_read_b32 v129, v225\n"
        "v_perm_b32 v226, v216, v236, s62\n"
        "v_mfma_f32_16x16x32_bf16 v[52:55], v[152:155], v[200:203], v[52:55]\n"
        "ds_read_b32 v130, v226\n"
        "v_perm_b32 v227, v216, v236, s63\n"
        "v_mfma_f32_16x16x32_bf16 v[52:55], v[156:159], v[204:207], v[52:55]\n"
        "ds_read_b32 v131, v227\n"
        "v_perm_b32 v224, v217, v236, s60\n"
        "v_mfma_f32_16x16x32_bf16 v[48:51], v[148:151], v[204:207], v[48:51]\n"
        "ds_read_b32 v132, v224\n"
        "v_perm_b32 v225, v217, v236, s61\n"
        "v_mfma_f32_16x16x32_bf16 v[48:51], v[144:147], v[200:203], v[48:51]\n"
        "ds_read_b32 v133, v225\n"
        "s_waitcnt lgkmcnt(15)\n"
        "v_mfma_f32_16x16x32_bf16 v[64:67], v[144:147], v[176:179], v[64:67]\n"
        "ds_read_b128 v[200:203], v230 offset:38912\n"
        "v_mfma_f32_16x16x32_bf16 v[64:67], v[148:151], v[180:183], v[64:67]\n"
        "ds_read_b128 v[204:207], v231 offset:38912\n"
        "v_perm_b32 v226, v217, v236, s62\n"
        "v_mfma_f32_16x16x32_bf16 v[68:71], v[156:159], v[180:183], v[68:71]\n"
        "ds_read_b32 v134, v226\n"
        "v_perm_b32 v227, v217, v236, s63\n"
        "v_mfma_f32_16x16x32_bf16 v[68:71], v[152:155], v[176:179], v[68:71]\n"
        "ds_read_b32 v135, v227\n"
        "v_perm_b32 v224, v220, v236, s60\n"
        "v_mfma_f32_16x16x32_bf16 v[72:75], v[160:163], v[176:179], v[72:75]\n"
        "ds_read_b32 v136, v224\n"
        "v_perm_b32 v225, v220, v236, s61\n"
        "v_mfma_f32_16x16x32_bf16 v[72:75], v[164:167], v[180:183], v[72:75]\n"
        "ds_read_b32 v137, v225\n"
        "v_perm_b32 v226, v220, v236, s62\n"
        "s_waitcnt lgkmcnt(15)\n"
        "v_mfma_f32_16x16x32_bf16 v[84:87], v[164:167], v[188:191], v[84:87]\n"
        "ds_read_b32 v138, v226\n"
        "v_perm_b32 v227, v220, v236, s63\n"
        "v_mfma_f32_16x16x32_bf16 v[84:87], v[160:163], v[184:187], v[84:87]\n"
        "ds_read_b32 v139, v227\n"
        "v_perm_b32 v224, v221, v236, s60\n"
        "v_mfma_f32_16x16x32_bf16 v[80:83], v[152:155], v[184:187], v[80:83]\n"
        "ds_read_b32 v140, v224\n"
        "v_perm_b32 v225, v221, v236, s61\n"
        "v_mfma_f32_16x16x32_bf16 v[80:83], v[156:159], v[188:191], v[80:83]\n"
        "ds_read_b32 v141, v225\n"
        "v_perm_b32 v226, v221, v236, s62\n"
        "v_mfma_f32_16x16x32_bf16 v[76:79], v[148:151], v[188:191], v[76:79]\n"
        "ds_read_b32 v142, v226\n"
        "v_perm_b32 v227, v221, v236, s63\n"
        "v_mfma_f32_16x16x32_bf16 v[76:79], v[144:147], v[184:187], v[76:79]\n"
        "ds_read_b32 v143, v227\n"
        "s_waitcnt lgkmcnt(10)\n"
        "s_barrier\n"
        "v_mfma_f32_16x16x32_bf16 v[88:91], v[144:147], v[192:195], v[88:91]\n"
        "ds_read_b128 v[208:211], v232 offset:32768\n"
        "v_mfma_f32_16x16x32_bf16 v[88:91], v[148:151], v[196:199], v[88:91]\n"
        "ds_read_b128 v[212:215], v233 offset:32768\n"
        "v_mfma_f32_16x16x32_bf16 v[92:95], v[156:159], v[196:199], v[92:95]\n"
        "v_mfma_f32_16x16x32_bf16 v[92:95], v[152:155], v[192:195], v[92:95]\n"
        "ds_read_b128 v[216:219], v234 offset:32768\n"
        "v_mfma_f32_16x16x32_bf16 v[96:99], v[160:163], v[192:195], v[96:99]\n"
        "ds_read_b128 v[220:223], v235 offset:32768\n"
        "v_mfma_f32_16x16x32_bf16 v[96:99], v[164:167], v[196:199], v[96:99]\n"
        "v_mfma_f32_16x16x32_bf16 v[108:111], v[164:167], v[204:207], v[108:111]\n"
        "ds_read_b128 v[176:179], v228 offset:0\n"
        "v_mfma_f32_16x16x32_bf16 v[108:111], v[160:163], v[200:203], v[108:111]\n"
        "ds_read_b128 v[180:183], v229 offset:0\n"
        "v_mfma_f32_16x16x32_bf16 v[104:107], v[152:155], v[200:203], v[104:107]\n"
        "v_mfma_f32_16x16x32_bf16 v[104:107], v[156:159], v[204:207], v[104:107]\n"
        "ds_read_b128 v[184:187], v228 offset:2048\n"
        "v_mfma_f32_16x16x32_bf16 v[100:103], v[148:151], v[204:207], v[100:103]\n"
        "ds_read_b128 v[188:191], v229 offset:2048\n"
        "v_mfma_f32_16x16x32_bf16 v[100:103], v[144:147], v[200:203], v[100:103]\n"
        "s_waitcnt lgkmcnt(2)\n"
        "v_mfma_f32_16x16x32_bf16 v[0:3], v[112:115], v[176:179], v[0:3]\n"
        "ds_read_b128 v[192:195], v228 offset:4096\n"
        "v_bitop3_b32 v210, v210, v214, s34 bitop3:0x7e\n"
        "v_mfma_f32_16x16x32_bf16 v[0:3], v[116:119], v[180:183], v[0:3]\n"
        "ds_read_b128 v[196:199], v229 offset:4096\n"
        "v_bitop3_b32 v211, v211, v215, s39 bitop3:0x7e\n"
        "v_mfma_f32_16x16x32_bf16 v[4:7], v[124:127], v[180:183], v[4:7]\n"
        "ds_read_b128 v[200:203], v228 offset:6144\n"
        "v_bitop3_b32 v218, v218, v222, s34 bitop3:0x7e\n"
        "v_mfma_f32_16x16x32_bf16 v[4:7], v[120:123], v[176:179], v[4:7]\n"
        "ds_read_b128 v[204:207], v229 offset:6144\n"
        "v_perm_b32 v224, v208, v236, s60\n"
        "v_mfma_f32_16x16x32_bf16 v[8:11], v[128:131], v[176:179], v[8:11]\n"
        "ds_read_b32 v144, v224\n"
        "v_perm_b32 v225, v208, v236, s61\n"
        "v_mfma_f32_16x16x32_bf16 v[8:11], v[132:135], v[180:183], v[8:11]\n"
        "ds_read_b32 v145, v225\n"
        "v_perm_b32 v226, v208, v236, s62\n"
        "v_mfma_f32_16x16x32_bf16 v[12:15], v[140:143], v[180:183], v[12:15]\n"
        "ds_read_b32 v146, v226\n"
        "v_perm_b32 v227, v208, v236, s63\n"
        "v_mfma_f32_16x16x32_bf16 v[12:15], v[136:139], v[176:179], v[12:15]\n"
        "ds_read_b32 v147, v227\n"
        "v_bitop3_b32 v219, v219, v223, s39 bitop3:0x7e\n"
        "s_waitcnt lgkmcnt(8)\n"
        "v_mfma_f32_16x16x32_bf16 v[28:31], v[136:139], v[184:187], v[28:31]\n"
        "ds_read_b128 v[176:179], v230 offset:0\n"
        "v_or3_b32 v210, v210, v211, v218\n"
        "v_mfma_f32_16x16x32_bf16 v[28:31], v[140:143], v[188:191], v[28:31]\n"
        "ds_read_b128 v[180:183], v231 offset:0\n"
        "v_perm_b32 v224, v209, v236, s60\n"
        "v_mfma_f32_16x16x32_bf16 v[24:27], v[132:135], v[188:191], v[24:27]\n"
        "ds_read_b32 v148, v224\n"
        "v_perm_b32 v225, v209, v236, s61\n"
        "v_mfma_f32_16x16x32_bf16 v[24:27], v[128:131], v[184:187], v[24:27]\n"
        "ds_read_b32 v149, v225\n"
        "v_perm_b32 v226, v209, v236, s62\n"
        "v_mfma_f32_16x16x32_bf16 v[20:23], v[120:123], v[184:187], v[20:23]\n"
        "ds_read_b32 v150, v226\n"
        "v_perm_b32 v227, v209, v236, s63\n"
        "v_mfma_f32_16x16x32_bf16 v[20:23], v[124:127], v[188:191], v[20:23]\n"
        "ds_read_b32 v151, v227\n"
        "v_perm_b32 v224, v212, v236, s60\n"
        "v_mfma_f32_16x16x32_bf16 v[16:19], v[116:119], v[188:191], v[16:19]\n"
        "ds_read_b32 v152, v224\n"
        "v_perm_b32 v225, v212, v236, s61\n"
        "v_mfma_f32_16x16x32_bf16 v[16:19], v[112:115], v[184:187], v[16:19]\n"
        "ds_read_b32 v153, v225\n"
        "v_or_b32 v210, v210, v219\n"
        "s_waitcnt lgkmcnt(14)\n"
        "v_mfma_f32_16x16x32_bf16 v[32:35], v[112:115], v[192:195], v[32:35]\n"
        "ds_read_b128 v[184:187], v230 offset:2048\n"
        "v_cmp_ne_u32 vcc, 0, v210\n"
        "v_mfma_f32_16x16x32_bf16 v[32:35], v[116:119], v[196:199], v[32:35]\n"
        "ds_read_b128 v[188:191], v231 offset:2048\n"
        "v_perm_b32 v226, v212, v236, s62\n"
        "v_mfma_f32_16x16x32_bf16 v[36:39], v[124:127], v[196:199], v[36:39]\n"
        "ds_read_b32 v154, v226\n"
        "v_perm_b32 v227, v212, v236, s63\n"
        "v_mfma_f32_16x16x32_bf16 v[36:39], v[120:123], v[192:195], v[36:39]\n"
        "ds_read_b32 v155, v227\n"
        "v_perm_b32 v224, v213, v236, s60\n"
        "v_mfma_f32_16x16x32_bf16 v[40:43], v[128:131], v[192:195], v[40:43]\n"
        "ds_read_b32 v156, v224\n"
        "v_perm_b32 v225, v213, v236, s61\n"
        "v_mfma_f32_16x16x32_bf16 v[40:43], v[132:135], v[196:199], v[40:43]\n"
        "ds_read_b32 v157, v225\n"
        "v_perm_b32 v226, v213, v236, s62\n"
        "v_mfma_f32_16x16x32_bf16 v[44:47], v[140:143], v[196:199], v[44:47]\n"
        "ds_read_b32 v158, v226\n"
        "v_perm_b32 v227, v213, v236, s63\n"
        "v_mfma_f32_16x16x32_bf16 v[44:47], v[136:139], v[192:195], v[44:47]\n"
        "ds_read_b32 v159, v227\n"
        "s_waitcnt lgkmcnt(15)\n"
        "v_mfma_f32_16x16x32_bf16 v[60:63], v[136:139], v[200:203], v[60:63]\n"
        "ds_read_b128 v[192:195], v230 offset:4096\n"
        "s_cbranch_vccnz .Ln_f3\n"
        ".Ln_b3:\n"
        "v_mfma_f32_16x16x32_bf16 v[60:63], v[140:143], v[204:207], v[60:63]\n"
        "ds_read_b128 v[196:199], v231 offset:4096\n"
        "v_perm_b32 v224, v216, v236, s60\n"
        "v_mfma_f32_16x16x32_bf16 v[56:59], v[132:135], v[204:207], v[56:59]\n"
        "ds_read_b32 v160, v224\n"
        "v_perm_b32 v225, v216, v236, s61\n"
        "v_mfma_f32_16x16x32_bf16 v[56:59], v[128:131], v[200:203], v[56:59]\n"
        "ds_read_b32 v161, v225\n"
        "v_perm_b32 v226, v216, v236, s62\n"
        "v_mfma_f32_16x16x32_bf16 v[52:55], v[120:123], v[200:203], v[52:55]\n"
        "ds_read_b32 v162, v226\n"
        "v_perm_b32 v227, v216, v236, s63\n"
        "v_mfma_f32_16x16x32_bf16 v[52:55], v[124:127], v[204:207], v[52:55]\n"
        "ds_read_b32 v163, v227\n"
        "v_perm_b32 v224, v217, v236, s60\n"
        "v_mfma_f32_16x16x32_bf16 v[48:51], v[116:119], v[204:207], v[48:51]\n"
        "ds_read_b32 v164, v224\n"
        "v_perm_b32 v225, v217, v236, s61\n"
        "v_mfma_f32_16x16x32_bf16 v[48:51], v[112:115], v[200:203], v[48:51]\n"
        "ds_read_b32 v165, v225\n"
        "s_waitcnt lgkmcnt(15)\n"
        "v_mfma_f32_16x16x32_bf16 v[64:67], v[112:115], v[176:179], v[64:67]\n"
        "ds_read_b128 v[200:203], v230 offset:6144\n"
        "v_mfma_f32_16x16x32_bf16 v[64:67], v[116:119], v[180:183], v[64:67]\n"
        "ds_read_b128 v[204:207], v231 offset:6144\n"
        "v_perm_b32 v226, v217, v236, s62\n"
        "v_mfma_f32_16x16x32_bf16 v[68:71], v[124:127], v[180:183], v[68:71]\n"
        "ds_read_b32 v166, v226\n"
        "v_perm_b32 v227, v217, v236, s63\n"
        "v_mfma_f32_16x16x32_bf16 v[68:71], v[120:123], v[176:179], v[68:71]\n"
        "ds_read_b32 v167, v227\n"
        "v_perm_b32 v224, v220, v236, s60\n"
        "v_mfma_f32_16x16x32_bf16 v[72:75], v[128:131], v[176:179], v[72:75]\n"
        "ds_read_b32 v168, v224\n"
        "v_perm_b32 v225, v220, v236, s61\n"
        "v_mfma_f32_16x16x32_bf16 v[72:75], v[132:135], v[180:183], v[72:75]\n"
        "ds_read_b32 v169, v225\n"
        "v_perm_b32 v226, v220, v236, s62\n"
        "s_waitcnt lgkmcnt(15)\n"
        "v_mfma_f32_16x16x32_bf16 v[84:87], v[132:135], v[188:191], v[84:87]\n"
        "ds_read_b32 v170, v226\n"
        "v_perm_b32 v227, v220, v236, s63\n"
        "v_mfma_f32_16x16x32_bf16 v[84:87], v[128:131], v[184:187], v[84:87]\n"
        "ds_read_b32 v171, v227\n"
        "v_perm_b32 v224, v221, v236, s60\n"
        "v_mfma_f32_16x16x32_bf16 v[80:83], v[120:123], v[184:187], v[80:83]\n"
        "ds_read_b32 v172, v224\n"
        "v_perm_b32 v225, v221, v236, s61\n"
        "v_mfma_f32_16x16x32_bf16 v[80:83], v[124:127], v[188:191], v[80:83]\n"
        "ds_read_b32 v173, v225\n"
        "v_perm_b32 v226, v221, v236, s62\n"
        "v_mfma_f32_16x16x32_bf16 v[76:79], v[116:119], v[188:191], v[76:79]\n"
        "ds_read_b32 v174, v226\n"
        "v_perm_b32 v227, v221, v236, s63\n"
        "v_mfma_f32_16x16x32_bf16 v[76:79], v[112:115], v[184:187], v[76:79]\n"
        "ds_read_b32 v175, v227\n"
        "s_waitcnt lgkmcnt(10)\n"
        "s_barrier\n"
        "v_mfma_f32_16x16x32_bf16 v[88:91], v[112:115], v[192:195], v[88:91]\n"
        "ds_read_b128 v[208:211], v232 offset:0\n"
        "v_mfma_f32_16x16x32_bf16 v[88:91], v[116:119], v[196:199], v[88:91]\n"
        "ds_read_b128 v[212:215], v233 offset:0\n"
        "v_mfma_f32_16x16x32_bf16 v[92:95], v[124:127], v[196:199], v[92:95]\n"
        "v_mfma_f32_16x16x32_bf16 v[92:95], v[120:123], v[192:195], v[92:95]\n"
        "ds_read_b128 v[216:219], v234 offset:0\n"
        ".Ln_tp0:\n"
        "s_cbranch_execz .Ln_ret\n"
        "v_mfma_f32_16x16x32_bf16 v[96:99], v[128:131], v[192:195], v[96:99]\n"
        "ds_read_b128 v[220:223], v235 offset:0\n"
        "v_mfma_f32_16x16x32_bf16 v[96:99], v[132:135], v[196:199], v[96:99]\n"
        "v_mfma_f32_16x16x32_bf16 v[108:111], v[132:135], v[204:207], v[108:111]\n"
        "ds_read_b128 v[176:179], v228 offset:16384\n"
        "v_mfma_f32_16x16x32_bf16 v[108:111], v[128:131], v[200:203], v[108:111]\n"
        "ds_read_b128 v[180:183], v229 offset:16384\n"
        "v_mfma_f32_16x16x32_bf16 v[104:107], v[120:123], v[200:203], v[104:107]\n"
        "v_mfma_f32_16x16x32_bf16 v[104:107], v[124:127], v[204:207], v[104:107]\n"
        "ds_read_b128 v[184:187], v228 offset:18432\n"
        "v_mfma_f32_16x16x32_bf16 v[100:103], v[116:119], v[204:207], v[100:103]\n"
        "ds_read_b128 v[188:191], v229 offset:18432\n"
        "v_mfma_f32_16x16x32_bf16 v[100:103], v[112:115], v[200:203], v[100:103]\n"
        "s_waitcnt lgkmcnt(2)\n"
        "v_mfma_f32_16x16x32_bf16 v[0:3], v[144:147], v[176:179], v[0:3]\n"
        "ds_read_b128 v[192:195], v228 offset:20480\n"
        "v_bitop3_b32 v210, v210, v214, s34 bitop3:0x7e\n"
        "v_mfma_f32_16x16x32_bf16 v[0:3], v[148:151], v[180:183], v[0:3]\n"
        "ds_read_b128 v[196:199], v229 offset:20480\n"
        "v_bitop3_b32 v211, v211, v215, s39 bitop3:0x7e\n"
        "v_mfma_f32_16x16x32_bf16 v[4:7], v[156:159], v[180:183], v[4:7]\n"
        "ds_read_b128 v[200:203], v228 offset:22528\n"
        "v_bitop3_b32 v218, v218, v222, s34 bitop3:0x7e\n"
        "v_mfma_f32_16x16x32_bf16 v[4:7], v[152:155], v[176:179], v[4:7]\n"
        "ds_read_b128 v[204:207], v229 offset:22528\n"
        "v_perm_b32 v224, v208, v236, s60\n"
        "v_mfma_f32_16x16x32_bf16 v[8:11], v[160:163], v[176:179], v[8:11]\n"
        "ds_read_b32 v112, v224\n"
        "v_perm_b32 v225, v208, v236, s61\n"
        "v_mfma_f32_16x16x32_bf16 v[8:11], v[164:167], v[180:183], v[8:11]\n"
        "ds_read_b32 v113, v225\n"
        "v_perm_b32 v226, v208, v236, s62\n"
        "v_mfma_f32_16x16x32_bf16 v[12:15], v[172:175], v[180:183], v[12:15]\n"
        "ds_read_b32 v114, v226\n"
        "v_perm_b32 v227, v208, v236, s63\n"
        "v_mfma_f32_16x16x32_bf16 v[12:15], v[168:171], v[176:179], v[12:15]\n"
        "ds_read_b32 v115, v227\n"
        "v_bitop3_b32 v219, v219, v223, s39 bitop3:0x7e\n"
        "s_waitcnt lgkmcnt(8)\n"
        "v_mfma_f32_16x16x32_bf16 v[28:31], v[168:171], v[184:187], v[28:31]\n"
        "ds_read_b128 v[176:179], v230 offset:16384\n"
        "v_or3_b32 v210, v210, v211, v218\n"
        "v_mfma_f32_16x16x32_bf16 v[28:31], v[172:175], v[188:191], v[28:31]\n"
        "ds_read_b128 v[180:183], v231 offset:16384\n"
        "v_perm_b32 v224, v209, v236, s60\n"
        "v_mfma_f32_16x16x32_bf16 v[24:27], v[164:167], v[188:191], v[24:27]\n"
        "ds_read_b32 v116, v224\n"
        "v_perm_b32 v225, v209, v236, s61\n"
        "v_mfma_f32_16x16x32_bf16 v[24:27], v[160:163], v[184:187], v[24:27]\n"
        "ds_read_b32 v117, v225\n"
        "v_perm_b32 v226, v209, v236, s62\n"
        "v_mfma_f32_16x16x32_bf16 v[20:23], v[152:155], v[184:187], v[20:23]\n"
        "ds_read_b32 v118, v226\n"
        "v_perm_b32 v227, v209, v236, s63\n"
        "v_mfma_f32_16x16x32_bf16 v[20:23], v[156:159], v[188:191], v[20:23]\n"
        "ds_read_b32 v119, v227\n"
        "v_perm_b32 v224, v212, v236, s60\n"
        "v_mfma_f32_16x16x32_bf16 v[16:19], v[148:151], v[188:191], v[16:19]\n"
        "ds_read_b32 v120, v224\n"
        "v_perm_b32 v225, v212, v236, s61\n"
        "v_mfma_f32_16x16x32_bf16 v[16:19], v[144:147], v[184:187], v[16:19]\n"
        "ds_read_b32 v121, v225\n"
        "v_or_b32 v210, v210, v219\n"
        "s_waitcnt lgkmcnt(14)\n"
        "v_mfma_f32_16x16x32_bf16 v[32:35], v[144:147], v[192:195], v[32:35]\n"
        "ds_read_b128 v[184:187], v230 offset:18432\n"
        "v_cmp_ne_u32 vcc, 0, v210\n"
        "v_mfma_f32_16x16x32_bf16 v[32:35], v[148:151], v[196:199], v[32:35]\n"
        "ds_read_b128 v[188:191], v231 offset:18432\n"
        "v_perm_b32 v226, v212, v236, s62\n"
        "v_mfma_f32_16x16x32_bf16 v[36:39], v[156:159], v[196:199], v[36:39]\n"
        "ds_read_b32 v122, v226\n"
        "v_perm_b32 v227, v212, v236, s63\n"
        "v_mfma_f32_16x16x32_bf16 v[36:39], v[152:155], v[192:195], v[36:39]\n"
        "ds_read_b32 v123, v227\n"
        "v_perm_b32 v224, v213, v236, s60\n"
        "v_mfma_f32_16x16x32_bf16 v[40:43], v[160:163], v[192:195], v[40:43]\n"
        "ds_read_b32 v124, v224\n"
        "v_perm_b32 v225, v213, v236, s61\n"
        "v_mfma_f32_16x16x32_bf16 v[40:43], v[164:167], v[196:199], v[40:43]\n"
        "ds_read_b32 v125, v225\n"
        "v_perm_b32 v226, v213, v236, s62\n"
        "v_mfma_f32_16x16x32_bf16 v[44:47], v[172:175], v[196:199], v[44:47]\n"
        "ds_read_b32 v126, v226\n"
        "v_perm_b32 v227, v213, v236, s63\n"
        "v_mfma_f32_16x16x32_bf16 v[44:47], v[168:171], v[192:195], v[44:47]\n"
        "ds_read_b32 v127, v227\n"
        "s_waitcnt lgkmcnt(15)\n"
        "v_mfma_f32_16x16x32_bf16 v[60:63], v[168:171], v[200:203], v[60:63]\n"
        "ds_read_b128 v[192:195], v230 offset:20480\n"
        "s_cbranch_vccnz .Ln_f4\n"
        ".Ln_b4:\n"
        "v_mfma_f32_16x16x32_bf16 v[60:63], v[172:175], v[204:207], v[60:63]\n"
        "ds_read_b128 v[196:199], v231 offset:20480\n"
        "v_perm_b32 v224, v216, v236, s60\n"
        "v_mfma_f32_16x16x32_bf16 v[56:59], v[164:167], v[204:207], v[56:59]\n"
        "ds_read_b32 v128, v224\n"
        "v_perm_b32 v225, v216, v236, s61\n"
        "v_mfma_f32_16x16x32_bf16 v[56:59], v[160:163], v[200:203], v[56:59]\n"
        "ds_read_b32 v129, v225\n"
        "v_perm_b32 v226, v216, v236, s62\n"
        "v_mfma_f32_16x16x32_bf16 v[52:55], v[152:155], v[200:203], v[52:55]\n"
        "ds_read_b32 v130, v226\n"
        "v_perm_b32 v227, v216, v236, s63\n"
        "v_mfma_f32_16x16x32_bf16 v[52:55], v[156:159], v[204:207], v[52:55]\n"
        "ds_read_b32 v131, v227\n"
        "v_perm_b32 v224, v217, v236, s60\n"
        "v_mfma_f32_16x16x32_bf16 v[48:51], v[148:151], v[204:207], v[48:51]\n"
        "ds_read_b32 v132, v224\n"
        "v_perm_b32 v225, v217, v236, s61\n"
        "v_mfma_f32_16x16x32_bf16 v[48:51], v[144:147], v[200:203], v[48:51]\n"
        "ds_read_b32 v133, v225\n"
        "s_waitcnt lgkmcnt(15)\n"
        "v_mfma_f32_16x16x32_bf16 v[64:67], v[144:147], v[176:179], v[64:67]\n"
        "ds_read_b128 v[200:203], v230 offset:22528\n"
        "v_mfma_f32_16x16x32_bf16 v[64:67], v[148:151], v[180:183], v[64:67]\n"
        "ds_read_b128 v[204:207], v231 offset:22528\n"
        "v_perm_b32 v226, v217, v236, s62\n"
        "v_mfma_f32_16x16x32_bf16 v[68:71], v[156:159], v[180:183], v[68:71]\n"
        "ds_read_b32 v134, v226\n"
        "v_perm_b32 v227, v217, v236, s63\n"
        "v_mfma_f32_16x16x32_bf16 v[68:71], v[152:155], v[176:179], v[68:71]\n"
        "ds_read_b32 v135, v227\n"
        "v_perm_b32 v224, v220, v236, s60\n"
        "v_mfma_f32_16x16x32_bf16 v[72:75], v[160:163], v[176:179], v[72:75]\n"
        "ds_read_b32 v136, v224\n"
        "v_perm_b32 v225, v220, v236, s61\n"
        "v_mfma_f32_16x16x32_bf16 v[72:75], v[164:167], v[180:183], v[72:75]\n"
        "ds_read_b32 v137, v225\n"
        "v_perm_b32 v226, v220, v236, s62\n"
        "s_waitcnt lgkmcnt(15)\n"
        "v_mfma_f32_16x16x32_bf16 v[84:87], v[164:167], v[188:191], v[84:87]\n"
        "ds_read_b32 v138, v226\n"
        "v_perm_b32 v227, v220, v236, s63\n"
        "v_mfma_f32_16x16x32_bf16 v[84:87], v[160:163], v[184:187], v[84:87]\n"
        "ds_read_b32 v139, v227\n"
        "v_perm_b32 v224, v221, v236, s60\n"
        "v_mfma_f32_16x16x32_bf16 v[80:83], v[152:155], v[184:187], v[80:83]\n"
        "ds_read_b32 v140, v224\n"
        "v_perm_b32 v225, v221, v236, s61\n"
        "v_mfma_f32_16x16x32_bf16 v[80:83], v[156:159], v[188:191], v[80:83]\n"
        "ds_read_b32 v141, v225\n"
        "v_perm_b32 v226, v221, v236, s62\n"
        "v_mfma_f32_16x16x32_bf16 v[76:79], v[148:151], v[188:191], v[76:79]\n"
        "ds_read_b32 v142, v226\n"
        "v_perm_b32 v227, v221, v236, s63\n"
        "v_mfma_f32_16x16x32_bf16 v[76:79], v[144:147], v[184:187], v[76:79]\n"
        "ds_read_b32 v143, v227\n"
        "s_waitcnt lgkmcnt(10)\n"
        "s_barrier\n"
        "v_mfma_f32_16x16x32_bf16 v[88:91], v[144:147], v[192:195], v[88:91]\n"
        "ds_read_b128 v[208:211], v232 offset:16384\n"
        "v_mfma_f32_16x16x32_bf16 v[88:91], v[148:151], v[196:199], v[88:91]\n"
        "ds_read_b128 v[212:215], v233 offset:16384\n"
        "v_mfma_f32_16x16x32_bf16 v[92:95], v[156:159], v[196:199], v[92:95]\n"
        "v_mfma_f32_16x16x32_bf16 v[92:95], v[152:155], v[192:195], v[92:95]\n"
        "ds_read_b128 v[216:219], v234 offset:16384\n"
        "v_mfma_f32_16x16x32_bf16 v[96:99], v[160:163], v[192:195], v[96:99]\n"
        "ds_read_b128 v[220:223], v235 offset:16384\n"
        "v_mfma_f32_16x16x32_bf16 v[96:99], v[164:167], v[196:199], v[96:99]\n"
        "v_mfma_f32_16x16x32_bf16 v[108:111], v[164:167], v[204:207], v[108:111]\n"
        "ds_read_b128 v[176:179], v228 offset:32768\n"
        "v_mfma_f32_16x16x32_bf16 v[108:111], v[160:163], v[200:203], v[108:111]\n"
        "ds_read_b128 v[180:183], v229 offset:32768\n"
        "v_mfma_f32_16x16x32_bf16 v[104:107], v[152:155], v[200:203], v[104:107]\n"
        "v_mfma_f32_16x16x32_bf16 v[104:107], v[156:159], v[204:207], v[104:107]\n"
        "ds_read_b128 v[184:187], v228 offset:34816\n"
        "v_mfma_f32_16x16x32_bf16 v[100:103], v[148:151], v[204:207], v[100:103]\n"
        "ds_read_b128 v[188:191], v229 offset:34816\n"
        "v_mfma_f32_16x16x32_bf16 v[100:103], v[144:147], v[200:203], v[100:103]\n"
        "s_waitcnt lgkmcnt(2)\n"
        "v_mfma_f32_16x16x32_bf16 v[0:3], v[112:115], v[176:179], v[0:3]\n"
        "ds_read_b128 v[192:195], v228 offset:36864\n"
        "v_bitop3_b32 v210, v210, v214, s34 bitop3:0x7e\n"
        "v_mfma_f32_16x16x32_bf16 v[0:3], v[116:119], v[180:183], v[0:3]\n"
        "ds_read_b128 v[196:199], v229 offset:36864\n"
        "v_bitop3_b32 v211, v211, v215, s39 bitop3:0x7e\n"
        "v_mfma_f32_16x16x32_bf16 v[4:7], v[124:127], v[180:183], v[4:7]\n"
        "ds_read_b128 v[200:203], v228 offset:38912\n"
        "v_bitop3_b32 v218, v218, v222, s34 bitop3:0x7e\n"
        "v_mfma_f32_16x16x32_bf16 v[4:7], v[120:123], v[176:179], v[4:7]\n"
        "ds_read_b128 v[204:207], v229 offset:38912\n"
        "v_perm_b32 v224, v208, v236, s60\n"
        "v_mfma_f32_16x16x32_bf16 v[8:11], v[128:131], v[176:179], v[8:11]\n"
        "ds_read_b32 v144, v224\n"
        "v_perm_b32 v225, v208, v236, s61\n"
        "v_mfma_f32_16x16x32_bf16 v[8:11], v[132:135], v[180:183], v[8:11]\n"
        "ds_read_b32 v145, v225\n"
        "v_perm_b32 v226, v208, v236, s62\n"
        "v_mfma_f32_16x16x32_bf16 v[12:15], v[140:143], v[180:183], v[12:15]\n"
        "ds_read_b32 v146, v226\n"
        "v_perm_b32 v227, v208, v236, s63\n"
        "v_mfma_f32_16x16x32_bf16 v[12:15], v[136:139], v[176:179], v[12:15]\n"
        "ds_read_b32 v147, v227\n"
        "v_bitop3_b32 v219, v219, v223, s39 bitop3:0x7e\n"
        "s_waitcnt lgkmcnt(8)\n"
        "v_mfma_f32_16x16x32_bf16 v[28:31], v[136:139], v[184:187], v[28:31]\n"
        "ds_read_b128 v[176:179], v230 offset:32768\n"
        "v_or3_b32 v210, v210, v211, v218\n"
        "v_mfma_f32_16x16x32_bf16 v[28:31], v[140:143], v[188:191], v[28:31]\n"
        "ds_read_b128 v[180:183], v231 offset:32768\n"
        "v_perm_b32 v224, v209, v236, s60\n"
        "v_mfma_f32_16x16x32_bf16 v[24:27], v[132:135], v[188:191], v[24:27]\n"
        "ds_read_b32 v148, v224\n"
        "v_perm_b32 v225, v209, v236, s61\n"
        "v_mfma_f32_16x16x32_bf16 v[24:27], v[128:131], v[184:187], v[24:27]\n"
        "ds_read_b32 v149, v225\n"
        "v_perm_b32 v226, v209, v236, s62\n"
        "v_mfma_f32_16x16x32_bf16 v[20:23], v[120:123], v[184:187], v[20:23]\n"
        "ds_read_b32 v150, v226\n"
        "v_perm_b32 v227, v209, v236, s63\n"
        "v_mfma_f32_16x16x32_bf16 v[20:23], v[124:127], v[188:191], v[20:23]\n"
        "ds_read_b32 v151, v227\n"
        "v_perm_b32 v224, v212, v236, s60\n"
        "v_mfma_f32_16x16x32_bf16 v[16:19], v[116:119], v[188:191], v[16:19]\n"
        "ds_read_b32 v152, v224\n"
        "v_perm_b32 v225, v212, v236, s61\n"
        "v_mfma_f32_16x16x32_bf16 v[16:19], v[112:115], v[184:187], v[16:19]\n"
        "ds_read_b32 v153, v225\n"
        "v_or_b32 v210, v210, v219\n"
        "s_waitcnt lgkmcnt(14)\n"
        "v_mfma_f32_16x16x32_bf16 v[32:35], v[112:115], v[192:195], v[32:35]\n"
        "ds_read_b128 v[184:187], v230 offset:34816\n"
        "v_cmp_ne_u32 vcc, 0, v210\n"
        "v_mfma_f32_16x16x32_bf16 v[32:35], v[116:119], v[196:199], v[32:35]\n"
        "ds_read_b128 v[188:191], v231 offset:34816\n"
        "v_perm_b32 v226, v212, v236, s62\n"
        "v_mfma_f32_16x16x32_bf16 v[36:39], v[124:127], v[196:199], v[36:39]\n"
        "ds_read_b32 v154, v226\n"
        "v_perm_b32 v227, v212, v236, s63\n"
        "v_mfma_f32_16x16x32_bf16 v[36:39], v[120:123], v[192:195], v[36:39]\n"
        "ds_read_b32 v155, v227\n"
        "v_perm_b32 v224, v213, v236, s60\n"
        "v_mfma_f32_16x16x32_bf16 v[40:43], v[128:131], v[192:195], v[40:43]\n"
        "ds_read_b32 v156, v224\n"
        "v_perm_b32 v225, v213, v236, s61\n"
        "v_mfma_f32_16x16x32_bf16 v[40:43], v[132:135], v[196:199], v[40:43]\n"
        "ds_read_b32 v157, v225\n"
        "v_perm_b32 v226, v213, v236, s62\n"
        "v_mfma_f32_16x16x32_bf16 v[44:47], v[140:143], v[196:199], v[44:47]\n"
        "ds_read_b32 v158, v226\n"
        "v_perm_b32 v227, v213, v236, s63\n"
        "v_mfma_f32_16x16x32_bf16 v[44:47], v[136:139], v[192:195], v[44:47]\n"
        "ds_read_b32 v159, v227\n"
        "s_waitcnt lgkmcnt(15)\n"
        "v_mfma_f32_16x16x32_bf16 v[60:63], v[136:139], v[200:203], v[60:63]\n"
        "ds_read_b128 v[192:195], v230 offset:36864\n"
        "s_cbranch_vccnz .Ln_f5\n"
        ".Ln_b5:\n"
        "v_mfma_f32_16x16x32_bf16 v[60:63], v[140:143], v[204:207], v[60:63]\n"
        "ds_read_b128 v[196:199], v231 offset:36864\n"
        "v_perm_b32 v224, v216, v236, s60\n"
        "v_mfma_f32_16x16x32_bf16 v[56:59], v[132:135], v[204:207], v[56:59]\n"
        "ds_read_b32 v160, v224\n"
        "v_perm_b32 v225, v216, v236, s61\n"
        "v_mfma_f32_16x16x32_bf16 v[56:59], v[128:131], v[200:203], v[56:59]\n"
        "ds_read_b32 v161, v225\n"
        "v_perm_b32 v226, v216, v236, s62\n"
        "v_mfma_f32_16x16x32_bf16 v[52:55], v[120:123], v[200:203], v[52:55]\n"
        "ds_read_b32 v162, v226\n"
        "v_perm_b32 v227, v216, v236, s63\n"
        "v_mfma_f32_16x16x32_bf16 v[52:55], v[124:127], v[204:207], v[52:55]\n"
        "ds_read_b32 v163, v227\n"
        "v_perm_b32 v224, v217, v236, s60\n"
        "v_mfma_f32_16x16x32_bf16 v[48:51], v[116:119], v[204:207], v[48:51]\n"
        "ds_read_b32 v164, v224\n"
        "v_perm_b32 v225, v217, v236, s61\n"
        "v_mfma_f32_16x16x32_bf16 v[48:51], v[112:115], v[200:203], v[48:51]\n"
        "ds_read_b32 v165, v225\n"
        "s_waitcnt lgkmcnt(15)\n"
        "v_mfma_f32_16x16x32_bf16 v[64:67], v[112:115], v[176:179], v[64:67]\n"
        "ds_read_b128 v[200:203], v230 offset:38912\n"
        "v_mfma_f32_16x16x32_bf16 v[64:67], v[116:119], v[180:183], v[64:67]\n"
        "ds_read_b128 v[204:207], v231 offset:38912\n"
        "v_perm_b32 v226, v217, v236, s62\n"
        "v_mfma_f32_16x16x32_bf16 v[68:71], v[124:127], v[180:183], v[68:71]\n"
        "ds_read_b32 v166, v226\n"
        "v_perm_b32 v227, v217, v236, s63\n"
        "v_mfma_f32_16x16x32_bf16 v[68:71], v[120:123], v[176:179], v[68:71]\n"
        "ds_read_b32 v167, v227\n"
        "v_perm_b32 v224, v220, v236, s60\n"
        "v_mfma_f32_16x16x32_bf16 v[72:75], v[128:131], v[176:179], v[72:75]\n"
        "ds_read_b32 v168, v224\n"
        "v_perm_b32 v225, v220, v236, s61\n"
        "v_mfma_f32_16x16x32_bf16 v[72:75], v[132:135], v[180:183], v[72:75]\n"
        "ds_read_b32 v169, v225\n"
        "v_perm_b32 v226, v220, v236, s62\n"
        "s_waitcnt lgkmcnt(15)\n"
        "v_mfma_f32_16x16x32_bf16 v[84:87], v[132:135], v[188:191], v[84:87]\n"
        "ds_read_b32 v170, v226\n"
        "v_perm_b32 v227, v220, v236, s63\n"
        "v_mfma_f32_16x16x32_bf16 v[84:87], v[128:131], v[184:187], v[84:87]\n"
        "ds_read_b32 v171, v227\n"
        "v_perm_b32 v224, v221, v236, s60\n"
        "v_mfma_f32_16x16x32_bf16 v[80:83], v[120:123], v[184:187], v[80:83]\n"
        "ds_read_b32 v172, v224\n"
        "v_perm_b32 v225, v221, v236, s61\n"
        "v_mfma_f32_16x16x32_bf16 v[80:83], v[124:127], v[188:191], v[80:83]\n"
        "ds_read_b32 v173, v225\n"
        "v_perm_b32 v226, v221, v236, s62\n"
        "v_mfma_f32_16x16x32_bf16 v[76:79], v[116:119], v[188:191], v[76:79]\n"
        "ds_read_b32 v174, v226\n"
        "v_perm_b32 v227, v221, v236, s63\n"
        "v_mfma_f32_16x16x32_bf16 v[76:79], v[112:115], v[184:187], v[76:79]\n"
        "ds_read_b32 v175, v227\n"
        "s_waitcnt lgkmcnt(10)\n"
        "s_barrier\n"
        "v_mfma_f32_16x16x32_bf16 v[88:91], v[112:115], v[192:195], v[88:91]\n"
        "ds_read_b128 v[208:211], v232 offset:32768\n"
        "v_mfma_f32_16x16x32_bf16 v[88:91], v[116:119], v[196:199], v[88:91]\n"
        "ds_read_b128 v[212:215], v233 offset:32768\n"
        "v_mfma_f32_16x16x32_bf16 v[92:95], v[124:127], v[196:199], v[92:95]\n"
        "v_mfma_f32_16x16x32_bf16 v[92:95], v[120:123], v[192:195], v[92:95]\n"
        "ds_read_b128 v[216:219], v234 offset:32768\n"
        "v_mfma_f32_16x16x32_bf16 v[96:99], v[128:131], v[192:195], v[96:99]\n"
        "ds_read_b128 v[220:223], v235 offset:32768\n"
        "v_mfma_f32_16x16x32_bf16 v[96:99], v[132:135], v[196:199], v[96:99]\n"
        "v_mfma_f32_16x16x32_bf16 v[108:111], v[132:135], v[204:207], v[108:111]\n"
        "ds_read_b128 v[176:179], v228 offset:0\n"
        "v_mfma_f32_16x16x32_bf16 v[108:111], v[128:131], v[200:203], v[108:111]\n"
        "ds_read_b128 v[180:183], v229 offset:0\n"
        "v_mfma_f32_16x16x32_bf16 v[104:107], v[120:123], v[200:203], v[104:107]\n"
        "v_mfma_f32_16x16x32_bf16 v[104:107], v[124:127], v[204:207], v[104:107]\n"
        "ds_read_b128 v[184:187], v228 offset:2048\n"
        "v_mfma_f32_16x16x32_bf16 v[100:103], v[116:119], v[204:207], v[100:103]\n"
        "ds_read_b128 v[188:191], v229 offset:2048\n"
        "v_mfma_f32_16x16x32_bf16 v[100:103], v[112:115], v[200:203], v[100:103]\n"
        "s_waitcnt lgkmcnt(2)\n"
        "v_mfma_f32_16x16x32_bf16 v[0:3], v[144:147], v[176:179], v[0:3]\n"
        "ds_read_b128 v[192:195], v228 offset:4096\n"
        "v_bitop3_b32 v210, v210, v214, s34 bitop3:0x7e\n"
        "v_mfma_f32_16x16x32_bf16 v[0:3], v[148:151], v[180:183], v[0:3]\n"
        "ds_read_b128 v[196:199], v229 offset:4096\n"
        "v_bitop3_b32 v211, v211, v215, s39 bitop3:0x7e\n"
        "v_mfma_f32_16x16x32_bf16 v[4:7], v[156:159], v[180:183], v[4:7]\n"
        "ds_read_b128 v[200:203], v228 offset:6144\n"
        "v_bitop3_b32 v218, v218, v222, s34 bitop3:0x7e\n"
        "v_mfma_f32_16x16x32_bf16 v[4:7], v[152:155], v[176:179], v[4:7]\n"
        "ds_read_b128 v[204:207], v229 offset:6144\n"
        "v_perm_b32 v224, v208, v236, s60\n"
        "v_mfma_f32_16x16x32_bf16 v[8:11], v[160:163], v[176:179], v[8:11]\n"
        "ds_read_b32 v112, v224\n"
        "v_perm_b32 v225, v208, v236, s61\n"
        "v_mfma_f32_16x16x32_bf16 v[8:11], v[164:167], v[180:183], v[8:11]\n"
        "ds_read_b32 v113, v225\n"
        "v_perm_b32 v226, v208, v236, s62\n"
        "v_mfma_f32_16x16x32_bf16 v[12:15], v[172:175], v[180:183], v[12:15]\n"
        "ds_read_b32 v114, v226\n"
        "v_perm_b32 v227, v208, v236, s63\n"
        "v_mfma_f32_16x16x32_bf16 v[12:15], v[168:171], v[176:179], v[12:15]\n"
        "ds_read_b32 v115, v227\n"
        "v_bitop3_b32 v219, v219, v223, s39 bitop3:0x7e\n"
        "s_waitcnt lgkmcnt(8)\n"
        "v_mfma_f32_16x16x32_bf16 v[28:31], v[168:171], v[184:187], v[28:31]\n"
        "ds_read_b128 v[176:179], v230 offset:0\n"
        "v_or3_b32 v210, v210, v211, v218\n"
        "v_mfma_f32_16x16x32_bf16 v[28:31], v[172:175], v[188:191], v[28:31]\n"
        "ds_read_b128 v[180:183], v231 offset:0\n"
        "v_perm_b32 v224, v209, v236, s60\n"
        "v_mfma_f32_16x16x32_bf16 v[24:27], v[164:167], v[188:191], v[24:27]\n"
        "ds_read_b32 v116, v224\n"
        "v_perm_b32 v225, v209, v236, s61\n"
        "v_mfma_f32_16x16x32_bf16 v[24:27], v[160:163], v[184:187], v[24:27]\n"
        "ds_read_b32 v117, v225\n"
        "v_perm_b32 v226, v209, v236, s62\n"
        "v_mfma_f32_16x16x32_bf16 v[20:23], v[152:155], v[184:187], v[20:23]\n"
        "ds_read_b32 v118, v226\n"
        "v_perm_b32 v227, v209, v236, s63\n"
        "v_mfma_f32_16x16x32_bf16 v[20:23], v[156:159], v[188:191], v[20:23]\n"
        "ds_read_b32 v119, v227\n"
        "v_perm_b32 v224, v212, v236, s60\n"
        "v_mfma_f32_16x16x32_bf16 v[16:19], v[148:151], v[188:191], v[16:19]\n"
        "ds_read_b32 v120, v224\n"
        "v_perm_b32 v225, v212, v236, s61\n"
        "v_mfma_f32_16x16x32_bf16 v[16:19], v[144:147], v[184:187], v[16:19]\n"
        "ds_read_b32 v121, v225\n"
        "v_or_b32 v210, v210, v219\n"
        "s_waitcnt lgkmcnt(14)\n"
        "v_mfma_f32_16x16x32_bf16 v[32:35], v[144:147], v[192:195], v[32:35]\n"
        "ds_read_b128 v[184:187], v230 offset:2048\n"
        "v_cmp_ne_u32 vcc, 0, v210\n"
        "v_mfma_f32_16x16x32_bf16 v[32:35], v[148:151], v[196:199], v[32:35]\n"
        "ds_read_b128 v[188:191], v231 offset:2048\n"
        "v_perm_b32 v226, v212, v236, s62\n"
        "v_mfma_f32_16x16x32_bf16 v[36:39], v[156:159], v[196:199], v[36:39]\n"
        "ds_read_b32 v122, v226\n"
        "v_perm_b32 v227, v212, v236, s63\n"
        "v_mfma_f32_16x16x32_bf16 v[36:39], v[152:155], v[192:195], v[36:39]\n"
        "ds_read_b32 v123, v227\n"
        "v_perm_b32 v224, v213, v236, s60\n"
        "v_mfma_f32_16x16x32_bf16 v[40:43], v[160:163], v[192:195], v[40:43]\n"
        "ds_read_b32 v124, v224\n"
        "v_perm_b32 v225, v213, v236, s61\n"
        "v_mfma_f32_16x16x32_bf16 v[40:43], v[164:167], v[196:199], v[40:43]\n"
        "ds_read_b32 v125, v225\n"
        "v_perm_b32 v226, v213, v236, s62\n"
        "v_mfma_f32_16x16x32_bf16 v[44:47], v[172:175], v[196:199], v[44:47]\n"
        "ds_read_b32 v126, v226\n"
        "v_perm_b32 v227, v213, v236, s63\n"
        "v_mfma_f32_16x16x32_bf16 v[44:47], v[168:171], v[192:195], v[44:47]\n"
        "ds_read_b32 v127, v227\n"
        "s_waitcnt lgkmcnt(15)\n"
        "v_mfma_f32_16x16x32_bf16 v[60:63], v[168:171], v[200:203], v[60:63]\n"
        "ds_read_b128 v[192:195], v230 offset:4096\n"
        "s_cbranch_vccnz .Ln_f6\n"
        ".Ln_b6:\n"
        "v_mfma_f32_16x16x32_bf16 v[60:63], v[172:175], v[204:207], v[60:63]\n"
        "ds_read_b128 v[196:199], v231 offset:4096\n"
        "v_perm_b32 v224, v216, v236, s60\n"
        "v_mfma_f32_16x16x32_bf16 v[56:59], v[164:167], v[204:207], v[56:59]\n"
        "ds_read_b32 v128, v224\n"
        "v_perm_b32 v225, v216, v236, s61\n"
        "v_mfma_f32_16x16x32_bf16 v[56:59], v[160:163], v[200:203], v[56:59]\n"
        "ds_read_b32 v129, v225\n"
        "v_perm_b32 v226, v216, v236, s62\n"
        "v_mfma_f32_16x16x32_bf16 v[52:55], v[152:155], v[200:203], v[52:55]\n"
        "ds_read_b32 v130, v226\n"
        "v_perm_b32 v227, v216, v236, s63\n"
        "v_mfma_f32_16x16x32_bf16 v[52:55], v[156:159], v[204:207], v[52:55]\n"
        "ds_read_b32 v131, v227\n"
        "v_perm_b32 v224, v217, v236, s60\n"
        "v_mfma_f32_16x16x32_bf16 v[48:51], v[148:151], v[204:207], v[48:51]\n"
        "ds_read_b32 v132, v224\n"
        "v_perm_b32 v225, v217, v236, s61\n"
        "v_mfma_f32_16x16x32_bf16 v[48:51], v[144:147], v[200:203], v[48:51]\n"
        "ds_read_b32 v133, v225\n"
        "s_waitcnt lgkmcnt(15)\n"
        "v_mfma_f32_16x16x32_bf16 v[64:67], v[144:147], v[176:179], v[64:67]\n"
        "ds_read_b128 v[200:203], v230 offset:6144\n"
        "v_mfma_f32_16x16x32_bf16 v[64:67], v[148:151], v[180:183], v[64:67]\n"
        "ds_read_b128 v[204:207], v231 offset:6144\n"
        "v_perm_b32 v226, v217, v236, s62\n"
        "v_mfma_f32_16x16x32_bf16 v[68:71], v[156:159], v[180:183], v[68:71]\n"
        "ds_read_b32 v134, v226\n"
        "v_perm_b32 v227, v217, v236, s63\n"
        "v_mfma_f32_16x16x32_bf16 v[68:71], v[152:155], v[176:179], v[68:71]\n"
        "ds_read_b32 v135, v227\n"
        "v_perm_b32 v224, v220, v236, s60\n"
        "v_mfma_f32_16x16x32_bf16 v[72:75], v[160:163], v[176:179], v[72:75]\n"
        "ds_read_b32 v136, v224\n"
        "v_perm_b32 v225, v220, v236, s61\n"
        "v_mfma_f32_16x16x32_bf16 v[72:75], v[164:167], v[180:183], v[72:75]\n"
        "ds_read_b32 v137, v225\n"
        "v_perm_b32 v226, v220, v236, s62\n"
        "s_waitcnt lgkmcnt(15)\n"
        "v_mfma_f32_16x16x32_bf16 v[84:87], v[164:167], v[188:191], v[84:87]\n"
        "ds_read_b32 v138, v226\n"
        "v_perm_b32 v227, v220, v236, s63\n"
        "v_mfma_f32_16x16x32_bf16 v[84:87], v[160:163], v[184:187], v[84:87]\n"
        "ds_read_b32 v139, v227\n"
        "v_perm_b32 v224, v221, v236, s60\n"
        "v_mfma_f32_16x16x32_bf16 v[80:83], v[152:155], v[184:187], v[80:83]\n"
        "ds_read_b32 v140, v224\n"
        "v_perm_b32 v225, v221, v236, s61\n"
        "v_mfma_f32_16x16x32_bf16 v[80:83], v[156:159], v[188:191], v[80:83]\n"
        "ds_read_b32 v141, v225\n"
        "v_perm_b32 v226, v221, v236, s62\n"
        "v_mfma_f32_16x16x32_bf16 v[76:79], v[148:151], v[188:191], v[76:79]\n"
        "ds_read_b32 v142, v226\n"
        "v_perm_b32 v227, v221, v236, s63\n"
        "v_mfma_f32_16x16x32_bf16 v[76:79], v[144:147], v[184:187], v[76:79]\n"
        "ds_read_b32 v143, v227\n"
        "s_waitcnt lgkmcnt(10)\n"
        "s_barrier\n"
        "v_mfma_f32_16x16x32_bf16 v[88:91], v[144:147], v[192:195], v[88:91]\n"
        "ds_read_b128 v[208:211], v232 offset:0\n"
        "v_mfma_f32_16x16x32_bf16 v[88:91], v[148:151], v[196:199], v[88:91]\n"
        "ds_read_b128 v[212:215], v233 offset:0\n"
        "v_mfma_f32_16x16x32_bf16 v[92:95], v[156:159], v[196:199], v[92:95]\n"
        "v_mfma_f32_16x16x32_bf16 v[92:95], v[152:155], v[192:195], v[92:95]\n"
        "ds_read_b128 v[216:219], v234 offset:0\n"
        "v_mfma_f32_16x16x32_bf16 v[96:99], v[160:163], v[192:195], v[96:99]\n"
        "ds_read_b128 v[220:223], v235 offset:0\n"
        "v_mfma_f32_16x16x32_bf16 v[96:99], v[164:167], v[196:199], v[96:99]\n"
        "v_mfma_f32_16x16x32_bf16 v[108:111], v[164:167], v[204:207], v[108:111]\n"
        "ds_read_b128 v[176:179], v228 offset:16384\n"
        "v_mfma_f32_16x16x32_bf16 v[108:111], v[160:163], v[200:203], v[108:111]\n"
        "ds_read_b128 v[180:183], v229 offset:16384\n"
        "v_mfma_f32_16x16x32_bf16 v[104:107], v[152:155], v[200:203], v[104:107]\n"
        "v_mfma_f32_16x16x32_bf16 v[104:107], v[156:159], v[204:207], v[104:107]\n"
        "ds_read_b128 v[184:187], v228 offset:18432\n"
        "v_mfma_f32_16x16x32_bf16 v[100:103], v[148:151], v[204:207], v[100:103]\n"
        "ds_read_b128 v[188:191], v229 offset:18432\n"
        "v_mfma_f32_16x16x32_bf16 v[100:103], v[144:147], v[200:203], v[100:103]\n"
        "s_waitcnt lgkmcnt(2)\n"
        "v_mfma_f32_16x16x32_bf16 v[0:3], v[112:115], v[176:179], v[0:3]\n"
        "ds_read_b128 v[192:195], v228 offset:20480\n"
        "v_bitop3_b32 v210, v210, v214, s34 bitop3:0x7e\n"
        "v_mfma_f32_16x16x32_bf16 v[0:3], v[116:119], v[180:183], v[0:3]\n"
        "ds_read_b128 v[196:199], v229 offset:20480\n"
        "v_bitop3_b32 v211, v211, v215, s39 bitop3:0x7e\n"
        "v_mfma_f32_16x16x32_bf16 v[4:7], v[124:127], v[180:183], v[4:7]\n"
        "ds_read_b128 v[200:203], v228 offset:22528\n"
        "v_bitop3_b32 v218, v218, v222, s34 bitop3:0x7e\n"
        "v_mfma_f32_16x16x32_bf16 v[4:7], v[120:123], v[176:179], v[4:7]\n"
        "ds_read_b128 v[204:207], v229 offset:22528\n"
        "v_perm_b32 v224, v208, v236, s60\n"
        "v_mfma_f32_16x16x32_bf16 v[8:11], v[128:131], v[176:179], v[8:11]\n"
        "ds_read_b32 v144, v224\n"
        "v_perm_b32 v225, v208, v236, s61\n"
        "v_mfma_f32_16x16x32_bf16 v[8:11], v[132:135], v[180:183], v[8:11]\n"
        "ds_read_b32 v145, v225\n"
        "v_perm_b32 v226, v208, v236, s62\n"
        "v_mfma_f32_16x16x32_bf16 v[12:15], v[140:143], v[180:183], v[12:15]\n"
        "ds_read_b32 v146, v226\n"
        "v_perm_b32 v227, v208, v236, s63\n"
        "v_mfma_f32_16x16x32_bf16 v[12:15], v[136:139], v[176:179], v[12:15]\n"
        "ds_read_b32 v147, v227\n"
        "v_bitop3_b32 v219, v219, v223, s39 bitop3:0x7e\n"
        "s_waitcnt lgkmcnt(8)\n"
        "v_mfma_f32_16x16x32_bf16 v[28:31], v[136:139], v[184:187], v[28:31]\n"
        "ds_read_b128 v[176:179], v230 offset:16384\n"
        "v_or3_b32 v210, v210, v211, v218\n"
        "v_mfma_f32_16x16x32_bf16 v[28:31], v[140:143], v[188:191], v[28:31]\n"
        "ds_read_b128 v[180:183], v231 offset:16384\n"
        "v_perm_b32 v224, v209, v236, s60\n"
        "v_mfma_f32_16x16x32_bf16 v[24:27], v[132:135], v[188:191], v[24:27]\n"
        "ds_read_b32 v148, v224\n"
        "v_perm_b32 v225, v209, v236, s61\n"
        "v_mfma_f32_16x16x32_bf16 v[24:27], v[128:131], v[184:187], v[24:27]\n"
        "ds_read_b32 v149, v225\n"
        "v_perm_b32 v226, v209, v236, s62\n"
        "v_mfma_f32_16x16x32_bf16 v[20:23], v[120:123], v[184:187], v[20:23]\n"
        "ds_read_b32 v150, v226\n"
        "v_perm_b32 v227, v209, v236, s63\n"
        "v_mfma_f32_16x16x32_bf16 v[20:23], v[124:127], v[188:191], v[20:23]\n"
        "ds_read_b32 v151, v227\n"
        "v_perm_b32 v224, v212, v236, s60\n"
        "v_mfma_f32_16x16x32_bf16 v[16:19], v[116:119], v[188:191], v[16:19]\n"
        "ds_read_b32 v152, v224\n"
        "v_perm_b32 v225, v212, v236, s61\n"
        "v_mfma_f32_16x16x32_bf16 v[16:19], v[112:115], v[184:187], v[16:19]\n"
        "ds_read_b32 v153, v225\n"
        ".Ln_tp1:\n"
        "s_cbranch_execz .Ln_ret\n"
        "v_or_b32 v210, v210, v219\n"
        "s_waitcnt lgkmcnt(14)\n"
        "v_mfma_f32_16x16x32_bf16 v[32:35], v[112:115], v[192:195], v[32:35]\n"
        "ds_read_b128 v[184:187], v230 offset:18432\n"
        "v_cmp_ne_u32 vcc, 0, v210\n"
        "v_mfma_f32_16x16x32_bf16 v[32:35], v[116:119], v[196:199], v[32:35]\n"
        "ds_read_b128 v[188:191], v231 offset:18432\n"
        "v_perm_b32 v226, v212, v236, s62\n"
        "v_mfma_f32_16x16x32_bf16 v[36:39], v[124:127], v[196:199], v[36:39]\n"
        "ds_read_b32 v154, v226\n"
        "v_perm_b32 v227, v212, v236, s63\n"
        "v_mfma_f32_16x16x32_bf16 v[36:39], v[120:123], v[192:195], v[36:39]\n"
        "ds_read_b32 v155, v227\n"
        "v_perm_b32 v224, v213, v236, s60\n"
        "v_mfma_f32_16x16x32_bf16 v[40:43], v[128:131], v[192:195], v[40:43]\n"
        "ds_read_b32 v156, v224\n"
        "v_perm_b32 v225, v213, v236, s61\n"
        "v_mfma_f32_16x16x32_bf16 v[40:43], v[132:135], v[196:199], v[40:43]\n"
        "ds_read_b32 v157, v225\n"
        "v_perm_b32 v226, v213, v236, s62\n"
        "v_mfma_f32_16x16x32_bf16 v[44:47], v[140:143], v[196:199], v[44:47]\n"
        "ds_read_b32 v158, v226\n"
        "v_perm_b32 v227, v213, v236, s63\n"
        "v_mfma_f32_16x16x32_bf16 v[44:47], v[136:139], v[192:195], v[44:47]\n"
        "ds_read_b32 v159, v227\n"
        "s_waitcnt lgkmcnt(15)\n"
        "v_mfma_f32_16x16x32_bf16 v[60:63], v[136:139], v[200:203], v[60:63]\n"
        "ds_read_b128 v[192:195], v230 offset:20480\n"
        "s_cbranch_vccnz .Ln_f7\n"
        ".Ln_b7:\n"
        "v_mfma_f32_16x16x32_bf16 v[60:63], v[140:143], v[204:207], v[60:63]\n"
        "ds_read_b128 v[196:199], v231 offset:20480\n"
        "v_perm_b32 v224, v216, v236, s60\n"
        "v_mfma_f32_16x16x32_bf16 v[56:59], v[132:135], v[204:207], v[56:59]\n"
        "ds_read_b32 v160, v224\n"
        "v_perm_b32 v225, v216, v236, s61\n"
        "v_mfma_f32_16x16x32_bf16 v[56:59], v[128:131], v[200:203], v[56:59]\n"
        "ds_read_b32 v161, v225\n"
        "v_perm_b32 v226, v216, v236, s62\n"
        "v_mfma_f32_16x16x32_bf16 v[52:55], v[120:123], v[200:203], v[52:55]\n"
        "ds_read_b32 v162, v226\n"
        "v_perm_b32 v227, v216, v236, s63\n"
        "v_mfma_f32_16x16x32_bf16 v[52:55], v[124:127], v[204:207], v[52:55]\n"
        "ds_read_b32 v163, v227\n"
        "v_perm_b32 v224, v217, v236, s60\n"
        "v_mfma_f32_16x16x32_bf16 v[48:51], v[116:119], v[204:207], v[48:51]\n"
        "ds_read_b32 v164, v224\n"
        "v_perm_b32 v225, v217, v236, s61\n"
        "v_mfma_f32_16x16x32_bf16 v[48:51], v[112:115], v[200:203], v[48:51]\n"
        "ds_read_b32 v165, v225\n"
        "s_waitcnt lgkmcnt(15)\n"
        "v_mfma_f32_16x16x32_bf16 v[64:67], v[112:115], v[176:179], v[64:67]\n"
        "ds_read_b128 v[200:203], v230 offset:22528\n"
        "v_mfma_f32_16x16x32_bf16 v[64:67], v[116:119], v[180:183], v[64:67]\n"
        "ds_read_b128 v[204:207], v231 offset:22528\n"
        "v_perm_b32 v226, v217, v236, s62\n"
        "v_mfma_f32_16x16x32_bf16 v[68:71], v[124:127], v[180:183], v[68:71]\n"
        "ds_read_b32 v166, v226\n"
        "v_perm_b32 v227, v217, v236, s63\n"
        "v_mfma_f32_16x16x32_bf16 v[68:71], v[120:123], v[176:179], v[68:71]\n"
        "ds_read_b32 v167, v227\n"
        "v_perm_b32 v224, v220, v236, s60\n"
        "v_mfma_f32_16x16x32_bf16 v[72:75], v[128:131], v[176:179], v[72:75]\n"
        "ds_read_b32 v168, v224\n"
        "v_perm_b32 v225, v220, v236, s61\n"
        "v_mfma_f32_16x16x32_bf16 v[72:75], v[132:135], v[180:183], v[72:75]\n"
        "ds_read_b32 v169, v225\n"
        "v_perm_b32 v226, v220, v236, s62\n"
        "s_waitcnt lgkmcnt(15)\n"
        "v_mfma_f32_16x16x32_bf16 v[84:87], v[132:135], v[188:191], v[84:87]\n"
        "ds_read_b32 v170, v226\n"
        "v_perm_b32 v227, v220, v236, s63\n"
        "v_mfma_f32_16x16x32_bf16 v[84:87], v[128:131], v[184:187], v[84:87]\n"
        "ds_read_b32 v171, v227\n"
        "v_perm_b32 v224, v221, v236, s60\n"
        "v_mfma_f32_16x16x32_bf16 v[80:83], v[120:123], v[184:187], v[80:83]\n"
        "ds_read_b32 v172, v224\n"
        "v_perm_b32 v225, v221, v236, s61\n"
        "v_mfma_f32_16x16x32_bf16 v[80:83], v[124:127], v[188:191], v[80:83]\n"
        "ds_read_b32 v173, v225\n"
        "v_perm_b32 v226, v221, v236, s62\n"
        "v_mfma_f32_16x16x32_bf16 v[76:79], v[116:119], v[188:191], v[76:79]\n"
        "ds_read_b32 v174, v226\n"
        "v_perm_b32 v227, v221, v236, s63\n"
        "v_mfma_f32_16x16x32_bf16 v[76:79], v[112:115], v[184:187], v[76:79]\n"
        "ds_read_b32 v175, v227\n"
        "s_waitcnt lgkmcnt(10)\n"
        "s_barrier\n"
        "v_mfma_f32_16x16x32_bf16 v[88:91], v[112:115], v[192:195], v[88:91]\n"
        "ds_read_b128 v[208:211], v232 offset:16384\n"
        "v_mfma_f32_16x16x32_bf16 v[88:91], v[116:119], v[196:199], v[88:91]\n"
        "ds_read_b128 v[212:215], v233 offset:16384\n"
        "v_mfma_f32_16x16x32_bf16 v[92:95], v[124:127], v[196:199], v[92:95]\n"
        "v_mfma_f32_16x16x32_bf16 v[92:95], v[120:123], v[192:195], v[92:95]\n"
        "ds_read_b128 v[216:219], v234 offset:16384\n"
        "v_mfma_f32_16x16x32_bf16 v[96:99], v[128:131], v[192:195], v[96:99]\n"
        "ds_read_b128 v[220:223], v235 offset:16384\n"
        "v_mfma_f32_16x16x32_bf16 v[96:99], v[132:135], v[196:199], v[96:99]\n"
        "v_mfma_f32_16x16x32_bf16 v[108:111], v[132:135], v[204:207], v[108:111]\n"
        "ds_read_b128 v[176:179], v228 offset:32768\n"
        "v_mfma_f32_16x16x32_bf16 v[108:111], v[128:131], v[200:203], v[108:111]\n"
        "ds_read_b128 v[180:183], v229 offset:32768\n"
        "v_mfma_f32_16x16x32_bf16 v[104:107], v[120:123], v[200:203], v[104:107]\n"
        "v_mfma_f32_16x16x32_bf16 v[104:107], v[124:127], v[204:207], v[104:107]\n"
        "ds_read_b128 v[184:187], v228 offset:34816\n"
        "v_mfma_f32_16x16x32_bf16 v[100:103], v[116:119], v[204:207], v[100:103]\n"
        "ds_read_b128 v[188:191], v229 offset:34816\n"
        "v_mfma_f32_16x16x32_bf16 v[100:103], v[112:115], v[200:203], v[100:103]\n"
        "s_waitcnt lgkmcnt(2)\n"
        "v_mfma_f32_16x16x32_bf16 v[0:3], v[144:147], v[176:179], v[0:3]\n"
        "ds_read_b128 v[192:195], v228 offset:36864\n"
        "v_bitop3_b32 v210, v210, v214, s34 bitop3:0x7e\n"
        "v_mfma_f32_16x16x32_bf16 v[0:3], v[148:151], v[180:183], v[0:3]\n"
        "ds_read_b128 v[196:199], v229 offset:36864\n"
        "v_bitop3_b32 v211, v211, v215, s39 bitop3:0x7e\n"
        "v_mfma_f32_16x16x32_bf16 v[4:7], v[156:159], v[180:183], v[4:7]\n"
        "ds_read_b128 v[200:203], v228 offset:38912\n"
        "v_bitop3_b32 v218, v218, v222, s34 bitop3:0x7e\n"
        "v_mfma_f32_16x16x32_bf16 v[4:7], v[152:155], v[176:179], v[4:7]\n"
        "ds_read_b128 v[204:207], v229 offset:38912\n"
        "v_perm_b32 v224, v208, v236, s60\n"
        "v_mfma_f32_16x16x32_bf16 v[8:11], v[160:163], v[176:179], v[8:11]\n"
        "ds_read_b32 v112, v224\n"
        "v_perm_b32 v225, v208, v236, s61\n"
        "v_mfma_f32_16x16x32_bf16 v[8:11], v[164:167], v[180:183], v[8:11]\n"
        "ds_read_b32 v113, v225\n"
        "v_perm_b32 v226, v208, v236, s62\n"
        "v_mfma_f32_16x16x32_bf16 v[12:15], v[172:175], v[180:183], v[12:15]\n"
        "ds_read_b32 v114, v226\n"
        "v_perm_b32 v227, v208, v236, s63\n"
        "v_mfma_f32_16x16x32_bf16 v[12:15], v[168:171], v[176:179], v[12:15]\n"
        "ds_read_b32 v115, v227\n"
        "v_bitop3_b32 v219, v219, v223, s39 bitop3:0x7e\n"
        "s_waitcnt lgkmcnt(8)\n"
        "v_mfma_f32_16x16x32_bf16 v[28:31], v[168:171], v[184:187], v[28:31]\n"
        "ds_read_b128 v[176:179], v230 offset:32768\n"
        "v_or3_b32 v210, v210, v211, v218\n"
        "v_mfma_f32_16x16x32_bf16 v[28:31], v[172:175], v[188:191], v[28:31]\n"
        "ds_read_b128 v[180:183], v231 offset:32768\n"
        "v_perm_b32 v224, v209, v236, s60\n"
        "v_mfma_f32_16x16x32_bf16 v[24:27], v[164:167], v[188:191], v[24:27]\n"
        "ds_read_b32 v116, v224\n"
        "v_perm_b32 v225, v209, v236, s61\n"
        "v_mfma_f32_16x16x32_bf16 v[24:27], v[160:163], v[184:187], v[24:27]\n"
        "ds_read_b32 v117, v225\n"
        "v_perm_b32 v226, v209, v236, s62\n"
        "v_mfma_f32_16x16x32_bf16 v[20:23], v[152:155], v[184:187], v[20:23]\n"
        "ds_read_b32 v118, v226\n"
        "v_perm_b32 v227, v209, v236, s63\n"
        "v_mfma_f32_16x16x32_bf16 v[20:23], v[156:159], v[188:191], v[20:23]\n"
        "ds_read_b32 v119, v227\n"
        "v_perm_b32 v224, v212, v236, s60\n"
        "v_mfma_f32_16x16x32_bf16 v[16:19], v[148:151], v[188:191], v[16:19]\n"
        "ds_read_b32 v120, v224\n"
        "v_perm_b32 v225, v212, v236, s61\n"
        "v_mfma_f32_16x16x32_bf16 v[16:19], v[144:147], v[184:187], v[16:19]\n"
        "ds_read_b32 v121, v225\n"
        "v_or_b32 v210, v210, v219\n"
        "s_waitcnt lgkmcnt(14)\n"
        "v_mfma_f32_16x16x32_bf16 v[32:35], v[144:147], v[192:195], v[32:35]\n"
        "ds_read_b128 v[184:187], v230 offset:34816\n"
        "v_cmp_ne_u32 vcc, 0, v210\n"
        "v_mfma_f32_16x16x32_bf16 v[32:35], v[148:151], v[196:199], v[32:35]\n"
        "ds_read_b128 v[188:191], v231 offset:34816\n"
        "v_perm_b32 v226, v212, v236, s62\n"
        "v_mfma_f32_16x16x32_bf16 v[36:39], v[156:159], v[196:199], v[36:39]\n"
        "ds_read_b32 v122, v226\n"
        "v_perm_b32 v227, v212, v236, s63\n"
        "v_mfma_f32_16x16x32_bf16 v[36:39], v[152:155], v[192:195], v[36:39]\n"
        "ds_read_b32 v123, v227\n"
        "v_perm_b32 v224, v213, v236, s60\n"
        "v_mfma_f32_16x16x32_bf16 v[40:43], v[160:163], v[192:195], v[40:43]\n"
        "ds_read_b32 v124, v224\n"
        "v_perm_b32 v225, v213, v236, s61\n"
        "v_mfma_f32_16x16x32_bf16 v[40:43], v[164:167], v[196:199], v[40:43]\n"
        "ds_read_b32 v125, v225\n"
        "v_perm_b32 v226, v213, v236, s62\n"
        "v_mfma_f32_16x16x32_bf16 v[44:47], v[172:175], v[196:199], v[44:47]\n"
        "ds_read_b32 v126, v226\n"
        "v_perm_b32 v227, v213, v236, s63\n"
        "v_mfma_f32_16x16x32_bf16 v[44:47], v[168:171], v[192:195], v[44:47]\n"
        "ds_read_b32 v127, v227\n"
        "s_waitcnt lgkmcnt(15)\n"
        "v_mfma_f32_16x16x32_bf16 v[60:63], v[168:171], v[200:203], v[60:63]\n"
        "ds_read_b128 v[192:195], v230 offset:36864\n"
        "s_cbranch_vccnz .Ln_f8\n"
        ".Ln_b8:\n"
        "v_mfma_f32_16x16x32_bf16 v[60:63], v[172:175], v[204:207], v[60:63]\n"
        "ds_read_b128 v[196:199], v231 offset:36864\n"
        "v_perm_b32 v224, v216, v236, s60\n"
        "v_mfma_f32_16x16x32_bf16 v[56:59], v[164:167], v[204:207], v[56:59]\n"
        "ds_read_b32 v128, v224\n"
        "v_perm_b32 v225, v216, v236, s61\n"
        "v_mfma_f32_16x16x32_bf16 v[56:59], v[160:163], v[200:203], v[56:59]\n"
        "ds_read_b32 v129, v225\n"
        "v_perm_b32 v226, v216, v236, s62\n"
        "v_mfma_f32_16x16x32_bf16 v[52:55], v[152:155], v[200:203], v[52:55]\n"
        "ds_read_b32 v130, v226\n"
        "v_perm_b32 v227, v216, v236, s63\n"
        "v_mfma_f32_16x16x32_bf16 v[52:55], v[156:159], v[204:207], v[52:55]\n"
        "ds_read_b32 v131, v227\n"
        "v_perm_b32 v224, v217, v236, s60\n"
        "v_mfma_f32_16x16x32_bf16 v[48:51], v[148:151], v[204:207], v[48:51]\n"
        "ds_read_b32 v132, v224\n"
        "v_perm_b32 v225, v217, v236, s61\n"
        "v_mfma_f32_16x16x32_bf16 v[48:51], v[144:147], v[200:203], v[48:51]\n"
        "ds_read_b32 v133, v225\n"
        "s_waitcnt lgkmcnt(15)\n"
        "v_mfma_f32_16x16x32_bf16 v[64:67], v[144:147], v[176:179], v[64:67]\n"
        "ds_read_b128 v[200:203], v230 offset:38912\n"
        "v_mfma_f32_16x16x32_bf16 v[64:67], v[148:151], v[180:183], v[64:67]\n"
        "ds_read_b128 v[204:207], v231 offset:38912\n"
        "v_perm_b32 v226, v217, v236, s62\n"
        "v_mfma_f32_16x16x32_bf16 v[68:71], v[156:159], v[180:183], v[68:71]\n"
        "ds_read_b32 v134, v226\n"
        "v_perm_b32 v227, v217, v236, s63\n"
        "v_mfma_f32_16x16x32_bf16 v[68:71], v[152:155], v[176:179], v[68:71]\n"
        "ds_read_b32 v135, v227\n"
        "v_perm_b32 v224, v220, v236, s60\n"
        "v_mfma_f32_16x16x32_bf16 v[72:75], v[160:163], v[176:179], v[72:75]\n"
        "ds_read_b32 v136, v224\n"
        "v_perm_b32 v225, v220, v236, s61\n"
        "v_mfma_f32_16x16x32_bf16 v[72:75], v[164:167], v[180:183], v[72:75]\n"
        "ds_read_b32 v137, v225\n"
        "v_perm_b32 v226, v220, v236, s62\n"
        "s_waitcnt lgkmcnt(15)\n"
        "v_mfma_f32_16x16x32_bf16 v[84:87], v[164:167], v[188:191], v[84:87]\n"
        "ds_read_b32 v138, v226\n"
        "v_perm_b32 v227, v220, v236, s63\n"
        "v_mfma_f32_16x16x32_bf16 v[84:87], v[160:163], v[184:187], v[84:87]\n"
        "ds_read_b32 v139, v227\n"
        "v_perm_b32 v224, v221, v236, s60\n"
        "v_mfma_f32_16x16x32_bf16 v[80:83], v[152:155], v[184:187], v[80:83]\n"
        "ds_read_b32 v140, v224\n"
        "v_perm_b32 v225, v221, v236, s61\n"
        "v_mfma_f32_16x16x32_bf16 v[80:83], v[156:159], v[188:191], v[80:83]\n"
        "ds_read_b32 v141, v225\n"
        "v_perm_b32 v226, v221, v236, s62\n"
        "v_mfma_f32_16x16x32_bf16 v[76:79], v[148:151], v[188:191], v[76:79]\n"
        "ds_read_b32 v142, v226\n"
        "v_perm_b32 v227, v221, v236, s63\n"
        "v_mfma_f32_16x16x32_bf16 v[76:79], v[144:147], v[184:187], v[76:79]\n"
        "ds_read_b32 v143, v227\n"
        "s_waitcnt lgkmcnt(10)\n"
        "s_barrier\n"
        "v_mfma_f32_16x16x32_bf16 v[88:91], v[144:147], v[192:195], v[88:91]\n"
        "ds_read_b128 v[208:211], v232 offset:32768\n"
        "v_mfma_f32_16x16x32_bf16 v[88:91], v[148:151], v[196:199], v[88:91]\n"
        "ds_read_b128 v[212:215], v233 offset:32768\n"
        "v_mfma_f32_16x16x32_bf16 v[92:95], v[156:159], v[196:199], v[92:95]\n"
        "v_mfma_f32_16x16x32_bf16 v[92:95], v[152:155], v[192:195], v[92:95]\n"
        "ds_read_b128 v[216:219], v234 offset:32768\n"
        "v_mfma_f32_16x16x32_bf16 v[96:99], v[160:163], v[192:195], v[96:99]\n"
        "ds_read_b128 v[220:223], v235 offset:32768\n"
        "v_mfma_f32_16x16x32_bf16 v[96:99], v[164:167], v[196:199], v[96:99]\n"
        "v_mfma_f32_16x16x32_bf16 v[108:111], v[164:167], v[204:207], v[108:111]\n"
        "ds_read_b128 v[176:179], v228 offset:0\n"
        "v_mfma_f32_16x16x32_bf16 v[108:111], v[160:163], v[200:203], v[108:111]\n"
        "ds_read_b128 v[180:183], v229 offset:0\n"
        "v_mfma_f32_16x16x32_bf16 v[104:107], v[152:155], v[200:203], v[104:107]\n"
        "v_mfma_f32_16x16x32_bf16 v[104:107], v[156:159], v[204:207], v[104:107]\n"
        "ds_read_b128 v[184:187], v228 offset:2048\n"
        "v_mfma_f32_16x16x32_bf16 v[100:103], v[148:151], v[204:207], v[100:103]\n"
        "ds_read_b128 v[188:191], v229 offset:2048\n"
        "v_mfma_f32_16x16x32_bf16 v[100:103], v[144:147], v[200:203], v[100:103]\n"
        "s_waitcnt lgkmcnt(2)\n"
        "v_mfma_f32_16x16x32_bf16 v[0:3], v[112:115], v[176:179], v[0:3]\n"
        "ds_read_b128 v[192:195], v228 offset:4096\n"
        "v_bitop3_b32 v210, v210, v214, s34 bitop3:0x7e\n"
        "v_mfma_f32_16x16x32_bf16 v[0:3], v[116:119], v[180:183], v[0:3]\n"
        "ds_read_b128 v[196:199], v229 offset:4096\n"
        "v_bitop3_b32 v211, v211, v215, s39 bitop3:0x7e\n"
        "v_mfma_f32_16x16x32_bf16 v[4:7], v[124:127], v[180:183], v[4:7]\n"
        "ds_read_b128 v[200:203], v228 offset:6144\n"
        "v_bitop3_b32 v218, v218, v222, s34 bitop3:0x7e\n"
        "v_mfma_f32_16x16x32_bf16 v[4:7], v[120:123], v[176:179], v[4:7]\n"
        "ds_read_b128 v[204:207], v229 offset:6144\n"
        "v_perm_b32 v224, v208, v236, s60\n"
        "v_mfma_f32_16x16x32_bf16 v[8:11], v[128:131], v[176:179], v[8:11]\n"
        "ds_read_b32 v144, v224\n"
        "v_perm_b32 v225, v208, v236, s61\n"
        "v_mfma_f32_16x16x32_bf16 v[8:11], v[132:135], v[180:183], v[8:11]\n"
        "ds_read_b32 v145, v225\n"
        "v_perm_b32 v226, v208, v236, s62\n"
        "v_mfma_f32_16x16x32_bf16 v[12:15], v[140:143], v[180:183], v[12:15]\n"
        "ds_read_b32 v146, v226\n"
        "v_perm_b32 v227, v208, v236, s63\n"
        "v_mfma_f32_16x16x32_bf16 v[12:15], v[136:139], v[176:179], v[12:15]\n"
        "ds_read_b32 v147, v227\n"
        "v_bitop3_b32 v219, v219, v223, s39 bitop3:0x7e\n"
        "s_waitcnt lgkmcnt(8)\n"
        "v_mfma_f32_16x16x32_bf16 v[28:31], v[136:139], v[184:187], v[28:31]\n"
        "ds_read_b128 v[176:179], v230 offset:0\n"
        "v_or3_b32 v210, v210, v211, v218\n"
        "v_mfma_f32_16x16x32_bf16 v[28:31], v[140:143], v[188:191], v[28:31]\n"
        "ds_read_b128 v[180:183], v231 offset:0\n"
        "v_perm_b32 v224, v209, v236, s60\n"
        "v_mfma_f32_16x16x32_bf16 v[24:27], v[132:135], v[188:191], v[24:27]\n"
        "ds_read_b32 v148, v224\n"
        "v_perm_b32 v225, v209, v236, s61\n"
        "v_mfma_f32_16x16x32_bf16 v[24:27], v[128:131], v[184:187], v[24:27]\n"
        "ds_read_b32 v149, v225\n"
        "v_perm_b32 v226, v209, v236, s62\n"
        "v_mfma_f32_16x16x32_bf16 v[20:23], v[120:123], v[184:187], v[20:23]\n"
        "ds_read_b32 v150, v226\n"
        "v_perm_b32 v227, v209, v236, s63\n"
        "v_mfma_f32_16x16x32_bf16 v[20:23], v[124:127], v[188:191], v[20:23]\n"
        "ds_read_b32 v151, v227\n"
        "v_perm_b32 v224, v212, v236, s60\n"
        "v_mfma_f32_16x16x32_bf16 v[16:19], v[116:119], v[188:191], v[16:19]\n"
        "ds_read_b32 v152, v224\n"
        "v_perm_b32 v225, v212, v236, s61\n"
        "v_mfma_f32_16x16x32_bf16 v[16:19], v[112:115], v[184:187], v[16:19]\n"
        "ds_read_b32 v153, v225\n"
        "v_or_b32 v210, v210, v219\n"
        "s_waitcnt lgkmcnt(14)\n"
        "v_mfma_f32_16x16x32_bf16 v[32:35], v[112:115], v[192:195], v[32:35]\n"
        "ds_read_b128 v[184:187], v230 offset:2048\n"
        "v_cmp_ne_u32 vcc, 0, v210\n"
        "v_mfma_f32_16x16x32_bf16 v[32:35], v[116:119], v[196:199], v[32:35]\n"
        "ds_read_b128 v[188:191], v231 offset:2048\n"
        "v_perm_b32 v226, v212, v236, s62\n"
        "v_mfma_f32_16x16x32_bf16 v[36:39], v[124:127], v[196:199], v[36:39]\n"
        "ds_read_b32 v154, v226\n"
        "v_perm_b32 v227, v212, v236, s63\n"
        "v_mfma_f32_16x16x32_bf16 v[36:39], v[120:123], v[192:195], v[36:39]\n"
        "ds_read_b32 v155, v227\n"
        "v_perm_b32 v224, v213, v236, s60\n"
        "v_mfma_f32_16x16x32_bf16 v[40:43], v[128:131], v[192:195], v[40:43]\n"
        "ds_read_b32 v156, v224\n"
        "v_perm_b32 v225, v213, v236, s61\n"
        "v_mfma_f32_16x16x32_bf16 v[40:43], v[132:135], v[196:199], v[40:43]\n"
        "ds_read_b32 v157, v225\n"
        "v_perm_b32 v226, v213, v236, s62\n"
        "v_mfma_f32_16x16x32_bf16 v[44:47], v[140:143], v[196:199], v[44:47]\n"
        "ds_read_b32 v158, v226\n"
        "v_perm_b32 v227, v213, v236, s63\n"
        "v_mfma_f32_16x16x32_bf16 v[44:47], v[136:139], v[192:195], v[44:47]\n"
        "ds_read_b32 v159, v227\n"
        "s_waitcnt lgkmcnt(15)\n"
        "v_mfma_f32_16x16x32_bf16 v[60:63], v[136:139], v[200:203], v[60:63]\n"
        "ds_read_b128 v[192:195], v230 offset:4096\n"
        "s_cbranch_vccnz .Ln_f9\n"
        ".Ln_b9:\n"
        "v_mfma_f32_16x16x32_bf16 v[60:63], v[140:143], v[204:207], v[60:63]\n"
        "ds_read_b128 v[196:199], v231 offset:4096\n"
        "v_perm_b32 v224, v216, v236, s60\n"
        "v_mfma_f32_16x16x32_bf16 v[56:59], v[132:135], v[204:207], v[56:59]\n"
        "ds_read_b32 v160, v224\n"
        "v_perm_b32 v225, v216, v236, s61\n"
        "v_mfma_f32_16x16x32_bf16 v[56:59], v[128:131], v[200:203], v[56:59]\n"
        "ds_read_b32 v161, v225\n"
        "v_perm_b32 v226, v216, v236, s62\n"
        "v_mfma_f32_16x16x32_bf16 v[52:55], v[120:123], v[200:203], v[52:55]\n"
        "ds_read_b32 v162, v226\n"
        "v_perm_b32 v227, v216, v236, s63\n"
        "v_mfma_f32_16x16x32_bf16 v[52:55], v[124:127], v[204:207], v[52:55]\n"
        "ds_read_b32 v163, v227\n"
        "v_perm_b32 v224, v217, v236, s60\n"
        "v_mfma_f32_16x16x32_bf16 v[48:51], v[116:119], v[204:207], v[48:51]\n"
        "ds_read_b32 v164, v224\n"
        "v_perm_b32 v225, v217, v236, s61\n"
        "v_mfma_f32_16x16x32_bf16 v[48:51], v[112:115], v[200:203], v[48:51]\n"
        "ds_read_b32 v165, v225\n"
        "s_waitcnt lgkmcnt(15)\n"
        "v_mfma_f32_16x16x32_bf16 v[64:67], v[112:115], v[176:179], v[64:67]\n"
        "ds_read_b128 v[200:203], v230 offset:6144\n"
        "v_mfma_f32_16x16x32_bf16 v[64:67], v[116:119], v[180:183], v[64:67]\n"
        "ds_read_b128 v[204:207], v231 offset:6144\n"
        "v_perm_b32 v226, v217, v236, s62\n"
        "v_mfma_f32_16x16x32_bf16 v[68:71], v[124:127], v[180:183], v[68:71]\n"
        "ds_read_b32 v166, v226\n"
        "v_perm_b32 v227, v217, v236, s63\n"
        "v_mfma_f32_16x16x32_bf16 v[68:71], v[120:123], v[176:179], v[68:71]\n"
        "ds_read_b32 v167, v227\n"
        "v_perm_b32 v224, v220, v236, s60\n"
        "v_mfma_f32_16x16x32_bf16 v[72:75], v[128:131], v[176:179], v[72:75]\n"
        "ds_read_b32 v168, v224\n"
        "v_perm_b32 v225, v220, v236, s61\n"
        "v_mfma_f32_16x16x32_bf16 v[72:75], v[132:135], v[180:183], v[72:75]\n"
        "ds_read_b32 v169, v225\n"
        "v_perm_b32 v226, v220, v236, s62\n"
        "s_waitcnt lgkmcnt(15)\n"
        "v_mfma_f32_16x16x32_bf16 v[84:87], v[132:135], v[188:191], v[84:87]\n"
        "ds_read_b32 v170, v226\n"
        "v_perm_b32 v227, v220, v236, s63\n"
        "v_mfma_f32_16x16x32_bf16 v[84:87], v[128:131], v[184:187], v[84:87]\n"
        "ds_read_b32 v171, v227\n"
        "v_perm_b32 v224, v221, v236, s60\n"
        "v_mfma_f32_16x16x32_bf16 v[80:83], v[120:123], v[184:187], v[80:83]\n"
        "ds_read_b32 v172, v224\n"
        "v_perm_b32 v225, v221, v236, s61\n"
        "v_mfma_f32_16x16x32_bf16 v[80:83], v[124:127], v[188:191], v[80:83]\n"
        "ds_read_b32 v173, v225\n"
        "v_perm_b32 v226, v221, v236, s62\n"
        "v_mfma_f32_16x16x32_bf16 v[76:79], v[116:119], v[188:191], v[76:79]\n"
        "ds_read_b32 v174, v226\n"
        "v_perm_b32 v227, v221, v236, s63\n"
        "v_mfma_f32_16x16x32_bf16 v[76:79], v[112:115], v[184:187], v[76:79]\n"
        "ds_read_b32 v175, v227\n"
        "s_waitcnt lgkmcnt(10)\n"
        "s_barrier\n"
        "v_mfma_f32_16x16x32_bf16 v[88:91], v[112:115], v[192:195], v[88:91]\n"
        "ds_read_b128 v[208:211], v232 offset:0\n"
        "v_mfma_f32_16x16x32_bf16 v[88:91], v[116:119], v[196:199], v[88:91]\n"
        "ds_read_b128 v[212:215], v233 offset:0\n"
        "v_mfma_f32_16x16x32_bf16 v[92:95], v[124:127], v[196:199], v[92:95]\n"
        "v_mfma_f32_16x16x32_bf16 v[92:95], v[120:123], v[192:195], v[92:95]\n"
        "ds_read_b128 v[216:219], v234 offset:0\n"
        "v_mfma_f32_16x16x32_bf16 v[96:99], v[128:131], v[192:195], v[96:99]\n"
        "ds_read_b128 v[220:223], v235 offset:0\n"
        "v_mfma_f32_16x16x32_bf16 v[96:99], v[132:135], v[196:199], v[96:99]\n"
        "v_mfma_f32_16x16x32_bf16 v[108:111], v[132:135], v[204:207], v[108:111]\n"
        "ds_read_b128 v[176:179], v228 offset:16384\n"
        "v_mfma_f32_16x16x32_bf16 v[108:111], v[128:131], v[200:203], v[108:111]\n"
        "ds_read_b128 v[180:183], v229 offset:16384\n"
        "v_mfma_f32_16x16x32_bf16 v[104:107], v[120:123], v[200:203], v[104:107]\n"
        "v_mfma_f32_16x16x32_bf16 v[104:107], v[124:127], v[204:207], v[104:107]\n"
        "ds_read_b128 v[184:187], v228 offset:18432\n"
        "v_mfma_f32_16x16x32_bf16 v[100:103], v[116:119], v[204:207], v[100:103]\n"
        "ds_read_b128 v[188:191], v229 offset:18432\n"
        "v_mfma_f32_16x16x32_bf16 v[100:103], v[112:115], v[200:203], v[100:103]\n"
        "s_waitcnt lgkmcnt(2)\n"
        "v_mfma_f32_16x16x32_bf16 v[0:3], v[144:147], v[176:179], v[0:3]\n"
        "ds_read_b128 v[192:195], v228 offset:20480\n"
        "v_bitop3_b32 v210, v210, v214, s34 bitop3:0x7e\n"
        "v_mfma_f32_16x16x32_bf16 v[0:3], v[148:151], v[180:183], v[0:3]\n"
        "ds_read_b128 v[196:199], v229 offset:20480\n"
        "v_bitop3_b32 v211, v211, v215, s39 bitop3:0x7e\n"
        "v_mfma_f32_16x16x32_bf16 v[4:7], v[156:159], v[180:183], v[4:7]\n"
        "ds_read_b128 v[200:203], v228 offset:22528\n"
        "v_bitop3_b32 v218, v218, v222, s34 bitop3:0x7e\n"
        "v_mfma_f32_16x16x32_bf16 v[4:7], v[152:155], v[176:179], v[4:7]\n"
        "ds_read_b128 v[204:207], v229 offset:22528\n"
        "v_perm_b32 v224, v208, v236, s60\n"
        "v_mfma_f32_16x16x32_bf16 v[8:11], v[160:163], v[176:179], v[8:11]\n"
        "ds_read_b32 v112, v224\n"
        "v_perm_b32 v225, v208, v236, s61\n"
        "v_mfma_f32_16x16x32_bf16 v[8:11], v[164:167], v[180:183], v[8:11]\n"
        "ds_read_b32 v113, v225\n"
        "v_perm_b32 v226, v208, v236, s62\n"
        "v_mfma_f32_16x16x32_bf16 v[12:15], v[172:175], v[180:183], v[12:15]\n"
        "ds_read_b32 v114, v226\n"
        "v_perm_b32 v227, v208, v236, s63\n"
        "v_mfma_f32_16x16x32_bf16 v[12:15], v[168:171], v[176:179], v[12:15]\n"
        "ds_read_b32 v115, v227\n"
        "v_bitop3_b32 v219, v219, v223, s39 bitop3:0x7e\n"
        "s_waitcnt lgkmcnt(8)\n"
        "v_mfma_f32_16x16x32_bf16 v[28:31], v[168:171], v[184:187], v[28:31]\n"
        "ds_read_b128 v[176:179], v230 offset:16384\n"
        "v_or3_b32 v210, v210, v211, v218\n"
        "v_mfma_f32_16x16x32_bf16 v[28:31], v[172:175], v[188:191], v[28:31]\n"
        "ds_read_b128 v[180:183], v231 offset:16384\n"
        "v_perm_b32 v224, v209, v236, s60\n"
        "v_mfma_f32_16x16x32_bf16 v[24:27], v[164:167], v[188:191], v[24:27]\n"
        "ds_read_b32 v116, v224\n"
        "v_perm_b32 v225, v209, v236, s61\n"
        "v_mfma_f32_16x16x32_bf16 v[24:27], v[160:163], v[184:187], v[24:27]\n"
        "ds_read_b32 v117, v225\n"
        "v_perm_b32 v226, v209, v236, s62\n"
        "v_mfma_f32_16x16x32_bf16 v[20:23], v[152:155], v[184:187], v[20:23]\n"
        "ds_read_b32 v118, v226\n"
        "v_perm_b32 v227, v209, v236, s63\n"
        "v_mfma_f32_16x16x32_bf16 v[20:23], v[156:159], v[188:191], v[20:23]\n"
        "ds_read_b32 v119, v227\n"
        "v_perm_b32 v224, v212, v236, s60\n"
        "v_mfma_f32_16x16x32_bf16 v[16:19], v[148:151], v[188:191], v[16:19]\n"
        "ds_read_b32 v120, v224\n"
        "v_perm_b32 v225, v212, v236, s61\n"
        "v_mfma_f32_16x16x32_bf16 v[16:19], v[144:147], v[184:187], v[16:19]\n"
        "ds_read_b32 v121, v225\n"
        "v_or_b32 v210, v210, v219\n"
        "s_waitcnt lgkmcnt(14)\n"
        "v_mfma_f32_16x16x32_bf16 v[32:35], v[144:147], v[192:195], v[32:35]\n"
        "ds_read_b128 v[184:187], v230 offset:18432\n"
        "v_cmp_ne_u32 vcc, 0, v210\n"
        "v_mfma_f32_16x16x32_bf16 v[32:35], v[148:151], v[196:199], v[32:35]\n"
        "ds_read_b128 v[188:191], v231 offset:18432\n"
        "v_perm_b32 v226, v212, v236, s62\n"
        "v_mfma_f32_16x16x32_bf16 v[36:39], v[156:159], v[196:199], v[36:39]\n"
        "ds_read_b32 v122, v226\n"
        "v_perm_b32 v227, v212, v236, s63\n"
        "v_mfma_f32_16x16x32_bf16 v[36:39], v[152:155], v[192:195], v[36:39]\n"
        "ds_read_b32 v123, v227\n"
        "v_perm_b32 v224, v213, v236, s60\n"
        "v_mfma_f32_16x16x32_bf16 v[40:43], v[160:163], v[192:195], v[40:43]\n"
        "ds_read_b32 v124, v224\n"
        "v_perm_b32 v225, v213, v236, s61\n"
        "v_mfma_f32_16x16x32_bf16 v[40:43], v[164:167], v[196:199], v[40:43]\n"
        "ds_read_b32 v125, v225\n"
        "v_perm_b32 v226, v213, v236, s62\n"
        "v_mfma_f32_16x16x32_bf16 v[44:47], v[172:175], v[196:199], v[44:47]\n"
        "ds_read_b32 v126, v226\n"
        "v_perm_b32 v227, v213, v236, s63\n"
        "v_mfma_f32_16x16x32_bf16 v[44:47], v[168:171], v[192:195], v[44:47]\n"
        "ds_read_b32 v127, v227\n"
        "s_waitcnt lgkmcnt(15)\n"
        "v_mfma_f32_16x16x32_bf16 v[60:63], v[168:171], v[200:203], v[60:63]\n"
        "ds_read_b128 v[192:195], v230 offset:20480\n"
        "s_cbranch_vccnz .Ln_f10\n"
        ".Ln_b10:\n"
        "v_mfma_f32_16x16x32_bf16 v[60:63], v[172:175], v[204:207], v[60:63]\n"
        "ds_read_b128 v[196:199], v231 offset:20480\n"
        "v_perm_b32 v224, v216, v236, s60\n"
        "v_mfma_f32_16x16x32_bf16 v[56:59], v[164:167], v[204:207], v[56:59]\n"
        "ds_read_b32 v128, v224\n"
        "v_perm_b32 v225, v216, v236, s61\n"
        "v_mfma_f32_16x16x32_bf16 v[56:59], v[160:163], v[200:203], v[56:59]\n"
        "ds_read_b32 v129, v225\n"
        "v_perm_b32 v226, v216, v236, s62\n"
        "v_mfma_f32_16x16x32_bf16 v[52:55], v[152:155], v[200:203], v[52:55]\n"
        "ds_read_b32 v130, v226\n"
        "v_perm_b32 v227, v216, v236, s63\n"
        "v_mfma_f32_16x16x32_bf16 v[52:55], v[156:159], v[204:207], v[52:55]\n"
        "ds_read_b32 v131, v227\n"
        "v_perm_b32 v224, v217, v236, s60\n"
        "v_mfma_f32_16x16x32_bf16 v[48:51], v[148:151], v[204:207], v[48:51]\n"
        "ds_read_b32 v132, v224\n"
        "v_perm_b32 v225, v217, v236, s61\n"
        "v_mfma_f32_16x16x32_bf16 v[48:51], v[144:147], v[200:203], v[48:51]\n"
        "ds_read_b32 v133, v225\n"
        "s_waitcnt lgkmcnt(15)\n"
        "v_mfma_f32_16x16x32_bf16 v[64:67], v[144:147], v[176:179], v[64:67]\n"
        "ds_read_b128 v[200:203], v230 offset:22528\n"
        "v_mfma_f32_16x16x32_bf16 v[64:67], v[148:151], v[180:183], v[64:67]\n"
        "ds_read_b128 v[204:207], v231 offset:22528\n"
        "v_perm_b32 v226, v217, v236, s62\n"
        "v_mfma_f32_16x16x32_bf16 v[68:71], v[156:159], v[180:183], v[68:71]\n"
        "ds_read_b32 v134, v226\n"
        "v_perm_b32 v227, v217, v236, s63\n"
        "v_mfma_f32_16x16x32_bf16 v[68:71], v[152:155], v[176:179], v[68:71]\n"
        "ds_read_b32 v135, v227\n"
        "v_perm_b32 v224, v220, v236, s60\n"
        "v_mfma_f32_16x16x32_bf16 v[72:75], v[160:163], v[176:179], v[72:75]\n"
        "ds_read_b32 v136, v224\n"
        ".Ln_tp2:\n"
        "s_cbranch_execz .Ln_ret\n"
        "v_perm_b32 v225, v220, v236, s61\n"
        "v_mfma_f32_16x16x32_bf16 v[72:75], v[164:167], v[180:183], v[72:75]\n"
        "ds_read_b32 v137, v225\n"
        "v_perm_b32 v226, v220, v236, s62\n"
        "s_waitcnt lgkmcnt(15)\n"
        "v_mfma_f32_16x16x32_bf16 v[84:87], v[164:167], v[188:191], v[84:87]\n"
        "ds_read_b32 v138, v226\n"
        "v_perm_b32 v227, v220, v236, s63\n"
        "v_mfma_f32_16x16x32_bf16 v[84:87], v[160:163], v[184:187], v[84:87]\n"
        "ds_read_b32 v139, v227\n"
        "v_perm_b32 v224, v221, v236, s60\n"
        "v_mfma_f32_16x16x32_bf16 v[80:83], v[152:155], v[184:187], v[80:83]\n"
        "ds_read_b32 v140, v224\n"
        "v_perm_b32 v225, v221, v236, s61\n"
        "v_mfma_f32_16x16x32_bf16 v[80:83], v[156:159], v[188:191], v[80:83]\n"
        "ds_read_b32 v141, v225\n"
        "v_perm_b32 v226, v221, v236, s62\n"
        "v_mfma_f32_16x16x32_bf16 v[76:79], v[148:151], v[188:191], v[76:79]\n"
        "ds_read_b32 v142, v226\n"
        "v_perm_b32 v227, v221, v236, s63\n"
        "v_mfma_f32_16x16x32_bf16 v[76:79], v[144:147], v[184:187], v[76:79]\n"
        "ds_read_b32 v143, v227\n"
        "s_waitcnt lgkmcnt(10)\n"
        "s_barrier\n"
        "v_mfma_f32_16x16x32_bf16 v[88:91], v[144:147], v[192:195], v[88:91]\n"
        "ds_read_b128 v[208:211], v232 offset:16384\n"
        "v_mfma_f32_16x16x32_bf16 v[88:91], v[148:151], v[196:199], v[88:91]\n"
        "ds_read_b128 v[212:215], v233 offset:16384\n"
        "v_mfma_f32_16x16x32_bf16 v[92:95], v[156:159], v[196:199], v[92:95]\n"
        "v_mfma_f32_16x16x32_bf16 v[92:95], v[152:155], v[192:195], v[92:95]\n"
        "ds_read_b128 v[216:219], v234 offset:16384\n"
        "v_mfma_f32_16x16x32_bf16 v[96:99], v[160:163], v[192:195], v[96:99]\n"
        "ds_read_b128 v[220:223], v235 offset:16384\n"
        "v_mfma_f32_16x16x32_bf16 v[96:99], v[164:167], v[196:199], v[96:99]\n"
        "v_mfma_f32_16x16x32_bf16 v[108:111], v[164:167], v[204:207], v[108:111]\n"
        "ds_read_b128 v[176:179], v228 offset:32768\n"
        "v_mfma_f32_16x16x32_bf16 v[108:111], v[160:163], v[200:203], v[108:111]\n"
        "ds_read_b128 v[180:183], v229 offset:32768\n"
        "v_mfma_f32_16x16x32_bf16 v[104:107], v[152:155], v[200:203], v[104:107]\n"
        "v_mfma_f32_16x16x32_bf16 v[104:107], v[156:159], v[204:207], v[104:107]\n"
        "ds_read_b128 v[184:187], v228 offset:34816\n"
        "v_mfma_f32_16x16x32_bf16 v[100:103], v[148:151], v[204:207], v[100:103]\n"
        "ds_read_b128 v[188:191], v229 offset:34816\n"
        "v_mfma_f32_16x16x32_bf16 v[100:103], v[144:147], v[200:203], v[100:103]\n"
        "s_waitcnt lgkmcnt(2)\n"
        "v_mfma_f32_16x16x32_bf16 v[0:3], v[112:115], v[176:179], v[0:3]\n"
        "ds_read_b128 v[192:195], v228 offset:36864\n"
        "v_bitop3_b32 v210, v210, v214, s34 bitop3:0x7e\n"
        "v_mfma_f32_16x16x32_bf16 v[0:3], v[116:119], v[180:183], v[0:3]\n"
        "ds_read_b128 v[196:199], v229 offset:36864\n"
        "v_bitop3_b32 v211, v211, v215, s39 bitop3:0x7e\n"
        "v_mfma_f32_16x16x32_bf16 v[4:7], v[124:127], v[180:183], v[4:7]\n"
        "ds_read_b128 v[200:203], v228 offset:38912\n"
        "v_bitop3_b32 v218, v218, v222, s34 bitop3:0x7e\n"
        "v_mfma_f32_16x16x32_bf16 v[4:7], v[120:123], v[176:179], v[4:7]\n"
        "ds_read_b128 v[204:207], v229 offset:38912\n"
        "v_perm_b32 v224, v208, v236, s60\n"
        "v_mfma_f32_16x16x32_bf16 v[8:11], v[128:131], v[176:179], v[8:11]\n"
        "ds_read_b32 v144, v224\n"
        "v_perm_b32 v225, v208, v236, s61\n"
        "v_mfma_f32_16x16x32_bf16 v[8:11], v[132:135], v[180:183], v[8:11]\n"
        "ds_read_b32 v145, v225\n"
        "v_perm_b32 v226, v208, v236, s62\n"
        "v_mfma_f32_16x16x32_bf16 v[12:15], v[140:143], v[180:183], v[12:15]\n"
        "ds_read_b32 v146, v226\n"
        "v_perm_b32 v227, v208, v236, s63\n"
        "v_mfma_f32_16x16x32_bf16 v[12:15], v[136:139], v[176:179], v[12:15]\n"
        "ds_read_b32 v147, v227\n"
        "v_bitop3_b32 v219, v219, v223, s39 bitop3:0x7e\n"
        "s_waitcnt lgkmcnt(8)\n"
        "v_mfma_f32_16x16x32_bf16 v[28:31], v[136:139], v[184:187], v[28:31]\n"
        "ds_read_b128 v[176:179], v230 offset:32768\n"
        "v_or3_b32 v210, v210, v211, v218\n"
        "v_mfma_f32_16x16x32_bf16 v[28:31], v[140:143], v[188:191], v[28:31]\n"
        "ds_read_b128 v[180:183], v231 offset:32768\n"
        "v_perm_b32 v224, v209, v236, s60\n"
        "v_mfma_f32_16x16x32_bf16 v[24:27], v[132:135], v[188:191], v[24:27]\n"
        "ds_read_b32 v148, v224\n"
        "v_perm_b32 v225, v209, v236, s61\n"
        "v_mfma_f32_16x16x32_bf16 v[24:27], v[128:131], v[184:187], v[24:27]\n"
        "ds_read_b32 v149, v225\n"
        "v_perm_b32 v226, v209, v236, s62\n"
        "v_mfma_f32_16x16x32_bf16 v[20:23], v[120:123], v[184:187], v[20:23]\n"
        "ds_read_b32 v150, v226\n"
        "v_perm_b32 v227, v209, v236, s63\n"
        "v_mfma_f32_16x16x32_bf16 v[20:23], v[124:127], v[188:191], v[20:23]\n"
        "ds_read_b32 v151, v227\n"
        "v_perm_b32 v224, v212, v236, s60\n"
        "v_mfma_f32_16x16x32_bf16 v[16:19], v[116:119], v[188:191], v[16:19]\n"
        "ds_read_b32 v152, v224\n"
        "v_perm_b32 v225, v212, v236, s61\n"
        "v_mfma_f32_16x16x32_bf16 v[16:19], v[112:115], v[184:187], v[16:19]\n"
        "ds_read_b32 v153, v225\n"
        "v_or_b32 v210, v210, v219\n"
        "s_waitcnt lgkmcnt(14)\n"
        "v_mfma_f32_16x16x32_bf16 v[32:35], v[112:115], v[192:195], v[32:35]\n"
        "ds_read_b128 v[184:187], v230 offset:34816\n"
        "v_cmp_ne_u32 vcc, 0, v210\n"
        "v_mfma_f32_16x16x32_bf16 v[32:35], v[116:119], v[196:199], v[32:35]\n"
        "ds_read_b128 v[188:191], v231 offset:34816\n"
        "v_perm_b32 v226, v212, v236, s62\n"
        "v_mfma_f32_16x16x32_bf16 v[36:39], v[124:127], v[196:199], v[36:39]\n"
        "ds_read_b32 v154, v226\n"
        "v_perm_b32 v227, v212, v236, s63\n"
        "v_mfma_f32_16x16x32_bf16 v[36:39], v[120:123], v[192:195], v[36:39]\n"
        "ds_read_b32 v155, v227\n"
        "v_perm_b32 v224, v213, v236, s60\n"
        "v_mfma_f32_16x16x32_bf16 v[40:43], v[128:131], v[192:195], v[40:43]\n"
        "ds_read_b32 v156, v224\n"
        "v_perm_b32 v225, v213, v236, s61\n"
        "v_mfma_f32_16x16x32_bf16 v[40:43], v[132:135], v[196:199], v[40:43]\n"
        "ds_read_b32 v157, v225\n"
        "v_perm_b32 v226, v213, v236, s62\n"
        "v_mfma_f32_16x16x32_bf16 v[44:47], v[140:143], v[196:199], v[44:47]\n"
        "ds_read_b32 v158, v226\n"
        "v_perm_b32 v227, v213, v236, s63\n"
        "v_mfma_f32_16x16x32_bf16 v[44:47], v[136:139], v[192:195], v[44:47]\n"
        "ds_read_b32 v159, v227\n"
        "s_waitcnt lgkmcnt(15)\n"
        "v_mfma_f32_16x16x32_bf16 v[60:63], v[136:139], v[200:203], v[60:63]\n"
        "ds_read_b128 v[192:195], v230 offset:36864\n"
        "s_cbranch_vccnz .Ln_f11\n"
        ".Ln_b11:\n"
        "v_mfma_f32_16x16x32_bf16 v[60:63], v[140:143], v[204:207], v[60:63]\n"
        "ds_read_b128 v[196:199], v231 offset:36864\n"
        "v_perm_b32 v224, v216, v236, s60\n"
        "v_mfma_f32_16x16x32_bf16 v[56:59], v[132:135], v[204:207], v[56:59]\n"
        "ds_read_b32 v160, v224\n"
        "v_perm_b32 v225, v216, v236, s61\n"
        "v_mfma_f32_16x16x32_bf16 v[56:59], v[128:131], v[200:203], v[56:59]\n"
        "ds_read_b32 v161, v225\n"
        "v_perm_b32 v226, v216, v236, s62\n"
        "v_mfma_f32_16x16x32_bf16 v[52:55], v[120:123], v[200:203], v[52:55]\n"
        "ds_read_b32 v162, v226\n"
        "v_perm_b32 v227, v216, v236, s63\n"
        "v_mfma_f32_16x16x32_bf16 v[52:55], v[124:127], v[204:207], v[52:55]\n"
        "ds_read_b32 v163, v227\n"
        "v_perm_b32 v224, v217, v236, s60\n"
        "v_mfma_f32_16x16x32_bf16 v[48:51], v[116:119], v[204:207], v[48:51]\n"
        "ds_read_b32 v164, v224\n"
        "v_perm_b32 v225, v217, v236, s61\n"
        "v_mfma_f32_16x16x32_bf16 v[48:51], v[112:115], v[200:203], v[48:51]\n"
        "ds_read_b32 v165, v225\n"
        "s_waitcnt lgkmcnt(15)\n"
        "v_mfma_f32_16x16x32_bf16 v[64:67], v[112:115], v[176:179], v[64:67]\n"
        "ds_read_b128 v[200:203], v230 offset:38912\n"
        "v_mfma_f32_16x16x32_bf16 v[64:67], v[116:119], v[180:183], v[64:67]\n"
        "ds_read_b128 v[204:207], v231 offset:38912\n"
        "v_perm_b32 v226, v217, v236, s62\n"
        "v_mfma_f32_16x16x32_bf16 v[68:71], v[124:127], v[180:183], v[68:71]\n"
        "ds_read_b32 v166, v226\n"
        "v_perm_b32 v227, v217, v236, s63\n"
        "v_mfma_f32_16x16x32_bf16 v[68:71], v[120:123], v[176:179], v[68:71]\n"
        "ds_read_b32 v167, v227\n"
        "v_perm_b32 v224, v220, v236, s60\n"
        "v_mfma_f32_16x16x32_bf16 v[72:75], v[128:131], v[176:179], v[72:75]\n"
        "ds_read_b32 v168, v224\n"
        "v_perm_b32 v225, v220, v236, s61\n"
        "v_mfma_f32_16x16x32_bf16 v[72:75], v[132:135], v[180:183], v[72:75]\n"
        "ds_read_b32 v169, v225\n"
        "v_perm_b32 v226, v220, v236, s62\n"
        "s_waitcnt lgkmcnt(15)\n"
        "v_mfma_f32_16x16x32_bf16 v[84:87], v[132:135], v[188:191], v[84:87]\n"
        "ds_read_b32 v170, v226\n"
        "v_perm_b32 v227, v220, v236, s63\n"
        "v_mfma_f32_16x16x32_bf16 v[84:87], v[128:131], v[184:187], v[84:87]\n"
        "ds_read_b32 v171, v227\n"
        "v_perm_b32 v224, v221, v236, s60\n"
        "v_mfma_f32_16x16x32_bf16 v[80:83], v[120:123], v[184:187], v[80:83]\n"
        "ds_read_b32 v172, v224\n"
        "v_perm_b32 v225, v221, v236, s61\n"
        "v_mfma_f32_16x16x32_bf16 v[80:83], v[124:127], v[188:191], v[80:83]\n"
        "ds_read_b32 v173, v225\n"
        "v_perm_b32 v226, v221, v236, s62\n"
        "v_mfma_f32_16x16x32_bf16 v[76:79], v[116:119], v[188:191], v[76:79]\n"
        "ds_read_b32 v174, v226\n"
        "v_perm_b32 v227, v221, v236, s63\n"
        "v_mfma_f32_16x16x32_bf16 v[76:79], v[112:115], v[184:187], v[76:79]\n"
        "ds_read_b32 v175, v227\n"
        "s_waitcnt lgkmcnt(10)\n"
        "s_barrier\n"
        "v_mfma_f32_16x16x32_bf16 v[88:91], v[112:115], v[192:195], v[88:91]\n"
        "ds_read_b128 v[208:211], v232 offset:32768\n"
        "v_mfma_f32_16x16x32_bf16 v[88:91], v[116:119], v[196:199], v[88:91]\n"
        "ds_read_b128 v[212:215], v233 offset:32768\n"
        "v_mfma_f32_16x16x32_bf16 v[92:95], v[124:127], v[196:199], v[92:95]\n"
        "v_mfma_f32_16x16x32_bf16 v[92:95], v[120:123], v[192:195], v[92:95]\n"
        "ds_read_b128 v[216:219], v234 offset:32768\n"
        "v_mfma_f32_16x16x32_bf16 v[96:99], v[128:131], v[192:195], v[96:99]\n"
        "ds_read_b128 v[220:223], v235 offset:32768\n"
        "v_mfma_f32_16x16x32_bf16 v[96:99], v[132:135], v[196:199], v[96:99]\n"
        "v_mfma_f32_16x16x32_bf16 v[108:111], v[132:135], v[204:207], v[108:111]\n"
        "ds_read_b128 v[176:179], v228 offset:0\n"
        "v_mfma_f32_16x16x32_bf16 v[108:111], v[128:131], v[200:203], v[108:111]\n"
        "ds_read_b128 v[180:183], v229 offset:0\n"
        "v_mfma_f32_16x16x32_bf16 v[104:107], v[120:123], v[200:203], v[104:107]\n"
        "v_mfma_f32_16x16x32_bf16 v[104:107], v[124:127], v[204:207], v[104:107]\n"
        "ds_read_b128 v[184:187], v228 offset:2048\n"
        "v_mfma_f32_16x16x32_bf16 v[100:103], v[116:119], v[204:207], v[100:103]\n"
        "ds_read_b128 v[188:191], v229 offset:2048\n"
        "v_mfma_f32_16x16x32_bf16 v[100:103], v[112:115], v[200:203], v[100:103]\n"
        "s_waitcnt lgkmcnt(2)\n"
        "v_mfma_f32_16x16x32_bf16 v[0:3], v[144:147], v[176:179], v[0:3]\n"
        "ds_read_b128 v[192:195], v228 offset:4096\n"
        "v_bitop3_b32 v210, v210, v214, s34 bitop3:0x7e\n"
        "v_mfma_f32_16x16x32_bf16 v[0:3], v[148:151], v[180:183], v[0:3]\n"
        "ds_read_b128 v[196:199], v229 offset:4096\n"
        "v_bitop3_b32 v211, v211, v215, s39 bitop3:0x7e\n"
        "v_mfma_f32_16x16x32_bf16 v[4:7], v[156:159], v[180:183], v[4:7]\n"
        "ds_read_b128 v[200:203], v228 offset:6144\n"
        "v_bitop3_b32 v218, v218, v222, s34 bitop3:0x7e\n"
        "v_mfma_f32_16x16x32_bf16 v[4:7], v[152:155], v[176:179], v[4:7]\n"
        "ds_read_b128 v[204:207], v229 offset:6144\n"
        "v_perm_b32 v224, v208, v236, s60\n"
        "v_mfma_f32_16x16x32_bf16 v[8:11], v[160:163], v[176:179], v[8:11]\n"
        "ds_read_b32 v112, v224\n"
        "v_perm_b32 v225, v208, v236, s61\n"
        "v_mfma_f32_16x16x32_bf16 v[8:11], v[164:167], v[180:183], v[8:11]\n"
        "ds_read_b32 v113, v225\n"
        "v_perm_b32 v226, v208, v236, s62\n"
        "v_mfma_f32_16x16x32_bf16 v[12:15], v[172:175], v[180:183], v[12:15]\n"
        "ds_read_b32 v114, v226\n"
        "v_perm_b32 v227, v208, v236, s63\n"
        "v_mfma_f32_16x16x32_bf16 v[12:15], v[168:171], v[176:179], v[12:15]\n"
        "ds_read_b32 v115, v227\n"
        "v_bitop3_b32 v219, v219, v223, s39 bitop3:0x7e\n"
        "s_waitcnt lgkmcnt(8)\n"
        "v_mfma_f32_16x16x32_bf16 v[28:31], v[168:171], v[184:187], v[28:31]\n"
        "ds_read_b128 v[176:179], v230 offset:0\n"
        "v_or3_b32 v210, v210, v211, v218\n"
        "v_mfma_f32_16x16x32_bf16 v[28:31], v[172:175], v[188:191], v[28:31]\n"
        "ds_read_b128 v[180:183], v231 offset:0\n"
        "v_perm_b32 v224, v209, v236, s60\n"
        "v_mfma_f32_16x16x32_bf16 v[24:27], v[164:167], v[188:191], v[24:27]\n"
        "ds_read_b32 v116, v224\n"
        "v_perm_b32 v225, v209, v236, s61\n"
        "v_mfma_f32_16x16x32_bf16 v[24:27], v[160:163], v[184:187], v[24:27]\n"
        "ds_read_b32 v117, v225\n"
        "v_perm_b32 v226, v209, v236, s62\n"
        "v_mfma_f32_16x16x32_bf16 v[20:23], v[152:155], v[184:187], v[20:23]\n"
        "ds_read_b32 v118, v226\n"
        "v_perm_b32 v227, v209, v236, s63\n"
        "v_mfma_f32_16x16x32_bf16 v[20:23], v[156:159], v[188:191], v[20:23]\n"
        "ds_read_b32 v119, v227\n"
        "v_perm_b32 v224, v212, v236, s60\n"
        "v_mfma_f32_16x16x32_bf16 v[16:19], v[148:151], v[188:191], v[16:19]\n"
        "ds_read_b32 v120, v224\n"
        "v_perm_b32 v225, v212, v236, s61\n"
        "v_mfma_f32_16x16x32_bf16 v[16:19], v[144:147], v[184:187], v[16:19]\n"
        "ds_read_b32 v121, v225\n"
        "v_or_b32 v210, v210, v219\n"
        "s_waitcnt lgkmcnt(14)\n"
        "v_mfma_f32_16x16x32_bf16 v[32:35], v[144:147], v[192:195], v[32:35]\n"
        "ds_read_b128 v[184:187], v230 offset:2048\n"
        "v_cmp_ne_u32 vcc, 0, v210\n"
        "v_mfma_f32_16x16x32_bf16 v[32:35], v[148:151], v[196:199], v[32:35]\n"
        "ds_read_b128 v[188:191], v231 offset:2048\n"
        "v_perm_b32 v226, v212, v236, s62\n"
        "v_mfma_f32_16x16x32_bf16 v[36:39], v[156:159], v[196:199], v[36:39]\n"
        "ds_read_b32 v122, v226\n"
        "v_perm_b32 v227, v212, v236, s63\n"
        "v_mfma_f32_16x16x32_bf16 v[36:39], v[152:155], v[192:195], v[36:39]\n"
        "ds_read_b32 v123, v227\n"
        "v_perm_b32 v224, v213, v236, s60\n"
        "v_mfma_f32_16x16x32_bf16 v[40:43], v[160:163], v[192:195], v[40:43]\n"
        "ds_read_b32 v124, v224\n"
        "v_perm_b32 v225, v213, v236, s61\n"
        "v_mfma_f32_16x16x32_bf16 v[40:43], v[164:167], v[196:199], v[40:43]\n"
        "ds_read_b32 v125, v225\n"
        "v_perm_b32 v226, v213, v236, s62\n"
        "v_mfma_f32_16x16x32_bf16 v[44:47], v[172:175], v[196:199], v[44:47]\n"
        "ds_read_b32 v126, v226\n"
        "v_perm_b32 v227, v213, v236, s63\n"
        "v_mfma_f32_16x16x32_bf16 v[44:47], v[168:171], v[192:195], v[44:47]\n"
        "ds_read_b32 v127, v227\n"
        "s_waitcnt lgkmcnt(15)\n"
        "v_mfma_f32_16x16x32_bf16 v[60:63], v[168:171], v[200:203], v[60:63]\n"
        "ds_read_b128 v[192:195], v230 offset:4096\n"
        "s_cbranch_vccnz .Ln_f12\n"
        ".Ln_b12:\n"
        "v_mfma_f32_16x16x32_bf16 v[60:63], v[172:175], v[204:207], v[60:63]\n"
        "ds_read_b128 v[196:199], v231 offset:4096\n"
        "v_perm_b32 v224, v216, v236, s60\n"
        "v_mfma_f32_16x16x32_bf16 v[56:59], v[164:167], v[204:207], v[56:59]\n"
        "ds_read_b32 v128, v224\n"
        "v_perm_b32 v225, v216, v236, s61\n"
        "v_mfma_f32_16x16x32_bf16 v[56:59], v[160:163], v[200:203], v[56:59]\n"
        "ds_read_b32 v129, v225\n"
        "v_perm_b32 v226, v216, v236, s62\n"
        "v_mfma_f32_16x16x32_bf16 v[52:55], v[152:155], v[200:203], v[52:55]\n"
        "ds_read_b32 v130, v226\n"
        "v_perm_b32 v227, v216, v236, s63\n"
        "v_mfma_f32_16x16x32_bf16 v[52:55], v[156:159], v[204:207], v[52:55]\n"
        "ds_read_b32 v131, v227\n"
        "v_perm_b32 v224, v217, v236, s60\n"
        "v_mfma_f32_16x16x32_bf16 v[48:51], v[148:151], v[204:207], v[48:51]\n"
        "ds_read_b32 v132, v224\n"
        "v_perm_b32 v225, v217, v236, s61\n"
        "v_mfma_f32_16x16x32_bf16 v[48:51], v[144:147], v[200:203], v[48:51]\n"
        "ds_read_b32 v133, v225\n"
        "s_waitcnt lgkmcnt(15)\n"
        "v_mfma_f32_16x16x32_bf16 v[64:67], v[144:147], v[176:179], v[64:67]\n"
        "ds_read_b128 v[200:203], v230 offset:6144\n"
        "v_mfma_f32_16x16x32_bf16 v[64:67], v[148:151], v[180:183], v[64:67]\n"
        "ds_read_b128 v[204:207], v231 offset:6144\n"
        "v_perm_b32 v226, v217, v236, s62\n"
        "v_mfma_f32_16x16x32_bf16 v[68:71], v[156:159], v[180:183], v[68:71]\n"
        "ds_read_b32 v134, v226\n"
        "v_perm_b32 v227, v217, v236, s63\n"
        "v_mfma_f32_16x16x32_bf16 v[68:71], v[152:155], v[176:179], v[68:71]\n"
        "ds_read_b32 v135, v227\n"
        "v_perm_b32 v224, v220, v236, s60\n"
        "v_mfma_f32_16x16x32_bf16 v[72:75], v[160:163], v[176:179], v[72:75]\n"
        "ds_read_b32 v136, v224\n"
        "v_perm_b32 v225, v220, v236, s61\n"
        "v_mfma_f32_16x16x32_bf16 v[72:75], v[164:167], v[180:183], v[72:75]\n"
        "ds_read_b32 v137, v225\n"
        "v_perm_b32 v226, v220, v236, s62\n"
        "s_waitcnt lgkmcnt(15)\n"
        "v_mfma_f32_16x16x32_bf16 v[84:87], v[164:167], v[188:191], v[84:87]\n"
        "ds_read_b32 v138, v226\n"
        "v_perm_b32 v227, v220, v236, s63\n"
        "v_mfma_f32_16x16x32_bf16 v[84:87], v[160:163], v[184:187], v[84:87]\n"
        "ds_read_b32 v139, v227\n"
        "v_perm_b32 v224, v221, v236, s60\n"
        "v_mfma_f32_16x16x32_bf16 v[80:83], v[152:155], v[184:187], v[80:83]\n"
        "ds_read_b32 v140, v224\n"
        "v_perm_b32 v225, v221, v236, s61\n"
        "v_mfma_f32_16x16x32_bf16 v[80:83], v[156:159], v[188:191], v[80:83]\n"
        "ds_read_b32 v141, v225\n"
        "v_perm_b32 v226, v221, v236, s62\n"
        "v_mfma_f32_16x16x32_bf16 v[76:79], v[148:151], v[188:191], v[76:79]\n"
        "ds_read_b32 v142, v226\n"
        "v_perm_b32 v227, v221, v236, s63\n"
        "v_mfma_f32_16x16x32_bf16 v[76:79], v[144:147], v[184:187], v[76:79]\n"
        "ds_read_b32 v143, v227\n"
        "s_waitcnt lgkmcnt(10)\n"
        "s_barrier\n"
        "v_mfma_f32_16x16x32_bf16 v[88:91], v[144:147], v[192:195], v[88:91]\n"
        "ds_read_b128 v[208:211], v232 offset:0\n"
        "v_mfma_f32_16x16x32_bf16 v[88:91], v[148:151], v[196:199], v[88:91]\n"
        "ds_read_b128 v[212:215], v233 offset:0\n"
        "v_mfma_f32_16x16x32_bf16 v[92:95], v[156:159], v[196:199], v[92:95]\n"
        "v_mfma_f32_16x16x32_bf16 v[92:95], v[152:155], v[192:195], v[92:95]\n"
        "ds_read_b128 v[216:219], v234 offset:0\n"
        "v_mfma_f32_16x16x32_bf16 v[96:99], v[160:163], v[192:195], v[96:99]\n"
        "ds_read_b128 v[220:223], v235 offset:0\n"
        "v_mfma_f32_16x16x32_bf16 v[96:99], v[164:167], v[196:199], v[96:99]\n"
        "v_mfma_f32_16x16x32_bf16 v[108:111], v[164:167], v[204:207], v[108:111]\n"
        "ds_read_b128 v[176:179], v228 offset:16384\n"
        "v_mfma_f32_16x16x32_bf16 v[108:111], v[160:163], v[200:203], v[108:111]\n"
        "ds_read_b128 v[180:183], v229 offset:16384\n"
        "v_mfma_f32_16x16x32_bf16 v[104:107], v[152:155], v[200:203], v[104:107]\n"
        "v_mfma_f32_16x16x32_bf16 v[104:107], v[156:159], v[204:207], v[104:107]\n"
        "ds_read_b128 v[184:187], v228 offset:18432\n"
        "v_mfma_f32_16x16x32_bf16 v[100:103], v[148:151], v[204:207], v[100:103]\n"
        "ds_read_b128 v[188:191], v229 offset:18432\n"
        "v_mfma_f32_16x16x32_bf16 v[100:103], v[144:147], v[200:203], v[100:103]\n"
        "s_waitcnt lgkmcnt(2)\n"
        "v_mfma_f32_16x16x32_bf16 v[0:3], v[112:115], v[176:179], v[0:3]\n"
        "ds_read_b128 v[192:195], v228 offset:20480\n"
        "v_bitop3_b32 v210, v210, v214, s34 bitop3:0x7e\n"
        "v_mfma_f32_16x16x32_bf16 v[0:3], v[116:119], v[180:183], v[0:3]\n"
        "ds_read_b128 v[196:199], v229 offset:20480\n"
        "v_bitop3_b32 v211, v211, v215, s39 bitop3:0x7e\n"
        "v_mfma_f32_16x16x32_bf16 v[4:7], v[124:127], v[180:183], v[4:7]\n"
        "ds_read_b128 v[200:203], v228 offset:22528\n"
        "v_bitop3_b32 v218, v218, v222, s34 bitop3:0x7e\n"
        "v_mfma_f32_16x16x32_bf16 v[4:7], v[120:123], v[176:179], v[4:7]\n"
        "ds_read_b128 v[204:207], v229 offset:22528\n"
        "v_perm_b32 v224, v208, v236, s60\n"
        "v_mfma_f32_16x16x32_bf16 v[8:11], v[128:131], v[176:179], v[8:11]\n"
        "ds_read_b32 v144, v224\n"
        "v_perm_b32 v225, v208, v236, s61\n"
        "v_mfma_f32_16x16x32_bf16 v[8:11], v[132:135], v[180:183], v[8:11]\n"
        "ds_read_b32 v145, v225\n"
        "v_perm_b32 v226, v208, v236, s62\n"
        "v_mfma_f32_16x16x32_bf16 v[12:15], v[140:143], v[180:183], v[12:15]\n"
        "ds_read_b32 v146, v226\n"
        "v_perm_b32 v227, v208, v236, s63\n"
        "v_mfma_f32_16x16x32_bf16 v[12:15], v[136:139], v[176:179], v[12:15]\n"
        "ds_read_b32 v147, v227\n"
        "v_bitop3_b32 v219, v219, v223, s39 bitop3:0x7e\n"
        "s_waitcnt lgkmcnt(8)\n"
        "v_mfma_f32_16x16x32_bf16 v[28:31], v[136:139], v[184:187], v[28:31]\n"
        "ds_read_b128 v[176:179], v230 offset:16384\n"
        "v_or3_b32 v210, v210, v211, v218\n"
        "v_mfma_f32_16x16x32_bf16 v[28:31], v[140:143], v[188:191], v[28:31]\n"
        "ds_read_b128 v[180:183], v231 offset:16384\n"
        "v_perm_b32 v224, v209, v236, s60\n"
        "v_mfma_f32_16x16x32_bf16 v[24:27], v[132:135], v[188:191], v[24:27]\n"
        "ds_read_b32 v148, v224\n"
        "v_perm_b32 v225, v209, v236, s61\n"
        "v_mfma_f32_16x16x32_bf16 v[24:27], v[128:131], v[184:187], v[24:27]\n"
        "ds_read_b32 v149, v225\n"
        "v_perm_b32 v226, v209, v236, s62\n"
        "v_mfma_f32_16x16x32_bf16 v[20:23], v[120:123], v[184:187], v[20:23]\n"
        "ds_read_b32 v150, v226\n"
        "v_perm_b32 v227, v209, v236, s63\n"
        "v_mfma_f32_16x16x32_bf16 v[20:23], v[124:127], v[188:191], v[20:23]\n"
        "ds_read_b32 v151, v227\n"
        "v_perm_b32 v224, v212, v236, s60\n"
        "v_mfma_f32_16x16x32_bf16 v[16:19], v[116:119], v[188:191], v[16:19]\n"
        "ds_read_b32 v152, v224\n"
        "v_perm_b32 v225, v212, v236, s61\n"
        "v_mfma_f32_16x16x32_bf16 v[16:19], v[112:115], v[184:187], v[16:19]\n"
        "ds_read_b32 v153, v225\n"
        "v_or_b32 v210, v210, v219\n"
        "s_waitcnt lgkmcnt(14)\n"
        "v_mfma_f32_16x16x32_bf16 v[32:35], v[112:115], v[192:195], v[32:35]\n"
        "ds_read_b128 v[184:187], v230 offset:18432\n"
        "v_cmp_ne_u32 vcc, 0, v210\n"
        "v_mfma_f32_16x16x32_bf16 v[32:35], v[116:119], v[196:199], v[32:35]\n"
        "ds_read_b128 v[188:191], v231 offset:18432\n"
        "v_perm_b32 v226, v212, v236, s62\n"
        "v_mfma_f32_16x16x32_bf16 v[36:39], v[124:127], v[196:199], v[36:39]\n"
        "ds_read_b32 v154, v226\n"
        "v_perm_b32 v227, v212, v236, s63\n"
        "v_mfma_f32_16x16x32_bf16 v[36:39], v[120:123], v[192:195], v[36:39]\n"
        "ds_read_b32 v155, v227\n"
        "v_perm_b32 v224, v213, v236, s60\n"
        "v_mfma_f32_16x16x32_bf16 v[40:43], v[128:131], v[192:195], v[40:43]\n"
        "ds_read_b32 v156, v224\n"
        "v_perm_b32 v225, v213, v236, s61\n"
        "v_mfma_f32_16x16x32_bf16 v[40:43], v[132:135], v[196:199], v[40:43]\n"
        "ds_read_b32 v157, v225\n"
        "v_perm_b32 v226, v213, v236, s62\n"
        "v_mfma_f32_16x16x32_bf16 v[44:47], v[140:143], v[196:199], v[44:47]\n"
        "ds_read_b32 v158, v226\n"
        "v_perm_b32 v227, v213, v236, s63\n"
        "v_mfma_f32_16x16x32_bf16 v[44:47], v[136:139], v[192:195], v[44:47]\n"
        "ds_read_b32 v159, v227\n"
        "s_waitcnt lgkmcnt(15)\n"
        "v_mfma_f32_16x16x32_bf16 v[60:63], v[136:139], v[200:203], v[60:63]\n"
        "ds_read_b128 v[192:195], v230 offset:20480\n"
        "s_cbranch_vccnz .Ln_f13\n"
        ".Ln_b13:\n"
        "v_mfma_f32_16x16x32_bf16 v[60:63], v[140:143], v[204:207], v[60:63]\n"
        "ds_read_b128 v[196:199], v231 offset:20480\n"
        "v_perm_b32 v224, v216, v236, s60\n"
        "v_mfma_f32_16x16x32_bf16 v[56:59], v[132:135], v[204:207], v[56:59]\n"
        "ds_read_b32 v160, v224\n"
        "v_perm_b32 v225, v216, v236, s61\n"
        "v_mfma_f32_16x16x32_bf16 v[56:59], v[128:131], v[200:203], v[56:59]\n"
        "ds_read_b32 v161, v225\n"
        "v_perm_b32 v226, v216, v236, s62\n"
        "v_mfma_f32_16x16x32_bf16 v[52:55], v[120:123], v[200:203], v[52:55]\n"
        "ds_read_b32 v162, v226\n"
        "v_perm_b32 v227, v216, v236, s63\n"
        "v_mfma_f32_16x16x32_bf16 v[52:55], v[124:127], v[204:207], v[52:55]\n"
        "ds_read_b32 v163, v227\n"
        "v_perm_b32 v224, v217, v236, s60\n"
        "v_mfma_f32_16x16x32_bf16 v[48:51], v[116:119], v[204:207], v[48:51]\n"
        "ds_read_b32 v164, v224\n"
        "v_perm_b32 v225, v217, v236, s61\n"
        "v_mfma_f32_16x16x32_bf16 v[48:51], v[112:115], v[200:203], v[48:51]\n"
        "ds_read_b32 v165, v225\n"
        "s_waitcnt lgkmcnt(15)\n"
        "v_mfma_f32_16x16x32_bf16 v[64:67], v[112:115], v[176:179], v[64:67]\n"
        "ds_read_b128 v[200:203], v230 offset:22528\n"
        "v_mfma_f32_16x16x32_bf16 v[64:67], v[116:119], v[180:183], v[64:67]\n"
        "ds_read_b128 v[204:207], v231 offset:22528\n"
        "v_perm_b32 v226, v217, v236, s62\n"
        "v_mfma_f32_16x16x32_bf16 v[68:71], v[124:127], v[180:183], v[68:71]\n"
        "ds_read_b32 v166, v226\n"
        "v_perm_b32 v227, v217, v236, s63\n"
        "v_mfma_f32_16x16x32_bf16 v[68:71], v[120:123], v[176:179], v[68:71]\n"
        "ds_read_b32 v167, v227\n"
        "v_perm_b32 v224, v220, v236, s60\n"
        "v_mfma_f32_16x16x32_bf16 v[72:75], v[128:131], v[176:179], v[72:75]\n"
        "ds_read_b32 v168, v224\n"
        "v_perm_b32 v225, v220, v236, s61\n"
        "v_mfma_f32_16x16x32_bf16 v[72:75], v[132:135], v[180:183], v[72:75]\n"
        "ds_read_b32 v169, v225\n"
        "v_perm_b32 v226, v220, v236, s62\n"
        "s_waitcnt lgkmcnt(15)\n"
        "v_mfma_f32_16x16x32_bf16 v[84:87], v[132:135], v[188:191], v[84:87]\n"
        "ds_read_b32 v170, v226\n"
        "v_perm_b32 v227, v220, v236, s63\n"
        "v_mfma_f32_16x16x32_bf16 v[84:87], v[128:131], v[184:187], v[84:87]\n"
        "ds_read_b32 v171, v227\n"
        "v_perm_b32 v224, v221, v236, s60\n"
        "v_mfma_f32_16x16x32_bf16 v[80:83], v[120:123], v[184:187], v[80:83]\n"
        "ds_read_b32 v172, v224\n"
        "v_perm_b32 v225, v221, v236, s61\n"
        "v_mfma_f32_16x16x32_bf16 v[80:83], v[124:127], v[188:191], v[80:83]\n"
        "ds_read_b32 v173, v225\n"
        "v_perm_b32 v226, v221, v236, s62\n"
        "v_mfma_f32_16x16x32_bf16 v[76:79], v[116:119], v[188:191], v[76:79]\n"
        "ds_read_b32 v174, v226\n"
        "v_perm_b32 v227, v221, v236, s63\n"
        "v_mfma_f32_16x16x32_bf16 v[76:79], v[112:115], v[184:187], v[76:79]\n"
        "ds_read_b32 v175, v227\n"
        "s_waitcnt lgkmcnt(10)\n"
        "s_barrier\n"
        "v_mfma_f32_16x16x32_bf16 v[88:91], v[112:115], v[192:195], v[88:91]\n"
        "ds_read_b128 v[208:211], v232 offset:16384\n"
        "v_mfma_f32_16x16x32_bf16 v[88:91], v[116:119], v[196:199], v[88:91]\n"
        "ds_read_b128 v[212:215], v233 offset:16384\n"
        "v_mfma_f32_16x16x32_bf16 v[92:95], v[124:127], v[196:199], v[92:95]\n"
        "v_mfma_f32_16x16x32_bf16 v[92:95], v[120:123], v[192:195], v[92:95]\n"
        "ds_read_b128 v[216:219], v234 offset:16384\n"
        "v_mfma_f32_16x16x32_bf16 v[96:99], v[128:131], v[192:195], v[96:99]\n"
        "ds_read_b128 v[220:223], v235 offset:16384\n"
        "v_mfma_f32_16x16x32_bf16 v[96:99], v[132:135], v[196:199], v[96:99]\n"
        "v_mfma_f32_16x16x32_bf16 v[108:111], v[132:135], v[204:207], v[108:111]\n"
        "ds_read_b128 v[176:179], v228 offset:32768\n"
        "v_mfma_f32_16x16x32_bf16 v[108:111], v[128:131], v[200:203], v[108:111]\n"
        "ds_read_b128 v[180:183], v229 offset:32768\n"
        "v_mfma_f32_16x16x32_bf16 v[104:107], v[120:123], v[200:203], v[104:107]\n"
        "v_mfma_f32_16x16x32_bf16 v[104:107], v[124:127], v[204:207], v[104:107]\n"
        "ds_read_b128 v[184:187], v228 offset:34816\n"
        "v_mfma_f32_16x16x32_bf16 v[100:103], v[116:119], v[204:207], v[100:103]\n"
        "ds_read_b128 v[188:191], v229 offset:34816\n"
        "v_mfma_f32_16x16x32_bf16 v[100:103], v[112:115], v[200:203], v[100:103]\n"
        "s_waitcnt lgkmcnt(2)\n"
        "v_mfma_f32_16x16x32_bf16 v[0:3], v[144:147], v[176:179], v[0:3]\n"
        "ds_read_b128 v[192:195], v228 offset:36864\n"
        "v_bitop3_b32 v210, v210, v214, s34 bitop3:0x7e\n"
        "v_mfma_f32_16x16x32_bf16 v[0:3], v[148:151], v[180:183], v[0:3]\n"
        "ds_read_b128 v[196:199], v229 offset:36864\n"
        "v_bitop3_b32 v211, v211, v215, s39 bitop3:0x7e\n"
        "v_mfma_f32_16x16x32_bf16 v[4:7], v[156:159], v[180:183], v[4:7]\n"
        "ds_read_b128 v[200:203], v228 offset:38912\n"
        "v_bitop3_b32 v218, v218, v222, s34 bitop3:0x7e\n"
        "v_mfma_f32_16x16x32_bf16 v[4:7], v[152:155], v[176:179], v[4:7]\n"
        "ds_read_b128 v[204:207], v229 offset:38912\n"
        "v_perm_b32 v224, v208, v236, s60\n"
        "v_mfma_f32_16x16x32_bf16 v[8:11], v[160:163], v[176:179], v[8:11]\n"
        "ds_read_b32 v112, v224\n"
        "v_perm_b32 v225, v208, v236, s61\n"
        "v_mfma_f32_16x16x32_bf16 v[8:11], v[164:167], v[180:183], v[8:11]\n"
        "ds_read_b32 v113, v225\n"
        ".Ln_tp3:\n"
        "s_cbranch_execz .Ln_ret\n"
        "v_perm_b32 v226, v208, v236, s62\n"
        "v_mfma_f32_16x16x32_bf16 v[12:15], v[172:175], v[180:183], v[12:15]\n"
        "ds_read_b32 v114, v226\n"
        "v_perm_b32 v227, v208, v236, s63\n"
        "v_mfma_f32_16x16x32_bf16 v[12:15], v[168:171], v[176:179], v[12:15]\n"
        "ds_read_b32 v115, v227\n"
        "v_bitop3_b32 v219, v219, v223, s39 bitop3:0x7e\n"
        "s_waitcnt lgkmcnt(8)\n"
        "v_mfma_f32_16x16x32_bf16 v[28:31], v[168:171], v[184:187], v[28:31]\n"
        "ds_read_b128 v[176:179], v230 offset:32768\n"
        "v_or3_b32 v210, v210, v211, v218\n"
        "v_mfma_f32_16x16x32_bf16 v[28:31], v[172:175], v[188:191], v[28:31]\n"
        "ds_read_b128 v[180:183], v231 offset:32768\n"
        "v_perm_b32 v224, v209, v236, s60\n"
        "v_mfma_f32_16x16x32_bf16 v[24:27], v[164:167], v[188:191], v[24:27]\n"
        "ds_read_b32 v116, v224\n"
        "v_perm_b32 v225, v209, v236, s61\n"
        "v_mfma_f32_16x16x32_bf16 v[24:27], v[160:163], v[184:187], v[24:27]\n"
        "ds_read_b32 v117, v225\n"
        "v_perm_b32 v226, v209, v236, s62\n"
        "v_mfma_f32_16x16x32_bf16 v[20:23], v[152:155], v[184:187], v[20:23]\n"
        "ds_read_b32 v118, v226\n"
        "v_perm_b32 v227, v209, v236, s63\n"
        "v_mfma_f32_16x16x32_bf16 v[20:23], v[156:159], v[188:191], v[20:23]\n"
        "ds_read_b32 v119, v227\n"
        "v_perm_b32 v224, v212, v236, s60\n"
        "v_mfma_f32_16x16x32_bf16 v[16:19], v[148:151], v[188:191], v[16:19]\n"
        "ds_read_b32 v120, v224\n"
        "v_perm_b32 v225, v212, v236, s61\n"
        "v_mfma_f32_16x16x32_bf16 v[16:19], v[144:147], v[184:187], v[16:19]\n"
        "ds_read_b32 v121, v225\n"
        "v_or_b32 v210, v210, v219\n"
        "s_waitcnt lgkmcnt(14)\n"
        "v_mfma_f32_16x16x32_bf16 v[32:35], v[144:147], v[192:195], v[32:35]\n"
        "ds_read_b128 v[184:187], v230 offset:34816\n"
        "v_cmp_ne_u32 vcc, 0, v210\n"
        "v_mfma_f32_16x16x32_bf16 v[32:35], v[148:151], v[196:199], v[32:35]\n"
        "ds_read_b128 v[188:191], v231 offset:34816\n"
        "v_perm_b32 v226, v212, v236, s62\n"
        "v_mfma_f32_16x16x32_bf16 v[36:39], v[156:159], v[196:199], v[36:39]\n"
        "ds_read_b32 v122, v226\n"
        "v_perm_b32 v227, v212, v236, s63\n"
        "v_mfma_f32_16x16x32_bf16 v[36:39], v[152:155], v[192:195], v[36:39]\n"
        "ds_read_b32 v123, v227\n"
        "v_perm_b32 v224, v213, v236, s60\n"
        "v_mfma_f32_16x16x32_bf16 v[40:43], v[160:163], v[192:195], v[40:43]\n"
        "ds_read_b32 v124, v224\n"
        "v_perm_b32 v225, v213, v236, s61\n"
        "v_mfma_f32_16x16x32_bf16 v[40:43], v[164:167], v[196:199], v[40:43]\n"
        "ds_read_b32 v125, v225\n"
        "v_perm_b32 v226, v213, v236, s62\n"
        "v_mfma_f32_16x16x32_bf16 v[44:47], v[172:175], v[196:199], v[44:47]\n"
        "ds_read_b32 v126, v226\n"
        "v_perm_b32 v227, v213, v236, s63\n"
        "v_mfma_f32_16x16x32_bf16 v[44:47], v[168:171], v[192:195], v[44:47]\n"
        "ds_read_b32 v127, v227\n"
        "s_waitcnt lgkmcnt(15)\n"
        "v_mfma_f32_16x16x32_bf16 v[60:63], v[168:171], v[200:203], v[60:63]\n"
        "ds_read_b128 v[192:195], v230 offset:36864\n"
        "s_cbranch_vccnz .Ln_f14\n"
        ".Ln_b14:\n"
        "v_mfma_f32_16x16x32_bf16 v[60:63], v[172:175], v[204:207], v[60:63]\n"
        "ds_read_b128 v[196:199], v231 offset:36864\n"
        "v_perm_b32 v224, v216, v236, s60\n"
        "v_mfma_f32_16x16x32_bf16 v[56:59], v[164:167], v[204:207], v[56:59]\n"
        "ds_read_b32 v128, v224\n"
        "v_perm_b32 v225, v216, v236, s61\n"
        "v_mfma_f32_16x16x32_bf16 v[56:59], v[160:163], v[200:203], v[56:59]\n"
        "ds_read_b32 v129, v225\n"
        "v_perm_b32 v226, v216, v236, s62\n"
        "v_mfma_f32_16x16x32_bf16 v[52:55], v[152:155], v[200:203], v[52:55]\n"
        "ds_read_b32 v130, v226\n"
        "v_perm_b32 v227, v216, v236, s63\n"
        "v_mfma_f32_16x16x32_bf16 v[52:55], v[156:159], v[204:207], v[52:55]\n"
        "ds_read_b32 v131, v227\n"
        "v_perm_b32 v224, v217, v236, s60\n"
        "v_mfma_f32_16x16x32_bf16 v[48:51], v[148:151], v[204:207], v[48:51]\n"
        "ds_read_b32 v132, v224\n"
        "v_perm_b32 v225, v217, v236, s61\n"
        "v_mfma_f32_16x16x32_bf16 v[48:51], v[144:147], v[200:203], v[48:51]\n"
        "ds_read_b32 v133, v225\n"
        "s_waitcnt lgkmcnt(15)\n"
        "v_mfma_f32_16x16x32_bf16 v[64:67], v[144:147], v[176:179], v[64:67]\n"
        "ds_read_b128 v[200:203], v230 offset:38912\n"
        "v_mfma_f32_16x16x32_bf16 v[64:67], v[148:151], v[180:183], v[64:67]\n"
        "ds_read_b128 v[204:207], v231 offset:38912\n"
        "v_perm_b32 v226, v217, v236, s62\n"
        "v_mfma_f32_16x16x32_bf16 v[68:71], v[156:159], v[180:183], v[68:71]\n"
        "ds_read_b32 v134, v226\n"
        "v_perm_b32 v227, v217, v236, s63\n"
        "v_mfma_f32_16x16x32_bf16 v[68:71], v[152:155], v[176:179], v[68:71]\n"
        "ds_read_b32 v135, v227\n"
        "v_perm_b32 v224, v220, v236, s60\n"
        "v_mfma_f32_16x16x32_bf16 v[72:75], v[160:163], v[176:179], v[72:75]\n"
        "ds_read_b32 v136, v224\n"
        "v_perm_b32 v225, v220, v236, s61\n"
        "v_mfma_f32_16x16x32_bf16 v[72:75], v[164:167], v[180:183], v[72:75]\n"
        "ds_read_b32 v137, v225\n"
        "v_perm_b32 v226, v220, v236, s62\n"
        "s_waitcnt lgkmcnt(15)\n"
        "v_mfma_f32_16x16x32_bf16 v[84:87], v[164:167], v[188:191], v[84:87]\n"
        "ds_read_b32 v138, v226\n"
        "v_perm_b32 v227, v220, v236, s63\n"
        "v_mfma_f32_16x16x32_bf16 v[84:87], v[160:163], v[184:187], v[84:87]\n"
        "ds_read_b32 v139, v227\n"
        "v_perm_b32 v224, v221, v236, s60\n"
        "v_mfma_f32_16x16x32_bf16 v[80:83], v[152:155], v[184:187], v[80:83]\n"
        "ds_read_b32 v140, v224\n"
        "v_perm_b32 v225, v221, v236, s61\n"
        "v_mfma_f32_16x16x32_bf16 v[80:83], v[156:159], v[188:191], v[80:83]\n"
        "ds_read_b32 v141, v225\n"
        "v_perm_b32 v226, v221, v236, s62\n"
        "v_mfma_f32_16x16x32_bf16 v[76:79], v[148:151], v[188:191], v[76:79]\n"
        "ds_read_b32 v142, v226\n"
        "v_perm_b32 v227, v221, v236, s63\n"
        "v_mfma_f32_16x16x32_bf16 v[76:79], v[144:147], v[184:187], v[76:79]\n"
        "ds_read_b32 v143, v227\n"
        "s_waitcnt lgkmcnt(10)\n"
        "s_barrier\n"
        "v_mfma_f32_16x16x32_bf16 v[88:91], v[144:147], v[192:195], v[88:91]\n"
        "ds_read_b128 v[208:211], v232 offset:32768\n"
        "v_mfma_f32_16x16x32_bf16 v[88:91], v[148:151], v[196:199], v[88:91]\n"
        "ds_read_b128 v[212:215], v233 offset:32768\n"
        "v_mfma_f32_16x16x32_bf16 v[92:95], v[156:159], v[196:199], v[92:95]\n"
        "v_mfma_f32_16x16x32_bf16 v[92:95], v[152:155], v[192:195], v[92:95]\n"
        "ds_read_b128 v[216:219], v234 offset:32768\n"
        "v_mfma_f32_16x16x32_bf16 v[96:99], v[160:163], v[192:195], v[96:99]\n"
        "ds_read_b128 v[220:223], v235 offset:32768\n"
        "v_mfma_f32_16x16x32_bf16 v[96:99], v[164:167], v[196:199], v[96:99]\n"
        "v_mfma_f32_16x16x32_bf16 v[108:111], v[164:167], v[204:207], v[108:111]\n"
        "ds_read_b128 v[176:179], v228 offset:0\n"
        "v_mfma_f32_16x16x32_bf16 v[108:111], v[160:163], v[200:203], v[108:111]\n"
        "ds_read_b128 v[180:183], v229 offset:0\n"
        "v_mfma_f32_16x16x32_bf16 v[104:107], v[152:155], v[200:203], v[104:107]\n"
        "v_mfma_f32_16x16x32_bf16 v[104:107], v[156:159], v[204:207], v[104:107]\n"
        "ds_read_b128 v[184:187], v228 offset:2048\n"
        "v_mfma_f32_16x16x32_bf16 v[100:103], v[148:151], v[204:207], v[100:103]\n"
        "ds_read_b128 v[188:191], v229 offset:2048\n"
        "v_mfma_f32_16x16x32_bf16 v[100:103], v[144:147], v[200:203], v[100:103]\n"
        "s_waitcnt lgkmcnt(2)\n"
        "v_perm_b32 v224, v208, v236, s60\n"
        "v_mfma_f32_16x16x32_bf16 v[0:3], v[112:115], v[176:179], v[0:3]\n"
        "ds_read_b32 v144, v224\n"
        "ds_read_b128 v[192:195], v228 offset:16384\n"
        "v_perm_b32 v225, v208, v236, s61\n"
        "v_mfma_f32_16x16x32_bf16 v[0:3], v[116:119], v[180:183], v[0:3]\n"
        "ds_read_b32 v145, v225\n"
        "ds_read_b128 v[196:199], v229 offset:16384\n"
        "v_perm_b32 v226, v208, v236, s62\n"
        "v_mfma_f32_16x16x32_bf16 v[4:7], v[120:123], v[176:179], v[4:7]\n"
        "ds_read_b32 v146, v226\n"
        "ds_read_b128 v[200:203], v228 offset:18432\n"
        "v_perm_b32 v227, v208, v236, s63\n"
        "v_mfma_f32_16x16x32_bf16 v[4:7], v[124:127], v[180:183], v[4:7]\n"
        "ds_read_b32 v147, v227\n"
        "ds_read_b128 v[204:207], v229 offset:18432\n"
        "v_perm_b32 v224, v209, v236, s60\n"
        "v_mfma_f32_16x16x32_bf16 v[8:11], v[128:131], v[176:179], v[8:11]\n"
        "ds_read_b32 v148, v224\n"
        "v_perm_b32 v225, v209, v236, s61\n"
        "v_bitop3_b32 v210, v210, v214, s34 bitop3:0x7e\n"
        "v_mfma_f32_16x16x32_bf16 v[8:11], v[132:135], v[180:183], v[8:11]\n"
        "ds_read_b32 v149, v225\n"
        "v_perm_b32 v226, v209, v236, s62\n"
        "v_bitop3_b32 v211, v211, v215, s39 bitop3:0x7e\n"
        "v_mfma_f32_16x16x32_bf16 v[12:15], v[136:139], v[176:179], v[12:15]\n"
        "ds_read_b32 v150, v226\n"
        "v_perm_b32 v227, v209, v236, s63\n"
        "v_bitop3_b32 v218, v218, v222, s34 bitop3:0x7e\n"
        "v_mfma_f32_16x16x32_bf16 v[12:15], v[140:143], v[180:183], v[12:15]\n"
        "ds_read_b32 v151, v227\n"
        "v_perm_b32 v224, v212, v236, s60\n"
        "v_bitop3_b32 v219, v219, v223, s39 bitop3:0x7e\n"
        "s_waitcnt lgkmcnt(12)\n"
        "s_barrier\n"
        "v_mfma_f32_16x16x32_bf16 v[16:19], v[112:115], v[184:187], v[16:19]\n"
        "ds_read_b32 v152, v224\n"
        "ds_read_b128 v[176:179], v228 offset:4096\n"
        "v_perm_b32 v225, v212, v236, s61\n"
        "v_mfma_f32_16x16x32_bf16 v[16:19], v[116:119], v[188:191], v[16:19]\n"
        "ds_read_b32 v153, v225\n"
        "ds_read_b128 v[180:183], v229 offset:4096\n"
        "v_perm_b32 v226, v212, v236, s62\n"
        "v_mfma_f32_16x16x32_bf16 v[20:23], v[120:123], v[184:187], v[20:23]\n"
        "ds_read_b32 v154, v226\n"
        "v_perm_b32 v227, v212, v236, s63\n"
        "v_or3_b32 v210, v210, v211, v218\n"
        "v_mfma_f32_16x16x32_bf16 v[20:23], v[124:127], v[188:191], v[20:23]\n"
        "ds_read_b32 v155, v227\n"
        "v_perm_b32 v224, v213, v236, s60\n"
        "v_or_b32 v210, v210, v219\n"
        "v_mfma_f32_16x16x32_bf16 v[24:27], v[128:131], v[184:187], v[24:27]\n"
        "ds_read_b32 v156, v224\n"
        "v_perm_b32 v225, v213, v236, s61\n"
        "v_cmp_ne_u32 vcc, 0, v210\n"
        "v_mfma_f32_16x16x32_bf16 v[24:27], v[132:135], v[188:191], v[24:27]\n"
        "ds_read_b32 v157, v225\n"
        "v_perm_b32 v226, v213, v236, s62\n"
        "v_mfma_f32_16x16x32_bf16 v[28:31], v[136:139], v[184:187], v[28:31]\n"
        "ds_read_b32 v158, v226\n"
        "s_cbranch_vccnz .Ln_f15\n"
        ".Ln_b15:\n"
        "v_perm_b32 v227, v213, v236, s63\n"
        "v_mfma_f32_16x16x32_bf16 v[28:31], v[140:143], v[188:191], v[28:31]\n"
        "ds_read_b32 v159, v227\n"
        "v_perm_b32 v224, v216, v236, s60\n"
        "v_lshlrev_b32 v208, 16, v238\n"
        "s_waitcnt lgkmcnt(15)\n"
        "v_mfma_f32_16x16x32_bf16 v[0:3], v[144:147], v[192:195], v[0:3]\n"
        "ds_read_b32 v160, v224\n"
        "ds_read_b128 v[184:187], v228 offset:20480\n"
        "v_perm_b32 v225, v216, v236, s61\n"
        "s_waitcnt lgkmcnt(12)\n"
        "v_mfma_f32_16x16x32_bf16 v[0:3], v[148:151], v[196:199], v[0:3]\n"
        "ds_read_b32 v161, v225\n"
        "ds_read_b128 v[188:191], v229 offset:20480\n"
        "v_perm_b32 v226, v216, v236, s62\n"
        "v_mfma_f32_16x16x32_bf16 v[16:19], v[144:147], v[200:203], v[16:19]\n"
        "ds_read_b32 v162, v226\n"
        "v_perm_b32 v227, v216, v236, s63\n"
        "v_and_b32 v209, 0xffff0000, v238\n"
        "v_mfma_f32_16x16x32_bf16 v[16:19], v[148:151], v[204:207], v[16:19]\n"
        "ds_read_b32 v163, v227\n"
        "v_perm_b32 v224, v217, v236, s60\n"
        "v_lshlrev_b32 v210, 16, v239\n"
        "s_waitcnt lgkmcnt(10)\n"
        "v_mfma_f32_16x16x32_bf16 v[4:7], v[152:155], v[192:195], v[4:7]\n"
        "ds_read_b32 v164, v224\n"
        "v_perm_b32 v225, v217, v236, s61\n"
        "v_and_b32 v211, 0xffff0000, v239\n"
        "s_waitcnt lgkmcnt(7)\n"
        "v_mfma_f32_16x16x32_bf16 v[4:7], v[156:159], v[196:199], v[4:7]\n"
        "ds_read_b32 v165, v225\n"
        "v_perm_b32 v226, v217, v236, s62\n"
        "v_lshlrev_b32 v212, 16, v240\n"
        "v_mfma_f32_16x16x32_bf16 v[20:23], v[152:155], v[200:203], v[20:23]\n"
        "ds_read_b32 v166, v226\n"
        "v_perm_b32 v227, v217, v236, s63\n"
        "v_and_b32 v213, 0xffff0000, v240\n"
        "v_mfma_f32_16x16x32_bf16 v[20:23], v[156:159], v[204:207], v[20:23]\n"
        "ds_read_b32 v167, v227\n"
        "v_perm_b32 v224, v220, v236, s60\n"
        "v_lshlrev_b32 v214, 16, v241\n"
        "v_mfma_f32_16x16x32_bf16 v[32:35], v[112:115], v[176:179], v[32:35]\n"
        "ds_read_b32 v168, v224\n"
        "v_perm_b32 v225, v220, v236, s61\n"
        "v_and_b32 v215, 0xffff0000, v241\n"
        "v_mfma_f32_16x16x32_bf16 v[32:35], v[116:119], v[180:183], v[32:35]\n"
        "ds_read_b32 v169, v225\n"
        "v_perm_b32 v226, v220, v236, s62\n"
        "v_mul_f32 v0, v0, v208\n"
        "v_mfma_f32_16x16x32_bf16 v[36:39], v[120:123], v[176:179], v[36:39]\n"
        "ds_read_b32 v170, v226\n"
        "v_perm_b32 v227, v220, v236, s63\n"
        "v_mul_f32 v1, v1, v210\n"
        "v_mfma_f32_16x16x32_bf16 v[36:39], v[124:127], v[180:183], v[36:39]\n"
        "ds_read_b32 v171, v227\n"
        "v_perm_b32 v224, v221, v236, s60\n"
        "v_mul_f32 v2, v2, v212\n"
        "v_mfma_f32_16x16x32_bf16 v[40:43], v[128:131], v[176:179], v[40:43]\n"
        "ds_read_b32 v172, v224\n"
        "v_perm_b32 v225, v221, v236, s61\n"
        "v_mul_f32 v3, v3, v214\n"
        "v_mfma_f32_16x16x32_bf16 v[40:43], v[132:135], v[180:183], v[40:43]\n"
        "ds_read_b32 v173, v225\n"
        "v_perm_b32 v226, v221, v236, s62\n"
        "v_mul_f32 v4, v4, v209\n"
        "v_mfma_f32_16x16x32_bf16 v[44:47], v[136:139], v[176:179], v[44:47]\n"
        "ds_read_b32 v174, v226\n"
        "v_perm_b32 v227, v221, v236, s63\n"
        "v_cvt_pk_bf16_f32 v0, v0, v4\n"
        "v_mfma_f32_16x16x32_bf16 v[44:47], v[140:143], v[180:183], v[44:47]\n"
        "ds_read_b32 v175, v227\n"
        "v_mul_f32 v5, v5, v211\n"
        "v_cvt_pk_bf16_f32 v1, v1, v5\n"
        "s_waitcnt lgkmcnt(12)\n"
        "v_mfma_f32_16x16x32_bf16 v[8:11], v[160:163], v[192:195], v[8:11]\n"
        "ds_read_b128 v[176:179], v228 offset:6144\n"
        "v_mul_f32 v6, v6, v213\n"
        "v_cvt_pk_bf16_f32 v2, v2, v6\n"
        "s_waitcnt lgkmcnt(9)\n"
        "v_mfma_f32_16x16x32_bf16 v[8:11], v[164:167], v[196:199], v[8:11]\n"
        "ds_read_b128 v[180:183], v229 offset:6144\n"
        "v_mul_f32 v7, v7, v215\n"
        "v_cvt_pk_bf16_f32 v3, v3, v7\n"
        "v_mfma_f32_16x16x32_bf16 v[24:27], v[160:163], v[200:203], v[24:27]\n"
        "ds_write_b128 v246, v[0:3] offset:0\n"
        "ds_write_b32 v237, v237 offset:0\n"
        "v_mul_f32 v16, v16, v208\n"
        "v_mfma_f32_16x16x32_bf16 v[24:27], v[164:167], v[204:207], v[24:27]\n"
        "v_mul_f32 v17, v17, v210\n"
        "v_mul_f32 v18, v18, v212\n"
        "v_mul_f32 v19, v19, v214\n"
        "s_waitcnt lgkmcnt(8)\n"
        "v_mfma_f32_16x16x32_bf16 v[12:15], v[168:171], v[192:195], v[12:15]\n"
        "v_mul_f32 v20, v20, v209\n"
        "v_cvt_pk_bf16_f32 v16, v16, v20\n"
        "v_mul_f32 v21, v21, v211\n"
        "s_waitcnt lgkmcnt(4)\n"
        "v_mfma_f32_16x16x32_bf16 v[12:15], v[172:175], v[196:199], v[12:15]\n"
        "v_cvt_pk_bf16_f32 v17, v17, v21\n"
        "v_mul_f32 v22, v22, v213\n"
        "v_cvt_pk_bf16_f32 v18, v18, v22\n"
        "v_mfma_f32_16x16x32_bf16 v[28:31], v[168:171], v[200:203], v[28:31]\n"
        "ds_read_b128 v[192:195], v228 offset:22528\n"
        "v_mul_f32 v23, v23, v215\n"
        "v_cvt_pk_bf16_f32 v19, v19, v23\n"
        "v_mfma_f32_16x16x32_bf16 v[28:31], v[172:175], v[204:207], v[28:31]\n"
        "ds_read_b128 v[196:199], v229 offset:22528\n"
        "ds_write_b128 v246, v[16:19] offset:8192\n"
        "ds_write_b32 v237, v237 offset:256\n"
        "v_mfma_f32_16x16x32_bf16 v[32:35], v[144:147], v[184:187], v[32:35]\n"
        "ds_read_b128 v[200:203], v230 offset:0\n"
        "v_lshlrev_b32 v216, 16, v242\n"
        "v_and_b32 v217, 0xffff0000, v242\n"
        "v_mfma_f32_16x16x32_bf16 v[32:35], v[148:151], v[188:191], v[32:35]\n"
        "ds_read_b128 v[204:207], v231 offset:0\n"
        "v_lshlrev_b32 v218, 16, v243\n"
        "v_and_b32 v219, 0xffff0000, v243\n"
        "v_mfma_f32_16x16x32_bf16 v[36:39], v[152:155], v[184:187], v[36:39]\n"
        "v_lshlrev_b32 v220, 16, v244\n"
        "v_and_b32 v221, 0xffff0000, v244\n"
        "v_lshlrev_b32 v222, 16, v245\n"
        "v_mfma_f32_16x16x32_bf16 v[36:39], v[156:159], v[188:191], v[36:39]\n"
        "v_and_b32 v223, 0xffff0000, v245\n"
        "v_mul_f32 v8, v8, v216\n"
        "v_mul_f32 v9, v9, v217\n"
        "v_mfma_f32_16x16x32_bf16 v[40:43], v[160:163], v[184:187], v[40:43]\n"
        "v_mul_f32 v10, v10, v218\n"
        "v_mul_f32 v11, v11, v219\n"
        "v_cvt_pk_bf16_f32 v8, v8, v9\n"
        "v_mfma_f32_16x16x32_bf16 v[40:43], v[164:167], v[188:191], v[40:43]\n"
        "v_cvt_pk_bf16_f32 v9, v10, v11\n"
        "v_mfma_f32_16x16x32_bf16 v[44:47], v[168:171], v[184:187], v[44:47]\n"
        "ds_write_b64 v247, v[8:9] offset:0\n"
        "v_mul_f32 v12, v12, v220\n"
        "v_mul_f32 v13, v13, v221\n"
        "v_mfma_f32_16x16x32_bf16 v[44:47], v[172:175], v[188:191], v[44:47]\n"
        "v_mul_f32 v14, v14, v222\n"
        "v_mul_f32 v15, v15, v223\n"
        "v_cvt_pk_bf16_f32 v12, v12, v13\n"
        "s_waitcnt lgkmcnt(9)\n"
        "v_mfma_f32_16x16x32_bf16 v[48:51], v[112:115], v[176:179], v[48:51]\n"
        "ds_read_b128 v[184:187], v230 offset:16384\n"
        "v_cvt_pk_bf16_f32 v13, v14, v15\n"
        "v_mfma_f32_16x16x32_bf16 v[48:51], v[116:119], v[180:183], v[48:51]\n"
        "ds_read_b128 v[188:191], v231 offset:16384\n"
        "ds_write_b64 v248, v[12:13] offset:0\n"
        "ds_write_b32 v237, v237 offset:512\n"
        "s_waitcnt lgkmcnt(9)\n"
        "v_mfma_f32_16x16x32_bf16 v[48:51], v[144:147], v[192:195], v[48:51]\n"
        "v_mul_f32 v24, v24, v216\n"
        "v_mul_f32 v25, v25, v217\n"
        "v_mul_f32 v26, v26, v218\n"
        "v_mfma_f32_16x16x32_bf16 v[48:51], v[148:151], v[196:199], v[48:51]\n"
        "v_mul_f32 v27, v27, v219\n"
        "v_cvt_pk_bf16_f32 v24, v24, v25\n"
        "v_cvt_pk_bf16_f32 v25, v26, v27\n"
        "v_mfma_f32_16x16x32_bf16 v[52:55], v[120:123], v[176:179], v[52:55]\n"
        "ds_write_b64 v247, v[24:25] offset:8192\n"
        "v_mul_f32 v28, v28, v220\n"
        "v_mul_f32 v29, v29, v221\n"
        "v_mfma_f32_16x16x32_bf16 v[52:55], v[124:127], v[180:183], v[52:55]\n"
        "v_mul_f32 v30, v30, v222\n"
        "v_mul_f32 v31, v31, v223\n"
        "v_cvt_pk_bf16_f32 v28, v28, v29\n"
        "v_mfma_f32_16x16x32_bf16 v[52:55], v[152:155], v[192:195], v[52:55]\n"
        "v_cvt_pk_bf16_f32 v29, v30, v31\n"
        "v_mfma_f32_16x16x32_bf16 v[52:55], v[156:159], v[196:199], v[52:55]\n"
        "ds_write_b64 v248, v[28:29] offset:8192\n"
        "ds_write_b32 v237, v237 offset:768\n"
        "v_mul_f32 v32, v32, v208\n"
        "v_mfma_f32_16x16x32_bf16 v[56:59], v[128:131], v[176:179], v[56:59]\n"
        "v_mul_f32 v33, v33, v210\n"
        "v_mul_f32 v34, v34, v212\n"
        "v_mul_f32 v35, v35, v214\n"
        "v_mfma_f32_16x16x32_bf16 v[56:59], v[132:135], v[180:183], v[56:59]\n"
        "v_mul_f32 v36, v36, v209\n"
        "v_cvt_pk_bf16_f32 v32, v32, v36\n"
        "v_mul_f32 v37, v37, v211\n"
        "v_mfma_f32_16x16x32_bf16 v[56:59], v[160:163], v[192:195], v[56:59]\n"
        "v_cvt_pk_bf16_f32 v33, v33, v37\n"
        "v_mul_f32 v38, v38, v213\n"
        "v_cvt_pk_bf16_f32 v34, v34, v38\n"
        "v_mfma_f32_16x16x32_bf16 v[56:59], v[164:167], v[196:199], v[56:59]\n"
        "v_mul_f32 v39, v39, v215\n"
        "v_cvt_pk_bf16_f32 v35, v35, v39\n"
        "v_mfma_f32_16x16x32_bf16 v[60:63], v[136:139], v[176:179], v[60:63]\n"
        "ds_write_b128 v246, v[32:35] offset:16384\n"
        "v_mul_f32 v40, v40, v216\n"
        "v_mul_f32 v41, v41, v217\n"
        "v_mfma_f32_16x16x32_bf16 v[60:63], v[140:143], v[180:183], v[60:63]\n"
        "v_mul_f32 v42, v42, v218\n"
        "v_mul_f32 v43, v43, v219\n"
        "v_cvt_pk_bf16_f32 v40, v40, v41\n"
        "v_mfma_f32_16x16x32_bf16 v[60:63], v[168:171], v[192:195], v[60:63]\n"
        "ds_read_b128 v[176:179], v230 offset:2048\n"
        "v_cvt_pk_bf16_f32 v41, v42, v43\n"
        "v_mfma_f32_16x16x32_bf16 v[60:63], v[172:175], v[196:199], v[60:63]\n"
        "ds_read_b128 v[180:183], v231 offset:2048\n"
        "ds_write_b64 v247, v[40:41] offset:16384\n"
        "v_mul_f32 v44, v44, v220\n"
        "s_waitcnt lgkmcnt(12)\n"
        "v_mfma_f32_16x16x32_bf16 v[64:67], v[112:115], v[200:203], v[64:67]\n"
        "ds_read_b128 v[192:195], v230 offset:18432\n"
        "v_mul_f32 v45, v45, v221\n"
        "v_mul_f32 v46, v46, v222\n"
        "v_mfma_f32_16x16x32_bf16 v[64:67], v[116:119], v[204:207], v[64:67]\n"
        "ds_read_b128 v[196:199], v231 offset:18432\n"
        "v_mul_f32 v47, v47, v223\n"
        "v_cvt_pk_bf16_f32 v44, v44, v45\n"
        "s_waitcnt lgkmcnt(11)\n"
        "v_mfma_f32_16x16x32_bf16 v[64:67], v[144:147], v[184:187], v[64:67]\n"
        "v_cvt_pk_bf16_f32 v45, v46, v47\n"
        "v_mfma_f32_16x16x32_bf16 v[64:67], v[148:151], v[188:191], v[64:67]\n"
        "ds_write_b64 v248, v[44:45] offset:16384\n"
        "ds_write_b32 v237, v237 offset:1024\n"
        "v_mul_f32 v48, v48, v208\n"
        "v_mfma_f32_16x16x32_bf16 v[68:71], v[120:123], v[200:203], v[68:71]\n"
        "v_mul_f32 v49, v49, v210\n"
        "v_mul_f32 v50, v50, v212\n"
        "v_mul_f32 v51, v51, v214\n"
        "v_mfma_f32_16x16x32_bf16 v[68:71], v[124:127], v[204:207], v[68:71]\n"
        "v_mul_f32 v52, v52, v209\n"
        "v_cvt_pk_bf16_f32 v48, v48, v52\n"
        "v_mul_f32 v53, v53, v211\n"
        "v_mfma_f32_16x16x32_bf16 v[68:71], v[152:155], v[184:187], v[68:71]\n"
        "v_cvt_pk_bf16_f32 v49, v49, v53\n"
        "v_mul_f32 v54, v54, v213\n"
        "v_cvt_pk_bf16_f32 v50, v50, v54\n"
        "v_mfma_f32_16x16x32_bf16 v[68:71], v[156:159], v[188:191], v[68:71]\n"
        "v_mul_f32 v55, v55, v215\n"
        "v_cvt_pk_bf16_f32 v51, v51, v55\n"
        "v_mfma_f32_16x16x32_bf16 v[72:75], v[128:131], v[200:203], v[72:75]\n"
        "ds_write_b128 v246, v[48:51] offset:24576\n"
        "v_mul_f32 v56, v56, v216\n"
        "v_mul_f32 v57, v57, v217\n"
        "v_mfma_f32_16x16x32_bf16 v[72:75], v[132:135], v[204:207], v[72:75]\n"
        "v_mul_f32 v58, v58, v218\n"
        "v_mul_f32 v59, v59, v219\n"
        "v_cvt_pk_bf16_f32 v56, v56, v57\n"
        "v_mfma_f32_16x16x32_bf16 v[72:75], v[160:163], v[184:187], v[72:75]\n"
        "ds_read_b128 v[200:203], v230 offset:4096\n"
        "v_cvt_pk_bf16_f32 v57, v58, v59\n"
        "v_mfma_f32_16x16x32_bf16 v[72:75], v[164:167], v[188:191], v[72:75]\n"
        "ds_read_b128 v[204:207], v231 offset:4096\n"
        "ds_write_b64 v247, v[56:57] offset:24576\n"
        "v_mul_f32 v60, v60, v220\n"
        "s_waitcnt lgkmcnt(9)\n"
        "v_mfma_f32_16x16x32_bf16 v[76:79], v[112:115], v[176:179], v[76:79]\n"
        "ds_read_b128 v[184:187], v230 offset:20480\n"
        "v_mul_f32 v61, v61, v221\n"
        "v_mul_f32 v62, v62, v222\n"
        "v_mfma_f32_16x16x32_bf16 v[76:79], v[116:119], v[180:183], v[76:79]\n"
        "ds_read_b128 v[188:191], v231 offset:20480\n"
        "v_mul_f32 v63, v63, v223\n"
        "v_cvt_pk_bf16_f32 v60, v60, v61\n"
        "s_waitcnt lgkmcnt(8)\n"
        "v_mfma_f32_16x16x32_bf16 v[76:79], v[144:147], v[192:195], v[76:79]\n"
        "v_cvt_pk_bf16_f32 v61, v62, v63\n"
        "v_mfma_f32_16x16x32_bf16 v[76:79], v[148:151], v[196:199], v[76:79]\n"
        "ds_write_b64 v248, v[60:61] offset:24576\n"
        "ds_write_b32 v237, v237 offset:1280\n"
        "v_mul_f32 v64, v64, v208\n"
        "v_mfma_f32_16x16x32_bf16 v[80:83], v[120:123], v[176:179], v[80:83]\n"
        "v_mul_f32 v65, v65, v210\n"
        "v_mul_f32 v66, v66, v212\n"
        "v_mul_f32 v67, v67, v214\n"
        "v_mfma_f32_16x16x32_bf16 v[80:83], v[124:127], v[180:183], v[80:83]\n"
        "v_mul_f32 v68, v68, v209\n"
        "v_cvt_pk_bf16_f32 v64, v64, v68\n"
        "v_mul_f32 v69, v69, v211\n"
        "v_mfma_f32_16x16x32_bf16 v[80:83], v[152:155], v[192:195], v[80:83]\n"
        "v_cvt_pk_bf16_f32 v65, v65, v69\n"
        "v_mul_f32 v70, v70, v213\n"
        "v_cvt_pk_bf16_f32 v66, v66, v70\n"
        "v_mfma_f32_16x16x32_bf16 v[80:83], v[156:159], v[196:199], v[80:83]\n"
        "v_mul_f32 v71, v71, v215\n"
        "v_cvt_pk_bf16_f32 v67, v67, v71\n"
        "v_mfma_f32_16x16x32_bf16 v[84:87], v[128:131], v[176:179], v[84:87]\n"
        "ds_write_b128 v246, v[64:67] offset:32768\n"
        "v_mul_f32 v72, v72, v216\n"
        "v_mul_f32 v73, v73, v217\n"
        "v_mfma_f32_16x16x32_bf16 v[84:87], v[132:135], v[180:183], v[84:87]\n"
        "v_mul_f32 v74, v74, v218\n"
        "v_mul_f32 v75, v75, v219\n"
        "v_cvt_pk_bf16_f32 v72, v72, v73\n"
        "v_mfma_f32_16x16x32_bf16 v[84:87], v[160:163], v[192:195], v[84:87]\n"
        "ds_read_b128 v[176:179], v230 offset:6144\n"
        "v_cvt_pk_bf16_f32 v73, v74, v75\n"
        "v_mfma_f32_16x16x32_bf16 v[84:87], v[164:167], v[196:199], v[84:87]\n"
        "ds_read_b128 v[180:183], v231 offset:6144\n"
        "ds_write_b64 v247, v[72:73] offset:32768\n"
        "ds_write_b32 v237, v237 offset:1536\n"
        "s_waitcnt lgkmcnt(10)\n"
        "v_mfma_f32_16x16x32_bf16 v[88:91], v[112:115], v[200:203], v[88:91]\n"
        "ds_read_b128 v[192:195], v230 offset:22528\n"
        "v_mul_f32 v76, v76, v208\n"
        "v_mul_f32 v77, v77, v210\n"
        "v_mfma_f32_16x16x32_bf16 v[88:91], v[116:119], v[204:207], v[88:91]\n"
        "ds_read_b128 v[196:199], v231 offset:22528\n"
        "v_mul_f32 v78, v78, v212\n"
        "v_mul_f32 v79, v79, v214\n"
        "s_waitcnt lgkmcnt(9)\n"
        "v_mfma_f32_16x16x32_bf16 v[88:91], v[144:147], v[184:187], v[88:91]\n"
        "v_mul_f32 v80, v80, v209\n"
        "v_cvt_pk_bf16_f32 v76, v76, v80\n"
        "v_mul_f32 v81, v81, v211\n"
        "v_mfma_f32_16x16x32_bf16 v[88:91], v[148:151], v[188:191], v[88:91]\n"
        "v_cvt_pk_bf16_f32 v77, v77, v81\n"
        "v_mul_f32 v82, v82, v213\n"
        "v_cvt_pk_bf16_f32 v78, v78, v82\n"
        "v_mfma_f32_16x16x32_bf16 v[92:95], v[120:123], v[200:203], v[92:95]\n"
        "v_mul_f32 v83, v83, v215\n"
        "v_cvt_pk_bf16_f32 v79, v79, v83\n"
        "v_mfma_f32_16x16x32_bf16 v[92:95], v[124:127], v[204:207], v[92:95]\n"
        "ds_write_b128 v246, v[76:79] offset:40960\n"
        "v_mul_f32 v84, v84, v216\n"
        "v_mul_f32 v85, v85, v217\n"
        "v_mfma_f32_16x16x32_bf16 v[92:95], v[152:155], v[184:187], v[92:95]\n"
        "v_mul_f32 v86, v86, v218\n"
        "v_mul_f32 v87, v87, v219\n"
        "v_cvt_pk_bf16_f32 v84, v84, v85\n"
        "v_mfma_f32_16x16x32_bf16 v[92:95], v[156:159], v[188:191], v[92:95]\n"
        "v_cvt_pk_bf16_f32 v85, v86, v87\n"
        "v_mfma_f32_16x16x32_bf16 v[96:99], v[128:131], v[200:203], v[96:99]\n"
        "ds_write_b64 v247, v[84:85] offset:40960\n"
        "ds_write_b32 v237, v237 offset:1792\n"
        "v_mul_f32 v88, v88, v208\n"
        "v_mfma_f32_16x16x32_bf16 v[96:99], v[132:135], v[204:207], v[96:99]\n"
        "v_mul_f32 v89, v89, v210\n"
        "v_mul_f32 v90, v90, v212\n"
        "v_mul_f32 v91, v91, v214\n"
        "v_mfma_f32_16x16x32_bf16 v[96:99], v[160:163], v[184:187], v[96:99]\n"
        "v_mul_f32 v92, v92, v209\n"
        "v_cvt_pk_bf16_f32 v88, v88, v92\n"
        "v_mul_f32 v93, v93, v211\n"
        "v_mfma_f32_16x16x32_bf16 v[96:99], v[164:167], v[188:191], v[96:99]\n"
        "v_cvt_pk_bf16_f32 v89, v89, v93\n"
        "v_mul_f32 v94, v94, v213\n"
        "v_cvt_pk_bf16_f32 v90, v90, v94\n"
        "s_waitcnt lgkmcnt(7)\n"
        "v_mfma_f32_16x16x32_bf16 v[100:103], v[112:115], v[176:179], v[100:103]\n"
        "v_mul_f32 v95, v95, v215\n"
        "v_cvt_pk_bf16_f32 v91, v91, v95\n"
        "v_mfma_f32_16x16x32_bf16 v[100:103], v[116:119], v[180:183], v[100:103]\n"
        "ds_write_b128 v246, v[88:91] offset:49152\n"
        "v_mul_f32 v96, v96, v216\n"
        "v_mul_f32 v97, v97, v217\n"
        "s_waitcnt lgkmcnt(4)\n"
        "v_mfma_f32_16x16x32_bf16 v[100:103], v[144:147], v[192:195], v[100:103]\n"
        "v_mul_f32 v98, v98, v218\n"
        "v_mul_f32 v99, v99, v219\n"
        "v_cvt_pk_bf16_f32 v96, v96, v97\n"
        "v_mfma_f32_16x16x32_bf16 v[100:103], v[148:151], v[196:199], v[100:103]\n"
        "v_cvt_pk_bf16_f32 v97, v98, v99\n"
        "v_mfma_f32_16x16x32_bf16 v[104:107], v[120:123], v[176:179], v[104:107]\n"
        "ds_write_b64 v247, v[96:97] offset:49152\n"
        "ds_write_b32 v237, v237 offset:2048\n"
        "v_mfma_f32_16x16x32_bf16 v[104:107], v[124:127], v[180:183], v[104:107]\n"
        "v_mfma_f32_16x16x32_bf16 v[104:107], v[152:155], v[192:195], v[104:107]\n"
        "v_mfma_f32_16x16x32_bf16 v[104:107], v[156:159], v[196:199], v[104:107]\n"
        "v_mfma_f32_16x16x32_bf16 v[108:111], v[128:131], v[176:179], v[108:111]\n"
        "v_mul_f32 v100, v100, v208\n"
        "v_mul_f32 v101, v101, v210\n"
        "v_mul_f32 v102, v102, v212\n"
        "v_mfma_f32_16x16x32_bf16 v[108:111], v[132:135], v[180:183], v[108:111]\n"
        "v_mul_f32 v103, v103, v214\n"
        "v_mfma_f32_16x16x32_bf16 v[108:111], v[160:163], v[192:195], v[108:111]\n"
        "v_mfma_f32_16x16x32_bf16 v[108:111], v[164:167], v[196:199], v[108:111]\n"
        "v_mul_f32 v104, v104, v209\n"
        "v_cvt_pk_bf16_f32 v100, v100, v104\n"
        "v_mul_f32 v105, v105, v211\n"
        "s_nop 7\n"
        "v_cvt_pk_bf16_f32 v101, v101, v105\n"
        "v_mul_f32 v106, v106, v213\n"
        "v_cvt_pk_bf16_f32 v102, v102, v106\n"
        "v_mul_f32 v107, v107, v215\n"
        "v_cvt_pk_bf16_f32 v103, v103, v107\n"
        "ds_write_b128 v246, v[100:103] offset:57344\n"
        "v_mul_f32 v108, v108, v216\n"
        "v_mul_f32 v109, v109, v217\n"
        "v_mul_f32 v110, v110, v218\n"
        "v_mul_f32 v111, v111, v219\n"
        "v_cvt_pk_bf16_f32 v108, v108, v109\n"
        "v_cvt_pk_bf16_f32 v109, v110, v111\n"
        "ds_write_b64 v247, v[108:109] offset:57344\n"
        "ds_write_b32 v237, v237 offset:2304\n"
        "s_endpgm\n"
        ".Ln_f1:\n"
        "s_movk_i32 s77, 1\n"
        "s_getpc_b64 s[70:71]\n"
        "s_branch .Ln_rec1\n"
        "s_branch .Ln_b1\n"
        ".Ln_f2:\n"
        "s_movk_i32 s77, 2\n"
        "s_getpc_b64 s[70:71]\n"
        "s_branch .Ln_rec0\n"
        "s_branch .Ln_b2\n"
        ".Ln_f3:\n"
        "s_movk_i32 s77, 3\n"
        "s_getpc_b64 s[70:71]\n"
        "s_branch .Ln_rec1\n"
        "s_branch .Ln_b3\n"
        ".Ln_f4:\n"
        "s_movk_i32 s77, 4\n"
        "s_getpc_b64 s[70:71]\n"
        "s_branch .Ln_rec0\n"
        "s_branch .Ln_b4\n"
        ".Ln_f5:\n"
        "s_movk_i32 s77, 5\n"
        "s_getpc_b64 s[70:71]\n"
        "s_branch .Ln_rec1\n"
        "s_branch .Ln_b5\n"
        ".Ln_f6:\n"
        "s_movk_i32 s77, 6\n"
        "s_getpc_b64 s[70:71]\n"
        "s_branch .Ln_rec0\n"
        "s_branch .Ln_b6\n"
        ".Ln_f7:\n"
        "s_movk_i32 s77, 7\n"
        "s_getpc_b64 s[70:71]\n"
        "s_branch .Ln_rec1\n"
        "s_branch .Ln_b7\n"
        ".Ln_f8:\n"
        "s_movk_i32 s77, 8\n"
        "s_getpc_b64 s[70:71]\n"
        "s_branch .Ln_rec0\n"
        "s_branch .Ln_b8\n"
        ".Ln_f9:\n"
        "s_movk_i32 s77, 9\n"
        "s_getpc_b64 s[70:71]\n"
        "s_branch .Ln_rec1\n"
        "s_branch .Ln_b9\n"
        ".Ln_f10:\n"
        "s_movk_i32 s77, 10\n"
        "s_getpc_b64 s[70:71]\n"
        "s_branch .Ln_rec0\n"
        "s_branch .Ln_b10\n"
        ".Ln_f11:\n"
        "s_movk_i32 s77, 11\n"
        "s_getpc_b64 s[70:71]\n"
        "s_branch .Ln_rec1\n"
        "s_branch .Ln_b11\n"
        ".Ln_f12:\n"
        "s_movk_i32 s77, 12\n"
        "s_getpc_b64 s[70:71]\n"
        "s_branch .Ln_rec0\n"
        "s_branch .Ln_b12\n"
        ".Ln_f13:\n"
        "s_movk_i32 s77, 13\n"
        "s_getpc_b64 s[70:71]\n"
        "s_branch .Ln_rec1\n"
        "s_branch .Ln_b13\n"
        ".Ln_f14:\n"
        "s_movk_i32 s77, 14\n"
        "s_getpc_b64 s[70:71]\n"
        "s_branch .Ln_rec0\n"
        "s_branch .Ln_b14\n"
        ".Ln_f15:\n"
        "s_movk_i32 s77, 15\n"
        "s_getpc_b64 s[70:71]\n"
        "s_branch .Ln_rec1\n"
        "s_branch .Ln_b15\n"
        ".Ln_f0:\n"
        "s_movk_i32 s77, 0\n"
        "s_waitcnt vmcnt(0)\n"
        "v_mov_b32 v144, v208\n"
        "v_mov_b32 v145, v209\n"
        "v_mov_b32 v146, v210\n"
        "v_mov_b32 v147, v211\n"
        "v_mov_b32 v148, v212\n"
        "v_mov_b32 v149, v213\n"
        "v_mov_b32 v150, v214\n"
        "v_mov_b32 v151, v215\n"
        "v_mov_b32 v152, v216\n"
        "v_mov_b32 v153, v217\n"
        "v_mov_b32 v154, v218\n"
        "v_mov_b32 v155, v219\n"
        "v_mov_b32 v156, v220\n"
        "v_mov_b32 v157, v221\n"
        "v_mov_b32 v158, v222\n"
        "v_mov_b32 v159, v223\n"
        "s_getpc_b64 s[70:71]\n"
        "s_branch .Ln_rec0\n"
        "v_mov_b32 v64, v208\n"
        "v_mov_b32 v208, v144\n"
        "v_mov_b32 v65, v209\n"
        "v_mov_b32 v209, v145\n"
        "v_mov_b32 v66, v210\n"
        "v_mov_b32 v210, v146\n"
        "v_mov_b32 v67, v211\n"
        "v_mov_b32 v211, v147\n"
        "v_mov_b32 v68, v212\n"
        "v_mov_b32 v212, v148\n"
        "v_mov_b32 v69, v213\n"
        "v_mov_b32 v213, v149\n"
        "v_mov_b32 v70, v214\n"
        "v_mov_b32 v214, v150\n"
        "v_mov_b32 v71, v215\n"
        "v_mov_b32 v215, v151\n"
        "v_mov_b32 v72, v216\n"
        "v_mov_b32 v216, v152\n"
        "v_mov_b32 v73, v217\n"
        "v_mov_b32 v217, v153\n"
        "v_mov_b32 v74, v218\n"
        "v_mov_b32 v218, v154\n"
        "v_mov_b32 v75, v219\n"
        "v_mov_b32 v219, v155\n"
        "v_mov_b32 v76, v220\n"
        "v_mov_b32 v220, v156\n"
        "v_mov_b32 v77, v221\n"
        "v_mov_b32 v221, v157\n"
        "v_mov_b32 v78, v222\n"
        "v_mov_b32 v222, v158\n"
        "v_mov_b32 v79, v223\n"
        "v_mov_b32 v223, v159\n"
        "s_branch .Ln_b0\n"
        ".Ln_rec0:\n"
        "s_waitcnt lgkmcnt(0)\n"
        "s_lshl_b32 s76, s77, 14\n"
        "s_add_u32 s76, s76, s64\n"
        "3:\n"
        "s_add_u32 s73, s73, 1\n"
        "s_cmp_lt_u32 s73, s74\n"
        "s_cbranch_scc1 2f\n"
        "s_mov_b32 s74, 0\n"
        "s_mov_b32 s78, 0\n"
        "1:\n"
        "s_lshl_b32 s72, s66, 2\n"
        "s_add_u32 s72, s72, 2\n"
        "s_cmp_eq_u32 s78, 0\n"
        "s_cselect_b32 s72, s68, s72\n"
        "s_cmp_eq_u32 s78, 2\n"
        "s_cselect_b32 s72, 3, s72\n"
        "s_lshl_b32 s35, s77, 1\n"
        "s_lshr_b32 s36, s33, 3\n"
        "s_add_u32 s35, s35, s36\n"
        "s_mul_i32 s35, s35, 7\n"
        "s_add_u32 s72, s72, s35\n"
        "s_mul_i32 s35, s72, 9363\n"
        "s_lshr_b32 s35, s35, 16\n"
        "s_mul_i32 s36, s35, 7\n"
        "s_sub_u32 s36, s72, s36\n"
        "s_and_b32 s79, s35, 1\n"
        "s_lshl_b32 s79, s79, 3\n"
        "s_and_b32 s37, s33, 7\n"
        "s_or_b32 s79, s79, s37\n"
        "s_lshr_b32 s35, s35, 1\n"
        "s_mul_i32 s40, s35, 917504\n"
        "s_mul_i32 s37, s79, 896\n"
        "s_lshl_b32 s38, s36, 7\n"
        "s_add_u32 s40, s40, s37\n"
        "s_add_u32 s40, s40, s38\n"
        "s_add_u32 s41, s40, 14336\n"
        "s_add_u32 s42, s41, 14336\n"
        "s_add_u32 s43, s42, 14336\n"
        "s_add_u32 s44, s43, 14336\n"
        "s_add_u32 s45, s44, 14336\n"
        "s_add_u32 s46, s45, 14336\n"
        "s_add_u32 s47, s46, 14336\n"
        "s_lshl_b32 s75, s79, 4\n"
        "s_add_u32 s75, s75, s35\n"
        "s_lshl_b32 s75, s75, 3\n"
        "s_add_u32 s75, s75, s36\n"
        "s_lshl_b32 s75, s75, 11\n"
        "s_add_u32 s75, s75, 114688\n"
        "buffer_load_dwordx4 v[112:115], v249, s[8:11], s40 offen\n"
        "buffer_load_dwordx4 v[116:119], v249, s[8:11], s41 offen\n"
        "buffer_load_dwordx4 v[120:123], v249, s[8:11], s42 offen\n"
        "buffer_load_dwordx4 v[124:127], v249, s[8:11], s43 offen\n"
        "buffer_load_dwordx4 v[128:131], v249, s[8:11], s44 offen\n"
        "buffer_load_dwordx4 v[132:135], v249, s[8:11], s45 offen\n"
        "buffer_load_dwordx4 v[136:139], v249, s[8:11], s46 offen\n"
        "buffer_load_dwordx4 v[140:143], v249, s[8:11], s47 offen\n"
        "v_mov_b32 v210, s34\n"
        "v_mov_b32 v211, s39\n"
        "v_mov_b32 v214, s34\n"
        "v_mov_b32 v215, s39\n"
        "s_waitcnt vmcnt(0)\n"
        "v_lshl_add_u64 v[140:141], v[140:141], 4, v[136:137]\n"
        "v_lshl_add_u64 v[142:143], v[142:143], 4, v[138:139]\n"
        "v_lshl_add_u64 v[140:141], v[140:141], 4, v[132:133]\n"
        "v_lshl_add_u64 v[142:143], v[142:143], 4, v[134:135]\n"
        "v_lshl_add_u64 v[140:141], v[140:141], 4, v[128:129]\n"
        "v_lshl_add_u64 v[142:143], v[142:143], 4, v[130:131]\n"
        "v_lshl_add_u64 v[140:141], v[140:141], 4, v[124:125]\n"
        "v_lshl_add_u64 v[142:143], v[142:143], 4, v[126:127]\n"
        "v_lshl_add_u64 v[140:141], v[140:141], 4, v[120:121]\n"
        "v_lshl_add_u64 v[142:143], v[142:143], 4, v[122:123]\n"
        "v_lshl_add_u64 v[140:141], v[140:141], 4, v[116:117]\n"
        "v_lshl_add_u64 v[142:143], v[142:143], 4, v[118:119]\n"
        "v_lshl_add_u64 v[208:209], v[140:141], 4, v[112:113]\n"
        "v_lshl_add_u64 v[212:213], v[142:143], 4, v[114:115]\n"
        "s_nop 0\n"
        "v_permlane32_swap_b32 v208, v209\n"
        "v_permlane32_swap_b32 v212, v213\n"
        "buffer_store_dwordx4 v[208:211], v250, s[16:19], s75 offen sc1\n"
        "buffer_store_dwordx4 v[212:215], v251, s[16:19], s75 offen offset:1024 sc1\n"
        "s_waitcnt vmcnt(0)\n"
        "s_add_u32 s78, s78, 1\n"
        "s_cmp_lt_u32 s78, 3\n"
        "s_cbranch_scc1 1b\n"
        "2:\n"
        "s_sleep 1\n"
        "buffer_load_dwordx4 v[208:211], v232, s[16:19], s76 offen sc1\n"
        "buffer_load_dwordx4 v[212:215], v233, s[16:19], s76 offen sc1\n"
        "buffer_load_dwordx4 v[216:219], v234, s[16:19], s76 offen sc1\n"
        "buffer_load_dwordx4 v[220:223], v235, s[16:19], s76 offen sc1\n"
        "s_waitcnt vmcnt(0)\n"
        "v_bitop3_b32 v210, v210, v214, s34 bitop3:0x7e\n"
        "v_bitop3_b32 v211, v211, v215, s39 bitop3:0x7e\n"
        "v_bitop3_b32 v218, v218, v222, s34 bitop3:0x7e\n"
        "v_bitop3_b32 v219, v219, v223, s39 bitop3:0x7e\n"
        "v_or3_b32 v210, v210, v211, v218\n"
        "v_or_b32 v210, v210, v219\n"
        "v_cmp_ne_u32 vcc, 0, v210\n"
        "s_cbranch_vccnz 3b\n"
        "4:\n"
        "v_perm_b32 v224, v208, v236, s60\n"
        "v_perm_b32 v225, v208, v236, s61\n"
        "v_perm_b32 v226, v208, v236, s62\n"
        "v_perm_b32 v227, v208, v236, s63\n"
        "ds_read_b32 v112, v224\n"
        "v_perm_b32 v224, v209, v236, s60\n"
        "ds_read_b32 v113, v225\n"
        "v_perm_b32 v225, v209, v236, s61\n"
        "ds_read_b32 v114, v226\n"
        "v_perm_b32 v226, v209, v236, s62\n"
        "ds_read_b32 v115, v227\n"
        "v_perm_b32 v227, v209, v236, s63\n"
        "ds_read_b32 v116, v224\n"
        "v_perm_b32 v224, v212, v236, s60\n"
        "ds_read_b32 v117, v225\n"
        "v_perm_b32 v225, v212, v236, s61\n"
        "ds_read_b32 v118, v226\n"
        "v_perm_b32 v226, v212, v236, s62\n"
        "ds_read_b32 v119, v227\n"
        "v_perm_b32 v227, v212, v236, s63\n"
        "ds_read_b32 v120, v224\n"
        "v_perm_b32 v224, v213, v236, s60\n"
        "ds_read_b32 v121, v225\n"
        "v_perm_b32 v225, v213, v236, s61\n"
        "ds_read_b32 v122, v226\n"
        "v_perm_b32 v226, v213, v236, s62\n"
        "ds_read_b32 v123, v227\n"
        "v_perm_b32 v227, v213, v236, s63\n"
        "ds_read_b32 v124, v224\n"
        "v_perm_b32 v224, v216, v236, s60\n"
        "ds_read_b32 v125, v225\n"
        "v_perm_b32 v225, v216, v236, s61\n"
        "ds_read_b32 v126, v226\n"
        "v_perm_b32 v226, v216, v236, s62\n"
        "ds_read_b32 v127, v227\n"
        "v_perm_b32 v227, v216, v236, s63\n"
        "ds_read_b32 v128, v224\n"
        "v_perm_b32 v224, v217, v236, s60\n"
        "ds_read_b32 v129, v225\n"
        "v_perm_b32 v225, v217, v236, s61\n"
        "ds_read_b32 v130, v226\n"
        "v_perm_b32 v226, v217, v236, s62\n"
        "ds_read_b32 v131, v227\n"
        "v_perm_b32 v227, v217, v236, s63\n"
        "ds_read_b32 v132, v224\n"
        "v_perm_b32 v224, v220, v236, s60\n"
        "ds_read_b32 v133, v225\n"
        "v_perm_b32 v225, v220, v236, s61\n"
        "ds_read_b32 v134, v226\n"
        "v_perm_b32 v226, v220, v236, s62\n"
        "ds_read_b32 v135, v227\n"
        "v_perm_b32 v227, v220, v236, s63\n"
        "ds_read_b32 v136, v224\n"
        "v_perm_b32 v224, v221, v236, s60\n"
        "ds_read_b32 v137, v225\n"
        "v_perm_b32 v225, v221, v236, s61\n"
        "ds_read_b32 v138, v226\n"
        "v_perm_b32 v226, v221, v236, s62\n"
        "ds_read_b32 v139, v227\n"
        "v_perm_b32 v227, v221, v236, s63\n"
        "ds_read_b32 v140, v224\n"
        "ds_read_b32 v141, v225\n"
        "ds_read_b32 v142, v226\n"
        "ds_read_b32 v143, v227\n"
        "s_waitcnt lgkmcnt(0)\n"
        "s_add_u32 s70, s70, 4\n"
        "s_addc_u32 s71, s71, 0\n"
        "s_setpc_b64 s[70:71]\n"
        ".Ln_rec1:\n"
        "s_waitcnt lgkmcnt(0)\n"
        "s_lshl_b32 s76, s77, 14\n"
        "s_add_u32 s76, s76, s64\n"
        "3:\n"
        "s_add_u32 s73, s73, 1\n"
        "s_cmp_lt_u32 s73, s74\n"
        "s_cbranch_scc1 2f\n"
        "s_mov_b32 s74, 0\n"
        "s_mov_b32 s78, 0\n"
        "1:\n"
        "s_lshl_b32 s72, s66, 2\n"
        "s_add_u32 s72, s72, 2\n"
        "s_cmp_eq_u32 s78, 0\n"
        "s_cselect_b32 s72, s68, s72\n"
        "s_cmp_eq_u32 s78, 2\n"
        "s_cselect_b32 s72, 3, s72\n"
        "s_lshl_b32 s35, s77, 1\n"
        "s_lshr_b32 s36, s33, 3\n"
        "s_add_u32 s35, s35, s36\n"
        "s_mul_i32 s35, s35, 7\n"
        "s_add_u32 s72, s72, s35\n"
        "s_mul_i32 s35, s72, 9363\n"
        "s_lshr_b32 s35, s35, 16\n"
        "s_mul_i32 s36, s35, 7\n"
        "s_sub_u32 s36, s72, s36\n"
        "s_and_b32 s79, s35, 1\n"
        "s_lshl_b32 s79, s79, 3\n"
        "s_and_b32 s37, s33, 7\n"
        "s_or_b32 s79, s79, s37\n"
        "s_lshr_b32 s35, s35, 1\n"
        "s_mul_i32 s40, s35, 917504\n"
        "s_mul_i32 s37, s79, 896\n"
        "s_lshl_b32 s38, s36, 7\n"
        "s_add_u32 s40, s40, s37\n"
        "s_add_u32 s40, s40, s38\n"
        "s_add_u32 s41, s40, 14336\n"
        "s_add_u32 s42, s41, 14336\n"
        "s_add_u32 s43, s42, 14336\n"
        "s_add_u32 s44, s43, 14336\n"
        "s_add_u32 s45, s44, 14336\n"
        "s_add_u32 s46, s45, 14336\n"
        "s_add_u32 s47, s46, 14336\n"
        "s_lshl_b32 s75, s79, 4\n"
        "s_add_u32 s75, s75, s35\n"
        "s_lshl_b32 s75, s75, 3\n"
        "s_add_u32 s75, s75, s36\n"
        "s_lshl_b32 s75, s75, 11\n"
        "s_add_u32 s75, s75, 114688\n"
        "buffer_load_dwordx4 v[144:147], v249, s[8:11], s40 offen\n"
        "buffer_load_dwordx4 v[148:151], v249, s[8:11], s41 offen\n"
        "buffer_load_dwordx4 v[152:155], v249, s[8:11], s42 offen\n"
        "buffer_load_dwordx4 v[156:159], v249, s[8:11], s43 offen\n"
        "buffer_load_dwordx4 v[160:163], v249, s[8:11], s44 offen\n"
        "buffer_load_dwordx4 v[164:167], v249, s[8:11], s45 offen\n"
        "buffer_load_dwordx4 v[168:171], v249, s[8:11], s46 offen\n"
        "buffer_load_dwordx4 v[172:175], v249, s[8:11], s47 offen\n"
        "v_mov_b32 v210, s34\n"
        "v_mov_b32 v211, s39\n"
        "v_mov_b32 v214, s34\n"
        "v_mov_b32 v215, s39\n"
        "s_waitcnt vmcnt(0)\n"
        "v_lshl_add_u64 v[172:173], v[172:173], 4, v[168:169]\n"
        "v_lshl_add_u64 v[174:175], v[174:175], 4, v[170:171]\n"
        "v_lshl_add_u64 v[172:173], v[172:173], 4, v[164:165]\n"
        "v_lshl_add_u64 v[174:175], v[174:175], 4, v[166:167]\n"
        "v_lshl_add_u64 v[172:173], v[172:173], 4, v[160:161]\n"
        "v_lshl_add_u64 v[174:175], v[174:175], 4, v[162:163]\n"
        "v_lshl_add_u64 v[172:173], v[172:173], 4, v[156:157]\n"
        "v_lshl_add_u64 v[174:175], v[174:175], 4, v[158:159]\n"
        "v_lshl_add_u64 v[172:173], v[172:173], 4, v[152:153]\n"
        "v_lshl_add_u64 v[174:175], v[174:175], 4, v[154:155]\n"
        "v_lshl_add_u64 v[172:173], v[172:173], 4, v[148:149]\n"
        "v_lshl_add_u64 v[174:175], v[174:175], 4, v[150:151]\n"
        "v_lshl_add_u64 v[208:209], v[172:173], 4, v[144:145]\n"
        "v_lshl_add_u64 v[212:213], v[174:175], 4, v[146:147]\n"
        "s_nop 0\n"
        "v_permlane32_swap_b32 v208, v209\n"
        "v_permlane32_swap_b32 v212, v213\n"
        "buffer_store_dwordx4 v[208:211], v250, s[16:19], s75 offen sc1\n"
        "buffer_store_dwordx4 v[212:215], v251, s[16:19], s75 offen offset:1024 sc1\n"
        "s_waitcnt vmcnt(0)\n"
        "s_add_u32 s78, s78, 1\n"
        "s_cmp_lt_u32 s78, 3\n"
        "s_cbranch_scc1 1b\n"
        "2:\n"
        "s_sleep 1\n"
        "buffer_load_dwordx4 v[208:211], v232, s[16:19], s76 offen sc1\n"
        "buffer_load_dwordx4 v[212:215], v233, s[16:19], s76 offen sc1\n"
        "buffer_load_dwordx4 v[216:219], v234, s[16:19], s76 offen sc1\n"
        "buffer_load_dwordx4 v[220:223], v235, s[16:19], s76 offen sc1\n"
        "s_waitcnt vmcnt(0)\n"
        "v_bitop3_b32 v210, v210, v214, s34 bitop3:0x7e\n"
        "v_bitop3_b32 v211, v211, v215, s39 bitop3:0x7e\n"
        "v_bitop3_b32 v218, v218, v222, s34 bitop3:0x7e\n"
        "v_bitop3_b32 v219, v219, v223, s39 bitop3:0x7e\n"
        "v_or3_b32 v210, v210, v211, v218\n"
        "v_or_b32 v210, v210, v219\n"
        "v_cmp_ne_u32 vcc, 0, v210\n"
        "s_cbranch_vccnz 3b\n"
        "4:\n"
        "v_perm_b32 v224, v208, v236, s60\n"
        "v_perm_b32 v225, v208, v236, s61\n"
        "v_perm_b32 v226, v208, v236, s62\n"
        "v_perm_b32 v227, v208, v236, s63\n"
        "ds_read_b32 v144, v224\n"
        "v_perm_b32 v224, v209, v236, s60\n"
        "ds_read_b32 v145, v225\n"
        "v_perm_b32 v225, v209, v236, s61\n"
        "ds_read_b32 v146, v226\n"
        "v_perm_b32 v226, v209, v236, s62\n"
        "ds_read_b32 v147, v227\n"
        "v_perm_b32 v227, v209, v236, s63\n"
        "ds_read_b32 v148, v224\n"
        "v_perm_b32 v224, v212, v236, s60\n"
        "ds_read_b32 v149, v225\n"
        "v_perm_b32 v225, v212, v236, s61\n"
        "ds_read_b32 v150, v226\n"
        "v_perm_b32 v226, v212, v236, s62\n"
        "ds_read_b32 v151, v227\n"
        "v_perm_b32 v227, v212, v236, s63\n"
        "ds_read_b32 v152, v224\n"
        "v_perm_b32 v224, v213, v236, s60\n"
        "ds_read_b32 v153, v225\n"
        "v_perm_b32 v225, v213, v236, s61\n"
        "ds_read_b32 v154, v226\n"
        "v_perm_b32 v226, v213, v236, s62\n"
        "ds_read_b32 v155, v227\n"
        "v_perm_b32 v227, v213, v236, s63\n"
        "ds_read_b32 v156, v224\n"
        "v_perm_b32 v224, v216, v236, s60\n"
        "ds_read_b32 v157, v225\n"
        "v_perm_b32 v225, v216, v236, s61\n"
        "ds_read_b32 v158, v226\n"
        "v_perm_b32 v226, v216, v236, s62\n"
        "ds_read_b32 v159, v227\n"
        "v_perm_b32 v227, v216, v236, s63\n"
        "ds_read_b32 v160, v224\n"
        "v_perm_b32 v224, v217, v236, s60\n"
        "ds_read_b32 v161, v225\n"
        "v_perm_b32 v225, v217, v236, s61\n"
        "ds_read_b32 v162, v226\n"
        "v_perm_b32 v226, v217, v236, s62\n"
        "ds_read_b32 v163, v227\n"
        "v_perm_b32 v227, v217, v236, s63\n"
        "ds_read_b32 v164, v224\n"
        "v_perm_b32 v224, v220, v236, s60\n"
        "ds_read_b32 v165, v225\n"
        "v_perm_b32 v225, v220, v236, s61\n"
        "ds_read_b32 v166, v226\n"
        "v_perm_b32 v226, v220, v236, s62\n"
        "ds_read_b32 v167, v227\n"
        "v_perm_b32 v227, v220, v236, s63\n"
        "ds_read_b32 v168, v224\n"
        "v_perm_b32 v224, v221, v236, s60\n"
        "ds_read_b32 v169, v225\n"
        "v_perm_b32 v225, v221, v236, s61\n"
        "ds_read_b32 v170, v226\n"
        "v_perm_b32 v226, v221, v236, s62\n"
        "ds_read_b32 v171, v227\n"
        "v_perm_b32 v227, v221, v236, s63\n"
        "ds_read_b32 v172, v224\n"
        "ds_read_b32 v173, v225\n"
        "ds_read_b32 v174, v226\n"
        "ds_read_b32 v175, v227\n"
        "s_waitcnt lgkmcnt(0)\n"
        "s_add_u32 s70, s70, 4\n"
        "s_addc_u32 s71, s71, 0\n"
        "s_setpc_b64 s[70:71]\n"
        ".Ln_ret:\n"
        "s_setpc_b64 s[70:71]\n"
        ".Ln_end:\n"
        :: "{s[0:1]}"((u64)__builtin_amdgcn_dispatch_ptr()), "{s[2:3]}"((u64)__builtin_amdgcn_kernarg_segment_ptr()), "{s[4:5]}"(llvm_dispatch_id()),
           "{s6}"(blockIdx.x), "{v0}"(threadIdx.x), "s"((unsigned)(size_t)(__attribute__((address_space(3))) unsigned char*)smem)
        : "v1", "v2", "v3", "v4", "v5", "v6", "v7", "v8", "v9", "v10", "v11", "v12", "v13", "v14", "v15", "v16", "v17", "v18", "v19", "v20", "v21", "v22", "v23", "v24", "v25", "v26", "v27", "v28", "v29", "v30", "v31", "v32", "v33", "v34", "v35", "v36", "v37", "v38", "v39", "v40", "v41", "v42", "v43", "v44", "v45", "v46", "v47", "v48", "v49", "v50", "v51", "v52", "v53", "v54", "v55", "v56", "v57", "v58", "v59", "v60", "v61", "v62", "v63", "v64", "v65", "v66", "v67", "v68", "v69", "v70", "v71", "v72", "v73", "v74", "v75", "v76", "v77", "v78", "v79", "v80", "v81", "v82", "v83", "v84", "v85", "v86", "v87", "v88", "v89", "v90", "v91", "v92", "v93", "v94", "v95", "v96", "v97", "v98", "v99", "v100", "v101", "v102", "v103", "v104", "v105", "v106", "v107", "v108", "v109", "v110", "v111", "v112", "v113", "v114", "v115", "v116", "v117", "v118", "v119", "v120", "v121", "v122", "v123", "v124", "v125", "v126", "v127", "v128", "v129", "v130", "v131", "v132", "v133", "v134", "v135", "v136", "v137", "v138", "v139", "v140", "v141", "v142", "v143", "v144", "v145", "v146", "v147", "v148", "v149", "v150", "v151", "v152", "v153", "v154", "v155", "v156", "v157", "v158", "v159", "v160", "v161", "v162", "v163", "v164", "v165", "v166", "v167", "v168", "v169", "v170", "v171", "v172", "v173", "v174", "v175", "v176", "v177", "v178", "v179", "v180", "v181", "v182", "v183", "v184", "v185", "v186", "v187", "v188", "v189", "v190", "v191", "v192", "v193", "v194", "v195", "v196", "v197", "v198", "v199", "v200", "v201", "v202", "v203", "v204", "v205", "v206", "v207", "v208", "v209", "v210", "v211", "v212", "v213", "v214", "v215", "v216", "v217", "v218", "v219", "v220", "v221", "v222", "v223", "v224", "v225", "v226", "v227", "v228", "v229", "v230", "v231", "v232", "v233", "v234", "v235", "v236", "v237", "v238", "v239", "v240", "v241", "v242", "v243", "v244", "v245", "v246", "v247", "v248", "v249", "v250", "v251", "v252", "v253", "v254", "v255", "s7", "s8", "s9", "s10", "s11", "s12", "s13", "s14", "s15", "s16", "s17", "s18", "s19", "s20", "s21", "s22", "s23", "s24", "s25", "s26", "s27", "s28", "s29", "s30", "s31", "s32", "s33", "s34", "s35", "s36", "s37", "s38", "s39", "s40", "s41", "s42", "s43", "s44", "s45", "s46", "s47", "s48", "s49", "s50", "s51", "s52", "s53", "s54", "s55", "s56", "s57", "s58", "s59", "s60", "s61", "s62", "s63", "s64", "s65", "s66", "s67", "s68", "s69", "s70", "s71", "s72", "s73", "s74", "s75", "s76", "s77", "s78", "s79", "s80", "s81", "s82", "s83", "s84", "s85", "s86", "s87", "s88", "s89", "s90", "s91", "s92", "s93", "s94", "s95", "vcc", "scc", "memory");
}
}

extern "C" void kernel_launch(void* const* d_in, const int* in_sizes, int n_in, void* d_out, int out_size, void* d_ws, size_t ws_size, hipStream_t stream) {
    const u64 fl = (u64)0x00020000 << 32;
    nf4_gemm_kernel<<<dim3(256), dim3(512), 0, stream>>>(d_in[1], fl | (1024u * 3584u * 4u), d_in[0], fl | (2048u * 1024u * 2u), (char*)d_ws + (32u << 20), fl | ((4u << 20) + 114688u),
                                                         d_out, fl | (2048u * 3584u * 2u), d_in[2], d_in[3], d_ws);
}
```
